```python
import torch

def _softmax(x, axis=-1):
    return torch.softmax(x, dim=axis)

def _gelu(x):
    return torch.nn.functional.gelu(x, approximate='tanh')

def _silu(x):
    return torch.nn.functional.silu(x)

def _sigmoid(x):
    return torch.sigmoid(x)

def _layernorm(x, axis=-1, eps=1e-05):
    m = x.mean(dim=axis, keepdim=True)
    v = x.var(dim=axis, keepdim=True, unbiased=False)
    return (x - m) / torch.sqrt(v + eps)

def _scatter_add(tgt, idx, src, axis=0):
    return tgt.clone().index_add_(axis, idx, src)

def _segment_sum(src, seg, n):
    out = torch.zeros((n,) + src.shape[1:], dtype=src.dtype)
    return out.index_add_(0, seg, src)

def _pad(x, pw):
    flat = tuple((v for pair in reversed(pw) for v in pair))
    return torch.nn.functional.pad(x, flat)
kernel_name = 'recurrent_scan__mha_block_cached__mixer_gmlp__prompt'

def setup_inputs():

    _w = lambda a: (lambda t: t.to(torch.bfloat16) if t.is_floating_point() else t)(torch.as_tensor(a))

    def _A_setup():
        B, Sq, D = (1, 2048, 448)
        H, Sk, Dh = (16, 2048, 256)
        return {'x': _w((lambda *s: torch.randn(*s, dtype=torch.bfloat16) * 0.05)(B, Sq, D)), 'k_cache': _w((lambda *s: torch.randn(*s, dtype=torch.bfloat16) * 0.8)(B, H, Sk, Dh)), 'v_cache': _w((lambda *s: torch.randn(*s, dtype=torch.bfloat16) * 0.2)(B, H, Sk, Dh)), 'wq': _w((lambda *s: torch.randn(*s, dtype=torch.bfloat16) * 0.8)(D, H * Dh) * D ** (-0.5)), 'wo': _w((lambda *s: torch.randn(*s, dtype=torch.bfloat16) * 0.2)(H * Dh, D) * (H * Dh) ** (-0.5))}
    _A_in = _A_setup()

    def _B_setup():
        B, S, D, F = (1, 2048, 448, 1792)
        Fh = F // 2
        return {'x': _w((lambda *s: torch.randn(*s, dtype=torch.bfloat16) * 0.05)(B, S, D)), 'w_in': _w((lambda *s: torch.randn(*s, dtype=torch.bfloat16) * 0.4)(D, 2 * Fh)), 'w_sp': _w(0.01 * (lambda *s: torch.randn(*s, dtype=torch.bfloat16) * 0.4)(S, S)), 'b_sp': _w(1.0 + 0.01 * (lambda *s: torch.randn(*s, dtype=torch.bfloat16) * 0.1)(S)), 'w_out': _w((lambda *s: torch.randn(*s, dtype=torch.bfloat16) * 0.1)(Fh, D))}
    _B_in = _B_setup()
    return {'A_x': _A_in['x'], 'A_k_cache': _A_in['k_cache'], 'A_v_cache': _A_in['v_cache'], 'A_wq': _A_in['wq'], 'A_wo': _A_in['wo'], 'B_x': _B_in['x'], 'B_w_in': _B_in['w_in'], 'B_w_sp': _B_in['w_sp'], 'B_b_sp': _B_in['b_sp'], 'B_w_out': _B_in['w_out']}

def reference(A_x, A_k_cache, A_v_cache, A_wq, A_wo, B_x, B_w_in, B_w_sp, B_b_sp, B_w_out):

    def _A_ref(x, k_cache, v_cache, wq, wo):
        B, Sq, D = x.shape
        H, Dh = (16, 256)
        q = (lambda x, *d: x.permute(*d))((x @ wq).reshape(B, Sq, H, Dh), 0, 2, 1, 3)
        scores = torch.einsum('bhqd,bhkd->bhqk', q, k_cache) * Dh ** (-0.5)
        p = _softmax(scores, axis=-1)
        o = torch.einsum('bhqk,bhkd->bhqd', p, v_cache)
        return (lambda x, *d: x.permute(*d))(o, 0, 2, 1, 3).reshape(B, Sq, H * Dh) @ wo

    def _B_ref(x, w_in, w_sp, b_sp, w_out):
        B, S, D = x.shape
        F = w_in.shape[-1]
        Fh = F // 2
        z = _gelu(torch.einsum('bsd,df->bsf', x, w_in))
        z1 = z[..., :Fh].clone()
        z2 = _layernorm(z[..., Fh:], axis=-1)
        g = torch.einsum('ts,bsf->btf', w_sp, z2) + b_sp[None, :, None]
        return torch.einsum('bsf,fd->bsd', z1 * g, w_out)
    x = A_x
    xn = _layernorm(x, axis=-1)
    s = _A_ref(xn, A_k_cache, A_v_cache, A_wq, A_wo)
    g = _silu(_B_ref(xn, B_w_in, B_w_sp, B_b_sp, B_w_out))
    return x + s * g
```

```cpp
#include <hip/hip_runtime.h>
#include <hip/hip_bf16.h>
#include <stdint.h>
typedef unsigned short u16;
typedef __attribute__((ext_vector_type(4))) float f32x4;
typedef __attribute__((ext_vector_type(2))) float f32x2;
typedef __attribute__((ext_vector_type(4))) unsigned u32x4;
typedef __attribute__((ext_vector_type(2))) unsigned u32x2;
typedef __attribute__((ext_vector_type(2))) __bf16 bf16x2;
#define DI __device__ __forceinline__
DI unsigned cvtpk(float lo, float hi) { f32x2 v = {lo, hi}; bf16x2 b = __builtin_convertvector(v, bf16x2); return __builtin_bit_cast(unsigned, b); }
DI float lo16(unsigned u) { return __uint_as_float(u << 16); }
DI float hi16(unsigned u) { return __uint_as_float(u & 0xffff0000u); }
DI float xsum4(float v) {
  auto a = __builtin_amdgcn_permlane32_swap(__float_as_uint(v), __float_as_uint(v), false, false);
  v = __uint_as_float(a[0]) + __uint_as_float(a[1]);
  auto b = __builtin_amdgcn_permlane16_swap(__float_as_uint(v), __float_as_uint(v), false, false);
  return __uint_as_float(b[0]) + __uint_as_float(b[1]);
}
DI void st8(void* p, u32x2 v) { asm volatile("global_store_dwordx2 %0, %1, off sc1" :: "v"(p), "v"(v) : "memory"); }
DI float gelu_tanh(float x) {
  const float t = x * x;
  const float p = __builtin_fmaf(t, -0.10294324f, -2.3022082f);
  const float e = __builtin_amdgcn_exp2f(x * p);
  return x * __builtin_amdgcn_rcpf(1.f + e);
}

#define STAMP(i)
#define WS_STATS  65536u
#define WS_Z1     (24u << 20)
#define WS_Z2N    (28u << 20)

__global__ __launch_bounds__(256, 1)
void k1(const u16* __restrict__ K, const u16* __restrict__ V, const u16* __restrict__ wq, const u16* __restrict__ w_in, char* __restrict__ ws,
        const u16* __restrict__ w_sp, const u16* __restrict__ b_sp, const u16* __restrict__ wo, const u16* __restrict__ w_out, const u16* __restrict__ x, u16* __restrict__ out) {
  extern __shared__ __attribute__((aligned(16))) char lds[];
  const unsigned tid = threadIdx.x, lane = tid & 63u, r = lane & 31u, h = lane >> 5, r16 = lane & 15u, g = lane >> 4;
  const unsigned w = __builtin_amdgcn_readfirstlane(tid >> 6);
  const unsigned b = blockIdx.x, xg = b & 7u, idx = b >> 3, head = 2u * xg + (idx >> 4), qb = idx & 15u;
  const unsigned zs = (xg>>1)*2u+(idx>>4)+8u*(xg&1u);
  const unsigned ldsb = (unsigned)(size_t)lds;
  const unsigned m0b = ldsb + w * 4096u;
  const unsigned q4 = r16 >> 2, p4 = lane & 3u;
  unsigned vwi[4], vwq[4], vok[4], vov[4];
#pragma unroll
  for (int i = 0; i < 4; ++i) {
    { const unsigned row = 16u * w + 4u * i + g, swz = g | ((unsigned)(i >> 1) << 2);
      const unsigned cs = r16 ^ (swz << 1);
      vwi[i] = cs < 14u ? row * 3584u + 224u * zs + (cs << 4) - 1024u * i : 0x40000000u; }
    { const unsigned row = 8u * w + 2u * i + h, swz = (row & 3u) | ((row >> 1) & 4u);
      vwq[i] = row * 8192u + 512u * head + ((r ^ (swz << 1)) << 4) - 1024u * i; }
    vok[i] = (8u * w + h) * 512u + ((r ^ ((8u * (w & 1u) + 2u * i + h) & 15u)) << 4);
    vov[i] = (8u * w + h) * 512u + ((r ^ (((2u * i + h) & 7u) << 1)) << 4);
  }
  const unsigned swzn = q4 | ((g & 1u) << 2);
  const unsigned vbi = ldsb + (8u * g + q4) * 256u + ((2u * swzn + (p4 >> 1)) << 4) + 8u * (p4 & 1u);
  const unsigned vbq = ldsb + (8u * g + q4) * 512u + ((2u * swzn + (p4 >> 1)) << 4) + 8u * (p4 & 1u);
  const unsigned vl16 = lane * 16u;
  const unsigned rot = qb % 7u;
  const unsigned cpi = (idx * 4u + w) & 15u;
  const char* kp = (const char*)K + (size_t)head * 1048576u;
  const char* vp = (const char*)V + (size_t)head * 1048576u;
  STAMP(0);
  const unsigned row0 = qb * 128u + w * 32u + r16;
  const char* xp = (const char*)x + (size_t)(qb * 128u + w * 32u) * 896u;
  unsigned vx[4];
#pragma unroll
  for (int i = 0; i < 4; ++i) vx[i] = (8u * i + (lane >> 3)) * 896u + (((lane & 7u) ^ (4u * (i & 1) + g)) << 4) - 1024u * i;
  const unsigned vxr = m0b + r16 * 128u + ((g ^ (r16 >> 1)) << 4);
  const unsigned rw = b * 4u + w;
  const unsigned rwp = rw / 224u, rr_ = rw - rwp * 224u;
  const unsigned rt = rw < 896u ? rwp * 224u + (rr_ < 96u ? rr_ : (rr_ < 112u ? rr_ + 96u : (rr_ < 208u ? rr_ - 16u : rr_))) : 0x100000u;
  const unsigned sl14 = lane / 14u, cc14 = lane - sl14 * 14u;
  const unsigned vrs = lane < 56u ? lane * 16u : 0x40000000u;
  const unsigned vrd0 = lane < 56u ? sl14 * (4096u * 224u) + cc14 * 16u : 0x40000000u, vrd1 = lane < 56u ? sl14 * (896u * 224u) + cc14 * 16u : 0x40000000u;
#define PLO(p) ((unsigned)(size_t)(p))
#define PHI(p) ((unsigned)((size_t)(p) >> 32))
  unsigned tlo, thi; float rsd[2];
  f32x4 za[14], qa[32];
  asm volatile(
    "s_waitcnt lgkmcnt(0)\n"
    "s_nop 4\n"
    "s_mov_b32 s64, %[xlo]\n"
    "s_and_b32 s65, %[xhi], 0xffff\n"
    "s_mov_b32 s66, 28672\n"
    "s_mov_b32 s67, 0x20000\n"
    "s_add_u32 m0, %[m0b], 49152\n"
    "s_add_u32 s68, %[rot], 0\n"
    "s_sub_u32 s52, s68, 7\n"
    "s_cmp_ge_u32 s68, 7\n"
    "s_cselect_b32 s68, s52, s68\n"
    "s_lshl_b32 s69, s68, 7\n"
    "buffer_load_dwordx4 %[vx0], s[64:67], s69 offen offset:0 lds\n"
    "buffer_load_dwordx4 %[vx1], s[64:67], s69 offen offset:1024 lds\n"
    "buffer_load_dwordx4 %[vx2], s[64:67], s69 offen offset:2048 lds\n"
    "buffer_load_dwordx4 %[vx3], s[64:67], s69 offen offset:3072 lds\n"
    "s_add_u32 m0, %[m0b], 65536\n"
    "s_add_u32 s68, %[rot], 1\n"
    "s_sub_u32 s52, s68, 7\n"
    "s_cmp_ge_u32 s68, 7\n"
    "s_cselect_b32 s68, s52, s68\n"
    "s_lshl_b32 s69, s68, 7\n"
    "buffer_load_dwordx4 %[vx0], s[64:67], s69 offen offset:0 lds\n"
    "buffer_load_dwordx4 %[vx1], s[64:67], s69 offen offset:1024 lds\n"
    "buffer_load_dwordx4 %[vx2], s[64:67], s69 offen offset:2048 lds\n"
    "buffer_load_dwordx4 %[vx3], s[64:67], s69 offen offset:3072 lds\n"
    "s_add_u32 m0, %[m0b], 81920\n"
    "s_add_u32 s68, %[rot], 2\n"
    "s_sub_u32 s52, s68, 7\n"
    "s_cmp_ge_u32 s68, 7\n"
    "s_cselect_b32 s68, s52, s68\n"
    "s_lshl_b32 s69, s68, 7\n"
    "buffer_load_dwordx4 %[vx0], s[64:67], s69 offen offset:0 lds\n"
    "buffer_load_dwordx4 %[vx1], s[64:67], s69 offen offset:1024 lds\n"
    "buffer_load_dwordx4 %[vx2], s[64:67], s69 offen offset:2048 lds\n"
    "buffer_load_dwordx4 %[vx3], s[64:67], s69 offen offset:3072 lds\n"
    "s_add_u32 m0, %[m0b], 98304\n"
    "s_add_u32 s68, %[rot], 3\n"
    "s_sub_u32 s52, s68, 7\n"
    "s_cmp_ge_u32 s68, 7\n"
    "s_cselect_b32 s68, s52, s68\n"
    "s_lshl_b32 s69, s68, 7\n"
    "buffer_load_dwordx4 %[vx0], s[64:67], s69 offen offset:0 lds\n"
    "buffer_load_dwordx4 %[vx1], s[64:67], s69 offen offset:1024 lds\n"
    "buffer_load_dwordx4 %[vx2], s[64:67], s69 offen offset:2048 lds\n"
    "buffer_load_dwordx4 %[vx3], s[64:67], s69 offen offset:3072 lds\n"
    "s_add_u32 m0, %[m0b], 114688\n"
    "s_add_u32 s68, %[rot], 4\n"
    "s_sub_u32 s52, s68, 7\n"
    "s_cmp_ge_u32 s68, 7\n"
    "s_cselect_b32 s68, s52, s68\n"
    "s_lshl_b32 s69, s68, 7\n"
    "buffer_load_dwordx4 %[vx0], s[64:67], s69 offen offset:0 lds\n"
    "buffer_load_dwordx4 %[vx1], s[64:67], s69 offen offset:1024 lds\n"
    "buffer_load_dwordx4 %[vx2], s[64:67], s69 offen offset:2048 lds\n"
    "buffer_load_dwordx4 %[vx3], s[64:67], s69 offen offset:3072 lds\n"
    "s_add_u32 m0, %[m0b], 131072\n"
    "s_add_u32 s68, %[rot], 5\n"
    "s_sub_u32 s52, s68, 7\n"
    "s_cmp_ge_u32 s68, 7\n"
    "s_cselect_b32 s68, s52, s68\n"
    "s_lshl_b32 s69, s68, 7\n"
    "buffer_load_dwordx4 %[vx0], s[64:67], s69 offen offset:0 lds\n"
    "buffer_load_dwordx4 %[vx1], s[64:67], s69 offen offset:1024 lds\n"
    "buffer_load_dwordx4 %[vx2], s[64:67], s69 offen offset:2048 lds\n"
    "buffer_load_dwordx4 %[vx3], s[64:67], s69 offen offset:3072 lds\n"
    "s_add_u32 m0, %[m0b], 147456\n"
    "s_add_u32 s68, %[rot], 6\n"
    "s_sub_u32 s52, s68, 7\n"
    "s_cmp_ge_u32 s68, 7\n"
    "s_cselect_b32 s68, s52, s68\n"
    "s_lshl_b32 s69, s68, 7\n"
    "buffer_load_dwordx4 %[vx0], s[64:67], s69 offen offset:0 lds\n"
    "buffer_load_dwordx4 %[vx1], s[64:67], s69 offen offset:1024 lds\n"
    "buffer_load_dwordx4 %[vx2], s[64:67], s69 offen offset:2048 lds\n"
    "buffer_load_dwordx4 %[vx3], s[64:67], s69 offen offset:3072 lds\n"
    "s_mov_b32 s56, %[wilo]\n"
    "s_and_b32 s57, %[wihi], 0xffff\n"
    "s_mov_b32 s58, 1605632\n"
    "s_mov_b32 s59, 0x20000\n"
    "s_mov_b32 s60, %[wqlo]\n"
    "s_and_b32 s61, %[wqhi], 0xffff\n"
    "s_mov_b32 s62, 3670016\n"
    "s_mov_b32 s63, 0x20000\n"
    "s_mov_b32 s50, %[m0b]\n"
    "s_getpc_b64 s[40:41]\n"
    ".Lk1_pc:\n"
    "s_and_b32 s41, s41, 0xffff\n"
    "s_mov_b32 s42, .Lk1_code_end-.Lk1_pc\n"
    "s_mov_b32 s43, 0x20000\n"
    "s_lshl_b32 s48, %[cpi], 13\n"
    "v_lshlrev_b32 v199, 3, %[vl16]\n"
    "buffer_load_dword v199, v199, s[40:43], s48 offen\n"
    "s_add_u32 s68, %[rot], 0\n"
    "s_sub_u32 s52, s68, 7\n"
    "s_cmp_ge_u32 s68, 7\n"
    "s_cselect_b32 s68, s52, s68\n"
    "s_add_u32 m0, s50, 32768\n"
    "s_lshl_b32 s71, s68, 19\n"
    "buffer_load_dwordx4 %[vwq0], s[60:63], s71 offen offset:0 lds\n"
    "buffer_load_dwordx4 %[vwq1], s[60:63], s71 offen offset:1024 lds\n"
    "buffer_load_dwordx4 %[vwq2], s[60:63], s71 offen offset:2048 lds\n"
    "buffer_load_dwordx4 %[vwq3], s[60:63], s71 offen offset:3072 lds\n"
    "s_add_u32 s68, %[rot], 0\n"
    "s_sub_u32 s52, s68, 7\n"
    "s_cmp_ge_u32 s68, 7\n"
    "s_cselect_b32 s68, s52, s68\n"
    "s_add_u32 m0, s50, 0\n"
    "s_mul_i32 s71, s68, 229376\n"
    "buffer_load_dwordx4 %[vwi0], s[56:59], s71 offen offset:0 lds\n"
    "buffer_load_dwordx4 %[vwi1], s[56:59], s71 offen offset:1024 lds\n"
    "buffer_load_dwordx4 %[vwi2], s[56:59], s71 offen offset:2048 lds\n"
    "buffer_load_dwordx4 %[vwi3], s[56:59], s71 offen offset:3072 lds\n"
    "v_xor_b32 v200, 0, %[vbq]\n"
    "v_xor_b32 v201, 32, %[vbq]\n"
    "v_xor_b32 v202, 64, %[vbq]\n"
    "v_xor_b32 v203, 96, %[vbq]\n"
    "v_xor_b32 v204, 128, %[vbq]\n"
    "v_xor_b32 v205, 160, %[vbq]\n"
    "v_xor_b32 v206, 192, %[vbq]\n"
    "v_xor_b32 v207, 224, %[vbq]\n"
    "v_xor_b32 v224, 0, %[vbi]\n"
    "v_xor_b32 v225, 32, %[vbi]\n"
    "v_xor_b32 v226, 64, %[vbi]\n"
    "v_xor_b32 v227, 96, %[vbi]\n"
    "v_xor_b32 v228, 128, %[vbi]\n"
    "v_xor_b32 v229, 160, %[vbi]\n"
    "v_xor_b32 v230, 192, %[vbi]\n"
    "v_xor_b32 v208, 0, %[vbq]\n"
    "v_add_u32 v208, 65536, v208\n"
    "v_xor_b32 v209, 32, %[vbq]\n"
    "v_add_u32 v209, 65536, v209\n"
    "v_xor_b32 v210, 64, %[vbq]\n"
    "v_add_u32 v210, 65536, v210\n"
    "v_xor_b32 v211, 96, %[vbq]\n"
    "v_add_u32 v211, 65536, v211\n"
    "v_xor_b32 v212, 128, %[vbq]\n"
    "v_add_u32 v212, 65536, v212\n"
    "v_xor_b32 v213, 160, %[vbq]\n"
    "v_add_u32 v213, 65536, v213\n"
    "v_xor_b32 v214, 192, %[vbq]\n"
    "v_add_u32 v214, 65536, v214\n"
    "v_xor_b32 v215, 224, %[vbq]\n"
    "v_add_u32 v215, 65536, v215\n"
    "v_xor_b32 v231, 0, %[vbi]\n"
    "v_add_u32 v231, 65536, v231\n"
    "v_xor_b32 v232, 32, %[vbi]\n"
    "v_add_u32 v232, 65536, v232\n"
    "v_xor_b32 v233, 64, %[vbi]\n"
    "v_add_u32 v233, 65536, v233\n"
    "v_xor_b32 v234, 96, %[vbi]\n"
    "v_add_u32 v234, 65536, v234\n"
    "v_xor_b32 v235, 128, %[vbi]\n"
    "v_add_u32 v235, 65536, v235\n"
    "v_xor_b32 v236, 160, %[vbi]\n"
    "v_add_u32 v236, 65536, v236\n"
    "v_xor_b32 v237, 192, %[vbi]\n"
    "v_add_u32 v237, 65536, v237\n"
    "v_xor_b32 v216, 0, %[vbq]\n"
    "v_add_u32 v216, 131072, v216\n"
    "v_xor_b32 v217, 32, %[vbq]\n"
    "v_add_u32 v217, 131072, v217\n"
    "v_xor_b32 v218, 64, %[vbq]\n"
    "v_add_u32 v218, 131072, v218\n"
    "v_xor_b32 v219, 96, %[vbq]\n"
    "v_add_u32 v219, 131072, v219\n"
    "v_xor_b32 v220, 128, %[vbq]\n"
    "v_add_u32 v220, 131072, v220\n"
    "v_xor_b32 v221, 160, %[vbq]\n"
    "v_add_u32 v221, 131072, v221\n"
    "v_xor_b32 v222, 192, %[vbq]\n"
    "v_add_u32 v222, 131072, v222\n"
    "v_xor_b32 v223, 224, %[vbq]\n"
    "v_add_u32 v223, 131072, v223\n"
    "v_xor_b32 v238, 0, %[vbi]\n"
    "v_add_u32 v238, 131072, v238\n"
    "v_xor_b32 v239, 32, %[vbi]\n"
    "v_add_u32 v239, 131072, v239\n"
    "v_xor_b32 v240, 64, %[vbi]\n"
    "v_add_u32 v240, 131072, v240\n"
    "v_xor_b32 v241, 96, %[vbi]\n"
    "v_add_u32 v241, 131072, v241\n"
    "v_xor_b32 v242, 128, %[vbi]\n"
    "v_add_u32 v242, 131072, v242\n"
    "v_xor_b32 v243, 160, %[vbi]\n"
    "v_add_u32 v243, 131072, v243\n"
    "v_xor_b32 v244, 192, %[vbi]\n"
    "v_add_u32 v244, 131072, v244\n"
    "v_xor_b32 v184, 64, %[vxr]\n"
    "v_add_u32 v185, 0x10000, %[vxr]\n"
    "v_add_u32 v186, 0x10000, v184\n"
    "v_add_u32 v187, 0x20000, %[vxr]\n"
    "v_add_u32 v188, 0x20000, v184\n"
    "v_mov_b32 v192, 0x3f803f80\n"
    "v_mov_b32 v193, 0x3f803f80\n"
    "v_mov_b32 v194, 0x3f803f80\n"
    "v_mov_b32 v195, 0x3f803f80\n"
    "s_waitcnt vmcnt(33)\n"
    "ds_read_b128 a[0:3], %[vxr] offset:49152\n"
    "ds_read_b128 a[4:7], %[vxr] offset:51200\n"
    "ds_read_b128 a[8:11], v184 offset:49152\n"
    "ds_read_b128 a[12:15], v184 offset:51200\n"
    "s_waitcnt vmcnt(29)\n"
    "ds_read_b128 a[16:19], v185 offset:0\n"
    "ds_read_b128 a[20:23], v185 offset:2048\n"
    "ds_read_b128 a[24:27], v186 offset:0\n"
    "ds_read_b128 a[28:31], v186 offset:2048\n"
    "s_waitcnt lgkmcnt(4)\n"
    "v_mfma_f32_16x16x32_bf16 v[168:171], v[192:195], a[0:3], 0\n"
    "v_mfma_f32_16x16x32_bf16 v[172:175], v[192:195], a[4:7], 0\n"
    "v_mfma_f32_16x16x32_bf16 v[168:171], v[192:195], a[8:11], v[168:171]\n"
    "v_mfma_f32_16x16x32_bf16 v[172:175], v[192:195], a[12:15], v[172:175]\n"
    "s_waitcnt vmcnt(25)\n"
    "ds_read_b128 a[32:35], v185 offset:16384\n"
    "ds_read_b128 a[36:39], v185 offset:18432\n"
    "ds_read_b128 a[40:43], v186 offset:16384\n"
    "ds_read_b128 a[44:47], v186 offset:18432\n"
    "s_waitcnt lgkmcnt(4)\n"
    "v_mfma_f32_16x16x32_bf16 v[168:171], v[192:195], a[16:19], v[168:171]\n"
    "v_mfma_f32_16x16x32_bf16 v[172:175], v[192:195], a[20:23], v[172:175]\n"
    "v_mfma_f32_16x16x32_bf16 v[168:171], v[192:195], a[24:27], v[168:171]\n"
    "v_mfma_f32_16x16x32_bf16 v[172:175], v[192:195], a[28:31], v[172:175]\n"
    "s_waitcnt vmcnt(21)\n"
    "ds_read_b128 a[48:51], v185 offset:32768\n"
    "ds_read_b128 a[52:55], v185 offset:34816\n"
    "ds_read_b128 a[56:59], v186 offset:32768\n"
    "ds_read_b128 a[60:63], v186 offset:34816\n"
    "s_waitcnt lgkmcnt(4)\n"
    "v_mfma_f32_16x16x32_bf16 v[168:171], v[192:195], a[32:35], v[168:171]\n"
    "v_mfma_f32_16x16x32_bf16 v[172:175], v[192:195], a[36:39], v[172:175]\n"
    "v_mfma_f32_16x16x32_bf16 v[168:171], v[192:195], a[40:43], v[168:171]\n"
    "v_mfma_f32_16x16x32_bf16 v[172:175], v[192:195], a[44:47], v[172:175]\n"
    "s_waitcnt vmcnt(17)\n"
    "ds_read_b128 a[64:67], v185 offset:49152\n"
    "ds_read_b128 a[68:71], v185 offset:51200\n"
    "ds_read_b128 a[72:75], v186 offset:49152\n"
    "ds_read_b128 a[76:79], v186 offset:51200\n"
    "s_waitcnt lgkmcnt(4)\n"
    "v_mfma_f32_16x16x32_bf16 v[168:171], v[192:195], a[48:51], v[168:171]\n"
    "v_mfma_f32_16x16x32_bf16 v[172:175], v[192:195], a[52:55], v[172:175]\n"
    "v_mfma_f32_16x16x32_bf16 v[168:171], v[192:195], a[56:59], v[168:171]\n"
    "v_mfma_f32_16x16x32_bf16 v[172:175], v[192:195], a[60:63], v[172:175]\n"
    "s_waitcnt vmcnt(13)\n"
    "ds_read_b128 a[80:83], v187 offset:0\n"
    "ds_read_b128 a[84:87], v187 offset:2048\n"
    "ds_read_b128 a[88:91], v188 offset:0\n"
    "ds_read_b128 a[92:95], v188 offset:2048\n"
    "s_waitcnt lgkmcnt(4)\n"
    "v_mfma_f32_16x16x32_bf16 v[168:171], v[192:195], a[64:67], v[168:171]\n"
    "v_mfma_f32_16x16x32_bf16 v[172:175], v[192:195], a[68:71], v[172:175]\n"
    "v_mfma_f32_16x16x32_bf16 v[168:171], v[192:195], a[72:75], v[168:171]\n"
    "v_mfma_f32_16x16x32_bf16 v[172:175], v[192:195], a[76:79], v[172:175]\n"
    "s_waitcnt vmcnt(9)\n"
    "ds_read_b128 a[96:99], v187 offset:16384\n"
    "ds_read_b128 a[100:103], v187 offset:18432\n"
    "ds_read_b128 a[104:107], v188 offset:16384\n"
    "ds_read_b128 a[108:111], v188 offset:18432\n"
    "s_waitcnt lgkmcnt(4)\n"
    "v_mfma_f32_16x16x32_bf16 v[168:171], v[192:195], a[80:83], v[168:171]\n"
    "v_mfma_f32_16x16x32_bf16 v[172:175], v[192:195], a[84:87], v[172:175]\n"
    "v_mfma_f32_16x16x32_bf16 v[168:171], v[192:195], a[88:91], v[168:171]\n"
    "v_mfma_f32_16x16x32_bf16 v[172:175], v[192:195], a[92:95], v[172:175]\n"
    "s_waitcnt lgkmcnt(0)\n"
    "s_barrier\n"
    "v_mfma_f32_16x16x32_bf16 v[168:171], v[192:195], a[96:99], v[168:171]\n"
    "v_mfma_f32_16x16x32_bf16 v[172:175], v[192:195], a[100:103], v[172:175]\n"
    "v_mfma_f32_16x16x32_bf16 v[168:171], v[192:195], a[104:107], v[168:171]\n"
    "v_mfma_f32_16x16x32_bf16 v[172:175], v[192:195], a[108:111], v[172:175]\n"
    "s_add_u32 s68, %[rot], 0\n"
    "s_sub_u32 s52, s68, 7\n"
    "s_cmp_ge_u32 s68, 7\n"
    "s_cselect_b32 s68, s52, s68\n"
    "s_add_u32 m0, s50, 49152\n"
    "s_lshl_b32 s71, s68, 19\n"
    "s_add_u32 s71, s71, 262144\n"
    "buffer_load_dwordx4 %[vwq0], s[60:63], s71 offen offset:0 lds\n"
    "buffer_load_dwordx4 %[vwq1], s[60:63], s71 offen offset:1024 lds\n"
    "buffer_load_dwordx4 %[vwq2], s[60:63], s71 offen offset:2048 lds\n"
    "buffer_load_dwordx4 %[vwq3], s[60:63], s71 offen offset:3072 lds\n"
    "s_add_u32 s68, %[rot], 1\n"
    "s_sub_u32 s52, s68, 7\n"
    "s_cmp_ge_u32 s68, 7\n"
    "s_cselect_b32 s68, s52, s68\n"
    "s_add_u32 m0, s50, 65536\n"
    "s_lshl_b32 s71, s68, 19\n"
    "buffer_load_dwordx4 %[vwq0], s[60:63], s71 offen offset:0 lds\n"
    "buffer_load_dwordx4 %[vwq1], s[60:63], s71 offen offset:1024 lds\n"
    "buffer_load_dwordx4 %[vwq2], s[60:63], s71 offen offset:2048 lds\n"
    "buffer_load_dwordx4 %[vwq3], s[60:63], s71 offen offset:3072 lds\n"
    "s_add_u32 s68, %[rot], 1\n"
    "s_sub_u32 s52, s68, 7\n"
    "s_cmp_ge_u32 s68, 7\n"
    "s_cselect_b32 s68, s52, s68\n"
    "s_add_u32 m0, s50, 16384\n"
    "s_mul_i32 s71, s68, 229376\n"
    "buffer_load_dwordx4 %[vwi0], s[56:59], s71 offen offset:0 lds\n"
    "buffer_load_dwordx4 %[vwi1], s[56:59], s71 offen offset:1024 lds\n"
    "buffer_load_dwordx4 %[vwi2], s[56:59], s71 offen offset:2048 lds\n"
    "buffer_load_dwordx4 %[vwi3], s[56:59], s71 offen offset:3072 lds\n"
    "s_mov_b32 s40, %[klo]\n"
    "s_and_b32 s41, %[khi], 0xffff\n"
    "s_mov_b32 s42, 1048576\n"
    "s_mov_b32 s43, 0x20000\n"
    "s_mov_b32 s44, %[vlo]\n"
    "s_and_b32 s45, %[vhi], 0xffff\n"
    "s_mov_b32 s46, 1048576\n"
    "s_mov_b32 s47, 0x20000\n"
    "v_mul_f32 v196, 0xbb124925, v168\n"
    "v_mul_f32 v197, 0xbb124925, v172\n"
    "v_accvgpr_read_b32 v184, a0\n"
    "v_accvgpr_read_b32 v185, a1\n"
    "v_accvgpr_read_b32 v186, a2\n"
    "v_accvgpr_read_b32 v187, a3\n"
    "v_lshlrev_b32 v188, 16, v184\n"
    "v_and_b32 v189, 0xffff0000, v184\n"
    "v_lshlrev_b32 v190, 16, v185\n"
    "v_and_b32 v191, 0xffff0000, v185\n"
    "v_lshlrev_b32 v192, 16, v186\n"
    "v_and_b32 v193, 0xffff0000, v186\n"
    "v_lshlrev_b32 v194, 16, v187\n"
    "v_and_b32 v195, 0xffff0000, v187\n"
    "v_add_f32 v188, v188, v196\n"
    "v_add_f32 v189, v189, v196\n"
    "v_add_f32 v190, v190, v196\n"
    "v_add_f32 v191, v191, v196\n"
    "v_add_f32 v192, v192, v196\n"
    "v_add_f32 v193, v193, v196\n"
    "v_add_f32 v194, v194, v196\n"
    "v_add_f32 v195, v195, v196\n"
    "v_cvt_pk_bf16_f32 v168, v188, v189\n"
    "v_cvt_pk_bf16_f32 v169, v190, v191\n"
    "v_cvt_pk_bf16_f32 v170, v192, v193\n"
    "v_cvt_pk_bf16_f32 v171, v194, v195\n"
    "v_accvgpr_read_b32 v184, a4\n"
    "v_accvgpr_read_b32 v185, a5\n"
    "v_accvgpr_read_b32 v186, a6\n"
    "v_accvgpr_read_b32 v187, a7\n"
    "v_lshlrev_b32 v188, 16, v184\n"
    "v_and_b32 v189, 0xffff0000, v184\n"
    "v_lshlrev_b32 v190, 16, v185\n"
    "v_and_b32 v191, 0xffff0000, v185\n"
    "v_lshlrev_b32 v192, 16, v186\n"
    "v_and_b32 v193, 0xffff0000, v186\n"
    "v_lshlrev_b32 v194, 16, v187\n"
    "v_and_b32 v195, 0xffff0000, v187\n"
    "v_add_f32 v188, v188, v197\n"
    "v_add_f32 v189, v189, v197\n"
    "v_add_f32 v190, v190, v197\n"
    "v_add_f32 v191, v191, v197\n"
    "v_add_f32 v192, v192, v197\n"
    "v_add_f32 v193, v193, v197\n"
    "v_add_f32 v194, v194, v197\n"
    "v_add_f32 v195, v195, v197\n"
    "v_cvt_pk_bf16_f32 v172, v188, v189\n"
    "v_cvt_pk_bf16_f32 v173, v190, v191\n"
    "v_cvt_pk_bf16_f32 v174, v192, v193\n"
    "v_cvt_pk_bf16_f32 v175, v194, v195\n"
    "s_waitcnt vmcnt(12)\n"
    "s_barrier\n"
    "ds_read_b64_tr_b16 a[224:225], v200 offset:32768\n"
    "ds_read_b64_tr_b16 a[226:227], v200 offset:34816\n"
    "ds_read_b64_tr_b16 a[228:229], v201 offset:32768\n"
    "ds_read_b64_tr_b16 a[230:231], v201 offset:34816\n"
    "ds_read_b64_tr_b16 a[232:233], v202 offset:32768\n"
    "ds_read_b64_tr_b16 a[234:235], v202 offset:34816\n"
    "ds_read_b64_tr_b16 a[236:237], v203 offset:32768\n"
    "ds_read_b64_tr_b16 a[238:239], v203 offset:34816\n"
    "ds_read_b64_tr_b16 a[240:241], v204 offset:32768\n"
    "ds_read_b64_tr_b16 a[242:243], v204 offset:34816\n"
    "ds_read_b64_tr_b16 a[244:245], v205 offset:32768\n"
    "ds_read_b64_tr_b16 a[246:247], v205 offset:34816\n"
    "s_waitcnt lgkmcnt(8)\n"
    "s_nop 1\n"
    "v_mfma_f32_16x16x32_bf16 v[248:251], v[168:171], v[168:171], 0\n"
    "v_mfma_f32_16x16x32_bf16 v[252:255], v[172:175], v[172:175], 0\n"
    "v_mfma_f32_16x16x32_bf16 %[q0], a[224:227], v[168:171], 0\n"
    "ds_read_b64_tr_b16 a[248:249], v206 offset:32768\n"
    "s_add_u32 s68, %[rot], 1\n"
    "s_sub_u32 s52, s68, 7\n"
    "s_cmp_ge_u32 s68, 7\n"
    "s_cselect_b32 s68, s52, s68\n"
    "s_add_u32 m0, s50, 81920\n"
    "s_lshl_b32 s71, s68, 19\n"
    "s_add_u32 s71, s71, 262144\n"
    "buffer_load_dwordx4 %[vwq0], s[60:63], s71 offen offset:0 lds\n"
    "v_mfma_f32_16x16x32_bf16 %[q1], a[224:227], v[172:175], 0\n"
    "ds_read_b64_tr_b16 a[250:251], v206 offset:34816\n"
    "buffer_load_dwordx4 %[vwq1], s[60:63], s71 offen offset:1024 lds\n"
    "v_mfma_f32_16x16x32_bf16 %[q2], a[228:231], v[168:171], 0\n"
    "ds_read_b64_tr_b16 a[252:253], v207 offset:32768\n"
    "v_accvgpr_read_b32 v184, a8\n"
    "v_accvgpr_read_b32 v185, a9\n"
    "v_mfma_f32_16x16x32_bf16 %[q3], a[228:231], v[172:175], 0\n"
    "ds_read_b64_tr_b16 a[254:255], v207 offset:34816\n"
    "buffer_load_dwordx4 %[vwq2], s[60:63], s71 offen offset:2048 lds\n"
    "s_waitcnt lgkmcnt(8)\n"
    "v_mfma_f32_16x16x32_bf16 %[q4], a[232:235], v[168:171], 0\n"
    "ds_read_b64_tr_b16 a[224:225], v200 offset:33024\n"
    "v_accvgpr_read_b32 v186, a10\n"
    "v_accvgpr_read_b32 v187, a11\n"
    "v_mfma_f32_16x16x32_bf16 %[q5], a[232:235], v[172:175], 0\n"
    "ds_read_b64_tr_b16 a[226:227], v200 offset:35072\n"
    "buffer_load_dwordx4 %[vwq3], s[60:63], s71 offen offset:3072 lds\n"
    "v_mfma_f32_16x16x32_bf16 %[q6], a[236:239], v[168:171], 0\n"
    "ds_read_b64_tr_b16 a[228:229], v201 offset:33024\n"
    "v_lshlrev_b32 v188, 16, v184\n"
    "v_and_b32 v189, 0xffff0000, v184\n"
    "v_mfma_f32_16x16x32_bf16 %[q7], a[236:239], v[172:175], 0\n"
    "ds_read_b64_tr_b16 a[230:231], v201 offset:35072\n"
    "s_add_u32 s68, %[rot], 2\n"
    "s_sub_u32 s52, s68, 7\n"
    "s_cmp_ge_u32 s68, 7\n"
    "s_cselect_b32 s68, s52, s68\n"
    "s_add_u32 m0, s50, 98304\n"
    "s_lshl_b32 s71, s68, 19\n"
    "buffer_load_dwordx4 %[vwq0], s[60:63], s71 offen offset:0 lds\n"
    "s_waitcnt lgkmcnt(8)\n"
    "v_mfma_f32_16x16x32_bf16 %[q8], a[240:243], v[168:171], 0\n"
    "ds_read_b64_tr_b16 a[232:233], v202 offset:33024\n"
    "v_lshlrev_b32 v190, 16, v185\n"
    "v_and_b32 v191, 0xffff0000, v185\n"
    "v_mfma_f32_16x16x32_bf16 %[q9], a[240:243], v[172:175], 0\n"
    "ds_read_b64_tr_b16 a[234:235], v202 offset:35072\n"
    "buffer_load_dwordx4 %[vwq1], s[60:63], s71 offen offset:1024 lds\n"
    "v_mfma_f32_16x16x32_bf16 %[q10], a[244:247], v[168:171], 0\n"
    "ds_read_b64_tr_b16 a[236:237], v203 offset:33024\n"
    "v_lshlrev_b32 v192, 16, v186\n"
    "v_and_b32 v193, 0xffff0000, v186\n"
    "v_mfma_f32_16x16x32_bf16 %[q11], a[244:247], v[172:175], 0\n"
    "ds_read_b64_tr_b16 a[238:239], v203 offset:35072\n"
    "buffer_load_dwordx4 %[vwq2], s[60:63], s71 offen offset:2048 lds\n"
    "s_waitcnt lgkmcnt(8)\n"
    "v_mfma_f32_16x16x32_bf16 %[q12], a[248:251], v[168:171], 0\n"
    "ds_read_b64_tr_b16 a[240:241], v204 offset:33024\n"
    "v_lshlrev_b32 v194, 16, v187\n"
    "v_and_b32 v195, 0xffff0000, v187\n"
    "v_mfma_f32_16x16x32_bf16 %[q13], a[248:251], v[172:175], 0\n"
    "ds_read_b64_tr_b16 a[242:243], v204 offset:35072\n"
    "buffer_load_dwordx4 %[vwq3], s[60:63], s71 offen offset:3072 lds\n"
    "v_mfma_f32_16x16x32_bf16 %[q14], a[252:255], v[168:171], 0\n"
    "ds_read_b64_tr_b16 a[244:245], v205 offset:33024\n"
    "v_add_f32 v188, v188, v196\n"
    "v_add_f32 v189, v189, v196\n"
    "v_mfma_f32_16x16x32_bf16 %[q15], a[252:255], v[172:175], 0\n"
    "ds_read_b64_tr_b16 a[246:247], v205 offset:35072\n"
    "s_add_u32 s68, %[rot], 2\n"
    "s_sub_u32 s52, s68, 7\n"
    "s_cmp_ge_u32 s68, 7\n"
    "s_cselect_b32 s68, s52, s68\n"
    "s_add_u32 m0, s50, 147456\n"
    "s_mul_i32 s71, s68, 229376\n"
    "buffer_load_dwordx4 %[vwi0], s[56:59], s71 offen offset:0 lds\n"
    "s_waitcnt lgkmcnt(8)\n"
    "v_mfma_f32_16x16x32_bf16 %[q16], a[224:227], v[168:171], 0\n"
    "ds_read_b64_tr_b16 a[248:249], v206 offset:33024\n"
    "v_add_f32 v190, v190, v196\n"
    "v_add_f32 v191, v191, v196\n"
    "v_mfma_f32_16x16x32_bf16 %[q17], a[224:227], v[172:175], 0\n"
    "ds_read_b64_tr_b16 a[250:251], v206 offset:35072\n"
    "buffer_load_dwordx4 %[vwi1], s[56:59], s71 offen offset:1024 lds\n"
    "v_mfma_f32_16x16x32_bf16 %[q18], a[228:231], v[168:171], 0\n"
    "ds_read_b64_tr_b16 a[252:253], v207 offset:33024\n"
    "v_add_f32 v192, v192, v196\n"
    "v_add_f32 v193, v193, v196\n"
    "v_mfma_f32_16x16x32_bf16 %[q19], a[228:231], v[172:175], 0\n"
    "ds_read_b64_tr_b16 a[254:255], v207 offset:35072\n"
    "buffer_load_dwordx4 %[vwi2], s[56:59], s71 offen offset:2048 lds\n"
    "s_waitcnt lgkmcnt(8)\n"
    "v_mfma_f32_16x16x32_bf16 %[q20], a[232:235], v[168:171], 0\n"
    "ds_read_b64_tr_b16 a[224:225], v224 offset:0\n"
    "v_add_f32 v194, v194, v196\n"
    "v_mfma_f32_16x16x32_bf16 %[q21], a[232:235], v[172:175], 0\n"
    "ds_read_b64_tr_b16 a[226:227], v224 offset:1024\n"
    "buffer_load_dwordx4 %[vwi3], s[56:59], s71 offen offset:3072 lds\n"
    "v_mfma_f32_16x16x32_bf16 %[q22], a[236:239], v[168:171], 0\n"
    "ds_read_b64_tr_b16 a[228:229], v225 offset:0\n"
    "v_add_f32 v195, v195, v196\n"
    "v_mfma_f32_16x16x32_bf16 %[q23], a[236:239], v[172:175], 0\n"
    "ds_read_b64_tr_b16 a[230:231], v225 offset:1024\n"
    "v_cvt_pk_bf16_f32 v176, v188, v189\n"
    "s_waitcnt lgkmcnt(8)\n"
    "v_mfma_f32_16x16x32_bf16 %[q24], a[240:243], v[168:171], 0\n"
    "ds_read_b64_tr_b16 a[232:233], v226 offset:0\n"
    "v_cvt_pk_bf16_f32 v177, v190, v191\n"
    "v_mfma_f32_16x16x32_bf16 %[q25], a[240:243], v[172:175], 0\n"
    "ds_read_b64_tr_b16 a[234:235], v226 offset:1024\n"
    "v_cvt_pk_bf16_f32 v178, v192, v193\n"
    "v_mfma_f32_16x16x32_bf16 %[q26], a[244:247], v[168:171], 0\n"
    "ds_read_b64_tr_b16 a[236:237], v227 offset:0\n"
    "v_cvt_pk_bf16_f32 v179, v194, v195\n"
    "v_mfma_f32_16x16x32_bf16 %[q27], a[244:247], v[172:175], 0\n"
    "ds_read_b64_tr_b16 a[238:239], v227 offset:1024\n"
    "v_accvgpr_read_b32 v184, a12\n"
    "s_waitcnt lgkmcnt(8)\n"
    "v_mfma_f32_16x16x32_bf16 %[q28], a[248:251], v[168:171], 0\n"
    "ds_read_b64_tr_b16 a[240:241], v228 offset:0\n"
    "v_accvgpr_read_b32 v185, a13\n"
    "v_mfma_f32_16x16x32_bf16 %[q29], a[248:251], v[172:175], 0\n"
    "ds_read_b64_tr_b16 a[242:243], v228 offset:1024\n"
    "v_accvgpr_read_b32 v186, a14\n"
    "v_mfma_f32_16x16x32_bf16 %[q30], a[252:255], v[168:171], 0\n"
    "ds_read_b64_tr_b16 a[244:245], v229 offset:0\n"
    "v_accvgpr_read_b32 v187, a15\n"
    "v_mfma_f32_16x16x32_bf16 %[q31], a[252:255], v[172:175], 0\n"
    "ds_read_b64_tr_b16 a[246:247], v229 offset:1024\n"
    "v_lshlrev_b32 v188, 16, v184\n"
    "s_waitcnt lgkmcnt(8)\n"
    "v_mfma_f32_16x16x32_bf16 %[z0], a[224:227], v[168:171], 0\n"
    "ds_read_b64_tr_b16 a[248:249], v230 offset:0\n"
    "v_and_b32 v189, 0xffff0000, v184\n"
    "v_mfma_f32_16x16x32_bf16 %[z1], a[224:227], v[172:175], 0\n"
    "ds_read_b64_tr_b16 a[250:251], v230 offset:1024\n"
    "v_lshlrev_b32 v190, 16, v185\n"
    "s_waitcnt vmcnt(20)\n"
    "s_barrier\n"
    "v_mfma_f32_16x16x32_bf16 %[z2], a[228:231], v[168:171], 0\n"
    "ds_read_b64_tr_b16 a[252:253], v200 offset:49152\n"
    "v_and_b32 v191, 0xffff0000, v185\n"
    "v_mfma_f32_16x16x32_bf16 %[z3], a[228:231], v[172:175], 0\n"
    "ds_read_b64_tr_b16 a[254:255], v200 offset:51200\n"
    "v_lshlrev_b32 v192, 16, v186\n"
    "s_waitcnt lgkmcnt(8)\n"
    "v_mfma_f32_16x16x32_bf16 %[z4], a[232:235], v[168:171], 0\n"
    "ds_read_b64_tr_b16 a[224:225], v201 offset:49152\n"
    "v_and_b32 v193, 0xffff0000, v186\n"
    "v_mfma_f32_16x16x32_bf16 %[z5], a[232:235], v[172:175], 0\n"
    "ds_read_b64_tr_b16 a[226:227], v201 offset:51200\n"
    "v_lshlrev_b32 v194, 16, v187\n"
    "v_mfma_f32_16x16x32_bf16 %[z6], a[236:239], v[168:171], 0\n"
    "ds_read_b64_tr_b16 a[228:229], v202 offset:49152\n"
    "v_and_b32 v195, 0xffff0000, v187\n"
    "v_mfma_f32_16x16x32_bf16 %[z7], a[236:239], v[172:175], 0\n"
    "ds_read_b64_tr_b16 a[230:231], v202 offset:51200\n"
    "v_add_f32 v188, v188, v197\n"
    "s_waitcnt lgkmcnt(8)\n"
    "v_mfma_f32_16x16x32_bf16 %[z8], a[240:243], v[168:171], 0\n"
    "ds_read_b64_tr_b16 a[232:233], v203 offset:49152\n"
    "v_add_f32 v189, v189, v197\n"
    "v_mfma_f32_16x16x32_bf16 %[z9], a[240:243], v[172:175], 0\n"
    "ds_read_b64_tr_b16 a[234:235], v203 offset:51200\n"
    "v_add_f32 v190, v190, v197\n"
    "v_mfma_f32_16x16x32_bf16 %[z10], a[244:247], v[168:171], 0\n"
    "ds_read_b64_tr_b16 a[236:237], v204 offset:49152\n"
    "v_add_f32 v191, v191, v197\n"
    "v_mfma_f32_16x16x32_bf16 %[z11], a[244:247], v[172:175], 0\n"
    "ds_read_b64_tr_b16 a[238:239], v204 offset:51200\n"
    "v_add_f32 v192, v192, v197\n"
    "s_waitcnt lgkmcnt(8)\n"
    "v_mfma_f32_16x16x32_bf16 %[z12], a[248:251], v[168:171], 0\n"
    "ds_read_b64_tr_b16 a[240:241], v205 offset:49152\n"
    "v_add_f32 v193, v193, v197\n"
    "v_mfma_f32_16x16x32_bf16 %[z13], a[248:251], v[172:175], 0\n"
    "ds_read_b64_tr_b16 a[242:243], v205 offset:51200\n"
    "v_add_f32 v194, v194, v197\n"
    "v_add_f32 v195, v195, v197\n"
    "v_cvt_pk_bf16_f32 v180, v188, v189\n"
    "v_cvt_pk_bf16_f32 v181, v190, v191\n"
    "v_cvt_pk_bf16_f32 v182, v192, v193\n"
    "v_cvt_pk_bf16_f32 v183, v194, v195\n"
    "s_nop 1\n"
    "v_mfma_f32_16x16x32_bf16 v[248:251], v[176:179], v[176:179], v[248:251]\n"
    "v_mfma_f32_16x16x32_bf16 v[252:255], v[180:183], v[180:183], v[252:255]\n"
    "s_mov_b32 s72, %[wslo]\n"
    "s_and_b32 s73, %[wshi], 0xffff\n"
    "s_mov_b32 s74, 8\n"
    "s_mov_b32 s75, 0x20000\n"
    "buffer_load_dwordx2 a[220:221], off, s[72:75], 0 sc1\n"
    "s_mov_b32 s72, %[wolo]\n"
    "s_and_b32 s73, %[wohi], 0xffff\n"
    "s_mov_b32 s74, 3670016\n"
    "s_mov_b32 s75, 0x20000\n"
    "s_mov_b32 s76, %[wtlo]\n"
    "s_and_b32 s77, %[wthi], 0xffff\n"
    "s_mov_b32 s78, 802816\n"
    "s_mov_b32 s79, 0x20000\n"
    "s_mul_i32 s80, %[rw], 3584\n"
    "s_mul_i32 s81, %[rw], 896\n"
    "buffer_load_dwordx4 a[200:203], %[vrs], s[72:75], s80 offen offset:0\n"
    "buffer_load_dwordx4 a[204:207], %[vrs], s[72:75], s80 offen offset:896\n"
    "buffer_load_dwordx4 a[208:211], %[vrs], s[72:75], s80 offen offset:1792\n"
    "buffer_load_dwordx4 a[212:215], %[vrs], s[72:75], s80 offen offset:2688\n"
    "buffer_load_dwordx4 a[216:219], %[vrs], s[76:79], s81 offen\n"
    "v_mfma_f32_16x16x32_bf16 %[q0], a[252:255], v[176:179], %[q0]\n"
    "ds_read_b64_tr_b16 a[244:245], v206 offset:49152\n"
    "v_accvgpr_read_b32 v184, a16\n"
    "v_accvgpr_read_b32 v185, a17\n"
    "v_mfma_f32_16x16x32_bf16 %[q1], a[252:255], v[180:183], %[q1]\n"
    "ds_read_b64_tr_b16 a[246:247], v206 offset:51200\n"
    "v_accvgpr_read_b32 v186, a18\n"
    "v_accvgpr_read_b32 v187, a19\n"
    "s_waitcnt lgkmcnt(8)\n"
    "v_mfma_f32_16x16x32_bf16 %[q2], a[224:227], v[176:179], %[q2]\n"
    "ds_read_b64_tr_b16 a[248:249], v207 offset:49152\n"
    "v_lshlrev_b32 v188, 16, v184\n"
    "v_and_b32 v189, 0xffff0000, v184\n"
    "v_mfma_f32_16x16x32_bf16 %[q3], a[224:227], v[180:183], %[q3]\n"
    "ds_read_b64_tr_b16 a[250:251], v207 offset:51200\n"
    "v_lshlrev_b32 v190, 16, v185\n"
    "v_and_b32 v191, 0xffff0000, v185\n"
    "v_mfma_f32_16x16x32_bf16 %[q4], a[228:231], v[176:179], %[q4]\n"
    "ds_read_b64_tr_b16 a[252:253], v200 offset:49408\n"
    "v_lshlrev_b32 v192, 16, v186\n"
    "v_and_b32 v193, 0xffff0000, v186\n"
    "v_mfma_f32_16x16x32_bf16 %[q5], a[228:231], v[180:183], %[q5]\n"
    "ds_read_b64_tr_b16 a[254:255], v200 offset:51456\n"
    "v_lshlrev_b32 v194, 16, v187\n"
    "v_and_b32 v195, 0xffff0000, v187\n"
    "s_waitcnt lgkmcnt(8)\n"
    "v_mfma_f32_16x16x32_bf16 %[q6], a[232:235], v[176:179], %[q6]\n"
    "ds_read_b64_tr_b16 a[224:225], v201 offset:49408\n"
    "v_add_f32 v188, v188, v196\n"
    "v_add_f32 v189, v189, v196\n"
    "v_mfma_f32_16x16x32_bf16 %[q7], a[232:235], v[180:183], %[q7]\n"
    "ds_read_b64_tr_b16 a[226:227], v201 offset:51456\n"
    "v_add_f32 v190, v190, v196\n"
    "v_add_f32 v191, v191, v196\n"
    "v_mfma_f32_16x16x32_bf16 %[q8], a[236:239], v[176:179], %[q8]\n"
    "ds_read_b64_tr_b16 a[228:229], v202 offset:49408\n"
    "v_add_f32 v192, v192, v196\n"
    "v_add_f32 v193, v193, v196\n"
    "v_mfma_f32_16x16x32_bf16 %[q9], a[236:239], v[180:183], %[q9]\n"
    "ds_read_b64_tr_b16 a[230:231], v202 offset:51456\n"
    "v_add_f32 v194, v194, v196\n"
    "s_waitcnt lgkmcnt(8)\n"
    "v_mfma_f32_16x16x32_bf16 %[q10], a[240:243], v[176:179], %[q10]\n"
    "ds_read_b64_tr_b16 a[232:233], v203 offset:49408\n"
    "v_add_f32 v195, v195, v196\n"
    "v_mfma_f32_16x16x32_bf16 %[q11], a[240:243], v[180:183], %[q11]\n"
    "ds_read_b64_tr_b16 a[234:235], v203 offset:51456\n"
    "v_cvt_pk_bf16_f32 v168, v188, v189\n"
    "v_mfma_f32_16x16x32_bf16 %[q12], a[244:247], v[176:179], %[q12]\n"
    "ds_read_b64_tr_b16 a[236:237], v204 offset:49408\n"
    "v_cvt_pk_bf16_f32 v169, v190, v191\n"
    "v_mfma_f32_16x16x32_bf16 %[q13], a[244:247], v[180:183], %[q13]\n"
    "ds_read_b64_tr_b16 a[238:239], v204 offset:51456\n"
    "v_cvt_pk_bf16_f32 v170, v192, v193\n"
    "s_waitcnt lgkmcnt(8)\n"
    "v_mfma_f32_16x16x32_bf16 %[q14], a[248:251], v[176:179], %[q14]\n"
    "ds_read_b64_tr_b16 a[240:241], v205 offset:49408\n"
    "v_cvt_pk_bf16_f32 v171, v194, v195\n"
    "v_mfma_f32_16x16x32_bf16 %[q15], a[248:251], v[180:183], %[q15]\n"
    "ds_read_b64_tr_b16 a[242:243], v205 offset:51456\n"
    "v_accvgpr_read_b32 v184, a20\n"
    "v_mfma_f32_16x16x32_bf16 %[q16], a[252:255], v[176:179], %[q16]\n"
    "ds_read_b64_tr_b16 a[244:245], v206 offset:49408\n"
    "v_accvgpr_read_b32 v185, a21\n"
    "v_mfma_f32_16x16x32_bf16 %[q17], a[252:255], v[180:183], %[q17]\n"
    "ds_read_b64_tr_b16 a[246:247], v206 offset:51456\n"
    "v_accvgpr_read_b32 v186, a22\n"
    "s_waitcnt lgkmcnt(8)\n"
    "v_mfma_f32_16x16x32_bf16 %[q18], a[224:227], v[176:179], %[q18]\n"
    "ds_read_b64_tr_b16 a[248:249], v207 offset:49408\n"
    "v_accvgpr_read_b32 v187, a23\n"
    "v_mfma_f32_16x16x32_bf16 %[q19], a[224:227], v[180:183], %[q19]\n"
    "ds_read_b64_tr_b16 a[250:251], v207 offset:51456\n"
    "v_lshlrev_b32 v188, 16, v184\n"
    "v_mfma_f32_16x16x32_bf16 %[q20], a[228:231], v[176:179], %[q20]\n"
    "ds_read_b64_tr_b16 a[252:253], v224 offset:8192\n"
    "v_and_b32 v189, 0xffff0000, v184\n"
    "v_mfma_f32_16x16x32_bf16 %[q21], a[228:231], v[180:183], %[q21]\n"
    "ds_read_b64_tr_b16 a[254:255], v224 offset:9216\n"
    "v_lshlrev_b32 v190, 16, v185\n"
    "s_waitcnt lgkmcnt(8)\n"
    "v_mfma_f32_16x16x32_bf16 %[q22], a[232:235], v[176:179], %[q22]\n"
    "ds_read_b64_tr_b16 a[224:225], v225 offset:8192\n"
    "v_and_b32 v191, 0xffff0000, v185\n"
    "v_mfma_f32_16x16x32_bf16 %[q23], a[232:235], v[180:183], %[q23]\n"
    "ds_read_b64_tr_b16 a[226:227], v225 offset:9216\n"
    "v_lshlrev_b32 v192, 16, v186\n"
    "v_mfma_f32_16x16x32_bf16 %[q24], a[236:239], v[176:179], %[q24]\n"
    "ds_read_b64_tr_b16 a[228:229], v226 offset:8192\n"
    "v_and_b32 v193, 0xffff0000, v186\n"
    "v_mfma_f32_16x16x32_bf16 %[q25], a[236:239], v[180:183], %[q25]\n"
    "ds_read_b64_tr_b16 a[230:231], v226 offset:9216\n"
    "v_lshlrev_b32 v194, 16, v187\n"
    "s_waitcnt lgkmcnt(8)\n"
    "v_mfma_f32_16x16x32_bf16 %[q26], a[240:243], v[176:179], %[q26]\n"
    "ds_read_b64_tr_b16 a[232:233], v227 offset:8192\n"
    "v_and_b32 v195, 0xffff0000, v187\n"
    "v_mfma_f32_16x16x32_bf16 %[q27], a[240:243], v[180:183], %[q27]\n"
    "ds_read_b64_tr_b16 a[234:235], v227 offset:9216\n"
    "v_add_f32 v188, v188, v197\n"
    "v_mfma_f32_16x16x32_bf16 %[q28], a[244:247], v[176:179], %[q28]\n"
    "ds_read_b64_tr_b16 a[236:237], v228 offset:8192\n"
    "v_add_f32 v189, v189, v197\n"
    "v_mfma_f32_16x16x32_bf16 %[q29], a[244:247], v[180:183], %[q29]\n"
    "ds_read_b64_tr_b16 a[238:239], v228 offset:9216\n"
    "v_add_f32 v190, v190, v197\n"
    "s_waitcnt lgkmcnt(8)\n"
    "v_mfma_f32_16x16x32_bf16 %[q30], a[248:251], v[176:179], %[q30]\n"
    "ds_read_b64_tr_b16 a[240:241], v229 offset:8192\n"
    "v_add_f32 v191, v191, v197\n"
    "v_mfma_f32_16x16x32_bf16 %[q31], a[248:251], v[180:183], %[q31]\n"
    "ds_read_b64_tr_b16 a[242:243], v229 offset:9216\n"
    "v_add_f32 v192, v192, v197\n"
    "v_mfma_f32_16x16x32_bf16 %[z0], a[252:255], v[176:179], %[z0]\n"
    "ds_read_b64_tr_b16 a[244:245], v230 offset:8192\n"
    "v_add_f32 v193, v193, v197\n"
    "v_mfma_f32_16x16x32_bf16 %[z1], a[252:255], v[180:183], %[z1]\n"
    "ds_read_b64_tr_b16 a[246:247], v230 offset:9216\n"
    "v_add_f32 v194, v194, v197\n"
    "s_waitcnt lgkmcnt(8)\n"
    "s_waitcnt vmcnt(18)\n"
    "s_barrier\n"
    "v_mfma_f32_16x16x32_bf16 %[z2], a[224:227], v[176:179], %[z2]\n"
    "ds_read_b64_tr_b16 a[248:249], v208 offset:0\n"
    "v_add_f32 v195, v195, v197\n"
    "v_mfma_f32_16x16x32_bf16 %[z3], a[224:227], v[180:183], %[z3]\n"
    "ds_read_b64_tr_b16 a[250:251], v208 offset:2048\n"
    "s_add_u32 s68, %[rot], 2\n"
    "s_sub_u32 s52, s68, 7\n"
    "s_cmp_ge_u32 s68, 7\n"
    "s_cselect_b32 s68, s52, s68\n"
    "s_add_u32 m0, s50, 114688\n"
    "s_lshl_b32 s71, s68, 19\n"
    "s_add_u32 s71, s71, 262144\n"
    "buffer_load_dwordx4 %[vwq0], s[60:63], s71 offen offset:0 lds\n"
    "v_mfma_f32_16x16x32_bf16 %[z4], a[228:231], v[176:179], %[z4]\n"
    "ds_read_b64_tr_b16 a[252:253], v209 offset:0\n"
    "v_cvt_pk_bf16_f32 v172, v188, v189\n"
    "v_mfma_f32_16x16x32_bf16 %[z5], a[228:231], v[180:183], %[z5]\n"
    "ds_read_b64_tr_b16 a[254:255], v209 offset:2048\n"
    "buffer_load_dwordx4 %[vwq1], s[60:63], s71 offen offset:1024 lds\n"
    "s_waitcnt lgkmcnt(8)\n"
    "v_mfma_f32_16x16x32_bf16 %[z6], a[232:235], v[176:179], %[z6]\n"
    "ds_read_b64_tr_b16 a[224:225], v210 offset:0\n"
    "v_cvt_pk_bf16_f32 v173, v190, v191\n"
    "v_mfma_f32_16x16x32_bf16 %[z7], a[232:235], v[180:183], %[z7]\n"
    "ds_read_b64_tr_b16 a[226:227], v210 offset:2048\n"
    "buffer_load_dwordx4 %[vwq2], s[60:63], s71 offen offset:2048 lds\n"
    "v_mfma_f32_16x16x32_bf16 %[z8], a[236:239], v[176:179], %[z8]\n"
    "ds_read_b64_tr_b16 a[228:229], v211 offset:0\n"
    "v_cvt_pk_bf16_f32 v174, v192, v193\n"
    "v_mfma_f32_16x16x32_bf16 %[z9], a[236:239], v[180:183], %[z9]\n"
    "ds_read_b64_tr_b16 a[230:231], v211 offset:2048\n"
    "buffer_load_dwordx4 %[vwq3], s[60:63], s71 offen offset:3072 lds\n"
    "s_waitcnt lgkmcnt(8)\n"
    "v_mfma_f32_16x16x32_bf16 %[z10], a[240:243], v[176:179], %[z10]\n"
    "ds_read_b64_tr_b16 a[232:233], v212 offset:0\n"
    "v_cvt_pk_bf16_f32 v175, v194, v195\n"
    "v_mfma_f32_16x16x32_bf16 %[z11], a[240:243], v[180:183], %[z11]\n"
    "ds_read_b64_tr_b16 a[234:235], v212 offset:2048\n"
    "v_mfma_f32_16x16x32_bf16 %[z12], a[244:247], v[176:179], %[z12]\n"
    "ds_read_b64_tr_b16 a[236:237], v213 offset:0\n"
    "v_mfma_f32_16x16x32_bf16 %[z13], a[244:247], v[180:183], %[z13]\n"
    "ds_read_b64_tr_b16 a[238:239], v213 offset:2048\n"
    "s_waitcnt lgkmcnt(8)\n"
    "s_nop 1\n"
    "v_mfma_f32_16x16x32_bf16 v[248:251], v[168:171], v[168:171], v[248:251]\n"
    "v_mfma_f32_16x16x32_bf16 v[252:255], v[172:175], v[172:175], v[252:255]\n"
    "v_mfma_f32_16x16x32_bf16 %[q0], a[248:251], v[168:171], %[q0]\n"
    "ds_read_b64_tr_b16 a[240:241], v214 offset:0\n"
    "v_accvgpr_read_b32 v184, a24\n"
    "v_accvgpr_read_b32 v185, a25\n"
    "v_mfma_f32_16x16x32_bf16 %[q1], a[248:251], v[172:175], %[q1]\n"
    "ds_read_b64_tr_b16 a[242:243], v214 offset:2048\n"
    "v_accvgpr_read_b32 v186, a26\n"
    "v_accvgpr_read_b32 v187, a27\n"
    "v_mfma_f32_16x16x32_bf16 %[q2], a[252:255], v[168:171], %[q2]\n"
    "ds_read_b64_tr_b16 a[244:245], v215 offset:0\n"
    "v_lshlrev_b32 v188, 16, v184\n"
    "v_and_b32 v189, 0xffff0000, v184\n"
    "v_mfma_f32_16x16x32_bf16 %[q3], a[252:255], v[172:175], %[q3]\n"
    "ds_read_b64_tr_b16 a[246:247], v215 offset:2048\n"
    "v_lshlrev_b32 v190, 16, v185\n"
    "v_and_b32 v191, 0xffff0000, v185\n"
    "s_waitcnt lgkmcnt(8)\n"
    "v_mfma_f32_16x16x32_bf16 %[q4], a[224:227], v[168:171], %[q4]\n"
    "ds_read_b64_tr_b16 a[248:249], v208 offset:256\n"
    "v_lshlrev_b32 v192, 16, v186\n"
    "v_and_b32 v193, 0xffff0000, v186\n"
    "v_mfma_f32_16x16x32_bf16 %[q5], a[224:227], v[172:175], %[q5]\n"
    "ds_read_b64_tr_b16 a[250:251], v208 offset:2304\n"
    "v_lshlrev_b32 v194, 16, v187\n"
    "v_and_b32 v195, 0xffff0000, v187\n"
    "v_mfma_f32_16x16x32_bf16 %[q6], a[228:231], v[168:171], %[q6]\n"
    "ds_read_b64_tr_b16 a[252:253], v209 offset:256\n"
    "v_add_f32 v188, v188, v196\n"
    "v_add_f32 v189, v189, v196\n"
    "v_mfma_f32_16x16x32_bf16 %[q7], a[228:231], v[172:175], %[q7]\n"
    "ds_read_b64_tr_b16 a[254:255], v209 offset:2304\n"
    "v_add_f32 v190, v190, v196\n"
    "v_add_f32 v191, v191, v196\n"
    "s_waitcnt lgkmcnt(8)\n"
    "v_mfma_f32_16x16x32_bf16 %[q8], a[232:235], v[168:171], %[q8]\n"
    "ds_read_b64_tr_b16 a[224:225], v210 offset:256\n"
    "v_add_f32 v192, v192, v196\n"
    "v_add_f32 v193, v193, v196\n"
    "v_mfma_f32_16x16x32_bf16 %[q9], a[232:235], v[172:175], %[q9]\n"
    "ds_read_b64_tr_b16 a[226:227], v210 offset:2304\n"
    "v_add_f32 v194, v194, v196\n"
    "v_mfma_f32_16x16x32_bf16 %[q10], a[236:239], v[168:171], %[q10]\n"
    "ds_read_b64_tr_b16 a[228:229], v211 offset:256\n"
    "v_add_f32 v195, v195, v196\n"
    "v_mfma_f32_16x16x32_bf16 %[q11], a[236:239], v[172:175], %[q11]\n"
    "ds_read_b64_tr_b16 a[230:231], v211 offset:2304\n"
    "v_cvt_pk_bf16_f32 v176, v188, v189\n"
    "s_waitcnt lgkmcnt(8)\n"
    "v_mfma_f32_16x16x32_bf16 %[q12], a[240:243], v[168:171], %[q12]\n"
    "ds_read_b64_tr_b16 a[232:233], v212 offset:256\n"
    "v_cvt_pk_bf16_f32 v177, v190, v191\n"
    "v_mfma_f32_16x16x32_bf16 %[q13], a[240:243], v[172:175], %[q13]\n"
    "ds_read_b64_tr_b16 a[234:235], v212 offset:2304\n"
    "v_cvt_pk_bf16_f32 v178, v192, v193\n"
    "v_mfma_f32_16x16x32_bf16 %[q14], a[244:247], v[168:171], %[q14]\n"
    "ds_read_b64_tr_b16 a[236:237], v213 offset:256\n"
    "v_cvt_pk_bf16_f32 v179, v194, v195\n"
    "v_mfma_f32_16x16x32_bf16 %[q15], a[244:247], v[172:175], %[q15]\n"
    "ds_read_b64_tr_b16 a[238:239], v213 offset:2304\n"
    "v_accvgpr_read_b32 v184, a28\n"
    "s_waitcnt lgkmcnt(8)\n"
    "v_mfma_f32_16x16x32_bf16 %[q16], a[248:251], v[168:171], %[q16]\n"
    "ds_read_b64_tr_b16 a[240:241], v214 offset:256\n"
    "v_accvgpr_read_b32 v185, a29\n"
    "v_mfma_f32_16x16x32_bf16 %[q17], a[248:251], v[172:175], %[q17]\n"
    "ds_read_b64_tr_b16 a[242:243], v214 offset:2304\n"
    "v_accvgpr_read_b32 v186, a30\n"
    "v_mfma_f32_16x16x32_bf16 %[q18], a[252:255], v[168:171], %[q18]\n"
    "ds_read_b64_tr_b16 a[244:245], v215 offset:256\n"
    "v_accvgpr_read_b32 v187, a31\n"
    "v_mfma_f32_16x16x32_bf16 %[q19], a[252:255], v[172:175], %[q19]\n"
    "ds_read_b64_tr_b16 a[246:247], v215 offset:2304\n"
    "v_lshlrev_b32 v188, 16, v184\n"
    "s_waitcnt lgkmcnt(8)\n"
    "v_mfma_f32_16x16x32_bf16 %[q20], a[224:227], v[168:171], %[q20]\n"
    "ds_read_b64_tr_b16 a[248:249], v224 offset:16384\n"
    "v_and_b32 v189, 0xffff0000, v184\n"
    "v_mfma_f32_16x16x32_bf16 %[q21], a[224:227], v[172:175], %[q21]\n"
    "ds_read_b64_tr_b16 a[250:251], v224 offset:17408\n"
    "v_lshlrev_b32 v190, 16, v185\n"
    "v_mfma_f32_16x16x32_bf16 %[q22], a[228:231], v[168:171], %[q22]\n"
    "ds_read_b64_tr_b16 a[252:253], v225 offset:16384\n"
    "v_and_b32 v191, 0xffff0000, v185\n"
    "v_mfma_f32_16x16x32_bf16 %[q23], a[228:231], v[172:175], %[q23]\n"
    "ds_read_b64_tr_b16 a[254:255], v225 offset:17408\n"
    "v_lshlrev_b32 v192, 16, v186\n"
    "s_waitcnt lgkmcnt(8)\n"
    "v_mfma_f32_16x16x32_bf16 %[q24], a[232:235], v[168:171], %[q24]\n"
    "ds_read_b64_tr_b16 a[224:225], v226 offset:16384\n"
    "v_and_b32 v193, 0xffff0000, v186\n"
    "v_mfma_f32_16x16x32_bf16 %[q25], a[232:235], v[172:175], %[q25]\n"
    "ds_read_b64_tr_b16 a[226:227], v226 offset:17408\n"
    "v_lshlrev_b32 v194, 16, v187\n"
    "v_mfma_f32_16x16x32_bf16 %[q26], a[236:239], v[168:171], %[q26]\n"
    "ds_read_b64_tr_b16 a[228:229], v227 offset:16384\n"
    "v_and_b32 v195, 0xffff0000, v187\n"
    "v_mfma_f32_16x16x32_bf16 %[q27], a[236:239], v[172:175], %[q27]\n"
    "ds_read_b64_tr_b16 a[230:231], v227 offset:17408\n"
    "v_add_f32 v188, v188, v197\n"
    "s_waitcnt lgkmcnt(8)\n"
    "v_mfma_f32_16x16x32_bf16 %[q28], a[240:243], v[168:171], %[q28]\n"
    "ds_read_b64_tr_b16 a[232:233], v228 offset:16384\n"
    "v_add_f32 v189, v189, v197\n"
    "v_mfma_f32_16x16x32_bf16 %[q29], a[240:243], v[172:175], %[q29]\n"
    "ds_read_b64_tr_b16 a[234:235], v228 offset:17408\n"
    "v_add_f32 v190, v190, v197\n"
    "v_mfma_f32_16x16x32_bf16 %[q30], a[244:247], v[168:171], %[q30]\n"
    "ds_read_b64_tr_b16 a[236:237], v229 offset:16384\n"
    "v_add_f32 v191, v191, v197\n"
    "v_mfma_f32_16x16x32_bf16 %[q31], a[244:247], v[172:175], %[q31]\n"
    "ds_read_b64_tr_b16 a[238:239], v229 offset:17408\n"
    "v_add_f32 v192, v192, v197\n"
    "s_waitcnt lgkmcnt(8)\n"
    "v_mfma_f32_16x16x32_bf16 %[z0], a[248:251], v[168:171], %[z0]\n"
    "ds_read_b64_tr_b16 a[240:241], v230 offset:16384\n"
    "v_add_f32 v193, v193, v197\n"
    "v_mfma_f32_16x16x32_bf16 %[z1], a[248:251], v[172:175], %[z1]\n"
    "ds_read_b64_tr_b16 a[242:243], v230 offset:17408\n"
    "v_add_f32 v194, v194, v197\n"
    "s_waitcnt vmcnt(18)\n"
    "s_barrier\n"
    "v_mfma_f32_16x16x32_bf16 %[z2], a[252:255], v[168:171], %[z2]\n"
    "ds_read_b64_tr_b16 a[244:245], v208 offset:16384\n"
    "v_add_f32 v195, v195, v197\n"
    "v_mfma_f32_16x16x32_bf16 %[z3], a[252:255], v[172:175], %[z3]\n"
    "ds_read_b64_tr_b16 a[246:247], v208 offset:18432\n"
    "s_add_u32 s68, %[rot], 3\n"
    "s_sub_u32 s52, s68, 7\n"
    "s_cmp_ge_u32 s68, 7\n"
    "s_cselect_b32 s68, s52, s68\n"
    "s_add_u32 m0, s50, 131072\n"
    "s_lshl_b32 s71, s68, 19\n"
    "buffer_load_dwordx4 %[vwq0], s[60:63], s71 offen offset:0 lds\n"
    "s_waitcnt lgkmcnt(8)\n"
    "v_mfma_f32_16x16x32_bf16 %[z4], a[224:227], v[168:171], %[z4]\n"
    "ds_read_b64_tr_b16 a[248:249], v209 offset:16384\n"
    "v_cvt_pk_bf16_f32 v180, v188, v189\n"
    "v_mfma_f32_16x16x32_bf16 %[z5], a[224:227], v[172:175], %[z5]\n"
    "ds_read_b64_tr_b16 a[250:251], v209 offset:18432\n"
    "buffer_load_dwordx4 %[vwq1], s[60:63], s71 offen offset:1024 lds\n"
    "v_mfma_f32_16x16x32_bf16 %[z6], a[228:231], v[168:171], %[z6]\n"
    "ds_read_b64_tr_b16 a[252:253], v210 offset:16384\n"
    "v_cvt_pk_bf16_f32 v181, v190, v191\n"
    "v_mfma_f32_16x16x32_bf16 %[z7], a[228:231], v[172:175], %[z7]\n"
    "ds_read_b64_tr_b16 a[254:255], v210 offset:18432\n"
    "buffer_load_dwordx4 %[vwq2], s[60:63], s71 offen offset:2048 lds\n"
    "s_waitcnt lgkmcnt(8)\n"
    "v_mfma_f32_16x16x32_bf16 %[z8], a[232:235], v[168:171], %[z8]\n"
    "ds_read_b64_tr_b16 a[224:225], v211 offset:16384\n"
    "v_cvt_pk_bf16_f32 v182, v192, v193\n"
    "v_mfma_f32_16x16x32_bf16 %[z9], a[232:235], v[172:175], %[z9]\n"
    "ds_read_b64_tr_b16 a[226:227], v211 offset:18432\n"
    "buffer_load_dwordx4 %[vwq3], s[60:63], s71 offen offset:3072 lds\n"
    "v_mfma_f32_16x16x32_bf16 %[z10], a[236:239], v[168:171], %[z10]\n"
    "ds_read_b64_tr_b16 a[228:229], v212 offset:16384\n"
    "v_cvt_pk_bf16_f32 v183, v194, v195\n"
    "v_mfma_f32_16x16x32_bf16 %[z11], a[236:239], v[172:175], %[z11]\n"
    "ds_read_b64_tr_b16 a[230:231], v212 offset:18432\n"
    "s_add_u32 s68, %[rot], 3\n"
    "s_sub_u32 s52, s68, 7\n"
    "s_cmp_ge_u32 s68, 7\n"
    "s_cselect_b32 s68, s52, s68\n"
    "s_add_u32 m0, s50, 0\n"
    "s_mul_i32 s71, s68, 229376\n"
    "buffer_load_dwordx4 %[vwi0], s[56:59], s71 offen offset:0 lds\n"
    "s_waitcnt lgkmcnt(8)\n"
    "v_mfma_f32_16x16x32_bf16 %[z12], a[240:243], v[168:171], %[z12]\n"
    "ds_read_b64_tr_b16 a[232:233], v213 offset:16384\n"
    "v_mfma_f32_16x16x32_bf16 %[z13], a[240:243], v[172:175], %[z13]\n"
    "ds_read_b64_tr_b16 a[234:235], v213 offset:18432\n"
    "buffer_load_dwordx4 %[vwi1], s[56:59], s71 offen offset:1024 lds\n"
    "s_nop 1\n"
    "v_mfma_f32_16x16x32_bf16 v[248:251], v[176:179], v[176:179], v[248:251]\n"
    "v_mfma_f32_16x16x32_bf16 v[252:255], v[180:183], v[180:183], v[252:255]\n"
    "v_mfma_f32_16x16x32_bf16 %[q0], a[244:247], v[176:179], %[q0]\n"
    "ds_read_b64_tr_b16 a[236:237], v214 offset:16384\n"
    "v_accvgpr_read_b32 v184, a32\n"
    "v_accvgpr_read_b32 v185, a33\n"
    "v_mfma_f32_16x16x32_bf16 %[q1], a[244:247], v[180:183], %[q1]\n"
    "ds_read_b64_tr_b16 a[238:239], v214 offset:18432\n"
    "buffer_load_dwordx4 %[vwi2], s[56:59], s71 offen offset:2048 lds\n"
    "s_waitcnt lgkmcnt(8)\n"
    "v_mfma_f32_16x16x32_bf16 %[q2], a[248:251], v[176:179], %[q2]\n"
    "ds_read_b64_tr_b16 a[240:241], v215 offset:16384\n"
    "v_accvgpr_read_b32 v186, a34\n"
    "v_accvgpr_read_b32 v187, a35\n"
    "v_mfma_f32_16x16x32_bf16 %[q3], a[248:251], v[180:183], %[q3]\n"
    "ds_read_b64_tr_b16 a[242:243], v215 offset:18432\n"
    "buffer_load_dwordx4 %[vwi3], s[56:59], s71 offen offset:3072 lds\n"
    "v_mfma_f32_16x16x32_bf16 %[q4], a[252:255], v[176:179], %[q4]\n"
    "ds_read_b64_tr_b16 a[244:245], v208 offset:16640\n"
    "v_lshlrev_b32 v188, 16, v184\n"
    "v_and_b32 v189, 0xffff0000, v184\n"
    "v_mfma_f32_16x16x32_bf16 %[q5], a[252:255], v[180:183], %[q5]\n"
    "ds_read_b64_tr_b16 a[246:247], v208 offset:18688\n"
    "v_lshlrev_b32 v190, 16, v185\n"
    "v_and_b32 v191, 0xffff0000, v185\n"
    "s_waitcnt lgkmcnt(8)\n"
    "v_mfma_f32_16x16x32_bf16 %[q6], a[224:227], v[176:179], %[q6]\n"
    "ds_read_b64_tr_b16 a[248:249], v209 offset:16640\n"
    "v_lshlrev_b32 v192, 16, v186\n"
    "v_and_b32 v193, 0xffff0000, v186\n"
    "v_mfma_f32_16x16x32_bf16 %[q7], a[224:227], v[180:183], %[q7]\n"
    "ds_read_b64_tr_b16 a[250:251], v209 offset:18688\n"
    "v_lshlrev_b32 v194, 16, v187\n"
    "v_and_b32 v195, 0xffff0000, v187\n"
    "v_mfma_f32_16x16x32_bf16 %[q8], a[228:231], v[176:179], %[q8]\n"
    "ds_read_b64_tr_b16 a[252:253], v210 offset:16640\n"
    "v_add_f32 v188, v188, v196\n"
    "v_add_f32 v189, v189, v196\n"
    "v_mfma_f32_16x16x32_bf16 %[q9], a[228:231], v[180:183], %[q9]\n"
    "ds_read_b64_tr_b16 a[254:255], v210 offset:18688\n"
    "v_add_f32 v190, v190, v196\n"
    "v_add_f32 v191, v191, v196\n"
    "s_waitcnt lgkmcnt(8)\n"
    "v_mfma_f32_16x16x32_bf16 %[q10], a[232:235], v[176:179], %[q10]\n"
    "ds_read_b64_tr_b16 a[224:225], v211 offset:16640\n"
    "v_add_f32 v192, v192, v196\n"
    "v_add_f32 v193, v193, v196\n"
    "v_mfma_f32_16x16x32_bf16 %[q11], a[232:235], v[180:183], %[q11]\n"
    "ds_read_b64_tr_b16 a[226:227], v211 offset:18688\n"
    "v_add_f32 v194, v194, v196\n"
    "v_mfma_f32_16x16x32_bf16 %[q12], a[236:239], v[176:179], %[q12]\n"
    "ds_read_b64_tr_b16 a[228:229], v212 offset:16640\n"
    "v_add_f32 v195, v195, v196\n"
    "v_mfma_f32_16x16x32_bf16 %[q13], a[236:239], v[180:183], %[q13]\n"
    "ds_read_b64_tr_b16 a[230:231], v212 offset:18688\n"
    "v_cvt_pk_bf16_f32 v168, v188, v189\n"
    "s_waitcnt lgkmcnt(8)\n"
    "v_mfma_f32_16x16x32_bf16 %[q14], a[240:243], v[176:179], %[q14]\n"
    "ds_read_b64_tr_b16 a[232:233], v213 offset:16640\n"
    "v_cvt_pk_bf16_f32 v169, v190, v191\n"
    "v_mfma_f32_16x16x32_bf16 %[q15], a[240:243], v[180:183], %[q15]\n"
    "ds_read_b64_tr_b16 a[234:235], v213 offset:18688\n"
    "v_cvt_pk_bf16_f32 v170, v192, v193\n"
    "v_mfma_f32_16x16x32_bf16 %[q16], a[244:247], v[176:179], %[q16]\n"
    "ds_read_b64_tr_b16 a[236:237], v214 offset:16640\n"
    "v_cvt_pk_bf16_f32 v171, v194, v195\n"
    "v_mfma_f32_16x16x32_bf16 %[q17], a[244:247], v[180:183], %[q17]\n"
    "ds_read_b64_tr_b16 a[238:239], v214 offset:18688\n"
    "v_accvgpr_read_b32 v184, a36\n"
    "s_waitcnt lgkmcnt(8)\n"
    "v_mfma_f32_16x16x32_bf16 %[q18], a[248:251], v[176:179], %[q18]\n"
    "ds_read_b64_tr_b16 a[240:241], v215 offset:16640\n"
    "v_accvgpr_read_b32 v185, a37\n"
    "v_mfma_f32_16x16x32_bf16 %[q19], a[248:251], v[180:183], %[q19]\n"
    "ds_read_b64_tr_b16 a[242:243], v215 offset:18688\n"
    "v_accvgpr_read_b32 v186, a38\n"
    "v_mfma_f32_16x16x32_bf16 %[q20], a[252:255], v[176:179], %[q20]\n"
    "ds_read_b64_tr_b16 a[244:245], v224 offset:24576\n"
    "v_accvgpr_read_b32 v187, a39\n"
    "v_mfma_f32_16x16x32_bf16 %[q21], a[252:255], v[180:183], %[q21]\n"
    "ds_read_b64_tr_b16 a[246:247], v224 offset:25600\n"
    "v_lshlrev_b32 v188, 16, v184\n"
    "s_waitcnt lgkmcnt(8)\n"
    "v_mfma_f32_16x16x32_bf16 %[q22], a[224:227], v[176:179], %[q22]\n"
    "ds_read_b64_tr_b16 a[248:249], v225 offset:24576\n"
    "v_and_b32 v189, 0xffff0000, v184\n"
    "v_mfma_f32_16x16x32_bf16 %[q23], a[224:227], v[180:183], %[q23]\n"
    "ds_read_b64_tr_b16 a[250:251], v225 offset:25600\n"
    "v_lshlrev_b32 v190, 16, v185\n"
    "v_mfma_f32_16x16x32_bf16 %[q24], a[228:231], v[176:179], %[q24]\n"
    "ds_read_b64_tr_b16 a[252:253], v226 offset:24576\n"
    "v_and_b32 v191, 0xffff0000, v185\n"
    "v_mfma_f32_16x16x32_bf16 %[q25], a[228:231], v[180:183], %[q25]\n"
    "ds_read_b64_tr_b16 a[254:255], v226 offset:25600\n"
    "v_lshlrev_b32 v192, 16, v186\n"
    "s_waitcnt lgkmcnt(8)\n"
    "v_mfma_f32_16x16x32_bf16 %[q26], a[232:235], v[176:179], %[q26]\n"
    "ds_read_b64_tr_b16 a[224:225], v227 offset:24576\n"
    "v_and_b32 v193, 0xffff0000, v186\n"
    "v_mfma_f32_16x16x32_bf16 %[q27], a[232:235], v[180:183], %[q27]\n"
    "ds_read_b64_tr_b16 a[226:227], v227 offset:25600\n"
    "v_lshlrev_b32 v194, 16, v187\n"
    "v_mfma_f32_16x16x32_bf16 %[q28], a[236:239], v[176:179], %[q28]\n"
    "ds_read_b64_tr_b16 a[228:229], v228 offset:24576\n"
    "v_and_b32 v195, 0xffff0000, v187\n"
    "v_mfma_f32_16x16x32_bf16 %[q29], a[236:239], v[180:183], %[q29]\n"
    "ds_read_b64_tr_b16 a[230:231], v228 offset:25600\n"
    "v_add_f32 v188, v188, v197\n"
    "s_waitcnt lgkmcnt(8)\n"
    "v_mfma_f32_16x16x32_bf16 %[q30], a[240:243], v[176:179], %[q30]\n"
    "ds_read_b64_tr_b16 a[232:233], v229 offset:24576\n"
    "v_add_f32 v189, v189, v197\n"
    "v_mfma_f32_16x16x32_bf16 %[q31], a[240:243], v[180:183], %[q31]\n"
    "ds_read_b64_tr_b16 a[234:235], v229 offset:25600\n"
    "v_add_f32 v190, v190, v197\n"
    "v_mfma_f32_16x16x32_bf16 %[z0], a[244:247], v[176:179], %[z0]\n"
    "ds_read_b64_tr_b16 a[236:237], v230 offset:24576\n"
    "v_add_f32 v191, v191, v197\n"
    "v_mfma_f32_16x16x32_bf16 %[z1], a[244:247], v[180:183], %[z1]\n"
    "ds_read_b64_tr_b16 a[238:239], v230 offset:25600\n"
    "v_add_f32 v192, v192, v197\n"
    "s_waitcnt lgkmcnt(8)\n"
    "s_waitcnt vmcnt(18)\n"
    "s_barrier\n"
    "v_mfma_f32_16x16x32_bf16 %[z2], a[248:251], v[176:179], %[z2]\n"
    "ds_read_b64_tr_b16 a[240:241], v208 offset:32768\n"
    "v_add_f32 v193, v193, v197\n"
    "v_mfma_f32_16x16x32_bf16 %[z3], a[248:251], v[180:183], %[z3]\n"
    "ds_read_b64_tr_b16 a[242:243], v208 offset:34816\n"
    "s_add_u32 s68, %[rot], 3\n"
    "s_sub_u32 s52, s68, 7\n"
    "s_cmp_ge_u32 s68, 7\n"
    "s_cselect_b32 s68, s52, s68\n"
    "s_add_u32 m0, s50, 32768\n"
    "s_lshl_b32 s71, s68, 19\n"
    "s_add_u32 s71, s71, 262144\n"
    "buffer_load_dwordx4 %[vwq0], s[60:63], s71 offen offset:0 lds\n"
    "v_mfma_f32_16x16x32_bf16 %[z4], a[252:255], v[176:179], %[z4]\n"
    "ds_read_b64_tr_b16 a[244:245], v209 offset:32768\n"
    "v_add_f32 v194, v194, v197\n"
    "v_mfma_f32_16x16x32_bf16 %[z5], a[252:255], v[180:183], %[z5]\n"
    "ds_read_b64_tr_b16 a[246:247], v209 offset:34816\n"
    "buffer_load_dwordx4 %[vwq1], s[60:63], s71 offen offset:1024 lds\n"
    "s_waitcnt lgkmcnt(8)\n"
    "v_mfma_f32_16x16x32_bf16 %[z6], a[224:227], v[176:179], %[z6]\n"
    "ds_read_b64_tr_b16 a[248:249], v210 offset:32768\n"
    "v_add_f32 v195, v195, v197\n"
    "v_mfma_f32_16x16x32_bf16 %[z7], a[224:227], v[180:183], %[z7]\n"
    "ds_read_b64_tr_b16 a[250:251], v210 offset:34816\n"
    "buffer_load_dwordx4 %[vwq2], s[60:63], s71 offen offset:2048 lds\n"
    "v_mfma_f32_16x16x32_bf16 %[z8], a[228:231], v[176:179], %[z8]\n"
    "ds_read_b64_tr_b16 a[252:253], v211 offset:32768\n"
    "v_cvt_pk_bf16_f32 v172, v188, v189\n"
    "v_mfma_f32_16x16x32_bf16 %[z9], a[228:231], v[180:183], %[z9]\n"
    "ds_read_b64_tr_b16 a[254:255], v211 offset:34816\n"
    "buffer_load_dwordx4 %[vwq3], s[60:63], s71 offen offset:3072 lds\n"
    "s_waitcnt lgkmcnt(8)\n"
    "v_mfma_f32_16x16x32_bf16 %[z10], a[232:235], v[176:179], %[z10]\n"
    "ds_read_b64_tr_b16 a[224:225], v212 offset:32768\n"
    "v_cvt_pk_bf16_f32 v173, v190, v191\n"
    "v_mfma_f32_16x16x32_bf16 %[z11], a[232:235], v[180:183], %[z11]\n"
    "ds_read_b64_tr_b16 a[226:227], v212 offset:34816\n"
    "v_cvt_pk_bf16_f32 v174, v192, v193\n"
    "v_mfma_f32_16x16x32_bf16 %[z12], a[236:239], v[176:179], %[z12]\n"
    "ds_read_b64_tr_b16 a[228:229], v213 offset:32768\n"
    "v_cvt_pk_bf16_f32 v175, v194, v195\n"
    "v_mfma_f32_16x16x32_bf16 %[z13], a[236:239], v[180:183], %[z13]\n"
    "ds_read_b64_tr_b16 a[230:231], v213 offset:34816\n"
    "s_waitcnt lgkmcnt(8)\n"
    "s_nop 1\n"
    "v_mfma_f32_16x16x32_bf16 v[248:251], v[168:171], v[168:171], v[248:251]\n"
    "v_mfma_f32_16x16x32_bf16 v[252:255], v[172:175], v[172:175], v[252:255]\n"
    "v_mfma_f32_16x16x32_bf16 %[q0], a[240:243], v[168:171], %[q0]\n"
    "ds_read_b64_tr_b16 a[232:233], v214 offset:32768\n"
    "v_accvgpr_read_b32 v184, a40\n"
    "v_accvgpr_read_b32 v185, a41\n"
    "v_mfma_f32_16x16x32_bf16 %[q1], a[240:243], v[172:175], %[q1]\n"
    "ds_read_b64_tr_b16 a[234:235], v214 offset:34816\n"
    "v_accvgpr_read_b32 v186, a42\n"
    "v_accvgpr_read_b32 v187, a43\n"
    "v_mfma_f32_16x16x32_bf16 %[q2], a[244:247], v[168:171], %[q2]\n"
    "ds_read_b64_tr_b16 a[236:237], v215 offset:32768\n"
    "v_lshlrev_b32 v188, 16, v184\n"
    "v_and_b32 v189, 0xffff0000, v184\n"
    "v_mfma_f32_16x16x32_bf16 %[q3], a[244:247], v[172:175], %[q3]\n"
    "ds_read_b64_tr_b16 a[238:239], v215 offset:34816\n"
    "v_lshlrev_b32 v190, 16, v185\n"
    "v_and_b32 v191, 0xffff0000, v185\n"
    "s_waitcnt lgkmcnt(8)\n"
    "v_mfma_f32_16x16x32_bf16 %[q4], a[248:251], v[168:171], %[q4]\n"
    "ds_read_b64_tr_b16 a[240:241], v208 offset:33024\n"
    "v_lshlrev_b32 v192, 16, v186\n"
    "v_and_b32 v193, 0xffff0000, v186\n"
    "v_mfma_f32_16x16x32_bf16 %[q5], a[248:251], v[172:175], %[q5]\n"
    "ds_read_b64_tr_b16 a[242:243], v208 offset:35072\n"
    "v_lshlrev_b32 v194, 16, v187\n"
    "v_and_b32 v195, 0xffff0000, v187\n"
    "v_mfma_f32_16x16x32_bf16 %[q6], a[252:255], v[168:171], %[q6]\n"
    "ds_read_b64_tr_b16 a[244:245], v209 offset:33024\n"
    "v_add_f32 v188, v188, v196\n"
    "v_add_f32 v189, v189, v196\n"
    "v_mfma_f32_16x16x32_bf16 %[q7], a[252:255], v[172:175], %[q7]\n"
    "ds_read_b64_tr_b16 a[246:247], v209 offset:35072\n"
    "v_add_f32 v190, v190, v196\n"
    "v_add_f32 v191, v191, v196\n"
    "s_waitcnt lgkmcnt(8)\n"
    "v_mfma_f32_16x16x32_bf16 %[q8], a[224:227], v[168:171], %[q8]\n"
    "ds_read_b64_tr_b16 a[248:249], v210 offset:33024\n"
    "v_add_f32 v192, v192, v196\n"
    "v_add_f32 v193, v193, v196\n"
    "v_mfma_f32_16x16x32_bf16 %[q9], a[224:227], v[172:175], %[q9]\n"
    "ds_read_b64_tr_b16 a[250:251], v210 offset:35072\n"
    "v_add_f32 v194, v194, v196\n"
    "v_mfma_f32_16x16x32_bf16 %[q10], a[228:231], v[168:171], %[q10]\n"
    "ds_read_b64_tr_b16 a[252:253], v211 offset:33024\n"
    "v_add_f32 v195, v195, v196\n"
    "v_mfma_f32_16x16x32_bf16 %[q11], a[228:231], v[172:175], %[q11]\n"
    "ds_read_b64_tr_b16 a[254:255], v211 offset:35072\n"
    "v_cvt_pk_bf16_f32 v176, v188, v189\n"
    "s_waitcnt lgkmcnt(8)\n"
    "v_mfma_f32_16x16x32_bf16 %[q12], a[232:235], v[168:171], %[q12]\n"
    "ds_read_b64_tr_b16 a[224:225], v212 offset:33024\n"
    "v_cvt_pk_bf16_f32 v177, v190, v191\n"
    "v_mfma_f32_16x16x32_bf16 %[q13], a[232:235], v[172:175], %[q13]\n"
    "ds_read_b64_tr_b16 a[226:227], v212 offset:35072\n"
    "v_cvt_pk_bf16_f32 v178, v192, v193\n"
    "v_mfma_f32_16x16x32_bf16 %[q14], a[236:239], v[168:171], %[q14]\n"
    "ds_read_b64_tr_b16 a[228:229], v213 offset:33024\n"
    "v_cvt_pk_bf16_f32 v179, v194, v195\n"
    "v_mfma_f32_16x16x32_bf16 %[q15], a[236:239], v[172:175], %[q15]\n"
    "ds_read_b64_tr_b16 a[230:231], v213 offset:35072\n"
    "v_accvgpr_read_b32 v184, a44\n"
    "s_waitcnt lgkmcnt(8)\n"
    "v_mfma_f32_16x16x32_bf16 %[q16], a[240:243], v[168:171], %[q16]\n"
    "ds_read_b64_tr_b16 a[232:233], v214 offset:33024\n"
    "v_accvgpr_read_b32 v185, a45\n"
    "v_mfma_f32_16x16x32_bf16 %[q17], a[240:243], v[172:175], %[q17]\n"
    "ds_read_b64_tr_b16 a[234:235], v214 offset:35072\n"
    "v_accvgpr_read_b32 v186, a46\n"
    "v_mfma_f32_16x16x32_bf16 %[q18], a[244:247], v[168:171], %[q18]\n"
    "ds_read_b64_tr_b16 a[236:237], v215 offset:33024\n"
    "v_accvgpr_read_b32 v187, a47\n"
    "v_mfma_f32_16x16x32_bf16 %[q19], a[244:247], v[172:175], %[q19]\n"
    "ds_read_b64_tr_b16 a[238:239], v215 offset:35072\n"
    "v_lshlrev_b32 v188, 16, v184\n"
    "s_waitcnt lgkmcnt(8)\n"
    "v_mfma_f32_16x16x32_bf16 %[q20], a[248:251], v[168:171], %[q20]\n"
    "ds_read_b64_tr_b16 a[240:241], v238 offset:16384\n"
    "v_and_b32 v189, 0xffff0000, v184\n"
    "v_mfma_f32_16x16x32_bf16 %[q21], a[248:251], v[172:175], %[q21]\n"
    "ds_read_b64_tr_b16 a[242:243], v238 offset:17408\n"
    "v_lshlrev_b32 v190, 16, v185\n"
    "v_mfma_f32_16x16x32_bf16 %[q22], a[252:255], v[168:171], %[q22]\n"
    "ds_read_b64_tr_b16 a[244:245], v239 offset:16384\n"
    "v_and_b32 v191, 0xffff0000, v185\n"
    "v_mfma_f32_16x16x32_bf16 %[q23], a[252:255], v[172:175], %[q23]\n"
    "ds_read_b64_tr_b16 a[246:247], v239 offset:17408\n"
    "v_lshlrev_b32 v192, 16, v186\n"
    "s_waitcnt lgkmcnt(8)\n"
    "v_mfma_f32_16x16x32_bf16 %[q24], a[224:227], v[168:171], %[q24]\n"
    "ds_read_b64_tr_b16 a[248:249], v240 offset:16384\n"
    "v_and_b32 v193, 0xffff0000, v186\n"
    "v_mfma_f32_16x16x32_bf16 %[q25], a[224:227], v[172:175], %[q25]\n"
    "ds_read_b64_tr_b16 a[250:251], v240 offset:17408\n"
    "v_lshlrev_b32 v194, 16, v187\n"
    "v_mfma_f32_16x16x32_bf16 %[q26], a[228:231], v[168:171], %[q26]\n"
    "ds_read_b64_tr_b16 a[252:253], v241 offset:16384\n"
    "v_and_b32 v195, 0xffff0000, v187\n"
    "v_mfma_f32_16x16x32_bf16 %[q27], a[228:231], v[172:175], %[q27]\n"
    "ds_read_b64_tr_b16 a[254:255], v241 offset:17408\n"
    "v_add_f32 v188, v188, v197\n"
    "s_waitcnt lgkmcnt(8)\n"
    "v_mfma_f32_16x16x32_bf16 %[q28], a[232:235], v[168:171], %[q28]\n"
    "ds_read_b64_tr_b16 a[224:225], v242 offset:16384\n"
    "v_add_f32 v189, v189, v197\n"
    "v_mfma_f32_16x16x32_bf16 %[q29], a[232:235], v[172:175], %[q29]\n"
    "ds_read_b64_tr_b16 a[226:227], v242 offset:17408\n"
    "v_add_f32 v190, v190, v197\n"
    "v_mfma_f32_16x16x32_bf16 %[q30], a[236:239], v[168:171], %[q30]\n"
    "ds_read_b64_tr_b16 a[228:229], v243 offset:16384\n"
    "v_add_f32 v191, v191, v197\n"
    "v_mfma_f32_16x16x32_bf16 %[q31], a[236:239], v[172:175], %[q31]\n"
    "ds_read_b64_tr_b16 a[230:231], v243 offset:17408\n"
    "v_add_f32 v192, v192, v197\n"
    "s_waitcnt lgkmcnt(8)\n"
    "v_mfma_f32_16x16x32_bf16 %[z0], a[240:243], v[168:171], %[z0]\n"
    "ds_read_b64_tr_b16 a[232:233], v244 offset:16384\n"
    "v_add_f32 v193, v193, v197\n"
    "v_mfma_f32_16x16x32_bf16 %[z1], a[240:243], v[172:175], %[z1]\n"
    "ds_read_b64_tr_b16 a[234:235], v244 offset:17408\n"
    "v_add_f32 v194, v194, v197\n"
    "s_waitcnt vmcnt(12)\n"
    "s_barrier\n"
    "v_mfma_f32_16x16x32_bf16 %[z2], a[244:247], v[168:171], %[z2]\n"
    "ds_read_b64_tr_b16 a[236:237], v208 offset:49152\n"
    "v_add_f32 v195, v195, v197\n"
    "v_mfma_f32_16x16x32_bf16 %[z3], a[244:247], v[172:175], %[z3]\n"
    "ds_read_b64_tr_b16 a[238:239], v208 offset:51200\n"
    "s_add_u32 s68, %[rot], 4\n"
    "s_sub_u32 s52, s68, 7\n"
    "s_cmp_ge_u32 s68, 7\n"
    "s_cselect_b32 s68, s52, s68\n"
    "s_add_u32 m0, s50, 49152\n"
    "s_lshl_b32 s71, s68, 19\n"
    "buffer_load_dwordx4 %[vwq0], s[60:63], s71 offen offset:0 lds\n"
    "s_waitcnt lgkmcnt(8)\n"
    "v_mfma_f32_16x16x32_bf16 %[z4], a[248:251], v[168:171], %[z4]\n"
    "ds_read_b64_tr_b16 a[240:241], v209 offset:49152\n"
    "v_cvt_pk_bf16_f32 v180, v188, v189\n"
    "v_mfma_f32_16x16x32_bf16 %[z5], a[248:251], v[172:175], %[z5]\n"
    "ds_read_b64_tr_b16 a[242:243], v209 offset:51200\n"
    "buffer_load_dwordx4 %[vwq1], s[60:63], s71 offen offset:1024 lds\n"
    "v_mfma_f32_16x16x32_bf16 %[z6], a[252:255], v[168:171], %[z6]\n"
    "ds_read_b64_tr_b16 a[244:245], v210 offset:49152\n"
    "v_cvt_pk_bf16_f32 v181, v190, v191\n"
    "v_mfma_f32_16x16x32_bf16 %[z7], a[252:255], v[172:175], %[z7]\n"
    "ds_read_b64_tr_b16 a[246:247], v210 offset:51200\n"
    "buffer_load_dwordx4 %[vwq2], s[60:63], s71 offen offset:2048 lds\n"
    "s_waitcnt lgkmcnt(8)\n"
    "v_mfma_f32_16x16x32_bf16 %[z8], a[224:227], v[168:171], %[z8]\n"
    "ds_read_b64_tr_b16 a[248:249], v211 offset:49152\n"
    "v_cvt_pk_bf16_f32 v182, v192, v193\n"
    "v_mfma_f32_16x16x32_bf16 %[z9], a[224:227], v[172:175], %[z9]\n"
    "ds_read_b64_tr_b16 a[250:251], v211 offset:51200\n"
    "buffer_load_dwordx4 %[vwq3], s[60:63], s71 offen offset:3072 lds\n"
    "v_mfma_f32_16x16x32_bf16 %[z10], a[228:231], v[168:171], %[z10]\n"
    "ds_read_b64_tr_b16 a[252:253], v212 offset:49152\n"
    "v_cvt_pk_bf16_f32 v183, v194, v195\n"
    "v_mfma_f32_16x16x32_bf16 %[z11], a[228:231], v[172:175], %[z11]\n"
    "ds_read_b64_tr_b16 a[254:255], v212 offset:51200\n"
    "s_add_u32 s68, %[rot], 4\n"
    "s_sub_u32 s52, s68, 7\n"
    "s_cmp_ge_u32 s68, 7\n"
    "s_cselect_b32 s68, s52, s68\n"
    "s_add_u32 m0, s50, 16384\n"
    "s_mul_i32 s71, s68, 229376\n"
    "buffer_load_dwordx4 %[vwi0], s[56:59], s71 offen offset:0 lds\n"
    "s_waitcnt lgkmcnt(8)\n"
    "v_mfma_f32_16x16x32_bf16 %[z12], a[232:235], v[168:171], %[z12]\n"
    "ds_read_b64_tr_b16 a[224:225], v213 offset:49152\n"
    "v_mfma_f32_16x16x32_bf16 %[z13], a[232:235], v[172:175], %[z13]\n"
    "ds_read_b64_tr_b16 a[226:227], v213 offset:51200\n"
    "buffer_load_dwordx4 %[vwi1], s[56:59], s71 offen offset:1024 lds\n"
    "s_nop 1\n"
    "v_mfma_f32_16x16x32_bf16 v[248:251], v[176:179], v[176:179], v[248:251]\n"
    "v_mfma_f32_16x16x32_bf16 v[252:255], v[180:183], v[180:183], v[252:255]\n"
    "v_mfma_f32_16x16x32_bf16 %[q0], a[236:239], v[176:179], %[q0]\n"
    "ds_read_b64_tr_b16 a[228:229], v214 offset:49152\n"
    "v_accvgpr_read_b32 v184, a48\n"
    "v_accvgpr_read_b32 v185, a49\n"
    "v_mfma_f32_16x16x32_bf16 %[q1], a[236:239], v[180:183], %[q1]\n"
    "ds_read_b64_tr_b16 a[230:231], v214 offset:51200\n"
    "buffer_load_dwordx4 %[vwi2], s[56:59], s71 offen offset:2048 lds\n"
    "s_waitcnt lgkmcnt(8)\n"
    "v_mfma_f32_16x16x32_bf16 %[q2], a[240:243], v[176:179], %[q2]\n"
    "ds_read_b64_tr_b16 a[232:233], v215 offset:49152\n"
    "v_accvgpr_read_b32 v186, a50\n"
    "v_accvgpr_read_b32 v187, a51\n"
    "v_mfma_f32_16x16x32_bf16 %[q3], a[240:243], v[180:183], %[q3]\n"
    "ds_read_b64_tr_b16 a[234:235], v215 offset:51200\n"
    "buffer_load_dwordx4 %[vwi3], s[56:59], s71 offen offset:3072 lds\n"
    "v_mfma_f32_16x16x32_bf16 %[q4], a[244:247], v[176:179], %[q4]\n"
    "ds_read_b64_tr_b16 a[236:237], v208 offset:49408\n"
    "v_lshlrev_b32 v188, 16, v184\n"
    "v_and_b32 v189, 0xffff0000, v184\n"
    "v_mfma_f32_16x16x32_bf16 %[q5], a[244:247], v[180:183], %[q5]\n"
    "ds_read_b64_tr_b16 a[238:239], v208 offset:51456\n"
    "v_lshlrev_b32 v190, 16, v185\n"
    "v_and_b32 v191, 0xffff0000, v185\n"
    "s_waitcnt lgkmcnt(8)\n"
    "v_mfma_f32_16x16x32_bf16 %[q6], a[248:251], v[176:179], %[q6]\n"
    "ds_read_b64_tr_b16 a[240:241], v209 offset:49408\n"
    "v_lshlrev_b32 v192, 16, v186\n"
    "v_and_b32 v193, 0xffff0000, v186\n"
    "v_mfma_f32_16x16x32_bf16 %[q7], a[248:251], v[180:183], %[q7]\n"
    "ds_read_b64_tr_b16 a[242:243], v209 offset:51456\n"
    "v_lshlrev_b32 v194, 16, v187\n"
    "v_and_b32 v195, 0xffff0000, v187\n"
    "v_mfma_f32_16x16x32_bf16 %[q8], a[252:255], v[176:179], %[q8]\n"
    "ds_read_b64_tr_b16 a[244:245], v210 offset:49408\n"
    "v_add_f32 v188, v188, v196\n"
    "v_add_f32 v189, v189, v196\n"
    "v_mfma_f32_16x16x32_bf16 %[q9], a[252:255], v[180:183], %[q9]\n"
    "ds_read_b64_tr_b16 a[246:247], v210 offset:51456\n"
    "v_add_f32 v190, v190, v196\n"
    "v_add_f32 v191, v191, v196\n"
    "s_waitcnt lgkmcnt(8)\n"
    "v_mfma_f32_16x16x32_bf16 %[q10], a[224:227], v[176:179], %[q10]\n"
    "ds_read_b64_tr_b16 a[248:249], v211 offset:49408\n"
    "v_add_f32 v192, v192, v196\n"
    "v_add_f32 v193, v193, v196\n"
    "v_mfma_f32_16x16x32_bf16 %[q11], a[224:227], v[180:183], %[q11]\n"
    "ds_read_b64_tr_b16 a[250:251], v211 offset:51456\n"
    "v_add_f32 v194, v194, v196\n"
    "v_mfma_f32_16x16x32_bf16 %[q12], a[228:231], v[176:179], %[q12]\n"
    "ds_read_b64_tr_b16 a[252:253], v212 offset:49408\n"
    "v_add_f32 v195, v195, v196\n"
    "v_mfma_f32_16x16x32_bf16 %[q13], a[228:231], v[180:183], %[q13]\n"
    "ds_read_b64_tr_b16 a[254:255], v212 offset:51456\n"
    "v_cvt_pk_bf16_f32 v168, v188, v189\n"
    "s_waitcnt lgkmcnt(8)\n"
    "v_mfma_f32_16x16x32_bf16 %[q14], a[232:235], v[176:179], %[q14]\n"
    "ds_read_b64_tr_b16 a[224:225], v213 offset:49408\n"
    "v_cvt_pk_bf16_f32 v169, v190, v191\n"
    "v_mfma_f32_16x16x32_bf16 %[q15], a[232:235], v[180:183], %[q15]\n"
    "ds_read_b64_tr_b16 a[226:227], v213 offset:51456\n"
    "v_cvt_pk_bf16_f32 v170, v192, v193\n"
    "v_mfma_f32_16x16x32_bf16 %[q16], a[236:239], v[176:179], %[q16]\n"
    "ds_read_b64_tr_b16 a[228:229], v214 offset:49408\n"
    "v_cvt_pk_bf16_f32 v171, v194, v195\n"
    "v_mfma_f32_16x16x32_bf16 %[q17], a[236:239], v[180:183], %[q17]\n"
    "ds_read_b64_tr_b16 a[230:231], v214 offset:51456\n"
    "v_accvgpr_read_b32 v184, a52\n"
    "s_waitcnt lgkmcnt(8)\n"
    "v_mfma_f32_16x16x32_bf16 %[q18], a[240:243], v[176:179], %[q18]\n"
    "ds_read_b64_tr_b16 a[232:233], v215 offset:49408\n"
    "v_accvgpr_read_b32 v185, a53\n"
    "v_mfma_f32_16x16x32_bf16 %[q19], a[240:243], v[180:183], %[q19]\n"
    "ds_read_b64_tr_b16 a[234:235], v215 offset:51456\n"
    "v_accvgpr_read_b32 v186, a54\n"
    "v_mfma_f32_16x16x32_bf16 %[q20], a[244:247], v[176:179], %[q20]\n"
    "ds_read_b64_tr_b16 a[236:237], v238 offset:24576\n"
    "v_accvgpr_read_b32 v187, a55\n"
    "v_mfma_f32_16x16x32_bf16 %[q21], a[244:247], v[180:183], %[q21]\n"
    "ds_read_b64_tr_b16 a[238:239], v238 offset:25600\n"
    "v_lshlrev_b32 v188, 16, v184\n"
    "s_waitcnt lgkmcnt(8)\n"
    "v_mfma_f32_16x16x32_bf16 %[q22], a[248:251], v[176:179], %[q22]\n"
    "ds_read_b64_tr_b16 a[240:241], v239 offset:24576\n"
    "v_and_b32 v189, 0xffff0000, v184\n"
    "v_mfma_f32_16x16x32_bf16 %[q23], a[248:251], v[180:183], %[q23]\n"
    "ds_read_b64_tr_b16 a[242:243], v239 offset:25600\n"
    "v_lshlrev_b32 v190, 16, v185\n"
    "v_mfma_f32_16x16x32_bf16 %[q24], a[252:255], v[176:179], %[q24]\n"
    "ds_read_b64_tr_b16 a[244:245], v240 offset:24576\n"
    "v_and_b32 v191, 0xffff0000, v185\n"
    "v_mfma_f32_16x16x32_bf16 %[q25], a[252:255], v[180:183], %[q25]\n"
    "ds_read_b64_tr_b16 a[246:247], v240 offset:25600\n"
    "v_lshlrev_b32 v192, 16, v186\n"
    "s_waitcnt lgkmcnt(8)\n"
    "v_mfma_f32_16x16x32_bf16 %[q26], a[224:227], v[176:179], %[q26]\n"
    "ds_read_b64_tr_b16 a[248:249], v241 offset:24576\n"
    "v_and_b32 v193, 0xffff0000, v186\n"
    "v_mfma_f32_16x16x32_bf16 %[q27], a[224:227], v[180:183], %[q27]\n"
    "ds_read_b64_tr_b16 a[250:251], v241 offset:25600\n"
    "v_lshlrev_b32 v194, 16, v187\n"
    "v_mfma_f32_16x16x32_bf16 %[q28], a[228:231], v[176:179], %[q28]\n"
    "ds_read_b64_tr_b16 a[252:253], v242 offset:24576\n"
    "v_and_b32 v195, 0xffff0000, v187\n"
    "v_mfma_f32_16x16x32_bf16 %[q29], a[228:231], v[180:183], %[q29]\n"
    "ds_read_b64_tr_b16 a[254:255], v242 offset:25600\n"
    "v_add_f32 v188, v188, v197\n"
    "s_waitcnt lgkmcnt(8)\n"
    "v_mfma_f32_16x16x32_bf16 %[q30], a[232:235], v[176:179], %[q30]\n"
    "ds_read_b64_tr_b16 a[224:225], v243 offset:24576\n"
    "v_add_f32 v189, v189, v197\n"
    "v_mfma_f32_16x16x32_bf16 %[q31], a[232:235], v[180:183], %[q31]\n"
    "ds_read_b64_tr_b16 a[226:227], v243 offset:25600\n"
    "v_add_f32 v190, v190, v197\n"
    "v_mfma_f32_16x16x32_bf16 %[z0], a[236:239], v[176:179], %[z0]\n"
    "ds_read_b64_tr_b16 a[228:229], v244 offset:24576\n"
    "v_add_f32 v191, v191, v197\n"
    "v_mfma_f32_16x16x32_bf16 %[z1], a[236:239], v[180:183], %[z1]\n"
    "ds_read_b64_tr_b16 a[230:231], v244 offset:25600\n"
    "v_add_f32 v192, v192, v197\n"
    "s_waitcnt lgkmcnt(8)\n"
    "s_waitcnt vmcnt(12)\n"
    "s_barrier\n"
    "v_mfma_f32_16x16x32_bf16 %[z2], a[240:243], v[176:179], %[z2]\n"
    "ds_read_b64_tr_b16 a[232:233], v216 offset:0\n"
    "v_add_f32 v193, v193, v197\n"
    "v_mfma_f32_16x16x32_bf16 %[z3], a[240:243], v[180:183], %[z3]\n"
    "ds_read_b64_tr_b16 a[234:235], v216 offset:2048\n"
    "s_add_u32 s68, %[rot], 4\n"
    "s_sub_u32 s52, s68, 7\n"
    "s_cmp_ge_u32 s68, 7\n"
    "s_cselect_b32 s68, s52, s68\n"
    "s_add_u32 m0, s50, 65536\n"
    "s_lshl_b32 s71, s68, 19\n"
    "s_add_u32 s71, s71, 262144\n"
    "buffer_load_dwordx4 %[vwq0], s[60:63], s71 offen offset:0 lds\n"
    "v_mfma_f32_16x16x32_bf16 %[z4], a[244:247], v[176:179], %[z4]\n"
    "ds_read_b64_tr_b16 a[236:237], v217 offset:0\n"
    "v_add_f32 v194, v194, v197\n"
    "v_mfma_f32_16x16x32_bf16 %[z5], a[244:247], v[180:183], %[z5]\n"
    "ds_read_b64_tr_b16 a[238:239], v217 offset:2048\n"
    "buffer_load_dwordx4 %[vwq1], s[60:63], s71 offen offset:1024 lds\n"
    "s_waitcnt lgkmcnt(8)\n"
    "v_mfma_f32_16x16x32_bf16 %[z6], a[248:251], v[176:179], %[z6]\n"
    "ds_read_b64_tr_b16 a[240:241], v218 offset:0\n"
    "v_add_f32 v195, v195, v197\n"
    "v_mfma_f32_16x16x32_bf16 %[z7], a[248:251], v[180:183], %[z7]\n"
    "ds_read_b64_tr_b16 a[242:243], v218 offset:2048\n"
    "buffer_load_dwordx4 %[vwq2], s[60:63], s71 offen offset:2048 lds\n"
    "v_mfma_f32_16x16x32_bf16 %[z8], a[252:255], v[176:179], %[z8]\n"
    "ds_read_b64_tr_b16 a[244:245], v219 offset:0\n"
    "v_cvt_pk_bf16_f32 v172, v188, v189\n"
    "v_mfma_f32_16x16x32_bf16 %[z9], a[252:255], v[180:183], %[z9]\n"
    "ds_read_b64_tr_b16 a[246:247], v219 offset:2048\n"
    "buffer_load_dwordx4 %[vwq3], s[60:63], s71 offen offset:3072 lds\n"
    "s_waitcnt lgkmcnt(8)\n"
    "v_mfma_f32_16x16x32_bf16 %[z10], a[224:227], v[176:179], %[z10]\n"
    "ds_read_b64_tr_b16 a[248:249], v220 offset:0\n"
    "v_cvt_pk_bf16_f32 v173, v190, v191\n"
    "v_mfma_f32_16x16x32_bf16 %[z11], a[224:227], v[180:183], %[z11]\n"
    "ds_read_b64_tr_b16 a[250:251], v220 offset:2048\n"
    "v_cvt_pk_bf16_f32 v174, v192, v193\n"
    "v_mfma_f32_16x16x32_bf16 %[z12], a[228:231], v[176:179], %[z12]\n"
    "ds_read_b64_tr_b16 a[252:253], v221 offset:0\n"
    "v_cvt_pk_bf16_f32 v175, v194, v195\n"
    "v_mfma_f32_16x16x32_bf16 %[z13], a[228:231], v[180:183], %[z13]\n"
    "ds_read_b64_tr_b16 a[254:255], v221 offset:2048\n"
    "s_waitcnt lgkmcnt(8)\n"
    "s_nop 1\n"
    "v_mfma_f32_16x16x32_bf16 v[248:251], v[168:171], v[168:171], v[248:251]\n"
    "v_mfma_f32_16x16x32_bf16 v[252:255], v[172:175], v[172:175], v[252:255]\n"
    "s_add_u32 s72, %[wslo], 58720256\n"
    "s_addc_u32 s73, %[wshi], 0\n"
    "s_and_b32 s73, s73, 0xffff\n"
    "s_mov_b32 s74, 3670016\n"
    "s_mov_b32 s75, 0x20000\n"
    "s_add_u32 s76, %[wslo], 62914560\n"
    "s_addc_u32 s77, %[wshi], 0\n"
    "s_and_b32 s77, s77, 0xffff\n"
    "s_mov_b32 s78, 802816\n"
    "s_mov_b32 s79, 0x20000\n"
    "s_mul_i32 s80, %[rw], 896\n"
    "s_mul_i32 s81, %[rt], 224\n"
    "buffer_store_dwordx4 a[200:203], %[vrd0], s[72:75], s80 offen offset:0 sc1\n"
    "buffer_store_dwordx4 a[204:207], %[vrd0], s[72:75], s80 offen offset:224 sc1\n"
    "buffer_store_dwordx4 a[208:211], %[vrd0], s[72:75], s80 offen offset:448 sc1\n"
    "buffer_store_dwordx4 a[212:215], %[vrd0], s[72:75], s80 offen offset:672 sc1\n"
    "buffer_store_dwordx4 a[216:219], %[vrd1], s[76:79], s81 offen sc1\n"
    "v_mfma_f32_16x16x32_bf16 %[q0], a[232:235], v[168:171], %[q0]\n"
    "ds_read_b64_tr_b16 a[224:225], v222 offset:0\n"
    "v_accvgpr_read_b32 v184, a56\n"
    "v_accvgpr_read_b32 v185, a57\n"
    "v_mfma_f32_16x16x32_bf16 %[q1], a[232:235], v[172:175], %[q1]\n"
    "ds_read_b64_tr_b16 a[226:227], v222 offset:2048\n"
    "v_accvgpr_read_b32 v186, a58\n"
    "v_accvgpr_read_b32 v187, a59\n"
    "v_mfma_f32_16x16x32_bf16 %[q2], a[236:239], v[168:171], %[q2]\n"
    "ds_read_b64_tr_b16 a[228:229], v223 offset:0\n"
    "v_lshlrev_b32 v188, 16, v184\n"
    "v_and_b32 v189, 0xffff0000, v184\n"
    "v_mfma_f32_16x16x32_bf16 %[q3], a[236:239], v[172:175], %[q3]\n"
    "ds_read_b64_tr_b16 a[230:231], v223 offset:2048\n"
    "v_lshlrev_b32 v190, 16, v185\n"
    "v_and_b32 v191, 0xffff0000, v185\n"
    "s_waitcnt lgkmcnt(8)\n"
    "v_mfma_f32_16x16x32_bf16 %[q4], a[240:243], v[168:171], %[q4]\n"
    "ds_read_b64_tr_b16 a[232:233], v216 offset:256\n"
    "v_lshlrev_b32 v192, 16, v186\n"
    "v_and_b32 v193, 0xffff0000, v186\n"
    "v_mfma_f32_16x16x32_bf16 %[q5], a[240:243], v[172:175], %[q5]\n"
    "ds_read_b64_tr_b16 a[234:235], v216 offset:2304\n"
    "v_lshlrev_b32 v194, 16, v187\n"
    "v_and_b32 v195, 0xffff0000, v187\n"
    "v_mfma_f32_16x16x32_bf16 %[q6], a[244:247], v[168:171], %[q6]\n"
    "ds_read_b64_tr_b16 a[236:237], v217 offset:256\n"
    "v_add_f32 v188, v188, v196\n"
    "v_add_f32 v189, v189, v196\n"
    "v_mfma_f32_16x16x32_bf16 %[q7], a[244:247], v[172:175], %[q7]\n"
    "ds_read_b64_tr_b16 a[238:239], v217 offset:2304\n"
    "v_add_f32 v190, v190, v196\n"
    "v_add_f32 v191, v191, v196\n"
    "s_waitcnt lgkmcnt(8)\n"
    "v_mfma_f32_16x16x32_bf16 %[q8], a[248:251], v[168:171], %[q8]\n"
    "ds_read_b64_tr_b16 a[240:241], v218 offset:256\n"
    "v_add_f32 v192, v192, v196\n"
    "v_add_f32 v193, v193, v196\n"
    "v_mfma_f32_16x16x32_bf16 %[q9], a[248:251], v[172:175], %[q9]\n"
    "ds_read_b64_tr_b16 a[242:243], v218 offset:2304\n"
    "v_add_f32 v194, v194, v196\n"
    "v_mfma_f32_16x16x32_bf16 %[q10], a[252:255], v[168:171], %[q10]\n"
    "ds_read_b64_tr_b16 a[244:245], v219 offset:256\n"
    "v_add_f32 v195, v195, v196\n"
    "v_mfma_f32_16x16x32_bf16 %[q11], a[252:255], v[172:175], %[q11]\n"
    "ds_read_b64_tr_b16 a[246:247], v219 offset:2304\n"
    "v_cvt_pk_bf16_f32 v176, v188, v189\n"
    "s_waitcnt lgkmcnt(8)\n"
    "v_mfma_f32_16x16x32_bf16 %[q12], a[224:227], v[168:171], %[q12]\n"
    "ds_read_b64_tr_b16 a[248:249], v220 offset:256\n"
    "v_cvt_pk_bf16_f32 v177, v190, v191\n"
    "v_mfma_f32_16x16x32_bf16 %[q13], a[224:227], v[172:175], %[q13]\n"
    "ds_read_b64_tr_b16 a[250:251], v220 offset:2304\n"
    "v_cvt_pk_bf16_f32 v178, v192, v193\n"
    "v_mfma_f32_16x16x32_bf16 %[q14], a[228:231], v[168:171], %[q14]\n"
    "ds_read_b64_tr_b16 a[252:253], v221 offset:256\n"
    "v_cvt_pk_bf16_f32 v179, v194, v195\n"
    "v_mfma_f32_16x16x32_bf16 %[q15], a[228:231], v[172:175], %[q15]\n"
    "ds_read_b64_tr_b16 a[254:255], v221 offset:2304\n"
    "v_accvgpr_read_b32 v184, a60\n"
    "s_waitcnt lgkmcnt(8)\n"
    "v_mfma_f32_16x16x32_bf16 %[q16], a[232:235], v[168:171], %[q16]\n"
    "ds_read_b64_tr_b16 a[224:225], v222 offset:256\n"
    "v_accvgpr_read_b32 v185, a61\n"
    "v_mfma_f32_16x16x32_bf16 %[q17], a[232:235], v[172:175], %[q17]\n"
    "ds_read_b64_tr_b16 a[226:227], v222 offset:2304\n"
    "v_accvgpr_read_b32 v186, a62\n"
    "v_mfma_f32_16x16x32_bf16 %[q18], a[236:239], v[168:171], %[q18]\n"
    "ds_read_b64_tr_b16 a[228:229], v223 offset:256\n"
    "v_accvgpr_read_b32 v187, a63\n"
    "v_mfma_f32_16x16x32_bf16 %[q19], a[236:239], v[172:175], %[q19]\n"
    "ds_read_b64_tr_b16 a[230:231], v223 offset:2304\n"
    "v_lshlrev_b32 v188, 16, v184\n"
    "s_waitcnt lgkmcnt(8)\n"
    "v_mfma_f32_16x16x32_bf16 %[q20], a[240:243], v[168:171], %[q20]\n"
    "ds_read_b64_tr_b16 a[232:233], v224 offset:0\n"
    "v_and_b32 v189, 0xffff0000, v184\n"
    "v_mfma_f32_16x16x32_bf16 %[q21], a[240:243], v[172:175], %[q21]\n"
    "ds_read_b64_tr_b16 a[234:235], v224 offset:1024\n"
    "v_lshlrev_b32 v190, 16, v185\n"
    "v_mfma_f32_16x16x32_bf16 %[q22], a[244:247], v[168:171], %[q22]\n"
    "ds_read_b64_tr_b16 a[236:237], v225 offset:0\n"
    "v_and_b32 v191, 0xffff0000, v185\n"
    "v_mfma_f32_16x16x32_bf16 %[q23], a[244:247], v[172:175], %[q23]\n"
    "ds_read_b64_tr_b16 a[238:239], v225 offset:1024\n"
    "v_lshlrev_b32 v192, 16, v186\n"
    "s_waitcnt lgkmcnt(8)\n"
    "v_mfma_f32_16x16x32_bf16 %[q24], a[248:251], v[168:171], %[q24]\n"
    "ds_read_b64_tr_b16 a[240:241], v226 offset:0\n"
    "v_and_b32 v193, 0xffff0000, v186\n"
    "v_mfma_f32_16x16x32_bf16 %[q25], a[248:251], v[172:175], %[q25]\n"
    "ds_read_b64_tr_b16 a[242:243], v226 offset:1024\n"
    "v_lshlrev_b32 v194, 16, v187\n"
    "v_mfma_f32_16x16x32_bf16 %[q26], a[252:255], v[168:171], %[q26]\n"
    "ds_read_b64_tr_b16 a[244:245], v227 offset:0\n"
    "v_and_b32 v195, 0xffff0000, v187\n"
    "v_mfma_f32_16x16x32_bf16 %[q27], a[252:255], v[172:175], %[q27]\n"
    "ds_read_b64_tr_b16 a[246:247], v227 offset:1024\n"
    "v_add_f32 v188, v188, v197\n"
    "s_waitcnt lgkmcnt(8)\n"
    "v_mfma_f32_16x16x32_bf16 %[q28], a[224:227], v[168:171], %[q28]\n"
    "ds_read_b64_tr_b16 a[248:249], v228 offset:0\n"
    "v_add_f32 v189, v189, v197\n"
    "v_mfma_f32_16x16x32_bf16 %[q29], a[224:227], v[172:175], %[q29]\n"
    "ds_read_b64_tr_b16 a[250:251], v228 offset:1024\n"
    "v_add_f32 v190, v190, v197\n"
    "v_mfma_f32_16x16x32_bf16 %[q30], a[228:231], v[168:171], %[q30]\n"
    "ds_read_b64_tr_b16 a[252:253], v229 offset:0\n"
    "v_add_f32 v191, v191, v197\n"
    "v_mfma_f32_16x16x32_bf16 %[q31], a[228:231], v[172:175], %[q31]\n"
    "ds_read_b64_tr_b16 a[254:255], v229 offset:1024\n"
    "v_add_f32 v192, v192, v197\n"
    "s_waitcnt lgkmcnt(8)\n"
    "v_mfma_f32_16x16x32_bf16 %[z0], a[232:235], v[168:171], %[z0]\n"
    "ds_read_b64_tr_b16 a[224:225], v230 offset:0\n"
    "v_add_f32 v193, v193, v197\n"
    "v_mfma_f32_16x16x32_bf16 %[z1], a[232:235], v[172:175], %[z1]\n"
    "ds_read_b64_tr_b16 a[226:227], v230 offset:1024\n"
    "v_add_f32 v194, v194, v197\n"
    "s_waitcnt vmcnt(17)\n"
    "s_barrier\n"
    "v_mfma_f32_16x16x32_bf16 %[z2], a[236:239], v[168:171], %[z2]\n"
    "ds_read_b64_tr_b16 a[228:229], v200 offset:32768\n"
    "v_add_f32 v195, v195, v197\n"
    "v_mfma_f32_16x16x32_bf16 %[z3], a[236:239], v[172:175], %[z3]\n"
    "ds_read_b64_tr_b16 a[230:231], v200 offset:34816\n"
    "s_add_u32 s68, %[rot], 5\n"
    "s_sub_u32 s52, s68, 7\n"
    "s_cmp_ge_u32 s68, 7\n"
    "s_cselect_b32 s68, s52, s68\n"
    "s_add_u32 m0, s50, 81920\n"
    "s_lshl_b32 s71, s68, 19\n"
    "buffer_load_dwordx4 %[vwq0], s[60:63], s71 offen offset:0 lds\n"
    "s_waitcnt lgkmcnt(8)\n"
    "v_mfma_f32_16x16x32_bf16 %[z4], a[240:243], v[168:171], %[z4]\n"
    "ds_read_b64_tr_b16 a[232:233], v201 offset:32768\n"
    "v_cvt_pk_bf16_f32 v180, v188, v189\n"
    "v_mfma_f32_16x16x32_bf16 %[z5], a[240:243], v[172:175], %[z5]\n"
    "ds_read_b64_tr_b16 a[234:235], v201 offset:34816\n"
    "buffer_load_dwordx4 %[vwq1], s[60:63], s71 offen offset:1024 lds\n"
    "v_mfma_f32_16x16x32_bf16 %[z6], a[244:247], v[168:171], %[z6]\n"
    "ds_read_b64_tr_b16 a[236:237], v202 offset:32768\n"
    "v_cvt_pk_bf16_f32 v181, v190, v191\n"
    "v_mfma_f32_16x16x32_bf16 %[z7], a[244:247], v[172:175], %[z7]\n"
    "ds_read_b64_tr_b16 a[238:239], v202 offset:34816\n"
    "buffer_load_dwordx4 %[vwq2], s[60:63], s71 offen offset:2048 lds\n"
    "s_waitcnt lgkmcnt(8)\n"
    "v_mfma_f32_16x16x32_bf16 %[z8], a[248:251], v[168:171], %[z8]\n"
    "ds_read_b64_tr_b16 a[240:241], v203 offset:32768\n"
    "v_cvt_pk_bf16_f32 v182, v192, v193\n"
    "v_mfma_f32_16x16x32_bf16 %[z9], a[248:251], v[172:175], %[z9]\n"
    "ds_read_b64_tr_b16 a[242:243], v203 offset:34816\n"
    "buffer_load_dwordx4 %[vwq3], s[60:63], s71 offen offset:3072 lds\n"
    "v_mfma_f32_16x16x32_bf16 %[z10], a[252:255], v[168:171], %[z10]\n"
    "ds_read_b64_tr_b16 a[244:245], v204 offset:32768\n"
    "v_cvt_pk_bf16_f32 v183, v194, v195\n"
    "v_mfma_f32_16x16x32_bf16 %[z11], a[252:255], v[172:175], %[z11]\n"
    "ds_read_b64_tr_b16 a[246:247], v204 offset:34816\n"
    "s_add_u32 s68, %[rot], 5\n"
    "s_sub_u32 s52, s68, 7\n"
    "s_cmp_ge_u32 s68, 7\n"
    "s_cselect_b32 s68, s52, s68\n"
    "s_add_u32 m0, s50, 147456\n"
    "s_mul_i32 s71, s68, 229376\n"
    "buffer_load_dwordx4 %[vwi0], s[56:59], s71 offen offset:0 lds\n"
    "s_waitcnt lgkmcnt(8)\n"
    "v_mfma_f32_16x16x32_bf16 %[z12], a[224:227], v[168:171], %[z12]\n"
    "ds_read_b64_tr_b16 a[248:249], v205 offset:32768\n"
    "v_mfma_f32_16x16x32_bf16 %[z13], a[224:227], v[172:175], %[z13]\n"
    "ds_read_b64_tr_b16 a[250:251], v205 offset:34816\n"
    "buffer_load_dwordx4 %[vwi1], s[56:59], s71 offen offset:1024 lds\n"
    "s_nop 1\n"
    "v_mfma_f32_16x16x32_bf16 v[248:251], v[176:179], v[176:179], v[248:251]\n"
    "v_mfma_f32_16x16x32_bf16 v[252:255], v[180:183], v[180:183], v[252:255]\n"
    "v_mfma_f32_16x16x32_bf16 %[q0], a[228:231], v[176:179], %[q0]\n"
    "ds_read_b64_tr_b16 a[252:253], v206 offset:32768\n"
    "v_accvgpr_read_b32 v184, a64\n"
    "v_accvgpr_read_b32 v185, a65\n"
    "v_mfma_f32_16x16x32_bf16 %[q1], a[228:231], v[180:183], %[q1]\n"
    "ds_read_b64_tr_b16 a[254:255], v206 offset:34816\n"
    "buffer_load_dwordx4 %[vwi2], s[56:59], s71 offen offset:2048 lds\n"
    "s_waitcnt lgkmcnt(8)\n"
    "v_mfma_f32_16x16x32_bf16 %[q2], a[232:235], v[176:179], %[q2]\n"
    "ds_read_b64_tr_b16 a[224:225], v207 offset:32768\n"
    "v_accvgpr_read_b32 v186, a66\n"
    "v_accvgpr_read_b32 v187, a67\n"
    "v_mfma_f32_16x16x32_bf16 %[q3], a[232:235], v[180:183], %[q3]\n"
    "ds_read_b64_tr_b16 a[226:227], v207 offset:34816\n"
    "buffer_load_dwordx4 %[vwi3], s[56:59], s71 offen offset:3072 lds\n"
    "v_mfma_f32_16x16x32_bf16 %[q4], a[236:239], v[176:179], %[q4]\n"
    "ds_read_b64_tr_b16 a[228:229], v200 offset:33024\n"
    "v_lshlrev_b32 v188, 16, v184\n"
    "v_and_b32 v189, 0xffff0000, v184\n"
    "v_mfma_f32_16x16x32_bf16 %[q5], a[236:239], v[180:183], %[q5]\n"
    "ds_read_b64_tr_b16 a[230:231], v200 offset:35072\n"
    "v_lshlrev_b32 v190, 16, v185\n"
    "v_and_b32 v191, 0xffff0000, v185\n"
    "s_waitcnt lgkmcnt(8)\n"
    "v_mfma_f32_16x16x32_bf16 %[q6], a[240:243], v[176:179], %[q6]\n"
    "ds_read_b64_tr_b16 a[232:233], v201 offset:33024\n"
    "v_lshlrev_b32 v192, 16, v186\n"
    "v_and_b32 v193, 0xffff0000, v186\n"
    "v_mfma_f32_16x16x32_bf16 %[q7], a[240:243], v[180:183], %[q7]\n"
    "ds_read_b64_tr_b16 a[234:235], v201 offset:35072\n"
    "v_lshlrev_b32 v194, 16, v187\n"
    "v_and_b32 v195, 0xffff0000, v187\n"
    "v_mfma_f32_16x16x32_bf16 %[q8], a[244:247], v[176:179], %[q8]\n"
    "ds_read_b64_tr_b16 a[236:237], v202 offset:33024\n"
    "v_add_f32 v188, v188, v196\n"
    "v_add_f32 v189, v189, v196\n"
    "v_mfma_f32_16x16x32_bf16 %[q9], a[244:247], v[180:183], %[q9]\n"
    "ds_read_b64_tr_b16 a[238:239], v202 offset:35072\n"
    "v_add_f32 v190, v190, v196\n"
    "v_add_f32 v191, v191, v196\n"
    "s_waitcnt lgkmcnt(8)\n"
    "v_mfma_f32_16x16x32_bf16 %[q10], a[248:251], v[176:179], %[q10]\n"
    "ds_read_b64_tr_b16 a[240:241], v203 offset:33024\n"
    "v_add_f32 v192, v192, v196\n"
    "v_add_f32 v193, v193, v196\n"
    "v_mfma_f32_16x16x32_bf16 %[q11], a[248:251], v[180:183], %[q11]\n"
    "ds_read_b64_tr_b16 a[242:243], v203 offset:35072\n"
    "v_add_f32 v194, v194, v196\n"
    "v_mfma_f32_16x16x32_bf16 %[q12], a[252:255], v[176:179], %[q12]\n"
    "ds_read_b64_tr_b16 a[244:245], v204 offset:33024\n"
    "v_add_f32 v195, v195, v196\n"
    "v_mfma_f32_16x16x32_bf16 %[q13], a[252:255], v[180:183], %[q13]\n"
    "ds_read_b64_tr_b16 a[246:247], v204 offset:35072\n"
    "v_cvt_pk_bf16_f32 v168, v188, v189\n"
    "s_waitcnt lgkmcnt(8)\n"
    "v_mfma_f32_16x16x32_bf16 %[q14], a[224:227], v[176:179], %[q14]\n"
    "ds_read_b64_tr_b16 a[248:249], v205 offset:33024\n"
    "v_cvt_pk_bf16_f32 v169, v190, v191\n"
    "v_mfma_f32_16x16x32_bf16 %[q15], a[224:227], v[180:183], %[q15]\n"
    "ds_read_b64_tr_b16 a[250:251], v205 offset:35072\n"
    "v_cvt_pk_bf16_f32 v170, v192, v193\n"
    "v_mfma_f32_16x16x32_bf16 %[q16], a[228:231], v[176:179], %[q16]\n"
    "ds_read_b64_tr_b16 a[252:253], v206 offset:33024\n"
    "v_cvt_pk_bf16_f32 v171, v194, v195\n"
    "v_mfma_f32_16x16x32_bf16 %[q17], a[228:231], v[180:183], %[q17]\n"
    "ds_read_b64_tr_b16 a[254:255], v206 offset:35072\n"
    "v_accvgpr_read_b32 v184, a68\n"
    "s_waitcnt lgkmcnt(8)\n"
    "v_mfma_f32_16x16x32_bf16 %[q18], a[232:235], v[176:179], %[q18]\n"
    "ds_read_b64_tr_b16 a[224:225], v207 offset:33024\n"
    "v_accvgpr_read_b32 v185, a69\n"
    "v_mfma_f32_16x16x32_bf16 %[q19], a[232:235], v[180:183], %[q19]\n"
    "ds_read_b64_tr_b16 a[226:227], v207 offset:35072\n"
    "v_accvgpr_read_b32 v186, a70\n"
    "v_mfma_f32_16x16x32_bf16 %[q20], a[236:239], v[176:179], %[q20]\n"
    "ds_read_b64_tr_b16 a[228:229], v224 offset:8192\n"
    "v_accvgpr_read_b32 v187, a71\n"
    "v_mfma_f32_16x16x32_bf16 %[q21], a[236:239], v[180:183], %[q21]\n"
    "ds_read_b64_tr_b16 a[230:231], v224 offset:9216\n"
    "v_lshlrev_b32 v188, 16, v184\n"
    "s_waitcnt lgkmcnt(8)\n"
    "v_mfma_f32_16x16x32_bf16 %[q22], a[240:243], v[176:179], %[q22]\n"
    "ds_read_b64_tr_b16 a[232:233], v225 offset:8192\n"
    "v_and_b32 v189, 0xffff0000, v184\n"
    "v_mfma_f32_16x16x32_bf16 %[q23], a[240:243], v[180:183], %[q23]\n"
    "ds_read_b64_tr_b16 a[234:235], v225 offset:9216\n"
    "v_lshlrev_b32 v190, 16, v185\n"
    "v_mfma_f32_16x16x32_bf16 %[q24], a[244:247], v[176:179], %[q24]\n"
    "ds_read_b64_tr_b16 a[236:237], v226 offset:8192\n"
    "v_and_b32 v191, 0xffff0000, v185\n"
    "v_mfma_f32_16x16x32_bf16 %[q25], a[244:247], v[180:183], %[q25]\n"
    "ds_read_b64_tr_b16 a[238:239], v226 offset:9216\n"
    "v_lshlrev_b32 v192, 16, v186\n"
    "s_waitcnt lgkmcnt(8)\n"
    "v_mfma_f32_16x16x32_bf16 %[q26], a[248:251], v[176:179], %[q26]\n"
    "ds_read_b64_tr_b16 a[240:241], v227 offset:8192\n"
    "v_and_b32 v193, 0xffff0000, v186\n"
    "v_mfma_f32_16x16x32_bf16 %[q27], a[248:251], v[180:183], %[q27]\n"
    "ds_read_b64_tr_b16 a[242:243], v227 offset:9216\n"
    "v_lshlrev_b32 v194, 16, v187\n"
    "v_mfma_f32_16x16x32_bf16 %[q28], a[252:255], v[176:179], %[q28]\n"
    "ds_read_b64_tr_b16 a[244:245], v228 offset:8192\n"
    "v_and_b32 v195, 0xffff0000, v187\n"
    "v_mfma_f32_16x16x32_bf16 %[q29], a[252:255], v[180:183], %[q29]\n"
    "ds_read_b64_tr_b16 a[246:247], v228 offset:9216\n"
    "v_add_f32 v188, v188, v197\n"
    "s_waitcnt lgkmcnt(8)\n"
    "v_mfma_f32_16x16x32_bf16 %[q30], a[224:227], v[176:179], %[q30]\n"
    "ds_read_b64_tr_b16 a[248:249], v229 offset:8192\n"
    "v_add_f32 v189, v189, v197\n"
    "v_mfma_f32_16x16x32_bf16 %[q31], a[224:227], v[180:183], %[q31]\n"
    "ds_read_b64_tr_b16 a[250:251], v229 offset:9216\n"
    "v_add_f32 v190, v190, v197\n"
    "v_mfma_f32_16x16x32_bf16 %[z0], a[228:231], v[176:179], %[z0]\n"
    "ds_read_b64_tr_b16 a[252:253], v230 offset:8192\n"
    "v_add_f32 v191, v191, v197\n"
    "v_mfma_f32_16x16x32_bf16 %[z1], a[228:231], v[180:183], %[z1]\n"
    "ds_read_b64_tr_b16 a[254:255], v230 offset:9216\n"
    "v_add_f32 v192, v192, v197\n"
    "s_waitcnt lgkmcnt(8)\n"
    "s_waitcnt vmcnt(17)\n"
    "s_barrier\n"
    "v_mfma_f32_16x16x32_bf16 %[z2], a[232:235], v[176:179], %[z2]\n"
    "ds_read_b64_tr_b16 a[224:225], v200 offset:49152\n"
    "v_add_f32 v193, v193, v197\n"
    "v_mfma_f32_16x16x32_bf16 %[z3], a[232:235], v[180:183], %[z3]\n"
    "ds_read_b64_tr_b16 a[226:227], v200 offset:51200\n"
    "s_add_u32 s68, %[rot], 5\n"
    "s_sub_u32 s52, s68, 7\n"
    "s_cmp_ge_u32 s68, 7\n"
    "s_cselect_b32 s68, s52, s68\n"
    "s_add_u32 m0, s50, 98304\n"
    "s_lshl_b32 s71, s68, 19\n"
    "s_add_u32 s71, s71, 262144\n"
    "buffer_load_dwordx4 %[vwq0], s[60:63], s71 offen offset:0 lds\n"
    "v_mfma_f32_16x16x32_bf16 %[z4], a[236:239], v[176:179], %[z4]\n"
    "ds_read_b64_tr_b16 a[228:229], v201 offset:49152\n"
    "v_add_f32 v194, v194, v197\n"
    "v_mfma_f32_16x16x32_bf16 %[z5], a[236:239], v[180:183], %[z5]\n"
    "ds_read_b64_tr_b16 a[230:231], v201 offset:51200\n"
    "buffer_load_dwordx4 %[vwq1], s[60:63], s71 offen offset:1024 lds\n"
    "s_waitcnt lgkmcnt(8)\n"
    "v_mfma_f32_16x16x32_bf16 %[z6], a[240:243], v[176:179], %[z6]\n"
    "ds_read_b64_tr_b16 a[232:233], v202 offset:49152\n"
    "v_add_f32 v195, v195, v197\n"
    "v_mfma_f32_16x16x32_bf16 %[z7], a[240:243], v[180:183], %[z7]\n"
    "ds_read_b64_tr_b16 a[234:235], v202 offset:51200\n"
    "buffer_load_dwordx4 %[vwq2], s[60:63], s71 offen offset:2048 lds\n"
    "v_mfma_f32_16x16x32_bf16 %[z8], a[244:247], v[176:179], %[z8]\n"
    "ds_read_b64_tr_b16 a[236:237], v203 offset:49152\n"
    "v_cvt_pk_bf16_f32 v172, v188, v189\n"
    "v_mfma_f32_16x16x32_bf16 %[z9], a[244:247], v[180:183], %[z9]\n"
    "ds_read_b64_tr_b16 a[238:239], v203 offset:51200\n"
    "buffer_load_dwordx4 %[vwq3], s[60:63], s71 offen offset:3072 lds\n"
    "s_waitcnt lgkmcnt(8)\n"
    "v_mfma_f32_16x16x32_bf16 %[z10], a[248:251], v[176:179], %[z10]\n"
    "ds_read_b64_tr_b16 a[240:241], v204 offset:49152\n"
    "v_cvt_pk_bf16_f32 v173, v190, v191\n"
    "v_mfma_f32_16x16x32_bf16 %[z11], a[248:251], v[180:183], %[z11]\n"
    "ds_read_b64_tr_b16 a[242:243], v204 offset:51200\n"
    "v_cvt_pk_bf16_f32 v174, v192, v193\n"
    "v_mfma_f32_16x16x32_bf16 %[z12], a[252:255], v[176:179], %[z12]\n"
    "ds_read_b64_tr_b16 a[244:245], v205 offset:49152\n"
    "v_cvt_pk_bf16_f32 v175, v194, v195\n"
    "v_mfma_f32_16x16x32_bf16 %[z13], a[252:255], v[180:183], %[z13]\n"
    "ds_read_b64_tr_b16 a[246:247], v205 offset:51200\n"
    "s_waitcnt lgkmcnt(8)\n"
    "s_nop 1\n"
    "v_mfma_f32_16x16x32_bf16 v[248:251], v[168:171], v[168:171], v[248:251]\n"
    "v_mfma_f32_16x16x32_bf16 v[252:255], v[172:175], v[172:175], v[252:255]\n"
    "v_mfma_f32_16x16x32_bf16 %[q0], a[224:227], v[168:171], %[q0]\n"
    "ds_read_b64_tr_b16 a[248:249], v206 offset:49152\n"
    "v_accvgpr_read_b32 v184, a72\n"
    "v_accvgpr_read_b32 v185, a73\n"
    "v_mfma_f32_16x16x32_bf16 %[q1], a[224:227], v[172:175], %[q1]\n"
    "ds_read_b64_tr_b16 a[250:251], v206 offset:51200\n"
    "v_accvgpr_read_b32 v186, a74\n"
    "v_accvgpr_read_b32 v187, a75\n"
    "v_mfma_f32_16x16x32_bf16 %[q2], a[228:231], v[168:171], %[q2]\n"
    "ds_read_b64_tr_b16 a[252:253], v207 offset:49152\n"
    "v_lshlrev_b32 v188, 16, v184\n"
    "v_and_b32 v189, 0xffff0000, v184\n"
    "v_mfma_f32_16x16x32_bf16 %[q3], a[228:231], v[172:175], %[q3]\n"
    "ds_read_b64_tr_b16 a[254:255], v207 offset:51200\n"
    "v_lshlrev_b32 v190, 16, v185\n"
    "v_and_b32 v191, 0xffff0000, v185\n"
    "s_waitcnt lgkmcnt(8)\n"
    "v_mfma_f32_16x16x32_bf16 %[q4], a[232:235], v[168:171], %[q4]\n"
    "ds_read_b64_tr_b16 a[224:225], v200 offset:49408\n"
    "v_lshlrev_b32 v192, 16, v186\n"
    "v_and_b32 v193, 0xffff0000, v186\n"
    "v_mfma_f32_16x16x32_bf16 %[q5], a[232:235], v[172:175], %[q5]\n"
    "ds_read_b64_tr_b16 a[226:227], v200 offset:51456\n"
    "v_lshlrev_b32 v194, 16, v187\n"
    "v_and_b32 v195, 0xffff0000, v187\n"
    "v_mfma_f32_16x16x32_bf16 %[q6], a[236:239], v[168:171], %[q6]\n"
    "ds_read_b64_tr_b16 a[228:229], v201 offset:49408\n"
    "v_add_f32 v188, v188, v196\n"
    "v_add_f32 v189, v189, v196\n"
    "v_mfma_f32_16x16x32_bf16 %[q7], a[236:239], v[172:175], %[q7]\n"
    "ds_read_b64_tr_b16 a[230:231], v201 offset:51456\n"
    "v_add_f32 v190, v190, v196\n"
    "v_add_f32 v191, v191, v196\n"
    "s_waitcnt lgkmcnt(8)\n"
    "v_mfma_f32_16x16x32_bf16 %[q8], a[240:243], v[168:171], %[q8]\n"
    "ds_read_b64_tr_b16 a[232:233], v202 offset:49408\n"
    "v_add_f32 v192, v192, v196\n"
    "v_add_f32 v193, v193, v196\n"
    "v_mfma_f32_16x16x32_bf16 %[q9], a[240:243], v[172:175], %[q9]\n"
    "ds_read_b64_tr_b16 a[234:235], v202 offset:51456\n"
    "v_add_f32 v194, v194, v196\n"
    "v_mfma_f32_16x16x32_bf16 %[q10], a[244:247], v[168:171], %[q10]\n"
    "ds_read_b64_tr_b16 a[236:237], v203 offset:49408\n"
    "v_add_f32 v195, v195, v196\n"
    "v_mfma_f32_16x16x32_bf16 %[q11], a[244:247], v[172:175], %[q11]\n"
    "ds_read_b64_tr_b16 a[238:239], v203 offset:51456\n"
    "v_cvt_pk_bf16_f32 v176, v188, v189\n"
    "s_waitcnt lgkmcnt(8)\n"
    "v_mfma_f32_16x16x32_bf16 %[q12], a[248:251], v[168:171], %[q12]\n"
    "ds_read_b64_tr_b16 a[240:241], v204 offset:49408\n"
    "v_cvt_pk_bf16_f32 v177, v190, v191\n"
    "v_mfma_f32_16x16x32_bf16 %[q13], a[248:251], v[172:175], %[q13]\n"
    "ds_read_b64_tr_b16 a[242:243], v204 offset:51456\n"
    "v_cvt_pk_bf16_f32 v178, v192, v193\n"
    "v_mfma_f32_16x16x32_bf16 %[q14], a[252:255], v[168:171], %[q14]\n"
    "ds_read_b64_tr_b16 a[244:245], v205 offset:49408\n"
    "v_cvt_pk_bf16_f32 v179, v194, v195\n"
    "v_mfma_f32_16x16x32_bf16 %[q15], a[252:255], v[172:175], %[q15]\n"
    "ds_read_b64_tr_b16 a[246:247], v205 offset:51456\n"
    "v_accvgpr_read_b32 v184, a76\n"
    "s_waitcnt lgkmcnt(8)\n"
    "v_mfma_f32_16x16x32_bf16 %[q16], a[224:227], v[168:171], %[q16]\n"
    "ds_read_b64_tr_b16 a[248:249], v206 offset:49408\n"
    "v_accvgpr_read_b32 v185, a77\n"
    "v_mfma_f32_16x16x32_bf16 %[q17], a[224:227], v[172:175], %[q17]\n"
    "ds_read_b64_tr_b16 a[250:251], v206 offset:51456\n"
    "v_accvgpr_read_b32 v186, a78\n"
    "v_mfma_f32_16x16x32_bf16 %[q18], a[228:231], v[168:171], %[q18]\n"
    "ds_read_b64_tr_b16 a[252:253], v207 offset:49408\n"
    "v_accvgpr_read_b32 v187, a79\n"
    "v_mfma_f32_16x16x32_bf16 %[q19], a[228:231], v[172:175], %[q19]\n"
    "ds_read_b64_tr_b16 a[254:255], v207 offset:51456\n"
    "v_lshlrev_b32 v188, 16, v184\n"
    "s_waitcnt lgkmcnt(8)\n"
    "v_mfma_f32_16x16x32_bf16 %[q20], a[232:235], v[168:171], %[q20]\n"
    "ds_read_b64_tr_b16 a[224:225], v224 offset:16384\n"
    "v_and_b32 v189, 0xffff0000, v184\n"
    "v_mfma_f32_16x16x32_bf16 %[q21], a[232:235], v[172:175], %[q21]\n"
    "ds_read_b64_tr_b16 a[226:227], v224 offset:17408\n"
    "v_lshlrev_b32 v190, 16, v185\n"
    "v_mfma_f32_16x16x32_bf16 %[q22], a[236:239], v[168:171], %[q22]\n"
    "ds_read_b64_tr_b16 a[228:229], v225 offset:16384\n"
    "v_and_b32 v191, 0xffff0000, v185\n"
    "v_mfma_f32_16x16x32_bf16 %[q23], a[236:239], v[172:175], %[q23]\n"
    "ds_read_b64_tr_b16 a[230:231], v225 offset:17408\n"
    "v_lshlrev_b32 v192, 16, v186\n"
    "s_waitcnt lgkmcnt(8)\n"
    "v_mfma_f32_16x16x32_bf16 %[q24], a[240:243], v[168:171], %[q24]\n"
    "ds_read_b64_tr_b16 a[232:233], v226 offset:16384\n"
    "v_and_b32 v193, 0xffff0000, v186\n"
    "v_mfma_f32_16x16x32_bf16 %[q25], a[240:243], v[172:175], %[q25]\n"
    "ds_read_b64_tr_b16 a[234:235], v226 offset:17408\n"
    "v_lshlrev_b32 v194, 16, v187\n"
    "v_mfma_f32_16x16x32_bf16 %[q26], a[244:247], v[168:171], %[q26]\n"
    "ds_read_b64_tr_b16 a[236:237], v227 offset:16384\n"
    "v_and_b32 v195, 0xffff0000, v187\n"
    "v_mfma_f32_16x16x32_bf16 %[q27], a[244:247], v[172:175], %[q27]\n"
    "ds_read_b64_tr_b16 a[238:239], v227 offset:17408\n"
    "v_add_f32 v188, v188, v197\n"
    "s_waitcnt lgkmcnt(8)\n"
    "v_mfma_f32_16x16x32_bf16 %[q28], a[248:251], v[168:171], %[q28]\n"
    "ds_read_b64_tr_b16 a[240:241], v228 offset:16384\n"
    "v_add_f32 v189, v189, v197\n"
    "v_mfma_f32_16x16x32_bf16 %[q29], a[248:251], v[172:175], %[q29]\n"
    "ds_read_b64_tr_b16 a[242:243], v228 offset:17408\n"
    "v_add_f32 v190, v190, v197\n"
    "v_mfma_f32_16x16x32_bf16 %[q30], a[252:255], v[168:171], %[q30]\n"
    "ds_read_b64_tr_b16 a[244:245], v229 offset:16384\n"
    "v_add_f32 v191, v191, v197\n"
    "v_mfma_f32_16x16x32_bf16 %[q31], a[252:255], v[172:175], %[q31]\n"
    "ds_read_b64_tr_b16 a[246:247], v229 offset:17408\n"
    "v_add_f32 v192, v192, v197\n"
    "s_waitcnt lgkmcnt(8)\n"
    "v_mfma_f32_16x16x32_bf16 %[z0], a[224:227], v[168:171], %[z0]\n"
    "ds_read_b64_tr_b16 a[248:249], v230 offset:16384\n"
    "v_add_f32 v193, v193, v197\n"
    "v_mfma_f32_16x16x32_bf16 %[z1], a[224:227], v[172:175], %[z1]\n"
    "ds_read_b64_tr_b16 a[250:251], v230 offset:17408\n"
    "v_add_f32 v194, v194, v197\n"
    "s_waitcnt vmcnt(17)\n"
    "s_barrier\n"
    "v_mfma_f32_16x16x32_bf16 %[z2], a[228:231], v[168:171], %[z2]\n"
    "ds_read_b64_tr_b16 a[252:253], v208 offset:0\n"
    "s_add_u32 s68, %[rot], 6\n"
    "s_sub_u32 s52, s68, 7\n"
    "s_cmp_ge_u32 s68, 7\n"
    "s_cselect_b32 s68, s52, s68\n"
    "s_add_u32 m0, s50, 114688\n"
    "s_lshl_b32 s71, s68, 19\n"
    "buffer_load_dwordx4 %[vwq0], s[60:63], s71 offen offset:0 lds\n"
    "v_mfma_f32_16x16x32_bf16 %[z3], a[228:231], v[172:175], %[z3]\n"
    "ds_read_b64_tr_b16 a[254:255], v208 offset:2048\n"
    "buffer_load_dwordx4 %[vwq1], s[60:63], s71 offen offset:1024 lds\n"
    "s_waitcnt lgkmcnt(8)\n"
    "v_mfma_f32_16x16x32_bf16 %[z4], a[232:235], v[168:171], %[z4]\n"
    "ds_read_b64_tr_b16 a[224:225], v209 offset:0\n"
    "v_add_f32 v195, v195, v197\n"
    "v_mfma_f32_16x16x32_bf16 %[z5], a[232:235], v[172:175], %[z5]\n"
    "ds_read_b64_tr_b16 a[226:227], v209 offset:2048\n"
    "buffer_load_dwordx4 %[vwq2], s[60:63], s71 offen offset:2048 lds\n"
    "v_mfma_f32_16x16x32_bf16 %[z6], a[236:239], v[168:171], %[z6]\n"
    "ds_read_b64_tr_b16 a[228:229], v210 offset:0\n"
    "v_cvt_pk_bf16_f32 v180, v188, v189\n"
    "v_mfma_f32_16x16x32_bf16 %[z7], a[236:239], v[172:175], %[z7]\n"
    "ds_read_b64_tr_b16 a[230:231], v210 offset:2048\n"
    "buffer_load_dwordx4 %[vwq3], s[60:63], s71 offen offset:3072 lds\n"
    "s_waitcnt lgkmcnt(8)\n"
    "v_mfma_f32_16x16x32_bf16 %[z8], a[240:243], v[168:171], %[z8]\n"
    "ds_read_b64_tr_b16 a[232:233], v211 offset:0\n"
    "v_cvt_pk_bf16_f32 v181, v190, v191\n"
    "v_mfma_f32_16x16x32_bf16 %[z9], a[240:243], v[172:175], %[z9]\n"
    "ds_read_b64_tr_b16 a[234:235], v211 offset:2048\n"
    "s_add_u32 s68, %[rot], 6\n"
    "s_sub_u32 s52, s68, 7\n"
    "s_cmp_ge_u32 s68, 7\n"
    "s_cselect_b32 s68, s52, s68\n"
    "s_add_u32 m0, s50, 0\n"
    "s_mul_i32 s71, s68, 229376\n"
    "buffer_load_dwordx4 %[vwi0], s[56:59], s71 offen offset:0 lds\n"
    "v_mfma_f32_16x16x32_bf16 %[z10], a[244:247], v[168:171], %[z10]\n"
    "ds_read_b64_tr_b16 a[236:237], v212 offset:0\n"
    "v_cvt_pk_bf16_f32 v182, v192, v193\n"
    "v_mfma_f32_16x16x32_bf16 %[z11], a[244:247], v[172:175], %[z11]\n"
    "ds_read_b64_tr_b16 a[238:239], v212 offset:2048\n"
    "buffer_load_dwordx4 %[vwi1], s[56:59], s71 offen offset:1024 lds\n"
    "s_waitcnt lgkmcnt(8)\n"
    "v_mfma_f32_16x16x32_bf16 %[z12], a[248:251], v[168:171], %[z12]\n"
    "ds_read_b64_tr_b16 a[240:241], v213 offset:0\n"
    "v_cvt_pk_bf16_f32 v183, v194, v195\n"
    "v_mfma_f32_16x16x32_bf16 %[z13], a[248:251], v[172:175], %[z13]\n"
    "ds_read_b64_tr_b16 a[242:243], v213 offset:2048\n"
    "buffer_load_dwordx4 %[vwi2], s[56:59], s71 offen offset:2048 lds\n"
    "s_nop 1\n"
    "v_mfma_f32_16x16x32_bf16 v[248:251], v[176:179], v[176:179], v[248:251]\n"
    "v_mfma_f32_16x16x32_bf16 v[252:255], v[180:183], v[180:183], v[252:255]\n"
    "v_mfma_f32_16x16x32_bf16 %[q0], a[252:255], v[176:179], %[q0]\n"
    "ds_read_b64_tr_b16 a[244:245], v214 offset:0\n"
    "v_accvgpr_read_b32 v184, a80\n"
    "v_accvgpr_read_b32 v185, a81\n"
    "v_mfma_f32_16x16x32_bf16 %[q1], a[252:255], v[180:183], %[q1]\n"
    "ds_read_b64_tr_b16 a[246:247], v214 offset:2048\n"
    "buffer_load_dwordx4 %[vwi3], s[56:59], s71 offen offset:3072 lds\n"
    "s_waitcnt lgkmcnt(8)\n"
    "v_mfma_f32_16x16x32_bf16 %[q2], a[224:227], v[176:179], %[q2]\n"
    "ds_read_b64_tr_b16 a[248:249], v215 offset:0\n"
    "v_accvgpr_read_b32 v186, a82\n"
    "v_accvgpr_read_b32 v187, a83\n"
    "v_mfma_f32_16x16x32_bf16 %[q3], a[224:227], v[180:183], %[q3]\n"
    "ds_read_b64_tr_b16 a[250:251], v215 offset:2048\n"
    "s_add_u32 m0, s50, 32768\n"
    "s_mov_b32 s48, 0\n"
    "buffer_load_dwordx4 %[vok0], s[40:43], s48 offen offset:0 nt lds\n"
    "v_mfma_f32_16x16x32_bf16 %[q4], a[228:231], v[176:179], %[q4]\n"
    "ds_read_b64_tr_b16 a[252:253], v208 offset:256\n"
    "v_lshlrev_b32 v188, 16, v184\n"
    "v_and_b32 v189, 0xffff0000, v184\n"
    "v_mfma_f32_16x16x32_bf16 %[q5], a[228:231], v[180:183], %[q5]\n"
    "ds_read_b64_tr_b16 a[254:255], v208 offset:2304\n"
    "buffer_load_dwordx4 %[vok1], s[40:43], s48 offen offset:1024 nt lds\n"
    "s_waitcnt lgkmcnt(8)\n"
    "v_mfma_f32_16x16x32_bf16 %[q6], a[232:235], v[176:179], %[q6]\n"
    "ds_read_b64_tr_b16 a[224:225], v209 offset:256\n"
    "v_lshlrev_b32 v190, 16, v185\n"
    "v_and_b32 v191, 0xffff0000, v185\n"
    "v_mfma_f32_16x16x32_bf16 %[q7], a[232:235], v[180:183], %[q7]\n"
    "ds_read_b64_tr_b16 a[226:227], v209 offset:2304\n"
    "buffer_load_dwordx4 %[vok2], s[40:43], s48 offen offset:2048 nt lds\n"
    "v_mfma_f32_16x16x32_bf16 %[q8], a[236:239], v[176:179], %[q8]\n"
    "ds_read_b64_tr_b16 a[228:229], v210 offset:256\n"
    "v_lshlrev_b32 v192, 16, v186\n"
    "v_and_b32 v193, 0xffff0000, v186\n"
    "v_mfma_f32_16x16x32_bf16 %[q9], a[236:239], v[180:183], %[q9]\n"
    "ds_read_b64_tr_b16 a[230:231], v210 offset:2304\n"
    "buffer_load_dwordx4 %[vok3], s[40:43], s48 offen offset:3072 nt lds\n"
    "s_waitcnt lgkmcnt(8)\n"
    "v_mfma_f32_16x16x32_bf16 %[q10], a[240:243], v[176:179], %[q10]\n"
    "ds_read_b64_tr_b16 a[232:233], v211 offset:256\n"
    "v_lshlrev_b32 v194, 16, v187\n"
    "v_and_b32 v195, 0xffff0000, v187\n"
    "v_mfma_f32_16x16x32_bf16 %[q11], a[240:243], v[180:183], %[q11]\n"
    "ds_read_b64_tr_b16 a[234:235], v211 offset:2304\n"
    "v_add_f32 v188, v188, v196\n"
    "v_add_f32 v189, v189, v196\n"
    "v_mfma_f32_16x16x32_bf16 %[q12], a[244:247], v[176:179], %[q12]\n"
    "ds_read_b64_tr_b16 a[236:237], v212 offset:256\n"
    "v_add_f32 v190, v190, v196\n"
    "v_add_f32 v191, v191, v196\n"
    "v_mfma_f32_16x16x32_bf16 %[q13], a[244:247], v[180:183], %[q13]\n"
    "ds_read_b64_tr_b16 a[238:239], v212 offset:2304\n"
    "v_add_f32 v192, v192, v196\n"
    "v_add_f32 v193, v193, v196\n"
    "s_waitcnt lgkmcnt(8)\n"
    "v_mfma_f32_16x16x32_bf16 %[q14], a[248:251], v[176:179], %[q14]\n"
    "ds_read_b64_tr_b16 a[240:241], v213 offset:256\n"
    "v_add_f32 v194, v194, v196\n"
    "v_mfma_f32_16x16x32_bf16 %[q15], a[248:251], v[180:183], %[q15]\n"
    "ds_read_b64_tr_b16 a[242:243], v213 offset:2304\n"
    "v_add_f32 v195, v195, v196\n"
    "v_mfma_f32_16x16x32_bf16 %[q16], a[252:255], v[176:179], %[q16]\n"
    "ds_read_b64_tr_b16 a[244:245], v214 offset:256\n"
    "v_cvt_pk_bf16_f32 v168, v188, v189\n"
    "v_mfma_f32_16x16x32_bf16 %[q17], a[252:255], v[180:183], %[q17]\n"
    "ds_read_b64_tr_b16 a[246:247], v214 offset:2304\n"
    "v_cvt_pk_bf16_f32 v169, v190, v191\n"
    "s_waitcnt lgkmcnt(8)\n"
    "v_mfma_f32_16x16x32_bf16 %[q18], a[224:227], v[176:179], %[q18]\n"
    "ds_read_b64_tr_b16 a[248:249], v215 offset:256\n"
    "v_cvt_pk_bf16_f32 v170, v192, v193\n"
    "v_mfma_f32_16x16x32_bf16 %[q19], a[224:227], v[180:183], %[q19]\n"
    "ds_read_b64_tr_b16 a[250:251], v215 offset:2304\n"
    "v_cvt_pk_bf16_f32 v171, v194, v195\n"
    "v_mfma_f32_16x16x32_bf16 %[q20], a[228:231], v[176:179], %[q20]\n"
    "ds_read_b64_tr_b16 a[252:253], v224 offset:24576\n"
    "v_accvgpr_read_b32 v184, a84\n"
    "v_mfma_f32_16x16x32_bf16 %[q21], a[228:231], v[180:183], %[q21]\n"
    "ds_read_b64_tr_b16 a[254:255], v224 offset:25600\n"
    "v_accvgpr_read_b32 v185, a85\n"
    "s_waitcnt lgkmcnt(8)\n"
    "v_mfma_f32_16x16x32_bf16 %[q22], a[232:235], v[176:179], %[q22]\n"
    "ds_read_b64_tr_b16 a[224:225], v225 offset:24576\n"
    "v_accvgpr_read_b32 v186, a86\n"
    "v_mfma_f32_16x16x32_bf16 %[q23], a[232:235], v[180:183], %[q23]\n"
    "ds_read_b64_tr_b16 a[226:227], v225 offset:25600\n"
    "v_accvgpr_read_b32 v187, a87\n"
    "v_mfma_f32_16x16x32_bf16 %[q24], a[236:239], v[176:179], %[q24]\n"
    "ds_read_b64_tr_b16 a[228:229], v226 offset:24576\n"
    "v_lshlrev_b32 v188, 16, v184\n"
    "v_mfma_f32_16x16x32_bf16 %[q25], a[236:239], v[180:183], %[q25]\n"
    "ds_read_b64_tr_b16 a[230:231], v226 offset:25600\n"
    "v_and_b32 v189, 0xffff0000, v184\n"
    "s_waitcnt lgkmcnt(8)\n"
    "v_mfma_f32_16x16x32_bf16 %[q26], a[240:243], v[176:179], %[q26]\n"
    "ds_read_b64_tr_b16 a[232:233], v227 offset:24576\n"
    "v_lshlrev_b32 v190, 16, v185\n"
    "v_mfma_f32_16x16x32_bf16 %[q27], a[240:243], v[180:183], %[q27]\n"
    "ds_read_b64_tr_b16 a[234:235], v227 offset:25600\n"
    "v_and_b32 v191, 0xffff0000, v185\n"
    "v_mfma_f32_16x16x32_bf16 %[q28], a[244:247], v[176:179], %[q28]\n"
    "ds_read_b64_tr_b16 a[236:237], v228 offset:24576\n"
    "v_lshlrev_b32 v192, 16, v186\n"
    "v_mfma_f32_16x16x32_bf16 %[q29], a[244:247], v[180:183], %[q29]\n"
    "ds_read_b64_tr_b16 a[238:239], v228 offset:25600\n"
    "v_and_b32 v193, 0xffff0000, v186\n"
    "s_waitcnt lgkmcnt(8)\n"
    "v_mfma_f32_16x16x32_bf16 %[q30], a[248:251], v[176:179], %[q30]\n"
    "ds_read_b64_tr_b16 a[240:241], v229 offset:24576\n"
    "v_lshlrev_b32 v194, 16, v187\n"
    "v_mfma_f32_16x16x32_bf16 %[q31], a[248:251], v[180:183], %[q31]\n"
    "ds_read_b64_tr_b16 a[242:243], v229 offset:25600\n"
    "v_and_b32 v195, 0xffff0000, v187\n"
    "v_mfma_f32_16x16x32_bf16 %[z0], a[252:255], v[176:179], %[z0]\n"
    "ds_read_b64_tr_b16 a[244:245], v230 offset:24576\n"
    "v_add_f32 v188, v188, v197\n"
    "v_mfma_f32_16x16x32_bf16 %[z1], a[252:255], v[180:183], %[z1]\n"
    "ds_read_b64_tr_b16 a[246:247], v230 offset:25600\n"
    "v_add_f32 v189, v189, v197\n"
    "s_waitcnt lgkmcnt(8)\n"
    "s_waitcnt vmcnt(16)\n"
    "s_barrier\n"
    "v_mfma_f32_16x16x32_bf16 %[z2], a[224:227], v[176:179], %[z2]\n"
    "ds_read_b64_tr_b16 a[248:249], v208 offset:16384\n"
    "v_add_f32 v190, v190, v197\n"
    "v_mfma_f32_16x16x32_bf16 %[z3], a[224:227], v[180:183], %[z3]\n"
    "ds_read_b64_tr_b16 a[250:251], v208 offset:18432\n"
    "s_add_u32 s68, %[rot], 6\n"
    "s_sub_u32 s52, s68, 7\n"
    "s_cmp_ge_u32 s68, 7\n"
    "s_cselect_b32 s68, s52, s68\n"
    "s_add_u32 m0, s50, 131072\n"
    "s_lshl_b32 s71, s68, 19\n"
    "s_add_u32 s71, s71, 262144\n"
    "buffer_load_dwordx4 %[vwq0], s[60:63], s71 offen offset:0 lds\n"
    "v_mfma_f32_16x16x32_bf16 %[z4], a[228:231], v[176:179], %[z4]\n"
    "ds_read_b64_tr_b16 a[252:253], v209 offset:16384\n"
    "v_add_f32 v191, v191, v197\n"
    "v_mfma_f32_16x16x32_bf16 %[z5], a[228:231], v[180:183], %[z5]\n"
    "ds_read_b64_tr_b16 a[254:255], v209 offset:18432\n"
    "buffer_load_dwordx4 %[vwq1], s[60:63], s71 offen offset:1024 lds\n"
    "s_waitcnt lgkmcnt(8)\n"
    "v_mfma_f32_16x16x32_bf16 %[z6], a[232:235], v[176:179], %[z6]\n"
    "ds_read_b64_tr_b16 a[224:225], v210 offset:16384\n"
    "v_add_f32 v192, v192, v197\n"
    "v_mfma_f32_16x16x32_bf16 %[z7], a[232:235], v[180:183], %[z7]\n"
    "ds_read_b64_tr_b16 a[226:227], v210 offset:18432\n"
    "buffer_load_dwordx4 %[vwq2], s[60:63], s71 offen offset:2048 lds\n"
    "v_mfma_f32_16x16x32_bf16 %[z8], a[236:239], v[176:179], %[z8]\n"
    "ds_read_b64_tr_b16 a[228:229], v211 offset:16384\n"
    "v_add_f32 v193, v193, v197\n"
    "v_mfma_f32_16x16x32_bf16 %[z9], a[236:239], v[180:183], %[z9]\n"
    "ds_read_b64_tr_b16 a[230:231], v211 offset:18432\n"
    "buffer_load_dwordx4 %[vwq3], s[60:63], s71 offen offset:3072 lds\n"
    "s_waitcnt lgkmcnt(8)\n"
    "v_mfma_f32_16x16x32_bf16 %[z10], a[240:243], v[176:179], %[z10]\n"
    "ds_read_b64_tr_b16 a[232:233], v212 offset:16384\n"
    "v_add_f32 v194, v194, v197\n"
    "v_mfma_f32_16x16x32_bf16 %[z11], a[240:243], v[180:183], %[z11]\n"
    "ds_read_b64_tr_b16 a[234:235], v212 offset:18432\n"
    "s_add_u32 m0, s50, 49152\n"
    "s_mov_b32 s48, 16384\n"
    "buffer_load_dwordx4 %[vok0], s[40:43], s48 offen offset:0 nt lds\n"
    "v_mfma_f32_16x16x32_bf16 %[z12], a[244:247], v[176:179], %[z12]\n"
    "ds_read_b64_tr_b16 a[236:237], v213 offset:16384\n"
    "v_add_f32 v195, v195, v197\n"
    "v_mfma_f32_16x16x32_bf16 %[z13], a[244:247], v[180:183], %[z13]\n"
    "ds_read_b64_tr_b16 a[238:239], v213 offset:18432\n"
    "buffer_load_dwordx4 %[vok1], s[40:43], s48 offen offset:1024 nt lds\n"
    "s_waitcnt lgkmcnt(8)\n"
    "v_cvt_pk_bf16_f32 v172, v188, v189\n"
    "v_cvt_pk_bf16_f32 v173, v190, v191\n"
    "v_cvt_pk_bf16_f32 v174, v192, v193\n"
    "v_cvt_pk_bf16_f32 v175, v194, v195\n"
    "s_nop 1\n"
    "v_mfma_f32_16x16x32_bf16 v[248:251], v[168:171], v[168:171], v[248:251]\n"
    "v_mfma_f32_16x16x32_bf16 v[252:255], v[172:175], v[172:175], v[252:255]\n"
    "v_mfma_f32_16x16x32_bf16 %[q0], a[248:251], v[168:171], %[q0]\n"
    "ds_read_b64_tr_b16 a[240:241], v214 offset:16384\n"
    "v_accvgpr_read_b32 v184, a88\n"
    "v_accvgpr_read_b32 v185, a89\n"
    "v_mfma_f32_16x16x32_bf16 %[q1], a[248:251], v[172:175], %[q1]\n"
    "ds_read_b64_tr_b16 a[242:243], v214 offset:18432\n"
    "buffer_load_dwordx4 %[vok2], s[40:43], s48 offen offset:2048 nt lds\n"
    "v_mfma_f32_16x16x32_bf16 %[q2], a[252:255], v[168:171], %[q2]\n"
    "ds_read_b64_tr_b16 a[244:245], v215 offset:16384\n"
    "v_accvgpr_read_b32 v186, a90\n"
    "v_accvgpr_read_b32 v187, a91\n"
    "v_mfma_f32_16x16x32_bf16 %[q3], a[252:255], v[172:175], %[q3]\n"
    "ds_read_b64_tr_b16 a[246:247], v215 offset:18432\n"
    "buffer_load_dwordx4 %[vok3], s[40:43], s48 offen offset:3072 nt lds\n"
    "s_waitcnt lgkmcnt(8)\n"
    "v_mfma_f32_16x16x32_bf16 %[q4], a[224:227], v[168:171], %[q4]\n"
    "ds_read_b64_tr_b16 a[248:249], v208 offset:16640\n"
    "v_lshlrev_b32 v188, 16, v184\n"
    "v_and_b32 v189, 0xffff0000, v184\n"
    "v_mfma_f32_16x16x32_bf16 %[q5], a[224:227], v[172:175], %[q5]\n"
    "ds_read_b64_tr_b16 a[250:251], v208 offset:18688\n"
    "v_lshlrev_b32 v190, 16, v185\n"
    "v_and_b32 v191, 0xffff0000, v185\n"
    "v_mfma_f32_16x16x32_bf16 %[q6], a[228:231], v[168:171], %[q6]\n"
    "ds_read_b64_tr_b16 a[252:253], v209 offset:16640\n"
    "v_lshlrev_b32 v192, 16, v186\n"
    "v_and_b32 v193, 0xffff0000, v186\n"
    "v_mfma_f32_16x16x32_bf16 %[q7], a[228:231], v[172:175], %[q7]\n"
    "ds_read_b64_tr_b16 a[254:255], v209 offset:18688\n"
    "v_lshlrev_b32 v194, 16, v187\n"
    "v_and_b32 v195, 0xffff0000, v187\n"
    "s_waitcnt lgkmcnt(8)\n"
    "v_mfma_f32_16x16x32_bf16 %[q8], a[232:235], v[168:171], %[q8]\n"
    "ds_read_b64_tr_b16 a[224:225], v210 offset:16640\n"
    "v_add_f32 v188, v188, v196\n"
    "v_add_f32 v189, v189, v196\n"
    "v_mfma_f32_16x16x32_bf16 %[q9], a[232:235], v[172:175], %[q9]\n"
    "ds_read_b64_tr_b16 a[226:227], v210 offset:18688\n"
    "v_add_f32 v190, v190, v196\n"
    "v_add_f32 v191, v191, v196\n"
    "v_mfma_f32_16x16x32_bf16 %[q10], a[236:239], v[168:171], %[q10]\n"
    "ds_read_b64_tr_b16 a[228:229], v211 offset:16640\n"
    "v_add_f32 v192, v192, v196\n"
    "v_add_f32 v193, v193, v196\n"
    "v_mfma_f32_16x16x32_bf16 %[q11], a[236:239], v[172:175], %[q11]\n"
    "ds_read_b64_tr_b16 a[230:231], v211 offset:18688\n"
    "v_add_f32 v194, v194, v196\n"
    "s_waitcnt lgkmcnt(8)\n"
    "v_mfma_f32_16x16x32_bf16 %[q12], a[240:243], v[168:171], %[q12]\n"
    "ds_read_b64_tr_b16 a[232:233], v212 offset:16640\n"
    "v_add_f32 v195, v195, v196\n"
    "v_mfma_f32_16x16x32_bf16 %[q13], a[240:243], v[172:175], %[q13]\n"
    "ds_read_b64_tr_b16 a[234:235], v212 offset:18688\n"
    "v_cvt_pk_bf16_f32 v176, v188, v189\n"
    "v_mfma_f32_16x16x32_bf16 %[q14], a[244:247], v[168:171], %[q14]\n"
    "ds_read_b64_tr_b16 a[236:237], v213 offset:16640\n"
    "v_cvt_pk_bf16_f32 v177, v190, v191\n"
    "v_mfma_f32_16x16x32_bf16 %[q15], a[244:247], v[172:175], %[q15]\n"
    "ds_read_b64_tr_b16 a[238:239], v213 offset:18688\n"
    "v_cvt_pk_bf16_f32 v178, v192, v193\n"
    "s_waitcnt lgkmcnt(8)\n"
    "v_mfma_f32_16x16x32_bf16 %[q16], a[248:251], v[168:171], %[q16]\n"
    "ds_read_b64_tr_b16 a[240:241], v214 offset:16640\n"
    "v_cvt_pk_bf16_f32 v179, v194, v195\n"
    "v_mfma_f32_16x16x32_bf16 %[q17], a[248:251], v[172:175], %[q17]\n"
    "ds_read_b64_tr_b16 a[242:243], v214 offset:18688\n"
    "v_accvgpr_read_b32 v184, a92\n"
    "v_mfma_f32_16x16x32_bf16 %[q18], a[252:255], v[168:171], %[q18]\n"
    "ds_read_b64_tr_b16 a[244:245], v215 offset:16640\n"
    "v_accvgpr_read_b32 v185, a93\n"
    "v_mfma_f32_16x16x32_bf16 %[q19], a[252:255], v[172:175], %[q19]\n"
    "ds_read_b64_tr_b16 a[246:247], v215 offset:18688\n"
    "v_accvgpr_read_b32 v186, a94\n"
    "s_waitcnt lgkmcnt(8)\n"
    "v_mfma_f32_16x16x32_bf16 %[q20], a[224:227], v[168:171], %[q20]\n"
    "ds_read_b64_tr_b16 a[248:249], v238 offset:16384\n"
    "v_accvgpr_read_b32 v187, a95\n"
    "v_mfma_f32_16x16x32_bf16 %[q21], a[224:227], v[172:175], %[q21]\n"
    "ds_read_b64_tr_b16 a[250:251], v238 offset:17408\n"
    "v_lshlrev_b32 v188, 16, v184\n"
    "v_mfma_f32_16x16x32_bf16 %[q22], a[228:231], v[168:171], %[q22]\n"
    "ds_read_b64_tr_b16 a[252:253], v239 offset:16384\n"
    "v_and_b32 v189, 0xffff0000, v184\n"
    "v_mfma_f32_16x16x32_bf16 %[q23], a[228:231], v[172:175], %[q23]\n"
    "ds_read_b64_tr_b16 a[254:255], v239 offset:17408\n"
    "v_lshlrev_b32 v190, 16, v185\n"
    "s_waitcnt lgkmcnt(8)\n"
    "v_mfma_f32_16x16x32_bf16 %[q24], a[232:235], v[168:171], %[q24]\n"
    "ds_read_b64_tr_b16 a[224:225], v240 offset:16384\n"
    "v_and_b32 v191, 0xffff0000, v185\n"
    "v_mfma_f32_16x16x32_bf16 %[q25], a[232:235], v[172:175], %[q25]\n"
    "ds_read_b64_tr_b16 a[226:227], v240 offset:17408\n"
    "v_lshlrev_b32 v192, 16, v186\n"
    "v_mfma_f32_16x16x32_bf16 %[q26], a[236:239], v[168:171], %[q26]\n"
    "ds_read_b64_tr_b16 a[228:229], v241 offset:16384\n"
    "v_and_b32 v193, 0xffff0000, v186\n"
    "v_mfma_f32_16x16x32_bf16 %[q27], a[236:239], v[172:175], %[q27]\n"
    "ds_read_b64_tr_b16 a[230:231], v241 offset:17408\n"
    "v_lshlrev_b32 v194, 16, v187\n"
    "s_waitcnt lgkmcnt(8)\n"
    "v_mfma_f32_16x16x32_bf16 %[q28], a[240:243], v[168:171], %[q28]\n"
    "ds_read_b64_tr_b16 a[232:233], v242 offset:16384\n"
    "v_and_b32 v195, 0xffff0000, v187\n"
    "v_mfma_f32_16x16x32_bf16 %[q29], a[240:243], v[172:175], %[q29]\n"
    "ds_read_b64_tr_b16 a[234:235], v242 offset:17408\n"
    "v_add_f32 v188, v188, v197\n"
    "v_mfma_f32_16x16x32_bf16 %[q30], a[244:247], v[168:171], %[q30]\n"
    "ds_read_b64_tr_b16 a[236:237], v243 offset:16384\n"
    "v_add_f32 v189, v189, v197\n"
    "v_mfma_f32_16x16x32_bf16 %[q31], a[244:247], v[172:175], %[q31]\n"
    "ds_read_b64_tr_b16 a[238:239], v243 offset:17408\n"
    "v_add_f32 v190, v190, v197\n"
    "s_waitcnt lgkmcnt(8)\n"
    "v_mfma_f32_16x16x32_bf16 %[z0], a[248:251], v[168:171], %[z0]\n"
    "ds_read_b64_tr_b16 a[240:241], v244 offset:16384\n"
    "v_add_f32 v191, v191, v197\n"
    "v_mfma_f32_16x16x32_bf16 %[z1], a[248:251], v[172:175], %[z1]\n"
    "ds_read_b64_tr_b16 a[242:243], v244 offset:17408\n"
    "v_add_f32 v192, v192, v197\n"
    "s_waitcnt vmcnt(20)\n"
    "s_barrier\n"
    "v_mfma_f32_16x16x32_bf16 %[z2], a[252:255], v[168:171], %[z2]\n"
    "ds_read_b64_tr_b16 a[244:245], v208 offset:32768\n"
    "v_add_f32 v193, v193, v197\n"
    "v_mfma_f32_16x16x32_bf16 %[z3], a[252:255], v[172:175], %[z3]\n"
    "ds_read_b64_tr_b16 a[246:247], v208 offset:34816\n"
    "s_add_u32 m0, s50, 65536\n"
    "s_mov_b32 s48, 32768\n"
    "buffer_load_dwordx4 %[vok0], s[40:43], s48 offen offset:0 nt lds\n"
    "s_waitcnt lgkmcnt(8)\n"
    "v_mfma_f32_16x16x32_bf16 %[z4], a[224:227], v[168:171], %[z4]\n"
    "ds_read_b64_tr_b16 a[248:249], v209 offset:32768\n"
    "v_add_f32 v194, v194, v197\n"
    "v_mfma_f32_16x16x32_bf16 %[z5], a[224:227], v[172:175], %[z5]\n"
    "ds_read_b64_tr_b16 a[250:251], v209 offset:34816\n"
    "buffer_load_dwordx4 %[vok1], s[40:43], s48 offen offset:1024 nt lds\n"
    "v_mfma_f32_16x16x32_bf16 %[z6], a[228:231], v[168:171], %[z6]\n"
    "ds_read_b64_tr_b16 a[252:253], v210 offset:32768\n"
    "v_add_f32 v195, v195, v197\n"
    "v_mfma_f32_16x16x32_bf16 %[z7], a[228:231], v[172:175], %[z7]\n"
    "ds_read_b64_tr_b16 a[254:255], v210 offset:34816\n"
    "buffer_load_dwordx4 %[vok2], s[40:43], s48 offen offset:2048 nt lds\n"
    "s_waitcnt lgkmcnt(8)\n"
    "v_mfma_f32_16x16x32_bf16 %[z8], a[232:235], v[168:171], %[z8]\n"
    "ds_read_b64_tr_b16 a[224:225], v211 offset:32768\n"
    "v_cvt_pk_bf16_f32 v180, v188, v189\n"
    "v_mfma_f32_16x16x32_bf16 %[z9], a[232:235], v[172:175], %[z9]\n"
    "ds_read_b64_tr_b16 a[226:227], v211 offset:34816\n"
    "buffer_load_dwordx4 %[vok3], s[40:43], s48 offen offset:3072 nt lds\n"
    "v_mfma_f32_16x16x32_bf16 %[z10], a[236:239], v[168:171], %[z10]\n"
    "ds_read_b64_tr_b16 a[228:229], v212 offset:32768\n"
    "v_cvt_pk_bf16_f32 v181, v190, v191\n"
    "v_mfma_f32_16x16x32_bf16 %[z11], a[236:239], v[172:175], %[z11]\n"
    "ds_read_b64_tr_b16 a[230:231], v212 offset:34816\n"
    "v_cvt_pk_bf16_f32 v182, v192, v193\n"
    "s_waitcnt lgkmcnt(8)\n"
    "v_mfma_f32_16x16x32_bf16 %[z12], a[240:243], v[168:171], %[z12]\n"
    "ds_read_b64_tr_b16 a[232:233], v213 offset:32768\n"
    "v_cvt_pk_bf16_f32 v183, v194, v195\n"
    "v_mfma_f32_16x16x32_bf16 %[z13], a[240:243], v[172:175], %[z13]\n"
    "ds_read_b64_tr_b16 a[234:235], v213 offset:34816\n"
    "s_nop 1\n"
    "v_mfma_f32_16x16x32_bf16 v[248:251], v[176:179], v[176:179], v[248:251]\n"
    "v_mfma_f32_16x16x32_bf16 v[252:255], v[180:183], v[180:183], v[252:255]\n"
    "v_mfma_f32_16x16x32_bf16 %[q0], a[244:247], v[176:179], %[q0]\n"
    "ds_read_b64_tr_b16 a[236:237], v214 offset:32768\n"
    "v_accvgpr_read_b32 v184, a96\n"
    "v_accvgpr_read_b32 v185, a97\n"
    "v_mfma_f32_16x16x32_bf16 %[q1], a[244:247], v[180:183], %[q1]\n"
    "ds_read_b64_tr_b16 a[238:239], v214 offset:34816\n"
    "v_accvgpr_read_b32 v186, a98\n"
    "v_accvgpr_read_b32 v187, a99\n"
    "s_waitcnt lgkmcnt(8)\n"
    "v_mfma_f32_16x16x32_bf16 %[q2], a[248:251], v[176:179], %[q2]\n"
    "ds_read_b64_tr_b16 a[240:241], v215 offset:32768\n"
    "v_lshlrev_b32 v188, 16, v184\n"
    "v_and_b32 v189, 0xffff0000, v184\n"
    "v_mfma_f32_16x16x32_bf16 %[q3], a[248:251], v[180:183], %[q3]\n"
    "ds_read_b64_tr_b16 a[242:243], v215 offset:34816\n"
    "v_lshlrev_b32 v190, 16, v185\n"
    "v_and_b32 v191, 0xffff0000, v185\n"
    "v_mfma_f32_16x16x32_bf16 %[q4], a[252:255], v[176:179], %[q4]\n"
    "ds_read_b64_tr_b16 a[244:245], v208 offset:33024\n"
    "v_lshlrev_b32 v192, 16, v186\n"
    "v_and_b32 v193, 0xffff0000, v186\n"
    "v_mfma_f32_16x16x32_bf16 %[q5], a[252:255], v[180:183], %[q5]\n"
    "ds_read_b64_tr_b16 a[246:247], v208 offset:35072\n"
    "v_lshlrev_b32 v194, 16, v187\n"
    "v_and_b32 v195, 0xffff0000, v187\n"
    "s_waitcnt lgkmcnt(8)\n"
    "v_mfma_f32_16x16x32_bf16 %[q6], a[224:227], v[176:179], %[q6]\n"
    "ds_read_b64_tr_b16 a[248:249], v209 offset:33024\n"
    "v_add_f32 v188, v188, v196\n"
    "v_add_f32 v189, v189, v196\n"
    "v_mfma_f32_16x16x32_bf16 %[q7], a[224:227], v[180:183], %[q7]\n"
    "ds_read_b64_tr_b16 a[250:251], v209 offset:35072\n"
    "v_add_f32 v190, v190, v196\n"
    "v_add_f32 v191, v191, v196\n"
    "v_mfma_f32_16x16x32_bf16 %[q8], a[228:231], v[176:179], %[q8]\n"
    "ds_read_b64_tr_b16 a[252:253], v210 offset:33024\n"
    "v_add_f32 v192, v192, v196\n"
    "v_add_f32 v193, v193, v196\n"
    "v_mfma_f32_16x16x32_bf16 %[q9], a[228:231], v[180:183], %[q9]\n"
    "ds_read_b64_tr_b16 a[254:255], v210 offset:35072\n"
    "v_add_f32 v194, v194, v196\n"
    "s_waitcnt lgkmcnt(8)\n"
    "v_mfma_f32_16x16x32_bf16 %[q10], a[232:235], v[176:179], %[q10]\n"
    "ds_read_b64_tr_b16 a[224:225], v211 offset:33024\n"
    "v_add_f32 v195, v195, v196\n"
    "v_mfma_f32_16x16x32_bf16 %[q11], a[232:235], v[180:183], %[q11]\n"
    "ds_read_b64_tr_b16 a[226:227], v211 offset:35072\n"
    "v_cvt_pk_bf16_f32 v168, v188, v189\n"
    "v_mfma_f32_16x16x32_bf16 %[q12], a[236:239], v[176:179], %[q12]\n"
    "ds_read_b64_tr_b16 a[228:229], v212 offset:33024\n"
    "v_cvt_pk_bf16_f32 v169, v190, v191\n"
    "v_mfma_f32_16x16x32_bf16 %[q13], a[236:239], v[180:183], %[q13]\n"
    "ds_read_b64_tr_b16 a[230:231], v212 offset:35072\n"
    "v_cvt_pk_bf16_f32 v170, v192, v193\n"
    "s_waitcnt lgkmcnt(8)\n"
    "v_mfma_f32_16x16x32_bf16 %[q14], a[240:243], v[176:179], %[q14]\n"
    "ds_read_b64_tr_b16 a[232:233], v213 offset:33024\n"
    "v_cvt_pk_bf16_f32 v171, v194, v195\n"
    "v_mfma_f32_16x16x32_bf16 %[q15], a[240:243], v[180:183], %[q15]\n"
    "ds_read_b64_tr_b16 a[234:235], v213 offset:35072\n"
    "v_accvgpr_read_b32 v184, a100\n"
    "v_mfma_f32_16x16x32_bf16 %[q16], a[244:247], v[176:179], %[q16]\n"
    "ds_read_b64_tr_b16 a[236:237], v214 offset:33024\n"
    "v_accvgpr_read_b32 v185, a101\n"
    "v_mfma_f32_16x16x32_bf16 %[q17], a[244:247], v[180:183], %[q17]\n"
    "ds_read_b64_tr_b16 a[238:239], v214 offset:35072\n"
    "v_accvgpr_read_b32 v186, a102\n"
    "s_waitcnt lgkmcnt(8)\n"
    "v_mfma_f32_16x16x32_bf16 %[q18], a[248:251], v[176:179], %[q18]\n"
    "ds_read_b64_tr_b16 a[240:241], v215 offset:33024\n"
    "v_accvgpr_read_b32 v187, a103\n"
    "v_mfma_f32_16x16x32_bf16 %[q19], a[248:251], v[180:183], %[q19]\n"
    "ds_read_b64_tr_b16 a[242:243], v215 offset:35072\n"
    "v_lshlrev_b32 v188, 16, v184\n"
    "v_mfma_f32_16x16x32_bf16 %[q20], a[252:255], v[176:179], %[q20]\n"
    "ds_read_b64_tr_b16 a[244:245], v238 offset:24576\n"
    "v_and_b32 v189, 0xffff0000, v184\n"
    "v_mfma_f32_16x16x32_bf16 %[q21], a[252:255], v[180:183], %[q21]\n"
    "ds_read_b64_tr_b16 a[246:247], v238 offset:25600\n"
    "v_lshlrev_b32 v190, 16, v185\n"
    "s_waitcnt lgkmcnt(8)\n"
    "v_mfma_f32_16x16x32_bf16 %[q22], a[224:227], v[176:179], %[q22]\n"
    "ds_read_b64_tr_b16 a[248:249], v239 offset:24576\n"
    "v_and_b32 v191, 0xffff0000, v185\n"
    "v_mfma_f32_16x16x32_bf16 %[q23], a[224:227], v[180:183], %[q23]\n"
    "ds_read_b64_tr_b16 a[250:251], v239 offset:25600\n"
    "v_lshlrev_b32 v192, 16, v186\n"
    "v_mfma_f32_16x16x32_bf16 %[q24], a[228:231], v[176:179], %[q24]\n"
    "ds_read_b64_tr_b16 a[252:253], v240 offset:24576\n"
    "v_and_b32 v193, 0xffff0000, v186\n"
    "v_mfma_f32_16x16x32_bf16 %[q25], a[228:231], v[180:183], %[q25]\n"
    "ds_read_b64_tr_b16 a[254:255], v240 offset:25600\n"
    "v_lshlrev_b32 v194, 16, v187\n"
    "s_waitcnt lgkmcnt(8)\n"
    "v_mfma_f32_16x16x32_bf16 %[q26], a[232:235], v[176:179], %[q26]\n"
    "ds_read_b64_tr_b16 a[224:225], v241 offset:24576\n"
    "v_and_b32 v195, 0xffff0000, v187\n"
    "v_mfma_f32_16x16x32_bf16 %[q27], a[232:235], v[180:183], %[q27]\n"
    "ds_read_b64_tr_b16 a[226:227], v241 offset:25600\n"
    "v_add_f32 v188, v188, v197\n"
    "v_mfma_f32_16x16x32_bf16 %[q28], a[236:239], v[176:179], %[q28]\n"
    "ds_read_b64_tr_b16 a[228:229], v242 offset:24576\n"
    "v_add_f32 v189, v189, v197\n"
    "v_mfma_f32_16x16x32_bf16 %[q29], a[236:239], v[180:183], %[q29]\n"
    "ds_read_b64_tr_b16 a[230:231], v242 offset:25600\n"
    "v_add_f32 v190, v190, v197\n"
    "s_waitcnt lgkmcnt(8)\n"
    "v_mfma_f32_16x16x32_bf16 %[q30], a[240:243], v[176:179], %[q30]\n"
    "ds_read_b64_tr_b16 a[232:233], v243 offset:24576\n"
    "v_add_f32 v191, v191, v197\n"
    "v_mfma_f32_16x16x32_bf16 %[q31], a[240:243], v[180:183], %[q31]\n"
    "ds_read_b64_tr_b16 a[234:235], v243 offset:25600\n"
    "v_add_f32 v192, v192, v197\n"
    "v_mfma_f32_16x16x32_bf16 %[z0], a[244:247], v[176:179], %[z0]\n"
    "ds_read_b64_tr_b16 a[236:237], v244 offset:24576\n"
    "v_add_f32 v193, v193, v197\n"
    "v_mfma_f32_16x16x32_bf16 %[z1], a[244:247], v[180:183], %[z1]\n"
    "ds_read_b64_tr_b16 a[238:239], v244 offset:25600\n"
    "v_add_f32 v194, v194, v197\n"
    "s_waitcnt lgkmcnt(8)\n"
    "s_waitcnt vmcnt(16)\n"
    "s_barrier\n"
    "v_mfma_f32_16x16x32_bf16 %[z2], a[248:251], v[176:179], %[z2]\n"
    "ds_read_b64_tr_b16 a[240:241], v208 offset:49152\n"
    "v_add_f32 v195, v195, v197\n"
    "v_mfma_f32_16x16x32_bf16 %[z3], a[248:251], v[180:183], %[z3]\n"
    "ds_read_b64_tr_b16 a[242:243], v208 offset:51200\n"
    "s_add_u32 m0, s50, 81920\n"
    "s_mov_b32 s48, 49152\n"
    "buffer_load_dwordx4 %[vok0], s[40:43], s48 offen offset:0 nt lds\n"
    "v_mfma_f32_16x16x32_bf16 %[z4], a[252:255], v[176:179], %[z4]\n"
    "ds_read_b64_tr_b16 a[244:245], v209 offset:49152\n"
    "v_cvt_pk_bf16_f32 v172, v188, v189\n"
    "v_mfma_f32_16x16x32_bf16 %[z5], a[252:255], v[180:183], %[z5]\n"
    "ds_read_b64_tr_b16 a[246:247], v209 offset:51200\n"
    "buffer_load_dwordx4 %[vok1], s[40:43], s48 offen offset:1024 nt lds\n"
    "s_waitcnt lgkmcnt(8)\n"
    "v_mfma_f32_16x16x32_bf16 %[z6], a[224:227], v[176:179], %[z6]\n"
    "ds_read_b64_tr_b16 a[248:249], v210 offset:49152\n"
    "v_cvt_pk_bf16_f32 v173, v190, v191\n"
    "v_mfma_f32_16x16x32_bf16 %[z7], a[224:227], v[180:183], %[z7]\n"
    "ds_read_b64_tr_b16 a[250:251], v210 offset:51200\n"
    "buffer_load_dwordx4 %[vok2], s[40:43], s48 offen offset:2048 nt lds\n"
    "v_mfma_f32_16x16x32_bf16 %[z8], a[228:231], v[176:179], %[z8]\n"
    "ds_read_b64_tr_b16 a[252:253], v211 offset:49152\n"
    "v_cvt_pk_bf16_f32 v174, v192, v193\n"
    "v_mfma_f32_16x16x32_bf16 %[z9], a[228:231], v[180:183], %[z9]\n"
    "ds_read_b64_tr_b16 a[254:255], v211 offset:51200\n"
    "buffer_load_dwordx4 %[vok3], s[40:43], s48 offen offset:3072 nt lds\n"
    "s_waitcnt lgkmcnt(8)\n"
    "v_mfma_f32_16x16x32_bf16 %[z10], a[232:235], v[176:179], %[z10]\n"
    "ds_read_b64_tr_b16 a[224:225], v212 offset:49152\n"
    "v_cvt_pk_bf16_f32 v175, v194, v195\n"
    "v_mfma_f32_16x16x32_bf16 %[z11], a[232:235], v[180:183], %[z11]\n"
    "ds_read_b64_tr_b16 a[226:227], v212 offset:51200\n"
    "v_mfma_f32_16x16x32_bf16 %[z12], a[236:239], v[176:179], %[z12]\n"
    "ds_read_b64_tr_b16 a[228:229], v213 offset:49152\n"
    "v_mfma_f32_16x16x32_bf16 %[z13], a[236:239], v[180:183], %[z13]\n"
    "ds_read_b64_tr_b16 a[230:231], v213 offset:51200\n"
    "s_waitcnt lgkmcnt(8)\n"
    "s_nop 1\n"
    "v_mfma_f32_16x16x32_bf16 v[248:251], v[168:171], v[168:171], v[248:251]\n"
    "v_mfma_f32_16x16x32_bf16 v[252:255], v[172:175], v[172:175], v[252:255]\n"
    "v_mfma_f32_16x16x32_bf16 %[q0], a[240:243], v[168:171], %[q0]\n"
    "ds_read_b64_tr_b16 a[232:233], v214 offset:49152\n"
    "v_accvgpr_read_b32 v184, a104\n"
    "v_accvgpr_read_b32 v185, a105\n"
    "v_mfma_f32_16x16x32_bf16 %[q1], a[240:243], v[172:175], %[q1]\n"
    "ds_read_b64_tr_b16 a[234:235], v214 offset:51200\n"
    "v_accvgpr_read_b32 v186, a106\n"
    "v_accvgpr_read_b32 v187, a107\n"
    "v_mfma_f32_16x16x32_bf16 %[q2], a[244:247], v[168:171], %[q2]\n"
    "ds_read_b64_tr_b16 a[236:237], v215 offset:49152\n"
    "v_lshlrev_b32 v188, 16, v184\n"
    "v_and_b32 v189, 0xffff0000, v184\n"
    "v_mfma_f32_16x16x32_bf16 %[q3], a[244:247], v[172:175], %[q3]\n"
    "ds_read_b64_tr_b16 a[238:239], v215 offset:51200\n"
    "v_lshlrev_b32 v190, 16, v185\n"
    "v_and_b32 v191, 0xffff0000, v185\n"
    "s_waitcnt lgkmcnt(8)\n"
    "v_mfma_f32_16x16x32_bf16 %[q4], a[248:251], v[168:171], %[q4]\n"
    "ds_read_b64_tr_b16 a[240:241], v208 offset:49408\n"
    "v_lshlrev_b32 v192, 16, v186\n"
    "v_and_b32 v193, 0xffff0000, v186\n"
    "v_mfma_f32_16x16x32_bf16 %[q5], a[248:251], v[172:175], %[q5]\n"
    "ds_read_b64_tr_b16 a[242:243], v208 offset:51456\n"
    "v_lshlrev_b32 v194, 16, v187\n"
    "v_and_b32 v195, 0xffff0000, v187\n"
    "v_mfma_f32_16x16x32_bf16 %[q6], a[252:255], v[168:171], %[q6]\n"
    "ds_read_b64_tr_b16 a[244:245], v209 offset:49408\n"
    "v_add_f32 v188, v188, v196\n"
    "v_add_f32 v189, v189, v196\n"
    "v_mfma_f32_16x16x32_bf16 %[q7], a[252:255], v[172:175], %[q7]\n"
    "ds_read_b64_tr_b16 a[246:247], v209 offset:51456\n"
    "v_add_f32 v190, v190, v196\n"
    "v_add_f32 v191, v191, v196\n"
    "s_waitcnt lgkmcnt(8)\n"
    "v_mfma_f32_16x16x32_bf16 %[q8], a[224:227], v[168:171], %[q8]\n"
    "ds_read_b64_tr_b16 a[248:249], v210 offset:49408\n"
    "v_add_f32 v192, v192, v196\n"
    "v_add_f32 v193, v193, v196\n"
    "v_mfma_f32_16x16x32_bf16 %[q9], a[224:227], v[172:175], %[q9]\n"
    "ds_read_b64_tr_b16 a[250:251], v210 offset:51456\n"
    "v_add_f32 v194, v194, v196\n"
    "v_mfma_f32_16x16x32_bf16 %[q10], a[228:231], v[168:171], %[q10]\n"
    "ds_read_b64_tr_b16 a[252:253], v211 offset:49408\n"
    "v_add_f32 v195, v195, v196\n"
    "v_mfma_f32_16x16x32_bf16 %[q11], a[228:231], v[172:175], %[q11]\n"
    "ds_read_b64_tr_b16 a[254:255], v211 offset:51456\n"
    "v_cvt_pk_bf16_f32 v176, v188, v189\n"
    "s_waitcnt lgkmcnt(8)\n"
    "v_mfma_f32_16x16x32_bf16 %[q12], a[232:235], v[168:171], %[q12]\n"
    "ds_read_b64_tr_b16 a[224:225], v212 offset:49408\n"
    "v_cvt_pk_bf16_f32 v177, v190, v191\n"
    "v_mfma_f32_16x16x32_bf16 %[q13], a[232:235], v[172:175], %[q13]\n"
    "ds_read_b64_tr_b16 a[226:227], v212 offset:51456\n"
    "v_cvt_pk_bf16_f32 v178, v192, v193\n"
    "v_mfma_f32_16x16x32_bf16 %[q14], a[236:239], v[168:171], %[q14]\n"
    "ds_read_b64_tr_b16 a[228:229], v213 offset:49408\n"
    "v_cvt_pk_bf16_f32 v179, v194, v195\n"
    "v_mfma_f32_16x16x32_bf16 %[q15], a[236:239], v[172:175], %[q15]\n"
    "ds_read_b64_tr_b16 a[230:231], v213 offset:51456\n"
    "v_accvgpr_read_b32 v184, a108\n"
    "s_waitcnt lgkmcnt(8)\n"
    "v_mfma_f32_16x16x32_bf16 %[q16], a[240:243], v[168:171], %[q16]\n"
    "ds_read_b64_tr_b16 a[232:233], v214 offset:49408\n"
    "v_accvgpr_read_b32 v185, a109\n"
    "v_mfma_f32_16x16x32_bf16 %[q17], a[240:243], v[172:175], %[q17]\n"
    "ds_read_b64_tr_b16 a[234:235], v214 offset:51456\n"
    "v_accvgpr_read_b32 v186, a110\n"
    "v_mfma_f32_16x16x32_bf16 %[q18], a[244:247], v[168:171], %[q18]\n"
    "ds_read_b64_tr_b16 a[236:237], v215 offset:49408\n"
    "v_accvgpr_read_b32 v187, a111\n"
    "v_mfma_f32_16x16x32_bf16 %[q19], a[244:247], v[172:175], %[q19]\n"
    "ds_read_b64_tr_b16 a[238:239], v215 offset:51456\n"
    "v_lshlrev_b32 v188, 16, v184\n"
    "s_waitcnt lgkmcnt(8)\n"
    "v_mfma_f32_16x16x32_bf16 %[q20], a[248:251], v[168:171], %[q20]\n"
    "ds_read_b64_tr_b16 a[240:241], v224 offset:0\n"
    "v_and_b32 v189, 0xffff0000, v184\n"
    "v_mfma_f32_16x16x32_bf16 %[q21], a[248:251], v[172:175], %[q21]\n"
    "ds_read_b64_tr_b16 a[242:243], v224 offset:1024\n"
    "v_lshlrev_b32 v190, 16, v185\n"
    "v_mfma_f32_16x16x32_bf16 %[q22], a[252:255], v[168:171], %[q22]\n"
    "ds_read_b64_tr_b16 a[244:245], v225 offset:0\n"
    "v_and_b32 v191, 0xffff0000, v185\n"
    "v_mfma_f32_16x16x32_bf16 %[q23], a[252:255], v[172:175], %[q23]\n"
    "ds_read_b64_tr_b16 a[246:247], v225 offset:1024\n"
    "v_lshlrev_b32 v192, 16, v186\n"
    "s_waitcnt lgkmcnt(8)\n"
    "v_mfma_f32_16x16x32_bf16 %[q24], a[224:227], v[168:171], %[q24]\n"
    "ds_read_b64_tr_b16 a[248:249], v226 offset:0\n"
    "v_and_b32 v193, 0xffff0000, v186\n"
    "v_mfma_f32_16x16x32_bf16 %[q25], a[224:227], v[172:175], %[q25]\n"
    "ds_read_b64_tr_b16 a[250:251], v226 offset:1024\n"
    "v_lshlrev_b32 v194, 16, v187\n"
    "v_mfma_f32_16x16x32_bf16 %[q26], a[228:231], v[168:171], %[q26]\n"
    "ds_read_b64_tr_b16 a[252:253], v227 offset:0\n"
    "v_and_b32 v195, 0xffff0000, v187\n"
    "v_mfma_f32_16x16x32_bf16 %[q27], a[228:231], v[172:175], %[q27]\n"
    "ds_read_b64_tr_b16 a[254:255], v227 offset:1024\n"
    "v_add_f32 v188, v188, v197\n"
    "s_waitcnt lgkmcnt(8)\n"
    "v_mfma_f32_16x16x32_bf16 %[q28], a[232:235], v[168:171], %[q28]\n"
    "ds_read_b64_tr_b16 a[224:225], v228 offset:0\n"
    "v_add_f32 v189, v189, v197\n"
    "v_mfma_f32_16x16x32_bf16 %[q29], a[232:235], v[172:175], %[q29]\n"
    "ds_read_b64_tr_b16 a[226:227], v228 offset:1024\n"
    "v_add_f32 v190, v190, v197\n"
    "v_mfma_f32_16x16x32_bf16 %[q30], a[236:239], v[168:171], %[q30]\n"
    "ds_read_b64_tr_b16 a[228:229], v229 offset:0\n"
    "v_add_f32 v191, v191, v197\n"
    "v_mfma_f32_16x16x32_bf16 %[q31], a[236:239], v[172:175], %[q31]\n"
    "ds_read_b64_tr_b16 a[230:231], v229 offset:1024\n"
    "v_add_f32 v192, v192, v197\n"
    "s_waitcnt lgkmcnt(8)\n"
    "v_mfma_f32_16x16x32_bf16 %[z0], a[240:243], v[168:171], %[z0]\n"
    "ds_read_b64_tr_b16 a[232:233], v230 offset:0\n"
    "v_add_f32 v193, v193, v197\n"
    "v_mfma_f32_16x16x32_bf16 %[z1], a[240:243], v[172:175], %[z1]\n"
    "ds_read_b64_tr_b16 a[234:235], v230 offset:1024\n"
    "v_add_f32 v194, v194, v197\n"
    "s_waitcnt vmcnt(12)\n"
    "s_barrier\n"
    "v_mfma_f32_16x16x32_bf16 %[z2], a[244:247], v[168:171], %[z2]\n"
    "ds_read_b64_tr_b16 a[236:237], v216 offset:0\n"
    "v_add_f32 v195, v195, v197\n"
    "v_mfma_f32_16x16x32_bf16 %[z3], a[244:247], v[172:175], %[z3]\n"
    "ds_read_b64_tr_b16 a[238:239], v216 offset:2048\n"
    "s_add_u32 m0, s50, 98304\n"
    "s_mov_b32 s48, 0\n"
    "buffer_load_dwordx4 %[vov0], s[44:47], s48 offen offset:0 nt lds\n"
    "s_waitcnt lgkmcnt(8)\n"
    "v_mfma_f32_16x16x32_bf16 %[z4], a[248:251], v[168:171], %[z4]\n"
    "ds_read_b64_tr_b16 a[240:241], v217 offset:0\n"
    "v_cvt_pk_bf16_f32 v180, v188, v189\n"
    "v_mfma_f32_16x16x32_bf16 %[z5], a[248:251], v[172:175], %[z5]\n"
    "ds_read_b64_tr_b16 a[242:243], v217 offset:2048\n"
    "buffer_load_dwordx4 %[vov1], s[44:47], s48 offen offset:1024 nt lds\n"
    "v_mfma_f32_16x16x32_bf16 %[z6], a[252:255], v[168:171], %[z6]\n"
    "ds_read_b64_tr_b16 a[244:245], v218 offset:0\n"
    "v_cvt_pk_bf16_f32 v181, v190, v191\n"
    "v_mfma_f32_16x16x32_bf16 %[z7], a[252:255], v[172:175], %[z7]\n"
    "ds_read_b64_tr_b16 a[246:247], v218 offset:2048\n"
    "buffer_load_dwordx4 %[vov2], s[44:47], s48 offen offset:2048 nt lds\n"
    "s_waitcnt lgkmcnt(8)\n"
    "v_mfma_f32_16x16x32_bf16 %[z8], a[224:227], v[168:171], %[z8]\n"
    "ds_read_b64_tr_b16 a[248:249], v219 offset:0\n"
    "v_cvt_pk_bf16_f32 v182, v192, v193\n"
    "v_mfma_f32_16x16x32_bf16 %[z9], a[224:227], v[172:175], %[z9]\n"
    "ds_read_b64_tr_b16 a[250:251], v219 offset:2048\n"
    "buffer_load_dwordx4 %[vov3], s[44:47], s48 offen offset:3072 nt lds\n"
    "v_mfma_f32_16x16x32_bf16 %[z10], a[228:231], v[168:171], %[z10]\n"
    "ds_read_b64_tr_b16 a[252:253], v220 offset:0\n"
    "v_cvt_pk_bf16_f32 v183, v194, v195\n"
    "v_mfma_f32_16x16x32_bf16 %[z11], a[228:231], v[172:175], %[z11]\n"
    "ds_read_b64_tr_b16 a[254:255], v220 offset:2048\n"
    "s_waitcnt lgkmcnt(8)\n"
    "v_mfma_f32_16x16x32_bf16 %[z12], a[232:235], v[168:171], %[z12]\n"
    "ds_read_b64_tr_b16 a[224:225], v221 offset:0\n"
    "v_mfma_f32_16x16x32_bf16 %[z13], a[232:235], v[172:175], %[z13]\n"
    "ds_read_b64_tr_b16 a[226:227], v221 offset:2048\n"
    "s_nop 1\n"
    "v_mfma_f32_16x16x32_bf16 v[248:251], v[176:179], v[176:179], v[248:251]\n"
    "v_mfma_f32_16x16x32_bf16 v[252:255], v[180:183], v[180:183], v[252:255]\n"
    "v_mfma_f32_16x16x32_bf16 %[q0], a[236:239], v[176:179], %[q0]\n"
    "ds_read_b64_tr_b16 a[228:229], v222 offset:0\n"
    "v_mfma_f32_16x16x32_bf16 %[q1], a[236:239], v[180:183], %[q1]\n"
    "ds_read_b64_tr_b16 a[230:231], v222 offset:2048\n"
    "s_waitcnt lgkmcnt(8)\n"
    "v_mfma_f32_16x16x32_bf16 %[q2], a[240:243], v[176:179], %[q2]\n"
    "ds_read_b64_tr_b16 a[232:233], v223 offset:0\n"
    "v_mfma_f32_16x16x32_bf16 %[q3], a[240:243], v[180:183], %[q3]\n"
    "ds_read_b64_tr_b16 a[234:235], v223 offset:2048\n"
    "v_mfma_f32_16x16x32_bf16 %[q4], a[244:247], v[176:179], %[q4]\n"
    "ds_read_b64_tr_b16 a[236:237], v216 offset:256\n"
    "v_mfma_f32_16x16x32_bf16 %[q5], a[244:247], v[180:183], %[q5]\n"
    "ds_read_b64_tr_b16 a[238:239], v216 offset:2304\n"
    "s_waitcnt lgkmcnt(8)\n"
    "v_mfma_f32_16x16x32_bf16 %[q6], a[248:251], v[176:179], %[q6]\n"
    "ds_read_b64_tr_b16 a[240:241], v217 offset:256\n"
    "v_mfma_f32_16x16x32_bf16 %[q7], a[248:251], v[180:183], %[q7]\n"
    "ds_read_b64_tr_b16 a[242:243], v217 offset:2304\n"
    "v_mfma_f32_16x16x32_bf16 %[q8], a[252:255], v[176:179], %[q8]\n"
    "ds_read_b64_tr_b16 a[244:245], v218 offset:256\n"
    "v_mfma_f32_16x16x32_bf16 %[q9], a[252:255], v[180:183], %[q9]\n"
    "ds_read_b64_tr_b16 a[246:247], v218 offset:2304\n"
    "s_waitcnt lgkmcnt(8)\n"
    "v_mfma_f32_16x16x32_bf16 %[q10], a[224:227], v[176:179], %[q10]\n"
    "ds_read_b64_tr_b16 a[248:249], v219 offset:256\n"
    "v_mfma_f32_16x16x32_bf16 %[q11], a[224:227], v[180:183], %[q11]\n"
    "ds_read_b64_tr_b16 a[250:251], v219 offset:2304\n"
    "v_mfma_f32_16x16x32_bf16 %[q12], a[228:231], v[176:179], %[q12]\n"
    "ds_read_b64_tr_b16 a[252:253], v220 offset:256\n"
    "v_mfma_f32_16x16x32_bf16 %[q13], a[228:231], v[180:183], %[q13]\n"
    "ds_read_b64_tr_b16 a[254:255], v220 offset:2304\n"
    "s_waitcnt lgkmcnt(8)\n"
    "v_mfma_f32_16x16x32_bf16 %[q14], a[232:235], v[176:179], %[q14]\n"
    "ds_read_b64_tr_b16 a[224:225], v221 offset:256\n"
    "v_mfma_f32_16x16x32_bf16 %[q15], a[232:235], v[180:183], %[q15]\n"
    "ds_read_b64_tr_b16 a[226:227], v221 offset:2304\n"
    "v_mfma_f32_16x16x32_bf16 %[q16], a[236:239], v[176:179], %[q16]\n"
    "ds_read_b64_tr_b16 a[228:229], v222 offset:256\n"
    "v_mfma_f32_16x16x32_bf16 %[q17], a[236:239], v[180:183], %[q17]\n"
    "ds_read_b64_tr_b16 a[230:231], v222 offset:2304\n"
    "s_waitcnt lgkmcnt(8)\n"
    "v_mfma_f32_16x16x32_bf16 %[q18], a[240:243], v[176:179], %[q18]\n"
    "ds_read_b64_tr_b16 a[232:233], v223 offset:256\n"
    "v_mfma_f32_16x16x32_bf16 %[q19], a[240:243], v[180:183], %[q19]\n"
    "ds_read_b64_tr_b16 a[234:235], v223 offset:2304\n"
    "v_mfma_f32_16x16x32_bf16 %[q20], a[244:247], v[176:179], %[q20]\n"
    "ds_read_b64_tr_b16 a[236:237], v224 offset:8192\n"
    "v_mfma_f32_16x16x32_bf16 %[q21], a[244:247], v[180:183], %[q21]\n"
    "ds_read_b64_tr_b16 a[238:239], v224 offset:9216\n"
    "s_waitcnt lgkmcnt(8)\n"
    "v_mfma_f32_16x16x32_bf16 %[q22], a[248:251], v[176:179], %[q22]\n"
    "ds_read_b64_tr_b16 a[240:241], v225 offset:8192\n"
    "v_mfma_f32_16x16x32_bf16 %[q23], a[248:251], v[180:183], %[q23]\n"
    "ds_read_b64_tr_b16 a[242:243], v225 offset:9216\n"
    "v_mfma_f32_16x16x32_bf16 %[q24], a[252:255], v[176:179], %[q24]\n"
    "ds_read_b64_tr_b16 a[244:245], v226 offset:8192\n"
    "v_mfma_f32_16x16x32_bf16 %[q25], a[252:255], v[180:183], %[q25]\n"
    "ds_read_b64_tr_b16 a[246:247], v226 offset:9216\n"
    "s_waitcnt lgkmcnt(8)\n"
    "v_mfma_f32_16x16x32_bf16 %[q26], a[224:227], v[176:179], %[q26]\n"
    "ds_read_b64_tr_b16 a[248:249], v227 offset:8192\n"
    "v_mfma_f32_16x16x32_bf16 %[q27], a[224:227], v[180:183], %[q27]\n"
    "ds_read_b64_tr_b16 a[250:251], v227 offset:9216\n"
    "v_mfma_f32_16x16x32_bf16 %[q28], a[228:231], v[176:179], %[q28]\n"
    "ds_read_b64_tr_b16 a[252:253], v228 offset:8192\n"
    "v_mfma_f32_16x16x32_bf16 %[q29], a[228:231], v[180:183], %[q29]\n"
    "ds_read_b64_tr_b16 a[254:255], v228 offset:9216\n"
    "s_waitcnt lgkmcnt(8)\n"
    "v_mfma_f32_16x16x32_bf16 %[q30], a[232:235], v[176:179], %[q30]\n"
    "ds_read_b64_tr_b16 a[224:225], v229 offset:8192\n"
    "v_mfma_f32_16x16x32_bf16 %[q31], a[232:235], v[180:183], %[q31]\n"
    "ds_read_b64_tr_b16 a[226:227], v229 offset:9216\n"
    "v_mfma_f32_16x16x32_bf16 %[z0], a[236:239], v[176:179], %[z0]\n"
    "ds_read_b64_tr_b16 a[228:229], v230 offset:8192\n"
    "v_mfma_f32_16x16x32_bf16 %[z1], a[236:239], v[180:183], %[z1]\n"
    "ds_read_b64_tr_b16 a[230:231], v230 offset:9216\n"
    "s_waitcnt lgkmcnt(8)\n"
    "v_mfma_f32_16x16x32_bf16 %[z2], a[240:243], v[176:179], %[z2]\n"
    "v_mfma_f32_16x16x32_bf16 %[z3], a[240:243], v[180:183], %[z3]\n"
    "v_mfma_f32_16x16x32_bf16 %[z4], a[244:247], v[176:179], %[z4]\n"
    "v_mfma_f32_16x16x32_bf16 %[z5], a[244:247], v[180:183], %[z5]\n"
    "s_waitcnt lgkmcnt(4)\n"
    "v_mfma_f32_16x16x32_bf16 %[z6], a[248:251], v[176:179], %[z6]\n"
    "v_mfma_f32_16x16x32_bf16 %[z7], a[248:251], v[180:183], %[z7]\n"
    "v_mfma_f32_16x16x32_bf16 %[z8], a[252:255], v[176:179], %[z8]\n"
    "v_mfma_f32_16x16x32_bf16 %[z9], a[252:255], v[180:183], %[z9]\n"
    "s_waitcnt lgkmcnt(0)\n"
    "v_mfma_f32_16x16x32_bf16 %[z10], a[224:227], v[176:179], %[z10]\n"
    "v_mfma_f32_16x16x32_bf16 %[z11], a[224:227], v[180:183], %[z11]\n"
    "v_mfma_f32_16x16x32_bf16 %[z12], a[228:231], v[176:179], %[z12]\n"
    "v_mfma_f32_16x16x32_bf16 %[z13], a[228:231], v[180:183], %[z13]\n"
    "s_waitcnt lgkmcnt(0)\n"
    "s_barrier\n"
    "s_add_u32 m0, s50, 114688\n"
    "s_mov_b32 s48, 16384\n"
    "buffer_load_dwordx4 %[vov0], s[44:47], s48 offen offset:0 nt lds\n"
    "buffer_load_dwordx4 %[vov1], s[44:47], s48 offen offset:1024 nt lds\n"
    "buffer_load_dwordx4 %[vov2], s[44:47], s48 offen offset:2048 nt lds\n"
    "buffer_load_dwordx4 %[vov3], s[44:47], s48 offen offset:3072 nt lds\n"
    "v_mbcnt_lo_u32_b32 v184, -1, 0\n"
    "v_mbcnt_hi_u32_b32 v184, -1, v184\n"
    "v_lshrrev_b32 v185, 4, v184\n"
    "v_and_b32 v186, 15, v184\n"
    "v_lshlrev_b32 v185, 2, v185\n"
    "v_sub_u32 v185, v186, v185\n"
    "v_mov_b32 v188, 0\n"
    "v_cmp_eq_u32 vcc, 0, v185\n"
    "s_nop 1\n"
    "v_cndmask_b32 v188, v188, v248, vcc\n"
    "v_cmp_eq_u32 vcc, 1, v185\n"
    "s_nop 1\n"
    "v_cndmask_b32 v188, v188, v249, vcc\n"
    "v_cmp_eq_u32 vcc, 2, v185\n"
    "s_nop 1\n"
    "v_cndmask_b32 v188, v188, v250, vcc\n"
    "v_cmp_eq_u32 vcc, 3, v185\n"
    "s_nop 1\n"
    "v_cndmask_b32 v188, v188, v251, vcc\n"
    "v_mov_b32 v189, v188\n"
    "s_nop 1\n"
    "v_permlane32_swap_b32 v188, v189\n"
    "s_nop 1\n"
    "v_add_f32 v188, v188, v189\n"
    "v_mov_b32 v189, v188\n"
    "s_nop 1\n"
    "v_permlane16_swap_b32 v188, v189\n"
    "s_nop 1\n"
    "v_add_f32 v188, v188, v189\n"
    "v_mov_b32 v189, 0x3727c5ac\n"
    "v_fmac_f32 v189, 0x3b124925, v188\n"
    "v_rsq_f32 %[rs0], v189\n"
    "v_mov_b32 v188, 0\n"
    "v_cmp_eq_u32 vcc, 0, v185\n"
    "s_nop 1\n"
    "v_cndmask_b32 v188, v188, v252, vcc\n"
    "v_cmp_eq_u32 vcc, 1, v185\n"
    "s_nop 1\n"
    "v_cndmask_b32 v188, v188, v253, vcc\n"
    "v_cmp_eq_u32 vcc, 2, v185\n"
    "s_nop 1\n"
    "v_cndmask_b32 v188, v188, v254, vcc\n"
    "v_cmp_eq_u32 vcc, 3, v185\n"
    "s_nop 1\n"
    "v_cndmask_b32 v188, v188, v255, vcc\n"
    "v_mov_b32 v189, v188\n"
    "s_nop 1\n"
    "v_permlane32_swap_b32 v188, v189\n"
    "s_nop 1\n"
    "v_add_f32 v188, v188, v189\n"
    "v_mov_b32 v189, v188\n"
    "s_nop 1\n"
    "v_permlane16_swap_b32 v188, v189\n"
    "s_nop 1\n"
    "v_add_f32 v188, v188, v189\n"
    "v_mov_b32 v189, 0x3727c5ac\n"
    "v_fmac_f32 v189, 0x3b124925, v188\n"
    "v_rsq_f32 %[rs1], v189\n"
    "v_accvgpr_read_b32 v184, a220\n"
    "v_accvgpr_read_b32 v185, a221\n"
    "s_nop 1\n"
    "v_readfirstlane_b32 s80, v184\n"
    "v_readfirstlane_b32 s81, v185\n"
    "s_nop 3\n"
    "s_add_u32 %[tlo], s80, 1\n"
    "s_addc_u32 %[thi], s81, 0\n"
    "s_nop 7\n"
    : [q0]"=&v"(qa[0]), [q1]"=&v"(qa[1]), [q2]"=&v"(qa[2]), [q3]"=&v"(qa[3]), [q4]"=&v"(qa[4]), [q5]"=&v"(qa[5]), [q6]"=&v"(qa[6]), [q7]"=&v"(qa[7]), [q8]"=&v"(qa[8]), [q9]"=&v"(qa[9]), [q10]"=&v"(qa[10]), [q11]"=&v"(qa[11]), [q12]"=&v"(qa[12]), [q13]"=&v"(qa[13]), [q14]"=&v"(qa[14]), [q15]"=&v"(qa[15]), [q16]"=&v"(qa[16]), [q17]"=&v"(qa[17]), [q18]"=&v"(qa[18]), [q19]"=&v"(qa[19]), [q20]"=&v"(qa[20]), [q21]"=&v"(qa[21]), [q22]"=&v"(qa[22]), [q23]"=&v"(qa[23]), [q24]"=&v"(qa[24]), [q25]"=&v"(qa[25]), [q26]"=&v"(qa[26]), [q27]"=&v"(qa[27]), [q28]"=&v"(qa[28]), [q29]"=&v"(qa[29]), [q30]"=&v"(qa[30]), [q31]"=&v"(qa[31]), [z0]"=&a"(za[0]), [z1]"=&a"(za[1]), [z2]"=&a"(za[2]), [z3]"=&a"(za[3]), [z4]"=&a"(za[4]), [z5]"=&a"(za[5]), [z6]"=&a"(za[6]), [z7]"=&a"(za[7]), [z8]"=&a"(za[8]), [z9]"=&a"(za[9]), [z10]"=&a"(za[10]), [z11]"=&a"(za[11]), [z12]"=&a"(za[12]), [z13]"=&a"(za[13]), [tlo]"=&s"(tlo), [thi]"=&s"(thi), [rs0]"=&v"(rsd[0]), [rs1]"=&v"(rsd[1])
    : [vwi0]"v"(vwi[0]), [vwi1]"v"(vwi[1]), [vwi2]"v"(vwi[2]), [vwi3]"v"(vwi[3]), [vwq0]"v"(vwq[0]), [vwq1]"v"(vwq[1]), [vwq2]"v"(vwq[2]), [vwq3]"v"(vwq[3]),
      [vok0]"v"(vok[0]), [vok1]"v"(vok[1]), [vok2]"v"(vok[2]), [vok3]"v"(vok[3]), [vov0]"v"(vov[0]), [vov1]"v"(vov[1]), [vov2]"v"(vov[2]), [vov3]"v"(vov[3]),
      [vx0]"v"(vx[0]), [vx1]"v"(vx[1]), [vx2]"v"(vx[2]), [vx3]"v"(vx[3]), [vrs]"v"(vrs), [vrd0]"v"(vrd0), [vrd1]"v"(vrd1),
      [vbi]"v"(vbi), [vbq]"v"(vbq), [vxr]"v"(vxr), [vl16]"v"(vl16), [cpi]"s"(cpi), [m0b]"s"(m0b), [rot]"s"(rot), [rw]"s"(rw), [rt]"s"(rt),
      [xlo]"s"(__builtin_amdgcn_readfirstlane(PLO(xp))), [xhi]"s"(__builtin_amdgcn_readfirstlane(PHI(xp))),
      [wolo]"s"(PLO(wo)), [wohi]"s"(PHI(wo)), [wtlo]"s"(PLO(w_out)), [wthi]"s"(PHI(w_out)), [wslo]"s"(PLO(ws)), [wshi]"s"(PHI(ws)),
      [wilo]"s"(PLO(w_in)), [wihi]"s"(PHI(w_in)), [wqlo]"s"(PLO(wq)), [wqhi]"s"(PHI(wq)),
      [klo]"s"(PLO(kp)), [khi]"s"(PHI(kp)), [vlo]"s"(PLO(vp)), [vhi]"s"(PHI(vp))
    : "v168","v169","v170","v171","v172","v173","v174","v175","v176","v177","v178","v179","v180","v181","v182","v183","v184","v185","v186","v187","v188","v189","v190","v191","v192","v193","v194","v195","v196","v197","v198","v199","v200","v201","v202","v203","v204","v205","v206","v207","v208","v209","v210","v211","v212","v213","v214","v215","v216","v217","v218","v219","v220","v221","v222","v223","v224","v225","v226","v227","v228","v229","v230","v231","v232","v233","v234","v235","v236","v237","v238","v239","v240","v241","v242","v243","v244","v245","v246","v247","v248","v249","v250","v251","v252","v253","v254","v255","a0","a1","a2","a3","a4","a5","a6","a7","a8","a9","a10","a11","a12","a13","a14","a15","a16","a17","a18","a19","a20","a21","a22","a23","a24","a25","a26","a27","a28","a29","a30","a31","a32","a33","a34","a35","a36","a37","a38","a39","a40","a41","a42","a43","a44","a45","a46","a47","a48","a49","a50","a51","a52","a53","a54","a55","a56","a57","a58","a59","a60","a61","a62","a63","a64","a65","a66","a67","a68","a69","a70","a71","a72","a73","a74","a75","a76","a77","a78","a79","a80","a81","a82","a83","a84","a85","a86","a87","a88","a89","a90","a91","a92","a93","a94","a95","a96","a97","a98","a99","a100","a101","a102","a103","a104","a105","a106","a107","a108","a109","a110","a111","a200","a201","a202","a203","a204","a205","a206","a207","a208","a209","a210","a211","a212","a213","a214","a215","a216","a217","a218","a219","a220","a221","a222","a223","a224","a225","a226","a227","a228","a229","a230","a231","a232","a233","a234","a235","a236","a237","a238","a239","a240","a241","a242","a243","a244","a245","a246","a247","a248","a249","a250","a251","a252","a253","a254","a255","s37","s38","s39","s40","s41","s42","s43","s44","s45","s46","s47","s48","s49","s50","s51","s52","s53","s54","s55","s56","s57","s58","s59","s60","s61","s62","s63","s64","s65","s66","s67","s68","s69","s70","s71","s72","s73","s74","s75","s76","s77","s78","s79","s80","s81","s82","s83","vcc","scc","memory");
  STAMP(1);
  u32x2 zp[14];
#pragma unroll
  for (int i = 0; i < 14; ++i) {
    float t[4];
#pragma unroll
    for (int e = 0; e < 4; ++e) t[e] = gelu_tanh(za[i][e] * rsd[i & 1]);
    zp[i][0] = cvtpk(t[0], t[1]); zp[i][1] = cvtpk(t[2], t[3]);
  }
  if (zs < 8u) {
    u16* z1 = (u16*)(ws + WS_Z1);
#pragma unroll
    for (int f = 0; f < 7; ++f)
#pragma unroll
      for (int qs = 0; qs < 2; ++qs)
        st8((char*)z1 + ((size_t)(qb * 8u + 2u * w + qs) * 56u + 7u * zs + f) * 512u + lane * 8u, zp[2 * f + qs]);
  } else {
#pragma unroll
    for (int qs = 0; qs < 2; ++qs) {
      float s = 0.f;
#pragma unroll
      for (int f = 0; f < 7; ++f) s += (lo16(zp[2 * f + qs][0]) + hi16(zp[2 * f + qs][0])) + (lo16(zp[2 * f + qs][1]) + hi16(zp[2 * f + qs][1]));
      s = xsum4(s);
      const float ml = s * (1.f / 112.f);
      float q = 0.f;
#pragma unroll
      for (int f = 0; f < 7; ++f) {
        float d0 = lo16(zp[2 * f + qs][0]) - ml, d1 = hi16(zp[2 * f + qs][0]) - ml, d2 = lo16(zp[2 * f + qs][1]) - ml, d3 = hi16(zp[2 * f + qs][1]) - ml;
        q += (d0 * d0 + d1 * d1) + (d2 * d2 + d3 * d3);
      }
      q = xsum4(q);
      if (g == 0u) {
        const u32x4 gr = {__float_as_uint(ml), __float_as_uint(q), tlo, thi};
        asm volatile("global_store_dwordx4 %0, %1, off sc1\n\ts_nop 1" :: "v"(ws + WS_STATS + ((size_t)(row0 + 16u * qs) * 8u + (zs - 8u)) * 16u), "v"(gr) : "memory");
      }
    }
  }
  STAMP(2);
  u32x4 qf[16];
  {
#pragma unroll
    for (int s = 0; s < 8; ++s)
#pragma unroll
      for (int qs = 0; qs < 2; ++qs) {
        const float c = 0.09016844005556021f * rsd[qs];
        const f32x4 a = qa[2 * (2 * s) + qs], bb = qa[2 * (2 * s + 1) + qs];
        unsigned x0 = cvtpk(a[0] * c, a[1] * c), x1 = cvtpk(a[2] * c, a[3] * c), y0 = cvtpk(bb[0] * c, bb[1] * c), y1 = cvtpk(bb[2] * c, bb[3] * c);
        auto s0 = __builtin_amdgcn_permlane32_swap(x0, y0, false, false); auto s1 = __builtin_amdgcn_permlane32_swap(x1, y1, false, false);
        auto t0 = __builtin_amdgcn_permlane16_swap(s0[0], s0[1], false, false); auto t1 = __builtin_amdgcn_permlane16_swap(s1[0], s1[1], false, false);
        qf[2 * s + qs] = (u32x4){t0[0], t1[0], t0[1], t1[1]};
      }
  }
  STAMP(4);
  if (zs >= 8u) {
    u16* z2n = (u16*)(ws + WS_Z2N);
    const char* sp = ws + WS_STATS + ((size_t)row0 * 8u + 2u * g) * 16u;
    u32x4 sv[4];
    for (unsigned spins = 0; spins < (1u << 21); ++spins) {
      asm volatile("global_load_dwordx4 %0, %4, off sc1\n\tglobal_load_dwordx4 %1, %4, off offset:16 sc1\n\t"
                   "global_load_dwordx4 %2, %4, off offset:2048 sc1\n\tglobal_load_dwordx4 %3, %4, off offset:2064 sc1\n\ts_waitcnt vmcnt(0)"
                   : "=&v"(sv[0]), "=&v"(sv[1]), "=&v"(sv[2]), "=&v"(sv[3]) : "v"(sp) : "memory");
      bool ok = true;
#pragma unroll
      for (int i = 0; i < 4; ++i) ok &= (sv[i][2] == tlo) & (sv[i][3] == thi);
      if (__all(ok)) break;
      __builtin_amdgcn_s_sleep(1);
    }
#pragma unroll
    for (int qs = 0; qs < 2; ++qs) {
      const float m0 = __uint_as_float(sv[2 * qs][0]), q0 = __uint_as_float(sv[2 * qs][1]), m1 = __uint_as_float(sv[2 * qs + 1][0]), q1 = __uint_as_float(sv[2 * qs + 1][1]);
      const float mean = xsum4(m0 + m1) * 0.125f;
      const float dm0 = m0 - mean, dm1 = m1 - mean;
      const float M2 = xsum4(q0 + q1 + 112.f * (dm0 * dm0 + dm1 * dm1));
      const float rs = rsqrtf(M2 * (1.f / 896.f) + 1e-5f);
#pragma unroll
      for (int f = 0; f < 7; ++f) {
        u32x2 o;
        o[0] = cvtpk((lo16(zp[2 * f + qs][0]) - mean) * rs, (hi16(zp[2 * f + qs][0]) - mean) * rs);
        o[1] = cvtpk((lo16(zp[2 * f + qs][1]) - mean) * rs, (hi16(zp[2 * f + qs][1]) - mean) * rs);
        st8(z2n + ((size_t)(zs - 8u) * 2048u + row0 + 16u * qs) * 112u + 16u * f + 4u * g, o);
      }
    }
  }
  STAMP(5);
  const unsigned vk0 = ldsb + 32768u + r16 * 512u + ((g ^ r16) << 4);
  const unsigned vrow = 4u * g + q4;
  const unsigned vv0 = ldsb + 98304u + vrow * 512u + ((2u * (vrow & 7u) + (p4 >> 1)) << 4) + 8u * (p4 & 1u);
  const unsigned kh = xg & 1u, tp = 4u * (xg >> 1) + (idx >> 3), fs = idx & 7u;
  const unsigned kq = xg & 3u, tp2 = 8u * (xg >> 2) + (idx >> 2), nq = idx & 3u;
  const unsigned n0 = 4u+4u*(idx>>4)+(xg&1u);
  const unsigned rotk = (idx * 5u + xg * 3u) & 15u;
  const unsigned rot2 = (idx * 5u + xg * 3u) & 15u;
  asm volatile(
    "s_mov_b32 s40, %[klo]\n"
    "s_and_b32 s41, %[khi], 0xffff\n"
    "s_mov_b32 s42, 1048576\n"
    "s_mov_b32 s43, 0x20000\n"
    "s_mov_b32 s44, %[vlo]\n"
    "s_and_b32 s45, %[vhi], 0xffff\n"
    "s_mov_b32 s46, 1048576\n"
    "s_mov_b32 s47, 0x20000\n"
    "s_mov_b32 s50, %[m0b]\n"
    "s_mov_b32 s79, 1090519040\n"
    "s_mov_b32 s73, 0\n"
    "s_mov_b32 s74, 0\n"
    "s_lshl_b32 s39, %[n0], 16\n"
    "s_add_u32 s39, s39, 98304\n"
    "v_xor_b32 v48, 0, %[vk0]\n"
    "v_xor_b32 v49, 64, %[vk0]\n"
    "v_xor_b32 v50, 128, %[vk0]\n"
    "v_xor_b32 v51, 192, %[vk0]\n"
    "v_xor_b32 v52, 0, %[vv0]\n"
    "v_xor_b32 v53, 32, %[vv0]\n"
    "v_xor_b32 v54, 64, %[vv0]\n"
    "v_xor_b32 v55, 96, %[vv0]\n"
    "v_xor_b32 v56, 128, %[vv0]\n"
    "v_xor_b32 v57, 160, %[vv0]\n"
    "v_xor_b32 v58, 192, %[vv0]\n"
    "v_xor_b32 v59, 224, %[vv0]\n"
    "v_add_u32 v60, 0x8000, %[vok0]\n"
    "v_add_u32 v61, 0x8000, %[vok1]\n"
    "v_add_u32 v62, 0x8000, %[vok2]\n"
    "v_add_u32 v63, 0x8000, %[vok3]\n"
    "v_mov_b32 v112, 0\n"
    "v_mov_b32 v113, 0\n"
    "v_mov_b32 v114, 0\n"
    "v_mov_b32 v115, 0\n"
    "s_nop 1\n"
    "v_mfma_f32_32x32x16_bf16 v[128:143], v[112:115], v[112:115], 0\n"
    "v_mfma_f32_32x32x16_bf16 v[144:159], v[112:115], v[112:115], 0\n"
    "v_mfma_f32_32x32x16_bf16 v[160:175], v[112:115], v[112:115], 0\n"
    "v_mfma_f32_32x32x16_bf16 v[176:191], v[112:115], v[112:115], 0\n"
    "v_mfma_f32_32x32x16_bf16 v[192:207], v[112:115], v[112:115], 0\n"
    "v_mfma_f32_32x32x16_bf16 v[208:223], v[112:115], v[112:115], 0\n"
    "v_mfma_f32_32x32x16_bf16 v[224:239], v[112:115], v[112:115], 0\n"
    "v_mfma_f32_32x32x16_bf16 v[240:255], v[112:115], v[112:115], 0\n"
    "v_mov_b32 v26, 0\n"
    "v_mov_b32 v27, 0\n"
    "v_mov_b32 v28, 0\n"
    "v_mov_b32 v29, 0\n"
    "s_add_u32 s80, %[wslo], 0\n"
    "s_addc_u32 s81, %[wshi], 0\n"
    "s_and_b32 s81, s81, 0xffff\n"
    "s_mov_b32 s82, 49152\n"
    "s_mov_b32 s83, 0x20000\n"
    "v_mbcnt_lo_u32_b32 v30, -1, 0\n"
    "v_mbcnt_hi_u32_b32 v30, -1, v30\n"
    "v_and_b32 v31, 15, v30\n"
    "v_lshrrev_b32 v32, 4, v30\n"
    "v_lshlrev_b32 v33, 4, v30\n"
    "v_lshlrev_b32 v22, 3, v30\n"
    "v_mov_b32 v20, %[tlo]\n"
    "v_mov_b32 v21, %[thi]\n"
    "s_lshl_b32 s52, %[w], 4\n"
    "v_mov_b32 v122, 0x40000000\n"
    "v_or_b32 v120, 0, v32\n"
    "v_lshlrev_b32 v120, 1, v120\n"
    "v_xor_b32 v120, v120, v31\n"
    "v_cmp_gt_u32 vcc, 14, v120\n"
    "v_add_u32 v121, s52, v32\n"
    "v_mul_u32_u24 v121, 224, v121\n"
    "v_lshl_add_u32 v121, v120, 4, v121\n"
    "v_add_u32 v121, 4096, v121\n"
    "v_cndmask_b32 v34, v122, v121, vcc\n"
    "v_or_b32 v120, 0, v32\n"
    "v_lshlrev_b32 v120, 1, v120\n"
    "v_xor_b32 v120, v120, v31\n"
    "v_cmp_gt_u32 vcc, 14, v120\n"
    "v_add_u32 v121, s52, v32\n"
    "v_mul_u32_u24 v121, 224, v121\n"
    "v_lshl_add_u32 v121, v120, 4, v121\n"
    "v_add_u32 v121, 3968, v121\n"
    "v_cndmask_b32 v35, v122, v121, vcc\n"
    "v_or_b32 v120, 4, v32\n"
    "v_lshlrev_b32 v120, 1, v120\n"
    "v_xor_b32 v120, v120, v31\n"
    "v_cmp_gt_u32 vcc, 14, v120\n"
    "v_add_u32 v121, s52, v32\n"
    "v_mul_u32_u24 v121, 224, v121\n"
    "v_lshl_add_u32 v121, v120, 4, v121\n"
    "v_add_u32 v121, 3840, v121\n"
    "v_cndmask_b32 v36, v122, v121, vcc\n"
    "v_or_b32 v120, 4, v32\n"
    "v_lshlrev_b32 v120, 1, v120\n"
    "v_xor_b32 v120, v120, v31\n"
    "v_cmp_gt_u32 vcc, 14, v120\n"
    "v_add_u32 v121, s52, v32\n"
    "v_mul_u32_u24 v121, 224, v121\n"
    "v_lshl_add_u32 v121, v120, 4, v121\n"
    "v_add_u32 v121, 3712, v121\n"
    "v_cndmask_b32 v37, v122, v121, vcc\n"
    "s_lshl_b32 s52, %[w], 4\n"
    "v_mov_b32 v122, 0x40000000\n"
    "v_or_b32 v120, 0, v32\n"
    "v_lshlrev_b32 v120, 1, v120\n"
    "v_xor_b32 v120, v120, v31\n"
    "v_cmp_gt_u32 vcc, 14, v120\n"
    "v_add_u32 v121, s52, v32\n"
    "v_mul_u32_u24 v121, 224, v121\n"
    "v_lshl_add_u32 v121, v120, 4, v121\n"
    "v_add_u32 v121, 4096, v121\n"
    "v_cndmask_b32 v42, v122, v121, vcc\n"
    "v_or_b32 v120, 4, v32\n"
    "v_lshlrev_b32 v120, 1, v120\n"
    "v_xor_b32 v120, v120, v31\n"
    "v_cmp_gt_u32 vcc, 14, v120\n"
    "v_add_u32 v121, s52, v32\n"
    "v_mul_u32_u24 v121, 224, v121\n"
    "v_lshl_add_u32 v121, v120, 4, v121\n"
    "v_add_u32 v121, 3968, v121\n"
    "v_cndmask_b32 v43, v122, v121, vcc\n"
    "v_or_b32 v120, 0, v32\n"
    "v_lshlrev_b32 v120, 1, v120\n"
    "v_xor_b32 v120, v120, v31\n"
    "v_cmp_gt_u32 vcc, 14, v120\n"
    "v_add_u32 v121, s52, v32\n"
    "v_mul_u32_u24 v121, 224, v121\n"
    "v_lshl_add_u32 v121, v120, 4, v121\n"
    "v_add_u32 v121, 3840, v121\n"
    "v_cndmask_b32 v44, v122, v121, vcc\n"
    "v_or_b32 v120, 4, v32\n"
    "v_lshlrev_b32 v120, 1, v120\n"
    "v_xor_b32 v120, v120, v31\n"
    "v_cmp_gt_u32 vcc, 14, v120\n"
    "v_add_u32 v121, s52, v32\n"
    "v_mul_u32_u24 v121, 224, v121\n"
    "v_lshl_add_u32 v121, v120, 4, v121\n"
    "v_add_u32 v121, 3712, v121\n"
    "v_cndmask_b32 v45, v122, v121, vcc\n"
    "s_lshl_b32 s52, %[w], 5\n"
    "v_lshrrev_b32 v120, 3, v30\n"
    "v_add_u32 v120, s52, v120\n"
    "v_lshlrev_b32 v120, 12, v120\n"
    "v_and_b32 v121, 7, v30\n"
    "v_xor_b32 v121, v121, v32\n"
    "v_lshl_add_u32 v38, v121, 4, v120\n"
    "v_xor_b32 v39, 64, v38\n"
    "v_add_u32 v39, 31744, v39\n"
    "v_add_u32 v40, 63488, v38\n"
    "v_add_u32 v41, 63488, v39\n"
    "s_mul_i32 s52, %[kh], 229376\n"
    "s_mul_i32 s53, %[fs], 458752\n"
    "s_add_u32 s52, s52, s53\n"
    "s_add_u32 s52, s52, 29356032\n"
    "s_add_u32 s56, %[wslo], s52\n"
    "s_addc_u32 s57, %[wshi], 0\n"
    "s_and_b32 s57, s57, 0xffff\n"
    "s_mov_b32 s58, 233472\n"
    "s_mov_b32 s59, 0x20000\n"
    "s_lshl_b32 s52, %[tp], 19\n"
    "s_lshl_b32 s53, %[kh], 11\n"
    "s_add_u32 s52, s52, s53\n"
    "s_add_u32 s60, %[splo], s52\n"
    "s_addc_u32 s61, %[sphi], 0\n"
    "s_and_b32 s61, s61, 0xffff\n"
    "s_mov_b32 s62, 522240\n"
    "s_mov_b32 s63, 0x20000\n"
    "s_waitcnt vmcnt(14) lgkmcnt(0)\n"
    "s_barrier\n"
    "s_mov_b32 s48, 32768\n"
    "s_add_u32 m0, s50, 131072\n"
    "s_nop 0\n"
    "buffer_load_dwordx4 %[vov0], s[44:47], s48 offen offset:0 nt lds\n"
    "buffer_load_dwordx4 %[vov1], s[44:47], s48 offen offset:1024 nt lds\n"
    "buffer_load_dwordx4 %[vov2], s[44:47], s48 offen offset:2048 nt lds\n"
    "buffer_load_dwordx4 %[vov3], s[44:47], s48 offen offset:3072 nt lds\n"
    "ds_read_b128 a[192:195], v48 offset:0\n"
    "ds_read_b128 a[196:199], v49 offset:0\n"
    "ds_read_b128 a[200:203], v50 offset:0\n"
    "ds_read_b128 a[204:207], v51 offset:0\n"
    "ds_read_b128 a[208:211], v48 offset:256\n"
    "ds_read_b128 a[212:215], v49 offset:256\n"
    "ds_read_b128 a[216:219], v50 offset:256\n"
    "ds_read_b128 a[220:223], v51 offset:256\n"
    "ds_read_b128 a[224:227], v48 offset:8192\n"
    "ds_read_b128 a[228:231], v49 offset:8192\n"
    "ds_read_b128 a[232:235], v50 offset:8192\n"
    "ds_read_b128 a[236:239], v51 offset:8192\n"
    "ds_read_b128 a[240:243], v48 offset:8448\n"
    "ds_read_b128 a[244:247], v49 offset:8448\n"
    "ds_read_b128 a[248:251], v50 offset:8448\n"
    "ds_read_b128 a[252:255], v51 offset:8448\n"
    "s_waitcnt lgkmcnt(15)\n"
    "v_mfma_f32_16x16x32_bf16 v[64:67], a[192:195], %[q0], 0\n"
    "v_mfma_f32_16x16x32_bf16 v[68:71], a[192:195], %[q1], 0\n"
    "s_waitcnt lgkmcnt(14)\n"
    "v_mfma_f32_16x16x32_bf16 v[64:67], a[196:199], %[q2], v[64:67]\n"
    "v_mfma_f32_16x16x32_bf16 v[68:71], a[196:199], %[q3], v[68:71]\n"
    "s_waitcnt lgkmcnt(13)\n"
    "v_mfma_f32_16x16x32_bf16 v[64:67], a[200:203], %[q4], v[64:67]\n"
    "v_mfma_f32_16x16x32_bf16 v[68:71], a[200:203], %[q5], v[68:71]\n"
    "s_waitcnt lgkmcnt(12)\n"
    "v_mfma_f32_16x16x32_bf16 v[64:67], a[204:207], %[q6], v[64:67]\n"
    "v_mfma_f32_16x16x32_bf16 v[68:71], a[204:207], %[q7], v[68:71]\n"
    "s_waitcnt lgkmcnt(11)\n"
    "v_mfma_f32_16x16x32_bf16 v[64:67], a[208:211], %[q8], v[64:67]\n"
    "v_mfma_f32_16x16x32_bf16 v[68:71], a[208:211], %[q9], v[68:71]\n"
    "s_waitcnt lgkmcnt(10)\n"
    "v_mfma_f32_16x16x32_bf16 v[64:67], a[212:215], %[q10], v[64:67]\n"
    "v_mfma_f32_16x16x32_bf16 v[68:71], a[212:215], %[q11], v[68:71]\n"
    "s_waitcnt lgkmcnt(9)\n"
    "v_mfma_f32_16x16x32_bf16 v[64:67], a[216:219], %[q12], v[64:67]\n"
    "v_mfma_f32_16x16x32_bf16 v[68:71], a[216:219], %[q13], v[68:71]\n"
    "s_waitcnt lgkmcnt(8)\n"
    "v_mfma_f32_16x16x32_bf16 v[64:67], a[220:223], %[q14], v[64:67]\n"
    "v_mfma_f32_16x16x32_bf16 v[68:71], a[220:223], %[q15], v[68:71]\n"
    "s_waitcnt lgkmcnt(7)\n"
    "v_mfma_f32_16x16x32_bf16 v[72:75], a[224:227], %[q0], 0\n"
    "v_mfma_f32_16x16x32_bf16 v[76:79], a[224:227], %[q1], 0\n"
    "s_waitcnt lgkmcnt(6)\n"
    "v_mfma_f32_16x16x32_bf16 v[72:75], a[228:231], %[q2], v[72:75]\n"
    "v_mfma_f32_16x16x32_bf16 v[76:79], a[228:231], %[q3], v[76:79]\n"
    "s_waitcnt lgkmcnt(5)\n"
    "v_mfma_f32_16x16x32_bf16 v[72:75], a[232:235], %[q4], v[72:75]\n"
    "v_mfma_f32_16x16x32_bf16 v[76:79], a[232:235], %[q5], v[76:79]\n"
    "s_waitcnt lgkmcnt(4)\n"
    "v_mfma_f32_16x16x32_bf16 v[72:75], a[236:239], %[q6], v[72:75]\n"
    "v_mfma_f32_16x16x32_bf16 v[76:79], a[236:239], %[q7], v[76:79]\n"
    "s_waitcnt lgkmcnt(3)\n"
    "v_mfma_f32_16x16x32_bf16 v[72:75], a[240:243], %[q8], v[72:75]\n"
    "v_mfma_f32_16x16x32_bf16 v[76:79], a[240:243], %[q9], v[76:79]\n"
    "s_waitcnt lgkmcnt(2)\n"
    "v_mfma_f32_16x16x32_bf16 v[72:75], a[244:247], %[q10], v[72:75]\n"
    "v_mfma_f32_16x16x32_bf16 v[76:79], a[244:247], %[q11], v[76:79]\n"
    "s_waitcnt lgkmcnt(1)\n"
    "v_mfma_f32_16x16x32_bf16 v[72:75], a[248:251], %[q12], v[72:75]\n"
    "v_mfma_f32_16x16x32_bf16 v[76:79], a[248:251], %[q13], v[76:79]\n"
    "s_waitcnt lgkmcnt(0)\n"
    "v_mfma_f32_16x16x32_bf16 v[72:75], a[252:255], %[q14], v[72:75]\n"
    "v_mfma_f32_16x16x32_bf16 v[76:79], a[252:255], %[q15], v[76:79]\n"
    "s_barrier\n"
    "s_add_u32 m0, s50, 32768\n"
    "s_nop 0\n"
    "buffer_load_dwordx4 v60, s[40:43], s48 offen offset:0 nt lds\n"
    "buffer_load_dwordx4 v61, s[40:43], s48 offen offset:1024 nt lds\n"
    "buffer_load_dwordx4 v62, s[40:43], s48 offen offset:2048 nt lds\n"
    "buffer_load_dwordx4 v63, s[40:43], s48 offen offset:3072 nt lds\n"
    "ds_read_b128 a[192:195], v48 offset:16384\n"
    "ds_read_b128 a[196:199], v49 offset:16384\n"
    "ds_read_b128 a[200:203], v50 offset:16384\n"
    "ds_read_b128 a[204:207], v51 offset:16384\n"
    "ds_read_b128 a[208:211], v48 offset:16640\n"
    "ds_read_b128 a[212:215], v49 offset:16640\n"
    "ds_read_b128 a[216:219], v50 offset:16640\n"
    "ds_read_b128 a[220:223], v51 offset:16640\n"
    "ds_read_b128 a[224:227], v48 offset:24576\n"
    "ds_read_b128 a[228:231], v49 offset:24576\n"
    "ds_read_b128 a[232:235], v50 offset:24576\n"
    "ds_read_b128 a[236:239], v51 offset:24576\n"
    "s_nop 7\n"
    "v_max3_f32 v122, v64, v65, v66\n"
    "v_max3_f32 v122, v122, v67, v72\n"
    "v_max3_f32 v122, v122, v73, v74\n"
    "v_max_f32 v122, v122, v75\n"
    "v_mov_b32 v120, v122\n"
    "s_nop 1\n"
    "v_permlane32_swap_b32 v122, v120\n"
    "s_nop 1\n"
    "v_max_f32 v122, v122, v120\n"
    "v_mov_b32 v120, v122\n"
    "s_nop 1\n"
    "v_permlane16_swap_b32 v122, v120\n"
    "s_nop 1\n"
    "v_max_f32 v122, v122, v120\n"
    "v_mov_b32 v24, v122\n"
    "v_sub_f32 v64, v64, v122\n"
    "v_sub_f32 v72, v72, v122\n"
    "v_sub_f32 v65, v65, v122\n"
    "v_sub_f32 v73, v73, v122\n"
    "v_sub_f32 v66, v66, v122\n"
    "v_sub_f32 v74, v74, v122\n"
    "v_sub_f32 v67, v67, v122\n"
    "v_sub_f32 v75, v75, v122\n"
    "v_sub_f32 v96, 0, v24\n"
    "v_sub_f32 v97, 0, v24\n"
    "v_sub_f32 v98, 0, v24\n"
    "v_sub_f32 v99, 0, v24\n"
    "v_max3_f32 v123, v68, v69, v70\n"
    "v_max3_f32 v123, v123, v71, v76\n"
    "v_max3_f32 v123, v123, v77, v78\n"
    "v_max_f32 v123, v123, v79\n"
    "v_mov_b32 v120, v123\n"
    "s_nop 1\n"
    "v_permlane32_swap_b32 v123, v120\n"
    "s_nop 1\n"
    "v_max_f32 v123, v123, v120\n"
    "v_mov_b32 v120, v123\n"
    "s_nop 1\n"
    "v_permlane16_swap_b32 v123, v120\n"
    "s_nop 1\n"
    "v_max_f32 v123, v123, v120\n"
    "v_mov_b32 v25, v123\n"
    "v_sub_f32 v68, v68, v123\n"
    "v_sub_f32 v76, v76, v123\n"
    "v_sub_f32 v69, v69, v123\n"
    "v_sub_f32 v77, v77, v123\n"
    "v_sub_f32 v70, v70, v123\n"
    "v_sub_f32 v78, v78, v123\n"
    "v_sub_f32 v71, v71, v123\n"
    "v_sub_f32 v79, v79, v123\n"
    "v_sub_f32 v100, 0, v25\n"
    "v_sub_f32 v101, 0, v25\n"
    "v_sub_f32 v102, 0, v25\n"
    "v_sub_f32 v103, 0, v25\n"
    "s_nop 1\n"
    "s_mov_b32 s51, %[n0]\n"
    "s_mov_b32 s73, 0\n"
    ".p2align 6\n"
    ".Lloop_%=:\n"
    "s_waitcnt lgkmcnt(8)\n"
    "v_mfma_f32_16x16x32_bf16 v[80:83], a[192:195], %[q0], v[96:99]\n"
    "v_exp_f32 v64, v64\n"
    "v_mfma_f32_16x16x32_bf16 v[84:87], a[192:195], %[q1], v[100:103]\n"
    "v_exp_f32 v65, v65\n"
    "v_mfma_f32_16x16x32_bf16 v[80:83], a[196:199], %[q2], v[80:83]\n"
    "v_exp_f32 v66, v66\n"
    "v_mfma_f32_16x16x32_bf16 v[84:87], a[196:199], %[q3], v[84:87]\n"
    "v_exp_f32 v67, v67\n"
    "v_mfma_f32_16x16x32_bf16 v[80:83], a[200:203], %[q4], v[80:83]\n"
    "v_exp_f32 v72, v72\n"
    "v_mfma_f32_16x16x32_bf16 v[84:87], a[200:203], %[q5], v[84:87]\n"
    "v_exp_f32 v73, v73\n"
    "v_mfma_f32_16x16x32_bf16 v[80:83], a[204:207], %[q6], v[80:83]\n"
    "v_exp_f32 v74, v74\n"
    "v_mfma_f32_16x16x32_bf16 v[84:87], a[204:207], %[q7], v[84:87]\n"
    "v_exp_f32 v75, v75\n"
    "s_waitcnt lgkmcnt(4)\n"
    "v_mfma_f32_16x16x32_bf16 v[80:83], a[208:211], %[q8], v[80:83]\n"
    "v_exp_f32 v68, v68\n"
    "v_mfma_f32_16x16x32_bf16 v[84:87], a[208:211], %[q9], v[84:87]\n"
    "v_exp_f32 v69, v69\n"
    "v_mfma_f32_16x16x32_bf16 v[80:83], a[212:215], %[q10], v[80:83]\n"
    "v_exp_f32 v70, v70\n"
    "v_mfma_f32_16x16x32_bf16 v[84:87], a[212:215], %[q11], v[84:87]\n"
    "v_exp_f32 v71, v71\n"
    "v_mfma_f32_16x16x32_bf16 v[80:83], a[216:219], %[q12], v[80:83]\n"
    "v_exp_f32 v76, v76\n"
    "v_mfma_f32_16x16x32_bf16 v[84:87], a[216:219], %[q13], v[84:87]\n"
    "v_exp_f32 v77, v77\n"
    "v_mfma_f32_16x16x32_bf16 v[80:83], a[220:223], %[q14], v[80:83]\n"
    "v_exp_f32 v78, v78\n"
    "v_mfma_f32_16x16x32_bf16 v[84:87], a[220:223], %[q15], v[84:87]\n"
    "v_exp_f32 v79, v79\n"
    "s_waitcnt lgkmcnt(0)\n"
    "v_mfma_f32_16x16x32_bf16 v[88:91], a[224:227], %[q0], v[96:99]\n"
    "ds_read_b128 a[240:243], v48 offset:24832\n"
    "v_cvt_pk_bf16_f32 v112, v64, v65\n"
    "v_mfma_f32_16x16x32_bf16 v[92:95], a[224:227], %[q1], v[100:103]\n"
    "ds_read_b128 a[244:247], v49 offset:24832\n"
    "v_cvt_pk_bf16_f32 v113, v66, v67\n"
    "v_mfma_f32_16x16x32_bf16 v[88:91], a[228:231], %[q2], v[88:91]\n"
    "ds_read_b128 a[248:251], v50 offset:24832\n"
    "v_cvt_pk_bf16_f32 v114, v72, v73\n"
    "v_mfma_f32_16x16x32_bf16 v[92:95], a[228:231], %[q3], v[92:95]\n"
    "ds_read_b128 a[252:255], v51 offset:24832\n"
    "v_cvt_pk_bf16_f32 v115, v74, v75\n"
    "v_mfma_f32_16x16x32_bf16 v[88:91], a[232:235], %[q4], v[88:91]\n"
    "ds_read_b64_tr_b16 a[192:193], v52 offset:0\n"
    "v_cvt_pk_bf16_f32 v116, v68, v69\n"
    "v_mfma_f32_16x16x32_bf16 v[92:95], a[232:235], %[q5], v[92:95]\n"
    "ds_read_b64_tr_b16 a[194:195], v52 offset:8192\n"
    "v_cvt_pk_bf16_f32 v117, v70, v71\n"
    "v_mfma_f32_16x16x32_bf16 v[88:91], a[236:239], %[q6], v[88:91]\n"
    "ds_read_b64_tr_b16 a[196:197], v53 offset:0\n"
    "v_cvt_pk_bf16_f32 v118, v76, v77\n"
    "v_mfma_f32_16x16x32_bf16 v[92:95], a[236:239], %[q7], v[92:95]\n"
    "ds_read_b64_tr_b16 a[198:199], v53 offset:8192\n"
    "v_cvt_pk_bf16_f32 v119, v78, v79\n"
    "s_waitcnt lgkmcnt(4)\n"
    "v_mfma_f32_16x16x32_bf16 v[88:91], a[240:243], %[q8], v[88:91]\n"
    "ds_read_b64_tr_b16 a[200:201], v54 offset:0\n"
    "v_add_f32 v26, v26, v64\n"
    "v_mfma_f32_16x16x32_bf16 v[92:95], a[240:243], %[q9], v[92:95]\n"
    "ds_read_b64_tr_b16 a[202:203], v54 offset:8192\n"
    "v_add_f32 v27, v27, v65\n"
    "v_mfma_f32_16x16x32_bf16 v[88:91], a[244:247], %[q10], v[88:91]\n"
    "ds_read_b64_tr_b16 a[204:205], v55 offset:0\n"
    "v_add_f32 v26, v26, v66\n"
    "v_mfma_f32_16x16x32_bf16 v[92:95], a[244:247], %[q11], v[92:95]\n"
    "ds_read_b64_tr_b16 a[206:207], v55 offset:8192\n"
    "v_add_f32 v27, v27, v67\n"
    "v_mfma_f32_16x16x32_bf16 v[88:91], a[248:251], %[q12], v[88:91]\n"
    "ds_read_b64_tr_b16 a[208:209], v56 offset:0\n"
    "v_add_f32 v26, v26, v72\n"
    "v_mfma_f32_16x16x32_bf16 v[92:95], a[248:251], %[q13], v[92:95]\n"
    "ds_read_b64_tr_b16 a[210:211], v56 offset:8192\n"
    "v_add_f32 v27, v27, v73\n"
    "v_mfma_f32_16x16x32_bf16 v[88:91], a[252:255], %[q14], v[88:91]\n"
    "ds_read_b64_tr_b16 a[212:213], v57 offset:0\n"
    "v_add_f32 v26, v26, v74\n"
    "v_mfma_f32_16x16x32_bf16 v[92:95], a[252:255], %[q15], v[92:95]\n"
    "ds_read_b64_tr_b16 a[214:215], v57 offset:8192\n"
    "v_add_f32 v27, v27, v75\n"
    "s_waitcnt lgkmcnt(8)\n"
    "v_mfma_f32_16x16x32_bf16 v[128:131], a[192:195], v[112:115], v[128:131]\n"
    "ds_read_b64_tr_b16 a[216:217], v58 offset:0\n"
    "v_add_f32 v28, v28, v68\n"
    "v_mfma_f32_16x16x32_bf16 v[132:135], a[192:195], v[116:119], v[132:135]\n"
    "ds_read_b64_tr_b16 a[218:219], v58 offset:8192\n"
    "v_add_f32 v29, v29, v69\n"
    "v_mfma_f32_16x16x32_bf16 v[136:139], a[196:199], v[112:115], v[136:139]\n"
    "ds_read_b64_tr_b16 a[220:221], v59 offset:0\n"
    "v_add_f32 v28, v28, v70\n"
    "v_mfma_f32_16x16x32_bf16 v[140:143], a[196:199], v[116:119], v[140:143]\n"
    "ds_read_b64_tr_b16 a[222:223], v59 offset:8192\n"
    "v_add_f32 v29, v29, v71\n"
    "s_waitcnt lgkmcnt(8)\n"
    "v_mfma_f32_16x16x32_bf16 v[144:147], a[200:203], v[112:115], v[144:147]\n"
    "ds_read_b64_tr_b16 a[224:225], v52 offset:256\n"
    "v_add_f32 v28, v28, v76\n"
    "v_mfma_f32_16x16x32_bf16 v[148:151], a[200:203], v[116:119], v[148:151]\n"
    "ds_read_b64_tr_b16 a[226:227], v52 offset:8448\n"
    "v_add_f32 v29, v29, v77\n"
    "v_mfma_f32_16x16x32_bf16 v[152:155], a[204:207], v[112:115], v[152:155]\n"
    "ds_read_b64_tr_b16 a[228:229], v53 offset:256\n"
    "v_add_f32 v28, v28, v78\n"
    "v_mfma_f32_16x16x32_bf16 v[156:159], a[204:207], v[116:119], v[156:159]\n"
    "ds_read_b64_tr_b16 a[230:231], v53 offset:8448\n"
    "v_add_f32 v29, v29, v79\n"
    "s_waitcnt lgkmcnt(8)\n"
    "v_mfma_f32_16x16x32_bf16 v[160:163], a[208:211], v[112:115], v[160:163]\n"
    "ds_read_b64_tr_b16 a[232:233], v54 offset:256\n"
    "s_waitcnt vmcnt(12)\n"
    "s_barrier\n"
    "s_add_u32 s48, s48, 16384\n"
    "v_mfma_f32_16x16x32_bf16 v[164:167], a[208:211], v[116:119], v[164:167]\n"
    "ds_read_b64_tr_b16 a[234:235], v54 offset:8448\n"
    "v_max3_f32 v110, v80, v81, v82\n"
    "s_add_u32 m0, s50, 147456\n"
    "v_mfma_f32_16x16x32_bf16 v[168:171], a[212:215], v[112:115], v[168:171]\n"
    "ds_read_b64_tr_b16 a[236:237], v55 offset:256\n"
    "buffer_load_dwordx4 %[vov0], s[44:47], s48 offen offset:0 nt lds\n"
    "v_mfma_f32_16x16x32_bf16 v[172:175], a[212:215], v[116:119], v[172:175]\n"
    "ds_read_b64_tr_b16 a[238:239], v55 offset:8448\n"
    "v_max3_f32 v110, v110, v83, v84\n"
    "s_waitcnt lgkmcnt(8)\n"
    "v_mfma_f32_16x16x32_bf16 v[176:179], a[216:219], v[112:115], v[176:179]\n"
    "ds_read_b64_tr_b16 a[240:241], v56 offset:256\n"
    "v_max3_f32 v110, v110, v85, v86\n"
    "v_mfma_f32_16x16x32_bf16 v[180:183], a[216:219], v[116:119], v[180:183]\n"
    "ds_read_b64_tr_b16 a[242:243], v56 offset:8448\n"
    "buffer_load_dwordx4 %[vov1], s[44:47], s48 offen offset:1024 nt lds\n"
    "v_mfma_f32_16x16x32_bf16 v[184:187], a[220:223], v[112:115], v[184:187]\n"
    "ds_read_b64_tr_b16 a[244:245], v57 offset:256\n"
    "v_max3_f32 v110, v110, v87, v88\n"
    "v_mfma_f32_16x16x32_bf16 v[188:191], a[220:223], v[116:119], v[188:191]\n"
    "ds_read_b64_tr_b16 a[246:247], v57 offset:8448\n"
    "v_max3_f32 v110, v110, v89, v90\n"
    "s_waitcnt lgkmcnt(8)\n"
    "v_mfma_f32_16x16x32_bf16 v[192:195], a[224:227], v[112:115], v[192:195]\n"
    "ds_read_b64_tr_b16 a[248:249], v58 offset:256\n"
    "buffer_load_dwordx4 %[vov2], s[44:47], s48 offen offset:2048 nt lds\n"
    "v_mfma_f32_16x16x32_bf16 v[196:199], a[224:227], v[116:119], v[196:199]\n"
    "ds_read_b64_tr_b16 a[250:251], v58 offset:8448\n"
    "v_max3_f32 v110, v110, v91, v92\n"
    "v_mfma_f32_16x16x32_bf16 v[200:203], a[228:231], v[112:115], v[200:203]\n"
    "ds_read_b64_tr_b16 a[252:253], v59 offset:256\n"
    "v_max3_f32 v110, v110, v93, v94\n"
    "v_mfma_f32_16x16x32_bf16 v[204:207], a[228:231], v[116:119], v[204:207]\n"
    "ds_read_b64_tr_b16 a[254:255], v59 offset:8448\n"
    "buffer_load_dwordx4 %[vov3], s[44:47], s48 offen offset:3072 nt lds\n"
    "s_waitcnt lgkmcnt(8)\n"
    "v_mfma_f32_16x16x32_bf16 v[208:211], a[232:235], v[112:115], v[208:211]\n"
    "ds_read_b128 a[192:195], v48 offset:32768\n"
    "v_max_f32 v110, v110, v95\n"
    "v_mfma_f32_16x16x32_bf16 v[212:215], a[232:235], v[116:119], v[212:215]\n"
    "ds_read_b128 a[196:199], v49 offset:32768\n"
    "s_add_u32 m0, s50, 49152\n"
    "v_mfma_f32_16x16x32_bf16 v[216:219], a[236:239], v[112:115], v[216:219]\n"
    "ds_read_b128 a[200:203], v50 offset:32768\n"
    "buffer_load_dwordx4 v60, s[40:43], s48 offen offset:0 nt lds\n"
    "v_mfma_f32_16x16x32_bf16 v[220:223], a[236:239], v[116:119], v[220:223]\n"
    "ds_read_b128 a[204:207], v51 offset:32768\n"
    "v_cmp_lt_f32 vcc, s79, v110\n"
    "s_waitcnt lgkmcnt(8)\n"
    "v_mfma_f32_16x16x32_bf16 v[224:227], a[240:243], v[112:115], v[224:227]\n"
    "ds_read_b128 a[208:211], v48 offset:33024\n"
    "v_mfma_f32_16x16x32_bf16 v[228:231], a[240:243], v[116:119], v[228:231]\n"
    "ds_read_b128 a[212:215], v49 offset:33024\n"
    "buffer_load_dwordx4 v61, s[40:43], s48 offen offset:1024 nt lds\n"
    "v_mfma_f32_16x16x32_bf16 v[232:235], a[244:247], v[112:115], v[232:235]\n"
    "ds_read_b128 a[216:219], v50 offset:33024\n"
    "v_mfma_f32_16x16x32_bf16 v[236:239], a[244:247], v[116:119], v[236:239]\n"
    "ds_read_b128 a[220:223], v51 offset:33024\n"
    "s_waitcnt lgkmcnt(8)\n"
    "v_mfma_f32_16x16x32_bf16 v[240:243], a[248:251], v[112:115], v[240:243]\n"
    "ds_read_b128 a[224:227], v48 offset:40960\n"
    "buffer_load_dwordx4 v62, s[40:43], s48 offen offset:2048 nt lds\n"
    "v_mfma_f32_16x16x32_bf16 v[244:247], a[248:251], v[116:119], v[244:247]\n"
    "ds_read_b128 a[228:231], v49 offset:40960\n"
    "v_mfma_f32_16x16x32_bf16 v[248:251], a[252:255], v[112:115], v[248:251]\n"
    "ds_read_b128 a[232:235], v50 offset:40960\n"
    "v_mfma_f32_16x16x32_bf16 v[252:255], a[252:255], v[116:119], v[252:255]\n"
    "ds_read_b128 a[236:239], v51 offset:40960\n"
    "buffer_load_dwordx4 v63, s[40:43], s48 offen offset:3072 nt lds\n"
    "s_cbranch_vccnz .Lrare0_%=\n"
    ".Lret0_%=:\n"
    "s_waitcnt lgkmcnt(8)\n"
    "v_mfma_f32_16x16x32_bf16 v[64:67], a[192:195], %[q0], v[96:99]\n"
    "v_exp_f32 v80, v80\n"
    "v_mfma_f32_16x16x32_bf16 v[68:71], a[192:195], %[q1], v[100:103]\n"
    "v_exp_f32 v81, v81\n"
    "v_mfma_f32_16x16x32_bf16 v[64:67], a[196:199], %[q2], v[64:67]\n"
    "v_exp_f32 v82, v82\n"
    "v_mfma_f32_16x16x32_bf16 v[68:71], a[196:199], %[q3], v[68:71]\n"
    "v_exp_f32 v83, v83\n"
    "v_mfma_f32_16x16x32_bf16 v[64:67], a[200:203], %[q4], v[64:67]\n"
    "v_exp_f32 v88, v88\n"
    "v_mfma_f32_16x16x32_bf16 v[68:71], a[200:203], %[q5], v[68:71]\n"
    "v_exp_f32 v89, v89\n"
    "v_mfma_f32_16x16x32_bf16 v[64:67], a[204:207], %[q6], v[64:67]\n"
    "v_exp_f32 v90, v90\n"
    "v_mfma_f32_16x16x32_bf16 v[68:71], a[204:207], %[q7], v[68:71]\n"
    "v_exp_f32 v91, v91\n"
    "s_waitcnt lgkmcnt(4)\n"
    "v_mfma_f32_16x16x32_bf16 v[64:67], a[208:211], %[q8], v[64:67]\n"
    "v_exp_f32 v84, v84\n"
    "v_mfma_f32_16x16x32_bf16 v[68:71], a[208:211], %[q9], v[68:71]\n"
    "v_exp_f32 v85, v85\n"
    "v_mfma_f32_16x16x32_bf16 v[64:67], a[212:215], %[q10], v[64:67]\n"
    "v_exp_f32 v86, v86\n"
    "v_mfma_f32_16x16x32_bf16 v[68:71], a[212:215], %[q11], v[68:71]\n"
    "v_exp_f32 v87, v87\n"
    "v_mfma_f32_16x16x32_bf16 v[64:67], a[216:219], %[q12], v[64:67]\n"
    "v_exp_f32 v92, v92\n"
    "v_mfma_f32_16x16x32_bf16 v[68:71], a[216:219], %[q13], v[68:71]\n"
    "v_exp_f32 v93, v93\n"
    "v_mfma_f32_16x16x32_bf16 v[64:67], a[220:223], %[q14], v[64:67]\n"
    "v_exp_f32 v94, v94\n"
    "v_mfma_f32_16x16x32_bf16 v[68:71], a[220:223], %[q15], v[68:71]\n"
    "v_exp_f32 v95, v95\n"
    "s_waitcnt lgkmcnt(0)\n"
    "v_mfma_f32_16x16x32_bf16 v[72:75], a[224:227], %[q0], v[96:99]\n"
    "ds_read_b128 a[240:243], v48 offset:41216\n"
    "v_cvt_pk_bf16_f32 v112, v80, v81\n"
    "v_mfma_f32_16x16x32_bf16 v[76:79], a[224:227], %[q1], v[100:103]\n"
    "ds_read_b128 a[244:247], v49 offset:41216\n"
    "v_cvt_pk_bf16_f32 v113, v82, v83\n"
    "v_mfma_f32_16x16x32_bf16 v[72:75], a[228:231], %[q2], v[72:75]\n"
    "ds_read_b128 a[248:251], v50 offset:41216\n"
    "v_cvt_pk_bf16_f32 v114, v88, v89\n"
    "v_mfma_f32_16x16x32_bf16 v[76:79], a[228:231], %[q3], v[76:79]\n"
    "ds_read_b128 a[252:255], v51 offset:41216\n"
    "v_cvt_pk_bf16_f32 v115, v90, v91\n"
    "v_mfma_f32_16x16x32_bf16 v[72:75], a[232:235], %[q4], v[72:75]\n"
    "ds_read_b64_tr_b16 a[192:193], v52 offset:16384\n"
    "v_cvt_pk_bf16_f32 v116, v84, v85\n"
    "v_mfma_f32_16x16x32_bf16 v[76:79], a[232:235], %[q5], v[76:79]\n"
    "ds_read_b64_tr_b16 a[194:195], v52 offset:24576\n"
    "v_cvt_pk_bf16_f32 v117, v86, v87\n"
    "v_mfma_f32_16x16x32_bf16 v[72:75], a[236:239], %[q6], v[72:75]\n"
    "ds_read_b64_tr_b16 a[196:197], v53 offset:16384\n"
    "v_cvt_pk_bf16_f32 v118, v92, v93\n"
    "v_mfma_f32_16x16x32_bf16 v[76:79], a[236:239], %[q7], v[76:79]\n"
    "ds_read_b64_tr_b16 a[198:199], v53 offset:24576\n"
    "v_cvt_pk_bf16_f32 v119, v94, v95\n"
    "s_waitcnt lgkmcnt(4)\n"
    "v_mfma_f32_16x16x32_bf16 v[72:75], a[240:243], %[q8], v[72:75]\n"
    "ds_read_b64_tr_b16 a[200:201], v54 offset:16384\n"
    "v_add_f32 v26, v26, v80\n"
    "v_mfma_f32_16x16x32_bf16 v[76:79], a[240:243], %[q9], v[76:79]\n"
    "ds_read_b64_tr_b16 a[202:203], v54 offset:24576\n"
    "v_add_f32 v27, v27, v81\n"
    "v_mfma_f32_16x16x32_bf16 v[72:75], a[244:247], %[q10], v[72:75]\n"
    "ds_read_b64_tr_b16 a[204:205], v55 offset:16384\n"
    "v_add_f32 v26, v26, v82\n"
    "v_mfma_f32_16x16x32_bf16 v[76:79], a[244:247], %[q11], v[76:79]\n"
    "ds_read_b64_tr_b16 a[206:207], v55 offset:24576\n"
    "v_add_f32 v27, v27, v83\n"
    "v_mfma_f32_16x16x32_bf16 v[72:75], a[248:251], %[q12], v[72:75]\n"
    "ds_read_b64_tr_b16 a[208:209], v56 offset:16384\n"
    "v_add_f32 v26, v26, v88\n"
    "v_mfma_f32_16x16x32_bf16 v[76:79], a[248:251], %[q13], v[76:79]\n"
    "ds_read_b64_tr_b16 a[210:211], v56 offset:24576\n"
    "v_add_f32 v27, v27, v89\n"
    "v_mfma_f32_16x16x32_bf16 v[72:75], a[252:255], %[q14], v[72:75]\n"
    "ds_read_b64_tr_b16 a[212:213], v57 offset:16384\n"
    "v_add_f32 v26, v26, v90\n"
    "v_mfma_f32_16x16x32_bf16 v[76:79], a[252:255], %[q15], v[76:79]\n"
    "ds_read_b64_tr_b16 a[214:215], v57 offset:24576\n"
    "v_add_f32 v27, v27, v91\n"
    "s_waitcnt lgkmcnt(8)\n"
    "v_mfma_f32_16x16x32_bf16 v[128:131], a[192:195], v[112:115], v[128:131]\n"
    "ds_read_b64_tr_b16 a[216:217], v58 offset:16384\n"
    "v_add_f32 v28, v28, v84\n"
    "v_mfma_f32_16x16x32_bf16 v[132:135], a[192:195], v[116:119], v[132:135]\n"
    "ds_read_b64_tr_b16 a[218:219], v58 offset:24576\n"
    "v_add_f32 v29, v29, v85\n"
    "v_mfma_f32_16x16x32_bf16 v[136:139], a[196:199], v[112:115], v[136:139]\n"
    "ds_read_b64_tr_b16 a[220:221], v59 offset:16384\n"
    "v_add_f32 v28, v28, v86\n"
    "v_mfma_f32_16x16x32_bf16 v[140:143], a[196:199], v[116:119], v[140:143]\n"
    "ds_read_b64_tr_b16 a[222:223], v59 offset:24576\n"
    "v_add_f32 v29, v29, v87\n"
    "s_waitcnt lgkmcnt(8)\n"
    "v_mfma_f32_16x16x32_bf16 v[144:147], a[200:203], v[112:115], v[144:147]\n"
    "ds_read_b64_tr_b16 a[224:225], v52 offset:16640\n"
    "v_add_f32 v28, v28, v92\n"
    "v_mfma_f32_16x16x32_bf16 v[148:151], a[200:203], v[116:119], v[148:151]\n"
    "ds_read_b64_tr_b16 a[226:227], v52 offset:24832\n"
    "v_add_f32 v29, v29, v93\n"
    "v_mfma_f32_16x16x32_bf16 v[152:155], a[204:207], v[112:115], v[152:155]\n"
    "ds_read_b64_tr_b16 a[228:229], v53 offset:16640\n"
    "v_add_f32 v28, v28, v94\n"
    "v_mfma_f32_16x16x32_bf16 v[156:159], a[204:207], v[116:119], v[156:159]\n"
    "ds_read_b64_tr_b16 a[230:231], v53 offset:24832\n"
    "v_add_f32 v29, v29, v95\n"
    "s_waitcnt lgkmcnt(8)\n"
    "v_mfma_f32_16x16x32_bf16 v[160:163], a[208:211], v[112:115], v[160:163]\n"
    "ds_read_b64_tr_b16 a[232:233], v54 offset:16640\n"
    "s_waitcnt vmcnt(12)\n"
    "s_barrier\n"
    "s_add_u32 s48, s48, 16384\n"
    "v_mfma_f32_16x16x32_bf16 v[164:167], a[208:211], v[116:119], v[164:167]\n"
    "ds_read_b64_tr_b16 a[234:235], v54 offset:24832\n"
    "v_max3_f32 v110, v64, v65, v66\n"
    "s_add_u32 m0, s50, 98304\n"
    "v_mfma_f32_16x16x32_bf16 v[168:171], a[212:215], v[112:115], v[168:171]\n"
    "ds_read_b64_tr_b16 a[236:237], v55 offset:16640\n"
    "buffer_load_dwordx4 %[vov0], s[44:47], s48 offen offset:0 nt lds\n"
    "v_mfma_f32_16x16x32_bf16 v[172:175], a[212:215], v[116:119], v[172:175]\n"
    "ds_read_b64_tr_b16 a[238:239], v55 offset:24832\n"
    "v_max3_f32 v110, v110, v67, v68\n"
    "s_waitcnt lgkmcnt(8)\n"
    "v_mfma_f32_16x16x32_bf16 v[176:179], a[216:219], v[112:115], v[176:179]\n"
    "ds_read_b64_tr_b16 a[240:241], v56 offset:16640\n"
    "v_max3_f32 v110, v110, v69, v70\n"
    "v_mfma_f32_16x16x32_bf16 v[180:183], a[216:219], v[116:119], v[180:183]\n"
    "ds_read_b64_tr_b16 a[242:243], v56 offset:24832\n"
    "buffer_load_dwordx4 %[vov1], s[44:47], s48 offen offset:1024 nt lds\n"
    "v_mfma_f32_16x16x32_bf16 v[184:187], a[220:223], v[112:115], v[184:187]\n"
    "ds_read_b64_tr_b16 a[244:245], v57 offset:16640\n"
    "v_max3_f32 v110, v110, v71, v72\n"
    "v_mfma_f32_16x16x32_bf16 v[188:191], a[220:223], v[116:119], v[188:191]\n"
    "ds_read_b64_tr_b16 a[246:247], v57 offset:24832\n"
    "v_max3_f32 v110, v110, v73, v74\n"
    "s_waitcnt lgkmcnt(8)\n"
    "v_mfma_f32_16x16x32_bf16 v[192:195], a[224:227], v[112:115], v[192:195]\n"
    "ds_read_b64_tr_b16 a[248:249], v58 offset:16640\n"
    "buffer_load_dwordx4 %[vov2], s[44:47], s48 offen offset:2048 nt lds\n"
    "v_mfma_f32_16x16x32_bf16 v[196:199], a[224:227], v[116:119], v[196:199]\n"
    "ds_read_b64_tr_b16 a[250:251], v58 offset:24832\n"
    "v_max3_f32 v110, v110, v75, v76\n"
    "v_mfma_f32_16x16x32_bf16 v[200:203], a[228:231], v[112:115], v[200:203]\n"
    "ds_read_b64_tr_b16 a[252:253], v59 offset:16640\n"
    "v_max3_f32 v110, v110, v77, v78\n"
    "v_mfma_f32_16x16x32_bf16 v[204:207], a[228:231], v[116:119], v[204:207]\n"
    "ds_read_b64_tr_b16 a[254:255], v59 offset:24832\n"
    "buffer_load_dwordx4 %[vov3], s[44:47], s48 offen offset:3072 nt lds\n"
    "s_waitcnt lgkmcnt(8)\n"
    "v_mfma_f32_16x16x32_bf16 v[208:211], a[232:235], v[112:115], v[208:211]\n"
    "ds_read_b128 a[192:195], v48 offset:49152\n"
    "v_max_f32 v110, v110, v79\n"
    "v_mfma_f32_16x16x32_bf16 v[212:215], a[232:235], v[116:119], v[212:215]\n"
    "ds_read_b128 a[196:199], v49 offset:49152\n"
    "s_add_u32 m0, s50, 65536\n"
    "v_mfma_f32_16x16x32_bf16 v[216:219], a[236:239], v[112:115], v[216:219]\n"
    "ds_read_b128 a[200:203], v50 offset:49152\n"
    "buffer_load_dwordx4 v60, s[40:43], s48 offen offset:0 nt lds\n"
    "v_mfma_f32_16x16x32_bf16 v[220:223], a[236:239], v[116:119], v[220:223]\n"
    "ds_read_b128 a[204:207], v51 offset:49152\n"
    "v_cmp_lt_f32 vcc, s79, v110\n"
    "s_waitcnt lgkmcnt(8)\n"
    "v_mfma_f32_16x16x32_bf16 v[224:227], a[240:243], v[112:115], v[224:227]\n"
    "ds_read_b128 a[208:211], v48 offset:49408\n"
    "v_mfma_f32_16x16x32_bf16 v[228:231], a[240:243], v[116:119], v[228:231]\n"
    "ds_read_b128 a[212:215], v49 offset:49408\n"
    "buffer_load_dwordx4 v61, s[40:43], s48 offen offset:1024 nt lds\n"
    "v_mfma_f32_16x16x32_bf16 v[232:235], a[244:247], v[112:115], v[232:235]\n"
    "ds_read_b128 a[216:219], v50 offset:49408\n"
    "v_mfma_f32_16x16x32_bf16 v[236:239], a[244:247], v[116:119], v[236:239]\n"
    "ds_read_b128 a[220:223], v51 offset:49408\n"
    "s_waitcnt lgkmcnt(8)\n"
    "v_mfma_f32_16x16x32_bf16 v[240:243], a[248:251], v[112:115], v[240:243]\n"
    "ds_read_b128 a[224:227], v48 offset:57344\n"
    "buffer_load_dwordx4 v62, s[40:43], s48 offen offset:2048 nt lds\n"
    "v_mfma_f32_16x16x32_bf16 v[244:247], a[248:251], v[116:119], v[244:247]\n"
    "ds_read_b128 a[228:231], v49 offset:57344\n"
    "v_mfma_f32_16x16x32_bf16 v[248:251], a[252:255], v[112:115], v[248:251]\n"
    "ds_read_b128 a[232:235], v50 offset:57344\n"
    "v_mfma_f32_16x16x32_bf16 v[252:255], a[252:255], v[116:119], v[252:255]\n"
    "ds_read_b128 a[236:239], v51 offset:57344\n"
    "buffer_load_dwordx4 v63, s[40:43], s48 offen offset:3072 nt lds\n"
    "s_cbranch_vccnz .Lrare1_%=\n"
    ".Lret1_%=:\n"
    "s_waitcnt lgkmcnt(8)\n"
    "v_mfma_f32_16x16x32_bf16 v[80:83], a[192:195], %[q0], v[96:99]\n"
    "v_exp_f32 v64, v64\n"
    "v_mfma_f32_16x16x32_bf16 v[84:87], a[192:195], %[q1], v[100:103]\n"
    "v_exp_f32 v65, v65\n"
    "v_mfma_f32_16x16x32_bf16 v[80:83], a[196:199], %[q2], v[80:83]\n"
    "v_exp_f32 v66, v66\n"
    "v_mfma_f32_16x16x32_bf16 v[84:87], a[196:199], %[q3], v[84:87]\n"
    "v_exp_f32 v67, v67\n"
    "v_mfma_f32_16x16x32_bf16 v[80:83], a[200:203], %[q4], v[80:83]\n"
    "v_exp_f32 v72, v72\n"
    "v_mfma_f32_16x16x32_bf16 v[84:87], a[200:203], %[q5], v[84:87]\n"
    "v_exp_f32 v73, v73\n"
    "v_mfma_f32_16x16x32_bf16 v[80:83], a[204:207], %[q6], v[80:83]\n"
    "v_exp_f32 v74, v74\n"
    "v_mfma_f32_16x16x32_bf16 v[84:87], a[204:207], %[q7], v[84:87]\n"
    "v_exp_f32 v75, v75\n"
    "s_waitcnt lgkmcnt(4)\n"
    "v_mfma_f32_16x16x32_bf16 v[80:83], a[208:211], %[q8], v[80:83]\n"
    "v_exp_f32 v68, v68\n"
    "v_mfma_f32_16x16x32_bf16 v[84:87], a[208:211], %[q9], v[84:87]\n"
    "v_exp_f32 v69, v69\n"
    "v_mfma_f32_16x16x32_bf16 v[80:83], a[212:215], %[q10], v[80:83]\n"
    "v_exp_f32 v70, v70\n"
    "v_mfma_f32_16x16x32_bf16 v[84:87], a[212:215], %[q11], v[84:87]\n"
    "v_exp_f32 v71, v71\n"
    "v_mfma_f32_16x16x32_bf16 v[80:83], a[216:219], %[q12], v[80:83]\n"
    "v_exp_f32 v76, v76\n"
    "v_mfma_f32_16x16x32_bf16 v[84:87], a[216:219], %[q13], v[84:87]\n"
    "v_exp_f32 v77, v77\n"
    "v_mfma_f32_16x16x32_bf16 v[80:83], a[220:223], %[q14], v[80:83]\n"
    "v_exp_f32 v78, v78\n"
    "v_mfma_f32_16x16x32_bf16 v[84:87], a[220:223], %[q15], v[84:87]\n"
    "v_exp_f32 v79, v79\n"
    "s_waitcnt lgkmcnt(0)\n"
    "v_mfma_f32_16x16x32_bf16 v[88:91], a[224:227], %[q0], v[96:99]\n"
    "ds_read_b128 a[240:243], v48 offset:57600\n"
    "v_cvt_pk_bf16_f32 v112, v64, v65\n"
    "v_mfma_f32_16x16x32_bf16 v[92:95], a[224:227], %[q1], v[100:103]\n"
    "ds_read_b128 a[244:247], v49 offset:57600\n"
    "v_cvt_pk_bf16_f32 v113, v66, v67\n"
    "v_mfma_f32_16x16x32_bf16 v[88:91], a[228:231], %[q2], v[88:91]\n"
    "ds_read_b128 a[248:251], v50 offset:57600\n"
    "v_cvt_pk_bf16_f32 v114, v72, v73\n"
    "v_mfma_f32_16x16x32_bf16 v[92:95], a[228:231], %[q3], v[92:95]\n"
    "ds_read_b128 a[252:255], v51 offset:57600\n"
    "v_cvt_pk_bf16_f32 v115, v74, v75\n"
    "v_mfma_f32_16x16x32_bf16 v[88:91], a[232:235], %[q4], v[88:91]\n"
    "ds_read_b64_tr_b16 a[192:193], v52 offset:32768\n"
    "v_cvt_pk_bf16_f32 v116, v68, v69\n"
    "v_mfma_f32_16x16x32_bf16 v[92:95], a[232:235], %[q5], v[92:95]\n"
    "ds_read_b64_tr_b16 a[194:195], v52 offset:40960\n"
    "v_cvt_pk_bf16_f32 v117, v70, v71\n"
    "v_mfma_f32_16x16x32_bf16 v[88:91], a[236:239], %[q6], v[88:91]\n"
    "ds_read_b64_tr_b16 a[196:197], v53 offset:32768\n"
    "v_cvt_pk_bf16_f32 v118, v76, v77\n"
    "v_mfma_f32_16x16x32_bf16 v[92:95], a[236:239], %[q7], v[92:95]\n"
    "ds_read_b64_tr_b16 a[198:199], v53 offset:40960\n"
    "v_cvt_pk_bf16_f32 v119, v78, v79\n"
    "s_waitcnt lgkmcnt(4)\n"
    "v_mfma_f32_16x16x32_bf16 v[88:91], a[240:243], %[q8], v[88:91]\n"
    "ds_read_b64_tr_b16 a[200:201], v54 offset:32768\n"
    "v_add_f32 v26, v26, v64\n"
    "v_mfma_f32_16x16x32_bf16 v[92:95], a[240:243], %[q9], v[92:95]\n"
    "ds_read_b64_tr_b16 a[202:203], v54 offset:40960\n"
    "v_add_f32 v27, v27, v65\n"
    "v_mfma_f32_16x16x32_bf16 v[88:91], a[244:247], %[q10], v[88:91]\n"
    "ds_read_b64_tr_b16 a[204:205], v55 offset:32768\n"
    "v_add_f32 v26, v26, v66\n"
    "v_mfma_f32_16x16x32_bf16 v[92:95], a[244:247], %[q11], v[92:95]\n"
    "ds_read_b64_tr_b16 a[206:207], v55 offset:40960\n"
    "v_add_f32 v27, v27, v67\n"
    "v_mfma_f32_16x16x32_bf16 v[88:91], a[248:251], %[q12], v[88:91]\n"
    "ds_read_b64_tr_b16 a[208:209], v56 offset:32768\n"
    "v_add_f32 v26, v26, v72\n"
    "v_mfma_f32_16x16x32_bf16 v[92:95], a[248:251], %[q13], v[92:95]\n"
    "ds_read_b64_tr_b16 a[210:211], v56 offset:40960\n"
    "v_add_f32 v27, v27, v73\n"
    "v_mfma_f32_16x16x32_bf16 v[88:91], a[252:255], %[q14], v[88:91]\n"
    "ds_read_b64_tr_b16 a[212:213], v57 offset:32768\n"
    "v_add_f32 v26, v26, v74\n"
    "v_mfma_f32_16x16x32_bf16 v[92:95], a[252:255], %[q15], v[92:95]\n"
    "ds_read_b64_tr_b16 a[214:215], v57 offset:40960\n"
    "v_add_f32 v27, v27, v75\n"
    "s_waitcnt lgkmcnt(8)\n"
    "v_mfma_f32_16x16x32_bf16 v[128:131], a[192:195], v[112:115], v[128:131]\n"
    "ds_read_b64_tr_b16 a[216:217], v58 offset:32768\n"
    "v_add_f32 v28, v28, v68\n"
    "v_mfma_f32_16x16x32_bf16 v[132:135], a[192:195], v[116:119], v[132:135]\n"
    "ds_read_b64_tr_b16 a[218:219], v58 offset:40960\n"
    "v_add_f32 v29, v29, v69\n"
    "v_mfma_f32_16x16x32_bf16 v[136:139], a[196:199], v[112:115], v[136:139]\n"
    "ds_read_b64_tr_b16 a[220:221], v59 offset:32768\n"
    "v_add_f32 v28, v28, v70\n"
    "v_mfma_f32_16x16x32_bf16 v[140:143], a[196:199], v[116:119], v[140:143]\n"
    "ds_read_b64_tr_b16 a[222:223], v59 offset:40960\n"
    "v_add_f32 v29, v29, v71\n"
    "s_waitcnt lgkmcnt(8)\n"
    "v_mfma_f32_16x16x32_bf16 v[144:147], a[200:203], v[112:115], v[144:147]\n"
    "ds_read_b64_tr_b16 a[224:225], v52 offset:33024\n"
    "v_add_f32 v28, v28, v76\n"
    "v_mfma_f32_16x16x32_bf16 v[148:151], a[200:203], v[116:119], v[148:151]\n"
    "ds_read_b64_tr_b16 a[226:227], v52 offset:41216\n"
    "v_add_f32 v29, v29, v77\n"
    "v_mfma_f32_16x16x32_bf16 v[152:155], a[204:207], v[112:115], v[152:155]\n"
    "ds_read_b64_tr_b16 a[228:229], v53 offset:33024\n"
    "v_add_f32 v28, v28, v78\n"
    "v_mfma_f32_16x16x32_bf16 v[156:159], a[204:207], v[116:119], v[156:159]\n"
    "ds_read_b64_tr_b16 a[230:231], v53 offset:41216\n"
    "v_add_f32 v29, v29, v79\n"
    "s_waitcnt lgkmcnt(8)\n"
    "v_mfma_f32_16x16x32_bf16 v[160:163], a[208:211], v[112:115], v[160:163]\n"
    "ds_read_b64_tr_b16 a[232:233], v54 offset:33024\n"
    "s_waitcnt vmcnt(12)\n"
    "s_barrier\n"
    "s_add_u32 s48, s48, 16384\n"
    "v_mfma_f32_16x16x32_bf16 v[164:167], a[208:211], v[116:119], v[164:167]\n"
    "ds_read_b64_tr_b16 a[234:235], v54 offset:41216\n"
    "v_max3_f32 v110, v80, v81, v82\n"
    "s_add_u32 m0, s50, 114688\n"
    "v_mfma_f32_16x16x32_bf16 v[168:171], a[212:215], v[112:115], v[168:171]\n"
    "ds_read_b64_tr_b16 a[236:237], v55 offset:33024\n"
    "buffer_load_dwordx4 %[vov0], s[44:47], s48 offen offset:0 nt lds\n"
    "v_mfma_f32_16x16x32_bf16 v[172:175], a[212:215], v[116:119], v[172:175]\n"
    "ds_read_b64_tr_b16 a[238:239], v55 offset:41216\n"
    "v_max3_f32 v110, v110, v83, v84\n"
    "s_waitcnt lgkmcnt(8)\n"
    "v_mfma_f32_16x16x32_bf16 v[176:179], a[216:219], v[112:115], v[176:179]\n"
    "ds_read_b64_tr_b16 a[240:241], v56 offset:33024\n"
    "v_max3_f32 v110, v110, v85, v86\n"
    "v_mfma_f32_16x16x32_bf16 v[180:183], a[216:219], v[116:119], v[180:183]\n"
    "ds_read_b64_tr_b16 a[242:243], v56 offset:41216\n"
    "buffer_load_dwordx4 %[vov1], s[44:47], s48 offen offset:1024 nt lds\n"
    "v_mfma_f32_16x16x32_bf16 v[184:187], a[220:223], v[112:115], v[184:187]\n"
    "ds_read_b64_tr_b16 a[244:245], v57 offset:33024\n"
    "v_max3_f32 v110, v110, v87, v88\n"
    "v_mfma_f32_16x16x32_bf16 v[188:191], a[220:223], v[116:119], v[188:191]\n"
    "ds_read_b64_tr_b16 a[246:247], v57 offset:41216\n"
    "v_max3_f32 v110, v110, v89, v90\n"
    "s_waitcnt lgkmcnt(8)\n"
    "v_mfma_f32_16x16x32_bf16 v[192:195], a[224:227], v[112:115], v[192:195]\n"
    "ds_read_b64_tr_b16 a[248:249], v58 offset:33024\n"
    "buffer_load_dwordx4 %[vov2], s[44:47], s48 offen offset:2048 nt lds\n"
    "v_mfma_f32_16x16x32_bf16 v[196:199], a[224:227], v[116:119], v[196:199]\n"
    "ds_read_b64_tr_b16 a[250:251], v58 offset:41216\n"
    "v_max3_f32 v110, v110, v91, v92\n"
    "v_mfma_f32_16x16x32_bf16 v[200:203], a[228:231], v[112:115], v[200:203]\n"
    "ds_read_b64_tr_b16 a[252:253], v59 offset:33024\n"
    "v_max3_f32 v110, v110, v93, v94\n"
    "v_mfma_f32_16x16x32_bf16 v[204:207], a[228:231], v[116:119], v[204:207]\n"
    "ds_read_b64_tr_b16 a[254:255], v59 offset:41216\n"
    "buffer_load_dwordx4 %[vov3], s[44:47], s48 offen offset:3072 nt lds\n"
    "s_waitcnt lgkmcnt(8)\n"
    "v_mfma_f32_16x16x32_bf16 v[208:211], a[232:235], v[112:115], v[208:211]\n"
    "ds_read_b128 a[192:195], v48 offset:0\n"
    "v_max_f32 v110, v110, v95\n"
    "v_mfma_f32_16x16x32_bf16 v[212:215], a[232:235], v[116:119], v[212:215]\n"
    "ds_read_b128 a[196:199], v49 offset:0\n"
    "s_add_u32 m0, s50, 81920\n"
    "v_mfma_f32_16x16x32_bf16 v[216:219], a[236:239], v[112:115], v[216:219]\n"
    "ds_read_b128 a[200:203], v50 offset:0\n"
    "buffer_load_dwordx4 v60, s[40:43], s48 offen offset:0 nt lds\n"
    "v_mfma_f32_16x16x32_bf16 v[220:223], a[236:239], v[116:119], v[220:223]\n"
    "ds_read_b128 a[204:207], v51 offset:0\n"
    "v_cmp_lt_f32 vcc, s79, v110\n"
    "s_waitcnt lgkmcnt(8)\n"
    "v_mfma_f32_16x16x32_bf16 v[224:227], a[240:243], v[112:115], v[224:227]\n"
    "ds_read_b128 a[208:211], v48 offset:256\n"
    "v_mfma_f32_16x16x32_bf16 v[228:231], a[240:243], v[116:119], v[228:231]\n"
    "ds_read_b128 a[212:215], v49 offset:256\n"
    "buffer_load_dwordx4 v61, s[40:43], s48 offen offset:1024 nt lds\n"
    "v_mfma_f32_16x16x32_bf16 v[232:235], a[244:247], v[112:115], v[232:235]\n"
    "ds_read_b128 a[216:219], v50 offset:256\n"
    "v_mfma_f32_16x16x32_bf16 v[236:239], a[244:247], v[116:119], v[236:239]\n"
    "ds_read_b128 a[220:223], v51 offset:256\n"
    "s_waitcnt lgkmcnt(8)\n"
    "v_mfma_f32_16x16x32_bf16 v[240:243], a[248:251], v[112:115], v[240:243]\n"
    "ds_read_b128 a[224:227], v48 offset:8192\n"
    "buffer_load_dwordx4 v62, s[40:43], s48 offen offset:2048 nt lds\n"
    "v_mfma_f32_16x16x32_bf16 v[244:247], a[248:251], v[116:119], v[244:247]\n"
    "ds_read_b128 a[228:231], v49 offset:8192\n"
    "v_mfma_f32_16x16x32_bf16 v[248:251], a[252:255], v[112:115], v[248:251]\n"
    "ds_read_b128 a[232:235], v50 offset:8192\n"
    "v_mfma_f32_16x16x32_bf16 v[252:255], a[252:255], v[116:119], v[252:255]\n"
    "ds_read_b128 a[236:239], v51 offset:8192\n"
    "buffer_load_dwordx4 v63, s[40:43], s48 offen offset:3072 nt lds\n"
    "s_cbranch_vccnz .Lrare2_%=\n"
    ".Lret2_%=:\n"
    "s_waitcnt lgkmcnt(8)\n"
    "v_mfma_f32_16x16x32_bf16 v[64:67], a[192:195], %[q0], v[96:99]\n"
    "v_exp_f32 v80, v80\n"
    "v_mfma_f32_16x16x32_bf16 v[68:71], a[192:195], %[q1], v[100:103]\n"
    "v_exp_f32 v81, v81\n"
    "v_mfma_f32_16x16x32_bf16 v[64:67], a[196:199], %[q2], v[64:67]\n"
    "v_exp_f32 v82, v82\n"
    "v_mfma_f32_16x16x32_bf16 v[68:71], a[196:199], %[q3], v[68:71]\n"
    "v_exp_f32 v83, v83\n"
    "v_mfma_f32_16x16x32_bf16 v[64:67], a[200:203], %[q4], v[64:67]\n"
    "v_exp_f32 v88, v88\n"
    "v_mfma_f32_16x16x32_bf16 v[68:71], a[200:203], %[q5], v[68:71]\n"
    "v_exp_f32 v89, v89\n"
    "v_mfma_f32_16x16x32_bf16 v[64:67], a[204:207], %[q6], v[64:67]\n"
    "v_exp_f32 v90, v90\n"
    "v_mfma_f32_16x16x32_bf16 v[68:71], a[204:207], %[q7], v[68:71]\n"
    "v_exp_f32 v91, v91\n"
    "s_waitcnt lgkmcnt(4)\n"
    "v_mfma_f32_16x16x32_bf16 v[64:67], a[208:211], %[q8], v[64:67]\n"
    "v_exp_f32 v84, v84\n"
    "v_mfma_f32_16x16x32_bf16 v[68:71], a[208:211], %[q9], v[68:71]\n"
    "v_exp_f32 v85, v85\n"
    "v_mfma_f32_16x16x32_bf16 v[64:67], a[212:215], %[q10], v[64:67]\n"
    "v_exp_f32 v86, v86\n"
    "v_mfma_f32_16x16x32_bf16 v[68:71], a[212:215], %[q11], v[68:71]\n"
    "v_exp_f32 v87, v87\n"
    "v_mfma_f32_16x16x32_bf16 v[64:67], a[216:219], %[q12], v[64:67]\n"
    "v_exp_f32 v92, v92\n"
    "v_mfma_f32_16x16x32_bf16 v[68:71], a[216:219], %[q13], v[68:71]\n"
    "v_exp_f32 v93, v93\n"
    "v_mfma_f32_16x16x32_bf16 v[64:67], a[220:223], %[q14], v[64:67]\n"
    "v_exp_f32 v94, v94\n"
    "v_mfma_f32_16x16x32_bf16 v[68:71], a[220:223], %[q15], v[68:71]\n"
    "v_exp_f32 v95, v95\n"
    "s_waitcnt lgkmcnt(0)\n"
    "v_mfma_f32_16x16x32_bf16 v[72:75], a[224:227], %[q0], v[96:99]\n"
    "ds_read_b128 a[240:243], v48 offset:8448\n"
    "v_cvt_pk_bf16_f32 v112, v80, v81\n"
    "v_mfma_f32_16x16x32_bf16 v[76:79], a[224:227], %[q1], v[100:103]\n"
    "ds_read_b128 a[244:247], v49 offset:8448\n"
    "v_cvt_pk_bf16_f32 v113, v82, v83\n"
    "v_mfma_f32_16x16x32_bf16 v[72:75], a[228:231], %[q2], v[72:75]\n"
    "ds_read_b128 a[248:251], v50 offset:8448\n"
    "v_cvt_pk_bf16_f32 v114, v88, v89\n"
    "v_mfma_f32_16x16x32_bf16 v[76:79], a[228:231], %[q3], v[76:79]\n"
    "ds_read_b128 a[252:255], v51 offset:8448\n"
    "v_cvt_pk_bf16_f32 v115, v90, v91\n"
    "v_mfma_f32_16x16x32_bf16 v[72:75], a[232:235], %[q4], v[72:75]\n"
    "ds_read_b64_tr_b16 a[192:193], v52 offset:49152\n"
    "v_cvt_pk_bf16_f32 v116, v84, v85\n"
    "v_mfma_f32_16x16x32_bf16 v[76:79], a[232:235], %[q5], v[76:79]\n"
    "ds_read_b64_tr_b16 a[194:195], v52 offset:57344\n"
    "v_cvt_pk_bf16_f32 v117, v86, v87\n"
    "v_mfma_f32_16x16x32_bf16 v[72:75], a[236:239], %[q6], v[72:75]\n"
    "ds_read_b64_tr_b16 a[196:197], v53 offset:49152\n"
    "v_cvt_pk_bf16_f32 v118, v92, v93\n"
    "v_mfma_f32_16x16x32_bf16 v[76:79], a[236:239], %[q7], v[76:79]\n"
    "ds_read_b64_tr_b16 a[198:199], v53 offset:57344\n"
    "v_cvt_pk_bf16_f32 v119, v94, v95\n"
    "s_waitcnt lgkmcnt(4)\n"
    "v_mfma_f32_16x16x32_bf16 v[72:75], a[240:243], %[q8], v[72:75]\n"
    "ds_read_b64_tr_b16 a[200:201], v54 offset:49152\n"
    "v_add_f32 v26, v26, v80\n"
    "v_mfma_f32_16x16x32_bf16 v[76:79], a[240:243], %[q9], v[76:79]\n"
    "ds_read_b64_tr_b16 a[202:203], v54 offset:57344\n"
    "v_add_f32 v27, v27, v81\n"
    "v_mfma_f32_16x16x32_bf16 v[72:75], a[244:247], %[q10], v[72:75]\n"
    "ds_read_b64_tr_b16 a[204:205], v55 offset:49152\n"
    "v_add_f32 v26, v26, v82\n"
    "v_mfma_f32_16x16x32_bf16 v[76:79], a[244:247], %[q11], v[76:79]\n"
    "ds_read_b64_tr_b16 a[206:207], v55 offset:57344\n"
    "v_add_f32 v27, v27, v83\n"
    "v_mfma_f32_16x16x32_bf16 v[72:75], a[248:251], %[q12], v[72:75]\n"
    "ds_read_b64_tr_b16 a[208:209], v56 offset:49152\n"
    "v_add_f32 v26, v26, v88\n"
    "v_mfma_f32_16x16x32_bf16 v[76:79], a[248:251], %[q13], v[76:79]\n"
    "ds_read_b64_tr_b16 a[210:211], v56 offset:57344\n"
    "v_add_f32 v27, v27, v89\n"
    "v_mfma_f32_16x16x32_bf16 v[72:75], a[252:255], %[q14], v[72:75]\n"
    "ds_read_b64_tr_b16 a[212:213], v57 offset:49152\n"
    "v_add_f32 v26, v26, v90\n"
    "v_mfma_f32_16x16x32_bf16 v[76:79], a[252:255], %[q15], v[76:79]\n"
    "ds_read_b64_tr_b16 a[214:215], v57 offset:57344\n"
    "v_add_f32 v27, v27, v91\n"
    "s_waitcnt lgkmcnt(8)\n"
    "v_mfma_f32_16x16x32_bf16 v[128:131], a[192:195], v[112:115], v[128:131]\n"
    "ds_read_b64_tr_b16 a[216:217], v58 offset:49152\n"
    "v_add_f32 v28, v28, v84\n"
    "v_mfma_f32_16x16x32_bf16 v[132:135], a[192:195], v[116:119], v[132:135]\n"
    "ds_read_b64_tr_b16 a[218:219], v58 offset:57344\n"
    "v_add_f32 v29, v29, v85\n"
    "v_mfma_f32_16x16x32_bf16 v[136:139], a[196:199], v[112:115], v[136:139]\n"
    "ds_read_b64_tr_b16 a[220:221], v59 offset:49152\n"
    "v_add_f32 v28, v28, v86\n"
    "v_mfma_f32_16x16x32_bf16 v[140:143], a[196:199], v[116:119], v[140:143]\n"
    "ds_read_b64_tr_b16 a[222:223], v59 offset:57344\n"
    "v_add_f32 v29, v29, v87\n"
    "s_waitcnt lgkmcnt(8)\n"
    "v_mfma_f32_16x16x32_bf16 v[144:147], a[200:203], v[112:115], v[144:147]\n"
    "ds_read_b64_tr_b16 a[224:225], v52 offset:49408\n"
    "v_add_f32 v28, v28, v92\n"
    "v_mfma_f32_16x16x32_bf16 v[148:151], a[200:203], v[116:119], v[148:151]\n"
    "ds_read_b64_tr_b16 a[226:227], v52 offset:57600\n"
    "v_add_f32 v29, v29, v93\n"
    "v_mfma_f32_16x16x32_bf16 v[152:155], a[204:207], v[112:115], v[152:155]\n"
    "ds_read_b64_tr_b16 a[228:229], v53 offset:49408\n"
    "v_add_f32 v28, v28, v94\n"
    "v_mfma_f32_16x16x32_bf16 v[156:159], a[204:207], v[116:119], v[156:159]\n"
    "ds_read_b64_tr_b16 a[230:231], v53 offset:57600\n"
    "v_add_f32 v29, v29, v95\n"
    "s_waitcnt lgkmcnt(8)\n"
    "v_mfma_f32_16x16x32_bf16 v[160:163], a[208:211], v[112:115], v[160:163]\n"
    "ds_read_b64_tr_b16 a[232:233], v54 offset:49408\n"
    "s_waitcnt vmcnt(12)\n"
    "s_barrier\n"
    "s_add_u32 s48, s48, 16384\n"
    "v_mfma_f32_16x16x32_bf16 v[164:167], a[208:211], v[116:119], v[164:167]\n"
    "ds_read_b64_tr_b16 a[234:235], v54 offset:57600\n"
    "v_max3_f32 v110, v64, v65, v66\n"
    "s_add_u32 m0, s50, 131072\n"
    "v_mfma_f32_16x16x32_bf16 v[168:171], a[212:215], v[112:115], v[168:171]\n"
    "ds_read_b64_tr_b16 a[236:237], v55 offset:49408\n"
    "buffer_load_dwordx4 %[vov0], s[44:47], s48 offen offset:0 nt lds\n"
    "v_mfma_f32_16x16x32_bf16 v[172:175], a[212:215], v[116:119], v[172:175]\n"
    "ds_read_b64_tr_b16 a[238:239], v55 offset:57600\n"
    "v_max3_f32 v110, v110, v67, v68\n"
    "s_waitcnt lgkmcnt(8)\n"
    "v_mfma_f32_16x16x32_bf16 v[176:179], a[216:219], v[112:115], v[176:179]\n"
    "ds_read_b64_tr_b16 a[240:241], v56 offset:49408\n"
    "v_max3_f32 v110, v110, v69, v70\n"
    "v_mfma_f32_16x16x32_bf16 v[180:183], a[216:219], v[116:119], v[180:183]\n"
    "ds_read_b64_tr_b16 a[242:243], v56 offset:57600\n"
    "buffer_load_dwordx4 %[vov1], s[44:47], s48 offen offset:1024 nt lds\n"
    "v_mfma_f32_16x16x32_bf16 v[184:187], a[220:223], v[112:115], v[184:187]\n"
    "ds_read_b64_tr_b16 a[244:245], v57 offset:49408\n"
    "v_max3_f32 v110, v110, v71, v72\n"
    "v_mfma_f32_16x16x32_bf16 v[188:191], a[220:223], v[116:119], v[188:191]\n"
    "ds_read_b64_tr_b16 a[246:247], v57 offset:57600\n"
    "v_max3_f32 v110, v110, v73, v74\n"
    "s_waitcnt lgkmcnt(8)\n"
    "v_mfma_f32_16x16x32_bf16 v[192:195], a[224:227], v[112:115], v[192:195]\n"
    "ds_read_b64_tr_b16 a[248:249], v58 offset:49408\n"
    "buffer_load_dwordx4 %[vov2], s[44:47], s48 offen offset:2048 nt lds\n"
    "v_mfma_f32_16x16x32_bf16 v[196:199], a[224:227], v[116:119], v[196:199]\n"
    "ds_read_b64_tr_b16 a[250:251], v58 offset:57600\n"
    "v_max3_f32 v110, v110, v75, v76\n"
    "v_mfma_f32_16x16x32_bf16 v[200:203], a[228:231], v[112:115], v[200:203]\n"
    "ds_read_b64_tr_b16 a[252:253], v59 offset:49408\n"
    "v_max3_f32 v110, v110, v77, v78\n"
    "v_mfma_f32_16x16x32_bf16 v[204:207], a[228:231], v[116:119], v[204:207]\n"
    "ds_read_b64_tr_b16 a[254:255], v59 offset:57600\n"
    "buffer_load_dwordx4 %[vov3], s[44:47], s48 offen offset:3072 nt lds\n"
    "s_waitcnt lgkmcnt(8)\n"
    "v_mfma_f32_16x16x32_bf16 v[208:211], a[232:235], v[112:115], v[208:211]\n"
    "ds_read_b128 a[192:195], v48 offset:16384\n"
    "v_max_f32 v110, v110, v79\n"
    "v_mfma_f32_16x16x32_bf16 v[212:215], a[232:235], v[116:119], v[212:215]\n"
    "ds_read_b128 a[196:199], v49 offset:16384\n"
    "s_add_u32 m0, s50, 32768\n"
    "v_mfma_f32_16x16x32_bf16 v[216:219], a[236:239], v[112:115], v[216:219]\n"
    "ds_read_b128 a[200:203], v50 offset:16384\n"
    "buffer_load_dwordx4 v60, s[40:43], s48 offen offset:0 nt lds\n"
    "v_mfma_f32_16x16x32_bf16 v[220:223], a[236:239], v[116:119], v[220:223]\n"
    "ds_read_b128 a[204:207], v51 offset:16384\n"
    "v_cmp_lt_f32 vcc, s79, v110\n"
    "s_waitcnt lgkmcnt(8)\n"
    "v_mfma_f32_16x16x32_bf16 v[224:227], a[240:243], v[112:115], v[224:227]\n"
    "ds_read_b128 a[208:211], v48 offset:16640\n"
    "v_mfma_f32_16x16x32_bf16 v[228:231], a[240:243], v[116:119], v[228:231]\n"
    "ds_read_b128 a[212:215], v49 offset:16640\n"
    "buffer_load_dwordx4 v61, s[40:43], s48 offen offset:1024 nt lds\n"
    "v_mfma_f32_16x16x32_bf16 v[232:235], a[244:247], v[112:115], v[232:235]\n"
    "ds_read_b128 a[216:219], v50 offset:16640\n"
    "v_mfma_f32_16x16x32_bf16 v[236:239], a[244:247], v[116:119], v[236:239]\n"
    "ds_read_b128 a[220:223], v51 offset:16640\n"
    "s_waitcnt lgkmcnt(8)\n"
    "v_mfma_f32_16x16x32_bf16 v[240:243], a[248:251], v[112:115], v[240:243]\n"
    "ds_read_b128 a[224:227], v48 offset:24576\n"
    "buffer_load_dwordx4 v62, s[40:43], s48 offen offset:2048 nt lds\n"
    "v_mfma_f32_16x16x32_bf16 v[244:247], a[248:251], v[116:119], v[244:247]\n"
    "ds_read_b128 a[228:231], v49 offset:24576\n"
    "v_mfma_f32_16x16x32_bf16 v[248:251], a[252:255], v[112:115], v[248:251]\n"
    "ds_read_b128 a[232:235], v50 offset:24576\n"
    "v_mfma_f32_16x16x32_bf16 v[252:255], a[252:255], v[116:119], v[252:255]\n"
    "ds_read_b128 a[236:239], v51 offset:24576\n"
    "buffer_load_dwordx4 v63, s[40:43], s48 offen offset:3072 nt lds\n"
    "s_cbranch_vccnz .Lrare3_%=\n"
    ".Lret3_%=:\n"
    "s_cmp_eq_u32 s73, 0\n"
    "s_cbranch_scc1 .Ljobs_%=\n"
    ".Ljobret_%=:\n"
    "s_add_u32 s73, s73, 1\n"
    "s_sub_u32 s51, s51, 1\n"
    "s_cmp_lg_u32 s51, 0\n"
    "s_cbranch_scc1 .Lloop_%=\n"
    "s_cmp_lg_u32 s74, 0\n"
    "s_cbranch_scc1 .Lpass1tail_%=\n"
    "s_lshl_b32 s75, %[w], 9\n"
    "s_add_u32 s75, s75, 4096\n"
    "buffer_load_dwordx2 v[46:47], v22, s[80:83], s75 offen sc1\n"
    "s_waitcnt lgkmcnt(8)\n"
    "v_mfma_f32_16x16x32_bf16 v[80:83], a[192:195], %[q0], v[96:99]\n"
    "v_exp_f32 v64, v64\n"
    "v_mfma_f32_16x16x32_bf16 v[84:87], a[192:195], %[q1], v[100:103]\n"
    "v_exp_f32 v65, v65\n"
    "v_mfma_f32_16x16x32_bf16 v[80:83], a[196:199], %[q2], v[80:83]\n"
    "v_exp_f32 v66, v66\n"
    "v_mfma_f32_16x16x32_bf16 v[84:87], a[196:199], %[q3], v[84:87]\n"
    "v_exp_f32 v67, v67\n"
    "v_mfma_f32_16x16x32_bf16 v[80:83], a[200:203], %[q4], v[80:83]\n"
    "v_exp_f32 v72, v72\n"
    "v_mfma_f32_16x16x32_bf16 v[84:87], a[200:203], %[q5], v[84:87]\n"
    "v_exp_f32 v73, v73\n"
    "v_mfma_f32_16x16x32_bf16 v[80:83], a[204:207], %[q6], v[80:83]\n"
    "v_exp_f32 v74, v74\n"
    "v_mfma_f32_16x16x32_bf16 v[84:87], a[204:207], %[q7], v[84:87]\n"
    "v_exp_f32 v75, v75\n"
    "s_waitcnt lgkmcnt(4)\n"
    "v_mfma_f32_16x16x32_bf16 v[80:83], a[208:211], %[q8], v[80:83]\n"
    "v_exp_f32 v68, v68\n"
    "v_mfma_f32_16x16x32_bf16 v[84:87], a[208:211], %[q9], v[84:87]\n"
    "v_exp_f32 v69, v69\n"
    "v_mfma_f32_16x16x32_bf16 v[80:83], a[212:215], %[q10], v[80:83]\n"
    "v_exp_f32 v70, v70\n"
    "v_mfma_f32_16x16x32_bf16 v[84:87], a[212:215], %[q11], v[84:87]\n"
    "v_exp_f32 v71, v71\n"
    "v_mfma_f32_16x16x32_bf16 v[80:83], a[216:219], %[q12], v[80:83]\n"
    "v_exp_f32 v76, v76\n"
    "v_mfma_f32_16x16x32_bf16 v[84:87], a[216:219], %[q13], v[84:87]\n"
    "v_exp_f32 v77, v77\n"
    "v_mfma_f32_16x16x32_bf16 v[80:83], a[220:223], %[q14], v[80:83]\n"
    "v_exp_f32 v78, v78\n"
    "v_mfma_f32_16x16x32_bf16 v[84:87], a[220:223], %[q15], v[84:87]\n"
    "v_exp_f32 v79, v79\n"
    "s_waitcnt lgkmcnt(0)\n"
    "v_mfma_f32_16x16x32_bf16 v[88:91], a[224:227], %[q0], v[96:99]\n"
    "ds_read_b128 a[240:243], v48 offset:24832\n"
    "v_cvt_pk_bf16_f32 v112, v64, v65\n"
    "v_mfma_f32_16x16x32_bf16 v[92:95], a[224:227], %[q1], v[100:103]\n"
    "ds_read_b128 a[244:247], v49 offset:24832\n"
    "v_cvt_pk_bf16_f32 v113, v66, v67\n"
    "v_mfma_f32_16x16x32_bf16 v[88:91], a[228:231], %[q2], v[88:91]\n"
    "ds_read_b128 a[248:251], v50 offset:24832\n"
    "v_cvt_pk_bf16_f32 v114, v72, v73\n"
    "v_mfma_f32_16x16x32_bf16 v[92:95], a[228:231], %[q3], v[92:95]\n"
    "ds_read_b128 a[252:255], v51 offset:24832\n"
    "v_cvt_pk_bf16_f32 v115, v74, v75\n"
    "v_mfma_f32_16x16x32_bf16 v[88:91], a[232:235], %[q4], v[88:91]\n"
    "ds_read_b64_tr_b16 a[192:193], v52 offset:0\n"
    "v_cvt_pk_bf16_f32 v116, v68, v69\n"
    "v_mfma_f32_16x16x32_bf16 v[92:95], a[232:235], %[q5], v[92:95]\n"
    "ds_read_b64_tr_b16 a[194:195], v52 offset:8192\n"
    "v_cvt_pk_bf16_f32 v117, v70, v71\n"
    "v_mfma_f32_16x16x32_bf16 v[88:91], a[236:239], %[q6], v[88:91]\n"
    "ds_read_b64_tr_b16 a[196:197], v53 offset:0\n"
    "v_cvt_pk_bf16_f32 v118, v76, v77\n"
    "v_mfma_f32_16x16x32_bf16 v[92:95], a[236:239], %[q7], v[92:95]\n"
    "ds_read_b64_tr_b16 a[198:199], v53 offset:8192\n"
    "v_cvt_pk_bf16_f32 v119, v78, v79\n"
    "s_waitcnt lgkmcnt(4)\n"
    "v_mfma_f32_16x16x32_bf16 v[88:91], a[240:243], %[q8], v[88:91]\n"
    "ds_read_b64_tr_b16 a[200:201], v54 offset:0\n"
    "v_add_f32 v26, v26, v64\n"
    "v_mfma_f32_16x16x32_bf16 v[92:95], a[240:243], %[q9], v[92:95]\n"
    "ds_read_b64_tr_b16 a[202:203], v54 offset:8192\n"
    "v_add_f32 v27, v27, v65\n"
    "v_mfma_f32_16x16x32_bf16 v[88:91], a[244:247], %[q10], v[88:91]\n"
    "ds_read_b64_tr_b16 a[204:205], v55 offset:0\n"
    "v_add_f32 v26, v26, v66\n"
    "v_mfma_f32_16x16x32_bf16 v[92:95], a[244:247], %[q11], v[92:95]\n"
    "ds_read_b64_tr_b16 a[206:207], v55 offset:8192\n"
    "v_add_f32 v27, v27, v67\n"
    "v_mfma_f32_16x16x32_bf16 v[88:91], a[248:251], %[q12], v[88:91]\n"
    "ds_read_b64_tr_b16 a[208:209], v56 offset:0\n"
    "v_add_f32 v26, v26, v72\n"
    "v_mfma_f32_16x16x32_bf16 v[92:95], a[248:251], %[q13], v[92:95]\n"
    "ds_read_b64_tr_b16 a[210:211], v56 offset:8192\n"
    "v_add_f32 v27, v27, v73\n"
    "v_mfma_f32_16x16x32_bf16 v[88:91], a[252:255], %[q14], v[88:91]\n"
    "ds_read_b64_tr_b16 a[212:213], v57 offset:0\n"
    "v_add_f32 v26, v26, v74\n"
    "v_mfma_f32_16x16x32_bf16 v[92:95], a[252:255], %[q15], v[92:95]\n"
    "ds_read_b64_tr_b16 a[214:215], v57 offset:8192\n"
    "v_add_f32 v27, v27, v75\n"
    "s_waitcnt lgkmcnt(8)\n"
    "v_mfma_f32_16x16x32_bf16 v[128:131], a[192:195], v[112:115], v[128:131]\n"
    "ds_read_b64_tr_b16 a[216:217], v58 offset:0\n"
    "v_add_f32 v28, v28, v68\n"
    "v_mfma_f32_16x16x32_bf16 v[132:135], a[192:195], v[116:119], v[132:135]\n"
    "ds_read_b64_tr_b16 a[218:219], v58 offset:8192\n"
    "v_add_f32 v29, v29, v69\n"
    "v_mfma_f32_16x16x32_bf16 v[136:139], a[196:199], v[112:115], v[136:139]\n"
    "ds_read_b64_tr_b16 a[220:221], v59 offset:0\n"
    "v_add_f32 v28, v28, v70\n"
    "v_mfma_f32_16x16x32_bf16 v[140:143], a[196:199], v[116:119], v[140:143]\n"
    "ds_read_b64_tr_b16 a[222:223], v59 offset:8192\n"
    "v_add_f32 v29, v29, v71\n"
    "s_waitcnt lgkmcnt(8)\n"
    "v_mfma_f32_16x16x32_bf16 v[144:147], a[200:203], v[112:115], v[144:147]\n"
    "ds_read_b64_tr_b16 a[224:225], v52 offset:256\n"
    "v_add_f32 v28, v28, v76\n"
    "v_mfma_f32_16x16x32_bf16 v[148:151], a[200:203], v[116:119], v[148:151]\n"
    "ds_read_b64_tr_b16 a[226:227], v52 offset:8448\n"
    "v_add_f32 v29, v29, v77\n"
    "v_mfma_f32_16x16x32_bf16 v[152:155], a[204:207], v[112:115], v[152:155]\n"
    "ds_read_b64_tr_b16 a[228:229], v53 offset:256\n"
    "v_add_f32 v28, v28, v78\n"
    "v_mfma_f32_16x16x32_bf16 v[156:159], a[204:207], v[116:119], v[156:159]\n"
    "ds_read_b64_tr_b16 a[230:231], v53 offset:8448\n"
    "v_add_f32 v29, v29, v79\n"
    "s_waitcnt lgkmcnt(8)\n"
    "v_mfma_f32_16x16x32_bf16 v[160:163], a[208:211], v[112:115], v[160:163]\n"
    "ds_read_b64_tr_b16 a[232:233], v54 offset:256\n"
    "s_waitcnt vmcnt(13)\n"
    "s_barrier\n"
    "s_add_u32 s48, s48, 16384\n"
    "v_mfma_f32_16x16x32_bf16 v[164:167], a[208:211], v[116:119], v[164:167]\n"
    "ds_read_b64_tr_b16 a[234:235], v54 offset:8448\n"
    "v_max3_f32 v110, v80, v81, v82\n"
    "s_add_u32 m0, s50, 147456\n"
    "v_mfma_f32_16x16x32_bf16 v[168:171], a[212:215], v[112:115], v[168:171]\n"
    "ds_read_b64_tr_b16 a[236:237], v55 offset:256\n"
    "buffer_load_dwordx4 %[vov0], s[44:47], s48 offen offset:0 nt lds\n"
    "v_mfma_f32_16x16x32_bf16 v[172:175], a[212:215], v[116:119], v[172:175]\n"
    "ds_read_b64_tr_b16 a[238:239], v55 offset:8448\n"
    "v_max3_f32 v110, v110, v83, v84\n"
    "s_waitcnt lgkmcnt(8)\n"
    "v_mfma_f32_16x16x32_bf16 v[176:179], a[216:219], v[112:115], v[176:179]\n"
    "ds_read_b64_tr_b16 a[240:241], v56 offset:256\n"
    "v_max3_f32 v110, v110, v85, v86\n"
    "v_mfma_f32_16x16x32_bf16 v[180:183], a[216:219], v[116:119], v[180:183]\n"
    "ds_read_b64_tr_b16 a[242:243], v56 offset:8448\n"
    "buffer_load_dwordx4 %[vov1], s[44:47], s48 offen offset:1024 nt lds\n"
    "v_mfma_f32_16x16x32_bf16 v[184:187], a[220:223], v[112:115], v[184:187]\n"
    "ds_read_b64_tr_b16 a[244:245], v57 offset:256\n"
    "v_max3_f32 v110, v110, v87, v88\n"
    "v_mfma_f32_16x16x32_bf16 v[188:191], a[220:223], v[116:119], v[188:191]\n"
    "ds_read_b64_tr_b16 a[246:247], v57 offset:8448\n"
    "v_max3_f32 v110, v110, v89, v90\n"
    "s_waitcnt lgkmcnt(8)\n"
    "v_mfma_f32_16x16x32_bf16 v[192:195], a[224:227], v[112:115], v[192:195]\n"
    "ds_read_b64_tr_b16 a[248:249], v58 offset:256\n"
    "buffer_load_dwordx4 %[vov2], s[44:47], s48 offen offset:2048 nt lds\n"
    "v_mfma_f32_16x16x32_bf16 v[196:199], a[224:227], v[116:119], v[196:199]\n"
    "ds_read_b64_tr_b16 a[250:251], v58 offset:8448\n"
    "v_max3_f32 v110, v110, v91, v92\n"
    "v_mfma_f32_16x16x32_bf16 v[200:203], a[228:231], v[112:115], v[200:203]\n"
    "ds_read_b64_tr_b16 a[252:253], v59 offset:256\n"
    "v_max3_f32 v110, v110, v93, v94\n"
    "v_mfma_f32_16x16x32_bf16 v[204:207], a[228:231], v[116:119], v[204:207]\n"
    "ds_read_b64_tr_b16 a[254:255], v59 offset:8448\n"
    "buffer_load_dwordx4 %[vov3], s[44:47], s48 offen offset:3072 nt lds\n"
    "s_waitcnt lgkmcnt(8)\n"
    "v_mfma_f32_16x16x32_bf16 v[208:211], a[232:235], v[112:115], v[208:211]\n"
    "ds_read_b128 a[192:195], v48 offset:32768\n"
    "v_max_f32 v110, v110, v95\n"
    "v_mfma_f32_16x16x32_bf16 v[212:215], a[232:235], v[116:119], v[212:215]\n"
    "ds_read_b128 a[196:199], v49 offset:32768\n"
    "s_add_u32 m0, s50, 49152\n"
    "v_mfma_f32_16x16x32_bf16 v[216:219], a[236:239], v[112:115], v[216:219]\n"
    "ds_read_b128 a[200:203], v50 offset:32768\n"
    "buffer_load_dwordx4 v60, s[40:43], s48 offen offset:0 nt lds\n"
    "v_mfma_f32_16x16x32_bf16 v[220:223], a[236:239], v[116:119], v[220:223]\n"
    "ds_read_b128 a[204:207], v51 offset:32768\n"
    "v_cmp_lt_f32 vcc, s79, v110\n"
    "s_waitcnt lgkmcnt(8)\n"
    "v_mfma_f32_16x16x32_bf16 v[224:227], a[240:243], v[112:115], v[224:227]\n"
    "ds_read_b128 a[208:211], v48 offset:33024\n"
    "v_mfma_f32_16x16x32_bf16 v[228:231], a[240:243], v[116:119], v[228:231]\n"
    "ds_read_b128 a[212:215], v49 offset:33024\n"
    "buffer_load_dwordx4 v61, s[40:43], s48 offen offset:1024 nt lds\n"
    "v_mfma_f32_16x16x32_bf16 v[232:235], a[244:247], v[112:115], v[232:235]\n"
    "ds_read_b128 a[216:219], v50 offset:33024\n"
    "v_mfma_f32_16x16x32_bf16 v[236:239], a[244:247], v[116:119], v[236:239]\n"
    "ds_read_b128 a[220:223], v51 offset:33024\n"
    "s_waitcnt lgkmcnt(8)\n"
    "v_mfma_f32_16x16x32_bf16 v[240:243], a[248:251], v[112:115], v[240:243]\n"
    "ds_read_b128 a[224:227], v48 offset:40960\n"
    "buffer_load_dwordx4 v62, s[40:43], s48 offen offset:2048 nt lds\n"
    "v_mfma_f32_16x16x32_bf16 v[244:247], a[248:251], v[116:119], v[244:247]\n"
    "ds_read_b128 a[228:231], v49 offset:40960\n"
    "v_mfma_f32_16x16x32_bf16 v[248:251], a[252:255], v[112:115], v[248:251]\n"
    "ds_read_b128 a[232:235], v50 offset:40960\n"
    "v_mfma_f32_16x16x32_bf16 v[252:255], a[252:255], v[116:119], v[252:255]\n"
    "ds_read_b128 a[236:239], v51 offset:40960\n"
    "buffer_load_dwordx4 v63, s[40:43], s48 offen offset:3072 nt lds\n"
    "s_cbranch_vccnz .LrareP0T24_%=\n"
    ".LretP0T24_%=:\n"
    "s_waitcnt lgkmcnt(8)\n"
    "v_mfma_f32_16x16x32_bf16 v[64:67], a[192:195], %[q0], v[96:99]\n"
    "v_exp_f32 v80, v80\n"
    "v_mfma_f32_16x16x32_bf16 v[68:71], a[192:195], %[q1], v[100:103]\n"
    "v_exp_f32 v81, v81\n"
    "v_mfma_f32_16x16x32_bf16 v[64:67], a[196:199], %[q2], v[64:67]\n"
    "v_exp_f32 v82, v82\n"
    "v_mfma_f32_16x16x32_bf16 v[68:71], a[196:199], %[q3], v[68:71]\n"
    "v_exp_f32 v83, v83\n"
    "v_mfma_f32_16x16x32_bf16 v[64:67], a[200:203], %[q4], v[64:67]\n"
    "v_exp_f32 v88, v88\n"
    "v_mfma_f32_16x16x32_bf16 v[68:71], a[200:203], %[q5], v[68:71]\n"
    "v_exp_f32 v89, v89\n"
    "v_mfma_f32_16x16x32_bf16 v[64:67], a[204:207], %[q6], v[64:67]\n"
    "v_exp_f32 v90, v90\n"
    "v_mfma_f32_16x16x32_bf16 v[68:71], a[204:207], %[q7], v[68:71]\n"
    "v_exp_f32 v91, v91\n"
    "s_waitcnt lgkmcnt(4)\n"
    "v_mfma_f32_16x16x32_bf16 v[64:67], a[208:211], %[q8], v[64:67]\n"
    "v_exp_f32 v84, v84\n"
    "v_mfma_f32_16x16x32_bf16 v[68:71], a[208:211], %[q9], v[68:71]\n"
    "v_exp_f32 v85, v85\n"
    "v_mfma_f32_16x16x32_bf16 v[64:67], a[212:215], %[q10], v[64:67]\n"
    "v_exp_f32 v86, v86\n"
    "v_mfma_f32_16x16x32_bf16 v[68:71], a[212:215], %[q11], v[68:71]\n"
    "v_exp_f32 v87, v87\n"
    "v_mfma_f32_16x16x32_bf16 v[64:67], a[216:219], %[q12], v[64:67]\n"
    "v_exp_f32 v92, v92\n"
    "v_mfma_f32_16x16x32_bf16 v[68:71], a[216:219], %[q13], v[68:71]\n"
    "v_exp_f32 v93, v93\n"
    "v_mfma_f32_16x16x32_bf16 v[64:67], a[220:223], %[q14], v[64:67]\n"
    "v_exp_f32 v94, v94\n"
    "v_mfma_f32_16x16x32_bf16 v[68:71], a[220:223], %[q15], v[68:71]\n"
    "v_exp_f32 v95, v95\n"
    "s_waitcnt lgkmcnt(0)\n"
    "v_mfma_f32_16x16x32_bf16 v[72:75], a[224:227], %[q0], v[96:99]\n"
    "ds_read_b128 a[240:243], v48 offset:41216\n"
    "v_cvt_pk_bf16_f32 v112, v80, v81\n"
    "v_mfma_f32_16x16x32_bf16 v[76:79], a[224:227], %[q1], v[100:103]\n"
    "ds_read_b128 a[244:247], v49 offset:41216\n"
    "v_cvt_pk_bf16_f32 v113, v82, v83\n"
    "v_mfma_f32_16x16x32_bf16 v[72:75], a[228:231], %[q2], v[72:75]\n"
    "ds_read_b128 a[248:251], v50 offset:41216\n"
    "v_cvt_pk_bf16_f32 v114, v88, v89\n"
    "v_mfma_f32_16x16x32_bf16 v[76:79], a[228:231], %[q3], v[76:79]\n"
    "ds_read_b128 a[252:255], v51 offset:41216\n"
    "v_cvt_pk_bf16_f32 v115, v90, v91\n"
    "v_mfma_f32_16x16x32_bf16 v[72:75], a[232:235], %[q4], v[72:75]\n"
    "ds_read_b64_tr_b16 a[192:193], v52 offset:16384\n"
    "v_cvt_pk_bf16_f32 v116, v84, v85\n"
    "v_mfma_f32_16x16x32_bf16 v[76:79], a[232:235], %[q5], v[76:79]\n"
    "ds_read_b64_tr_b16 a[194:195], v52 offset:24576\n"
    "v_cvt_pk_bf16_f32 v117, v86, v87\n"
    "v_mfma_f32_16x16x32_bf16 v[72:75], a[236:239], %[q6], v[72:75]\n"
    "ds_read_b64_tr_b16 a[196:197], v53 offset:16384\n"
    "v_cvt_pk_bf16_f32 v118, v92, v93\n"
    "v_mfma_f32_16x16x32_bf16 v[76:79], a[236:239], %[q7], v[76:79]\n"
    "ds_read_b64_tr_b16 a[198:199], v53 offset:24576\n"
    "v_cvt_pk_bf16_f32 v119, v94, v95\n"
    "s_waitcnt lgkmcnt(4)\n"
    "v_mfma_f32_16x16x32_bf16 v[72:75], a[240:243], %[q8], v[72:75]\n"
    "ds_read_b64_tr_b16 a[200:201], v54 offset:16384\n"
    "v_add_f32 v26, v26, v80\n"
    "v_mfma_f32_16x16x32_bf16 v[76:79], a[240:243], %[q9], v[76:79]\n"
    "ds_read_b64_tr_b16 a[202:203], v54 offset:24576\n"
    "v_add_f32 v27, v27, v81\n"
    "v_mfma_f32_16x16x32_bf16 v[72:75], a[244:247], %[q10], v[72:75]\n"
    "ds_read_b64_tr_b16 a[204:205], v55 offset:16384\n"
    "v_add_f32 v26, v26, v82\n"
    "v_mfma_f32_16x16x32_bf16 v[76:79], a[244:247], %[q11], v[76:79]\n"
    "ds_read_b64_tr_b16 a[206:207], v55 offset:24576\n"
    "v_add_f32 v27, v27, v83\n"
    "v_mfma_f32_16x16x32_bf16 v[72:75], a[248:251], %[q12], v[72:75]\n"
    "ds_read_b64_tr_b16 a[208:209], v56 offset:16384\n"
    "v_add_f32 v26, v26, v88\n"
    "v_mfma_f32_16x16x32_bf16 v[76:79], a[248:251], %[q13], v[76:79]\n"
    "ds_read_b64_tr_b16 a[210:211], v56 offset:24576\n"
    "v_add_f32 v27, v27, v89\n"
    "v_mfma_f32_16x16x32_bf16 v[72:75], a[252:255], %[q14], v[72:75]\n"
    "ds_read_b64_tr_b16 a[212:213], v57 offset:16384\n"
    "v_add_f32 v26, v26, v90\n"
    "s_waitcnt vmcnt(8)\n"
    "v_cmp_eq_u64 s[52:53], v[46:47], v[20:21]\n"
    "s_cmp_eq_u64 s[52:53], -1\n"
    "s_cbranch_scc1 .Lt1_%=\n"
    "s_mov_b32 s38, 0\n"
    ".Lt2_%=:\n"
    "s_sleep 2\n"
    "buffer_load_dwordx2 v[46:47], v22, s[80:83], s75 offen sc1\n"
    "s_waitcnt vmcnt(0)\n"
    "v_cmp_eq_u64 s[52:53], v[46:47], v[20:21]\n"
    "s_cmp_eq_u64 s[52:53], -1\n"
    "s_cbranch_scc1 .Lt1_%=\n"
    "s_add_u32 s38, s38, 1\n"
    "s_cmp_lt_u32 s38, 0x200000\n"
    "s_cbranch_scc1 .Lt2_%=\n"
    ".Lt1_%=:\n"
    "v_mfma_f32_16x16x32_bf16 v[76:79], a[252:255], %[q15], v[76:79]\n"
    "ds_read_b64_tr_b16 a[214:215], v57 offset:24576\n"
    "v_add_f32 v27, v27, v91\n"
    "s_waitcnt lgkmcnt(8)\n"
    "v_mfma_f32_16x16x32_bf16 v[128:131], a[192:195], v[112:115], v[128:131]\n"
    "ds_read_b64_tr_b16 a[216:217], v58 offset:16384\n"
    "v_add_f32 v28, v28, v84\n"
    "v_mfma_f32_16x16x32_bf16 v[132:135], a[192:195], v[116:119], v[132:135]\n"
    "ds_read_b64_tr_b16 a[218:219], v58 offset:24576\n"
    "v_add_f32 v29, v29, v85\n"
    "v_mfma_f32_16x16x32_bf16 v[136:139], a[196:199], v[112:115], v[136:139]\n"
    "ds_read_b64_tr_b16 a[220:221], v59 offset:16384\n"
    "v_add_f32 v28, v28, v86\n"
    "v_mfma_f32_16x16x32_bf16 v[140:143], a[196:199], v[116:119], v[140:143]\n"
    "ds_read_b64_tr_b16 a[222:223], v59 offset:24576\n"
    "v_add_f32 v29, v29, v87\n"
    "s_waitcnt lgkmcnt(8)\n"
    "v_mfma_f32_16x16x32_bf16 v[144:147], a[200:203], v[112:115], v[144:147]\n"
    "ds_read_b64_tr_b16 a[224:225], v52 offset:16640\n"
    "v_add_f32 v28, v28, v92\n"
    "v_mfma_f32_16x16x32_bf16 v[148:151], a[200:203], v[116:119], v[148:151]\n"
    "ds_read_b64_tr_b16 a[226:227], v52 offset:24832\n"
    "v_add_f32 v29, v29, v93\n"
    "v_mfma_f32_16x16x32_bf16 v[152:155], a[204:207], v[112:115], v[152:155]\n"
    "ds_read_b64_tr_b16 a[228:229], v53 offset:16640\n"
    "v_add_f32 v28, v28, v94\n"
    "v_mfma_f32_16x16x32_bf16 v[156:159], a[204:207], v[116:119], v[156:159]\n"
    "ds_read_b64_tr_b16 a[230:231], v53 offset:24832\n"
    "v_add_f32 v29, v29, v95\n"
    "s_waitcnt lgkmcnt(8)\n"
    "v_mfma_f32_16x16x32_bf16 v[160:163], a[208:211], v[112:115], v[160:163]\n"
    "ds_read_b64_tr_b16 a[232:233], v54 offset:16640\n"
    "s_barrier\n"
    "s_add_u32 s48, s48, 16384\n"
    "v_mfma_f32_16x16x32_bf16 v[164:167], a[208:211], v[116:119], v[164:167]\n"
    "ds_read_b64_tr_b16 a[234:235], v54 offset:24832\n"
    "v_max3_f32 v110, v64, v65, v66\n"
    "s_add_u32 m0, s50, 98304\n"
    "v_mfma_f32_16x16x32_bf16 v[168:171], a[212:215], v[112:115], v[168:171]\n"
    "ds_read_b64_tr_b16 a[236:237], v55 offset:16640\n"
    "buffer_load_dwordx4 %[vov0], s[44:47], s48 offen offset:0 nt lds\n"
    "v_mfma_f32_16x16x32_bf16 v[172:175], a[212:215], v[116:119], v[172:175]\n"
    "ds_read_b64_tr_b16 a[238:239], v55 offset:24832\n"
    "v_max3_f32 v110, v110, v67, v68\n"
    "s_waitcnt lgkmcnt(8)\n"
    "v_mfma_f32_16x16x32_bf16 v[176:179], a[216:219], v[112:115], v[176:179]\n"
    "ds_read_b64_tr_b16 a[240:241], v56 offset:16640\n"
    "v_max3_f32 v110, v110, v69, v70\n"
    "v_mfma_f32_16x16x32_bf16 v[180:183], a[216:219], v[116:119], v[180:183]\n"
    "ds_read_b64_tr_b16 a[242:243], v56 offset:24832\n"
    "buffer_load_dwordx4 %[vov1], s[44:47], s48 offen offset:1024 nt lds\n"
    "v_mfma_f32_16x16x32_bf16 v[184:187], a[220:223], v[112:115], v[184:187]\n"
    "ds_read_b64_tr_b16 a[244:245], v57 offset:16640\n"
    "v_max3_f32 v110, v110, v71, v72\n"
    "v_mfma_f32_16x16x32_bf16 v[188:191], a[220:223], v[116:119], v[188:191]\n"
    "ds_read_b64_tr_b16 a[246:247], v57 offset:24832\n"
    "v_max3_f32 v110, v110, v73, v74\n"
    "s_waitcnt lgkmcnt(8)\n"
    "v_mfma_f32_16x16x32_bf16 v[192:195], a[224:227], v[112:115], v[192:195]\n"
    "ds_read_b64_tr_b16 a[248:249], v58 offset:16640\n"
    "buffer_load_dwordx4 %[vov2], s[44:47], s48 offen offset:2048 nt lds\n"
    "v_mfma_f32_16x16x32_bf16 v[196:199], a[224:227], v[116:119], v[196:199]\n"
    "ds_read_b64_tr_b16 a[250:251], v58 offset:24832\n"
    "v_max3_f32 v110, v110, v75, v76\n"
    "v_mfma_f32_16x16x32_bf16 v[200:203], a[228:231], v[112:115], v[200:203]\n"
    "ds_read_b64_tr_b16 a[252:253], v59 offset:16640\n"
    "v_max3_f32 v110, v110, v77, v78\n"
    "v_mfma_f32_16x16x32_bf16 v[204:207], a[228:231], v[116:119], v[204:207]\n"
    "ds_read_b64_tr_b16 a[254:255], v59 offset:24832\n"
    "buffer_load_dwordx4 %[vov3], s[44:47], s48 offen offset:3072 nt lds\n"
    "s_waitcnt lgkmcnt(8)\n"
    "v_mfma_f32_16x16x32_bf16 v[208:211], a[232:235], v[112:115], v[208:211]\n"
    "ds_read_b128 a[192:195], v48 offset:49152\n"
    "v_max_f32 v110, v110, v79\n"
    "v_mfma_f32_16x16x32_bf16 v[212:215], a[232:235], v[116:119], v[212:215]\n"
    "ds_read_b128 a[196:199], v49 offset:49152\n"
    "v_mfma_f32_16x16x32_bf16 v[216:219], a[236:239], v[112:115], v[216:219]\n"
    "ds_read_b128 a[200:203], v50 offset:49152\n"
    "s_add_u32 m0, s50, 0\n"
    "s_add_u32 s49, %[rot], 0\n"
    "s_and_b32 s49, s49, 15\n"
    "s_mul_i32 s49, s49, 14336\n"
    "buffer_load_dwordx4 v34, s[56:59], s49 offen offset:0 sc1 lds\n"
    "v_mfma_f32_16x16x32_bf16 v[220:223], a[236:239], v[116:119], v[220:223]\n"
    "ds_read_b128 a[204:207], v51 offset:49152\n"
    "v_cmp_lt_f32 vcc, s79, v110\n"
    "s_waitcnt lgkmcnt(8)\n"
    "v_mfma_f32_16x16x32_bf16 v[224:227], a[240:243], v[112:115], v[224:227]\n"
    "ds_read_b128 a[208:211], v48 offset:49408\n"
    "v_mfma_f32_16x16x32_bf16 v[228:231], a[240:243], v[116:119], v[228:231]\n"
    "ds_read_b128 a[212:215], v49 offset:49408\n"
    "buffer_load_dwordx4 v35, s[56:59], s49 offen offset:1024 sc1 lds\n"
    "v_mfma_f32_16x16x32_bf16 v[232:235], a[244:247], v[112:115], v[232:235]\n"
    "ds_read_b128 a[216:219], v50 offset:49408\n"
    "v_mfma_f32_16x16x32_bf16 v[236:239], a[244:247], v[116:119], v[236:239]\n"
    "ds_read_b128 a[220:223], v51 offset:49408\n"
    "s_waitcnt lgkmcnt(8)\n"
    "v_mfma_f32_16x16x32_bf16 v[240:243], a[248:251], v[112:115], v[240:243]\n"
    "ds_read_b128 a[224:227], v48 offset:57344\n"
    "buffer_load_dwordx4 v36, s[56:59], s49 offen offset:2048 sc1 lds\n"
    "v_mfma_f32_16x16x32_bf16 v[244:247], a[248:251], v[116:119], v[244:247]\n"
    "ds_read_b128 a[228:231], v49 offset:57344\n"
    "v_mfma_f32_16x16x32_bf16 v[248:251], a[252:255], v[112:115], v[248:251]\n"
    "ds_read_b128 a[232:235], v50 offset:57344\n"
    "v_mfma_f32_16x16x32_bf16 v[252:255], a[252:255], v[116:119], v[252:255]\n"
    "ds_read_b128 a[236:239], v51 offset:57344\n"
    "buffer_load_dwordx4 v37, s[56:59], s49 offen offset:3072 sc1 lds\n"
    "s_cbranch_vccnz .LrareP0T25_%=\n"
    ".LretP0T25_%=:\n"
    "s_waitcnt lgkmcnt(8)\n"
    "v_mfma_f32_16x16x32_bf16 v[80:83], a[192:195], %[q0], v[96:99]\n"
    "v_exp_f32 v64, v64\n"
    "v_mfma_f32_16x16x32_bf16 v[84:87], a[192:195], %[q1], v[100:103]\n"
    "v_exp_f32 v65, v65\n"
    "v_mfma_f32_16x16x32_bf16 v[80:83], a[196:199], %[q2], v[80:83]\n"
    "v_exp_f32 v66, v66\n"
    "v_mfma_f32_16x16x32_bf16 v[84:87], a[196:199], %[q3], v[84:87]\n"
    "v_exp_f32 v67, v67\n"
    "v_mfma_f32_16x16x32_bf16 v[80:83], a[200:203], %[q4], v[80:83]\n"
    "v_exp_f32 v72, v72\n"
    "v_mfma_f32_16x16x32_bf16 v[84:87], a[200:203], %[q5], v[84:87]\n"
    "v_exp_f32 v73, v73\n"
    "v_mfma_f32_16x16x32_bf16 v[80:83], a[204:207], %[q6], v[80:83]\n"
    "v_exp_f32 v74, v74\n"
    "v_mfma_f32_16x16x32_bf16 v[84:87], a[204:207], %[q7], v[84:87]\n"
    "v_exp_f32 v75, v75\n"
    "s_waitcnt lgkmcnt(4)\n"
    "v_mfma_f32_16x16x32_bf16 v[80:83], a[208:211], %[q8], v[80:83]\n"
    "v_exp_f32 v68, v68\n"
    "v_mfma_f32_16x16x32_bf16 v[84:87], a[208:211], %[q9], v[84:87]\n"
    "v_exp_f32 v69, v69\n"
    "v_mfma_f32_16x16x32_bf16 v[80:83], a[212:215], %[q10], v[80:83]\n"
    "v_exp_f32 v70, v70\n"
    "v_mfma_f32_16x16x32_bf16 v[84:87], a[212:215], %[q11], v[84:87]\n"
    "v_exp_f32 v71, v71\n"
    "v_mfma_f32_16x16x32_bf16 v[80:83], a[216:219], %[q12], v[80:83]\n"
    "v_exp_f32 v76, v76\n"
    "v_mfma_f32_16x16x32_bf16 v[84:87], a[216:219], %[q13], v[84:87]\n"
    "v_exp_f32 v77, v77\n"
    "v_mfma_f32_16x16x32_bf16 v[80:83], a[220:223], %[q14], v[80:83]\n"
    "v_exp_f32 v78, v78\n"
    "v_mfma_f32_16x16x32_bf16 v[84:87], a[220:223], %[q15], v[84:87]\n"
    "v_exp_f32 v79, v79\n"
    "s_waitcnt lgkmcnt(0)\n"
    "v_mfma_f32_16x16x32_bf16 v[88:91], a[224:227], %[q0], v[96:99]\n"
    "ds_read_b128 a[240:243], v48 offset:57600\n"
    "v_cvt_pk_bf16_f32 v112, v64, v65\n"
    "v_mfma_f32_16x16x32_bf16 v[92:95], a[224:227], %[q1], v[100:103]\n"
    "ds_read_b128 a[244:247], v49 offset:57600\n"
    "v_cvt_pk_bf16_f32 v113, v66, v67\n"
    "v_mfma_f32_16x16x32_bf16 v[88:91], a[228:231], %[q2], v[88:91]\n"
    "ds_read_b128 a[248:251], v50 offset:57600\n"
    "v_cvt_pk_bf16_f32 v114, v72, v73\n"
    "v_mfma_f32_16x16x32_bf16 v[92:95], a[228:231], %[q3], v[92:95]\n"
    "ds_read_b128 a[252:255], v51 offset:57600\n"
    "v_cvt_pk_bf16_f32 v115, v74, v75\n"
    "v_mfma_f32_16x16x32_bf16 v[88:91], a[232:235], %[q4], v[88:91]\n"
    "ds_read_b64_tr_b16 a[192:193], v52 offset:32768\n"
    "v_cvt_pk_bf16_f32 v116, v68, v69\n"
    "v_mfma_f32_16x16x32_bf16 v[92:95], a[232:235], %[q5], v[92:95]\n"
    "ds_read_b64_tr_b16 a[194:195], v52 offset:40960\n"
    "v_cvt_pk_bf16_f32 v117, v70, v71\n"
    "v_mfma_f32_16x16x32_bf16 v[88:91], a[236:239], %[q6], v[88:91]\n"
    "ds_read_b64_tr_b16 a[196:197], v53 offset:32768\n"
    "v_cvt_pk_bf16_f32 v118, v76, v77\n"
    "v_mfma_f32_16x16x32_bf16 v[92:95], a[236:239], %[q7], v[92:95]\n"
    "ds_read_b64_tr_b16 a[198:199], v53 offset:40960\n"
    "v_cvt_pk_bf16_f32 v119, v78, v79\n"
    "s_waitcnt lgkmcnt(4)\n"
    "v_mfma_f32_16x16x32_bf16 v[88:91], a[240:243], %[q8], v[88:91]\n"
    "ds_read_b64_tr_b16 a[200:201], v54 offset:32768\n"
    "v_add_f32 v26, v26, v64\n"
    "v_mfma_f32_16x16x32_bf16 v[92:95], a[240:243], %[q9], v[92:95]\n"
    "ds_read_b64_tr_b16 a[202:203], v54 offset:40960\n"
    "v_add_f32 v27, v27, v65\n"
    "v_mfma_f32_16x16x32_bf16 v[88:91], a[244:247], %[q10], v[88:91]\n"
    "ds_read_b64_tr_b16 a[204:205], v55 offset:32768\n"
    "v_add_f32 v26, v26, v66\n"
    "v_mfma_f32_16x16x32_bf16 v[92:95], a[244:247], %[q11], v[92:95]\n"
    "ds_read_b64_tr_b16 a[206:207], v55 offset:40960\n"
    "v_add_f32 v27, v27, v67\n"
    "v_mfma_f32_16x16x32_bf16 v[88:91], a[248:251], %[q12], v[88:91]\n"
    "ds_read_b64_tr_b16 a[208:209], v56 offset:32768\n"
    "v_add_f32 v26, v26, v72\n"
    "v_mfma_f32_16x16x32_bf16 v[92:95], a[248:251], %[q13], v[92:95]\n"
    "ds_read_b64_tr_b16 a[210:211], v56 offset:40960\n"
    "v_add_f32 v27, v27, v73\n"
    "v_mfma_f32_16x16x32_bf16 v[88:91], a[252:255], %[q14], v[88:91]\n"
    "ds_read_b64_tr_b16 a[212:213], v57 offset:32768\n"
    "v_add_f32 v26, v26, v74\n"
    "v_mfma_f32_16x16x32_bf16 v[92:95], a[252:255], %[q15], v[92:95]\n"
    "ds_read_b64_tr_b16 a[214:215], v57 offset:40960\n"
    "v_add_f32 v27, v27, v75\n"
    "s_waitcnt lgkmcnt(8)\n"
    "v_mfma_f32_16x16x32_bf16 v[128:131], a[192:195], v[112:115], v[128:131]\n"
    "ds_read_b64_tr_b16 a[216:217], v58 offset:32768\n"
    "v_add_f32 v28, v28, v68\n"
    "v_mfma_f32_16x16x32_bf16 v[132:135], a[192:195], v[116:119], v[132:135]\n"
    "ds_read_b64_tr_b16 a[218:219], v58 offset:40960\n"
    "v_add_f32 v29, v29, v69\n"
    "v_mfma_f32_16x16x32_bf16 v[136:139], a[196:199], v[112:115], v[136:139]\n"
    "ds_read_b64_tr_b16 a[220:221], v59 offset:32768\n"
    "v_add_f32 v28, v28, v70\n"
    "v_mfma_f32_16x16x32_bf16 v[140:143], a[196:199], v[116:119], v[140:143]\n"
    "ds_read_b64_tr_b16 a[222:223], v59 offset:40960\n"
    "v_add_f32 v29, v29, v71\n"
    "s_waitcnt lgkmcnt(8)\n"
    "v_mfma_f32_16x16x32_bf16 v[144:147], a[200:203], v[112:115], v[144:147]\n"
    "ds_read_b64_tr_b16 a[224:225], v52 offset:33024\n"
    "v_add_f32 v28, v28, v76\n"
    "v_mfma_f32_16x16x32_bf16 v[148:151], a[200:203], v[116:119], v[148:151]\n"
    "ds_read_b64_tr_b16 a[226:227], v52 offset:41216\n"
    "v_add_f32 v29, v29, v77\n"
    "v_mfma_f32_16x16x32_bf16 v[152:155], a[204:207], v[112:115], v[152:155]\n"
    "ds_read_b64_tr_b16 a[228:229], v53 offset:33024\n"
    "v_add_f32 v28, v28, v78\n"
    "v_mfma_f32_16x16x32_bf16 v[156:159], a[204:207], v[116:119], v[156:159]\n"
    "ds_read_b64_tr_b16 a[230:231], v53 offset:41216\n"
    "v_add_f32 v29, v29, v79\n"
    "s_waitcnt lgkmcnt(8)\n"
    "v_mfma_f32_16x16x32_bf16 v[160:163], a[208:211], v[112:115], v[160:163]\n"
    "ds_read_b64_tr_b16 a[232:233], v54 offset:33024\n"
    "s_waitcnt vmcnt(12)\n"
    "s_barrier\n"
    "s_add_u32 s48, s48, 16384\n"
    "v_mfma_f32_16x16x32_bf16 v[164:167], a[208:211], v[116:119], v[164:167]\n"
    "ds_read_b64_tr_b16 a[234:235], v54 offset:41216\n"
    "v_max3_f32 v110, v80, v81, v82\n"
    "s_add_u32 m0, s50, 114688\n"
    "v_mfma_f32_16x16x32_bf16 v[168:171], a[212:215], v[112:115], v[168:171]\n"
    "ds_read_b64_tr_b16 a[236:237], v55 offset:33024\n"
    "buffer_load_dwordx4 %[vov0], s[44:47], s48 offen offset:0 nt lds\n"
    "v_mfma_f32_16x16x32_bf16 v[172:175], a[212:215], v[116:119], v[172:175]\n"
    "ds_read_b64_tr_b16 a[238:239], v55 offset:41216\n"
    "v_max3_f32 v110, v110, v83, v84\n"
    "s_waitcnt lgkmcnt(8)\n"
    "v_mfma_f32_16x16x32_bf16 v[176:179], a[216:219], v[112:115], v[176:179]\n"
    "ds_read_b64_tr_b16 a[240:241], v56 offset:33024\n"
    "v_max3_f32 v110, v110, v85, v86\n"
    "v_mfma_f32_16x16x32_bf16 v[180:183], a[216:219], v[116:119], v[180:183]\n"
    "ds_read_b64_tr_b16 a[242:243], v56 offset:41216\n"
    "buffer_load_dwordx4 %[vov1], s[44:47], s48 offen offset:1024 nt lds\n"
    "v_mfma_f32_16x16x32_bf16 v[184:187], a[220:223], v[112:115], v[184:187]\n"
    "ds_read_b64_tr_b16 a[244:245], v57 offset:33024\n"
    "v_max3_f32 v110, v110, v87, v88\n"
    "v_mfma_f32_16x16x32_bf16 v[188:191], a[220:223], v[116:119], v[188:191]\n"
    "ds_read_b64_tr_b16 a[246:247], v57 offset:41216\n"
    "v_max3_f32 v110, v110, v89, v90\n"
    "s_waitcnt lgkmcnt(8)\n"
    "v_mfma_f32_16x16x32_bf16 v[192:195], a[224:227], v[112:115], v[192:195]\n"
    "ds_read_b64_tr_b16 a[248:249], v58 offset:33024\n"
    "buffer_load_dwordx4 %[vov2], s[44:47], s48 offen offset:2048 nt lds\n"
    "v_mfma_f32_16x16x32_bf16 v[196:199], a[224:227], v[116:119], v[196:199]\n"
    "ds_read_b64_tr_b16 a[250:251], v58 offset:41216\n"
    "v_max3_f32 v110, v110, v91, v92\n"
    "v_mfma_f32_16x16x32_bf16 v[200:203], a[228:231], v[112:115], v[200:203]\n"
    "ds_read_b64_tr_b16 a[252:253], v59 offset:33024\n"
    "v_max3_f32 v110, v110, v93, v94\n"
    "v_mfma_f32_16x16x32_bf16 v[204:207], a[228:231], v[116:119], v[204:207]\n"
    "ds_read_b64_tr_b16 a[254:255], v59 offset:41216\n"
    "buffer_load_dwordx4 %[vov3], s[44:47], s48 offen offset:3072 nt lds\n"
    "s_waitcnt lgkmcnt(8)\n"
    "v_mfma_f32_16x16x32_bf16 v[208:211], a[232:235], v[112:115], v[208:211]\n"
    "ds_read_b128 a[192:195], v48 offset:0\n"
    "v_max_f32 v110, v110, v95\n"
    "v_mfma_f32_16x16x32_bf16 v[212:215], a[232:235], v[116:119], v[212:215]\n"
    "ds_read_b128 a[196:199], v49 offset:0\n"
    "v_mfma_f32_16x16x32_bf16 v[216:219], a[236:239], v[112:115], v[216:219]\n"
    "ds_read_b128 a[200:203], v50 offset:0\n"
    "s_add_u32 m0, s50, 16384\n"
    "s_add_u32 s77, %[rot], 0\n"
    "s_and_b32 s77, s77, 15\n"
    "s_lshl_b32 s77, s77, 7\n"
    "buffer_load_dwordx4 v38, s[60:63], s77 offen offset:0 lds\n"
    "v_mfma_f32_16x16x32_bf16 v[220:223], a[236:239], v[116:119], v[220:223]\n"
    "ds_read_b128 a[204:207], v51 offset:0\n"
    "v_cmp_lt_f32 vcc, s79, v110\n"
    "s_waitcnt lgkmcnt(8)\n"
    "v_mfma_f32_16x16x32_bf16 v[224:227], a[240:243], v[112:115], v[224:227]\n"
    "ds_read_b128 a[208:211], v48 offset:256\n"
    "v_mfma_f32_16x16x32_bf16 v[228:231], a[240:243], v[116:119], v[228:231]\n"
    "ds_read_b128 a[212:215], v49 offset:256\n"
    "buffer_load_dwordx4 v39, s[60:63], s77 offen offset:1024 lds\n"
    "v_mfma_f32_16x16x32_bf16 v[232:235], a[244:247], v[112:115], v[232:235]\n"
    "ds_read_b128 a[216:219], v50 offset:256\n"
    "v_mfma_f32_16x16x32_bf16 v[236:239], a[244:247], v[116:119], v[236:239]\n"
    "ds_read_b128 a[220:223], v51 offset:256\n"
    "s_waitcnt lgkmcnt(8)\n"
    "v_mfma_f32_16x16x32_bf16 v[240:243], a[248:251], v[112:115], v[240:243]\n"
    "ds_read_b128 a[224:227], v48 offset:8192\n"
    "buffer_load_dwordx4 v40, s[60:63], s77 offen offset:2048 lds\n"
    "v_mfma_f32_16x16x32_bf16 v[244:247], a[248:251], v[116:119], v[244:247]\n"
    "ds_read_b128 a[228:231], v49 offset:8192\n"
    "v_mfma_f32_16x16x32_bf16 v[248:251], a[252:255], v[112:115], v[248:251]\n"
    "ds_read_b128 a[232:235], v50 offset:8192\n"
    "v_mfma_f32_16x16x32_bf16 v[252:255], a[252:255], v[116:119], v[252:255]\n"
    "ds_read_b128 a[236:239], v51 offset:8192\n"
    "buffer_load_dwordx4 v41, s[60:63], s77 offen offset:3072 lds\n"
    "s_cbranch_vccnz .LrareP0T26_%=\n"
    ".LretP0T26_%=:\n"
    "s_waitcnt lgkmcnt(8)\n"
    "v_mfma_f32_16x16x32_bf16 v[64:67], a[192:195], %[q0], v[96:99]\n"
    "v_exp_f32 v80, v80\n"
    "v_mfma_f32_16x16x32_bf16 v[68:71], a[192:195], %[q1], v[100:103]\n"
    "v_exp_f32 v81, v81\n"
    "v_mfma_f32_16x16x32_bf16 v[64:67], a[196:199], %[q2], v[64:67]\n"
    "v_exp_f32 v82, v82\n"
    "v_mfma_f32_16x16x32_bf16 v[68:71], a[196:199], %[q3], v[68:71]\n"
    "v_exp_f32 v83, v83\n"
    "v_mfma_f32_16x16x32_bf16 v[64:67], a[200:203], %[q4], v[64:67]\n"
    "v_exp_f32 v88, v88\n"
    "v_mfma_f32_16x16x32_bf16 v[68:71], a[200:203], %[q5], v[68:71]\n"
    "v_exp_f32 v89, v89\n"
    "v_mfma_f32_16x16x32_bf16 v[64:67], a[204:207], %[q6], v[64:67]\n"
    "v_exp_f32 v90, v90\n"
    "v_mfma_f32_16x16x32_bf16 v[68:71], a[204:207], %[q7], v[68:71]\n"
    "v_exp_f32 v91, v91\n"
    "s_waitcnt lgkmcnt(4)\n"
    "v_mfma_f32_16x16x32_bf16 v[64:67], a[208:211], %[q8], v[64:67]\n"
    "v_exp_f32 v84, v84\n"
    "v_mfma_f32_16x16x32_bf16 v[68:71], a[208:211], %[q9], v[68:71]\n"
    "v_exp_f32 v85, v85\n"
    "v_mfma_f32_16x16x32_bf16 v[64:67], a[212:215], %[q10], v[64:67]\n"
    "v_exp_f32 v86, v86\n"
    "v_mfma_f32_16x16x32_bf16 v[68:71], a[212:215], %[q11], v[68:71]\n"
    "v_exp_f32 v87, v87\n"
    "v_mfma_f32_16x16x32_bf16 v[64:67], a[216:219], %[q12], v[64:67]\n"
    "v_exp_f32 v92, v92\n"
    "v_mfma_f32_16x16x32_bf16 v[68:71], a[216:219], %[q13], v[68:71]\n"
    "v_exp_f32 v93, v93\n"
    "v_mfma_f32_16x16x32_bf16 v[64:67], a[220:223], %[q14], v[64:67]\n"
    "v_exp_f32 v94, v94\n"
    "v_mfma_f32_16x16x32_bf16 v[68:71], a[220:223], %[q15], v[68:71]\n"
    "v_exp_f32 v95, v95\n"
    "s_waitcnt lgkmcnt(0)\n"
    "v_mfma_f32_16x16x32_bf16 v[72:75], a[224:227], %[q0], v[96:99]\n"
    "ds_read_b128 a[240:243], v48 offset:8448\n"
    "v_cvt_pk_bf16_f32 v112, v80, v81\n"
    "v_mfma_f32_16x16x32_bf16 v[76:79], a[224:227], %[q1], v[100:103]\n"
    "ds_read_b128 a[244:247], v49 offset:8448\n"
    "v_cvt_pk_bf16_f32 v113, v82, v83\n"
    "v_mfma_f32_16x16x32_bf16 v[72:75], a[228:231], %[q2], v[72:75]\n"
    "ds_read_b128 a[248:251], v50 offset:8448\n"
    "v_cvt_pk_bf16_f32 v114, v88, v89\n"
    "v_mfma_f32_16x16x32_bf16 v[76:79], a[228:231], %[q3], v[76:79]\n"
    "ds_read_b128 a[252:255], v51 offset:8448\n"
    "v_cvt_pk_bf16_f32 v115, v90, v91\n"
    "v_mfma_f32_16x16x32_bf16 v[72:75], a[232:235], %[q4], v[72:75]\n"
    "ds_read_b64_tr_b16 a[192:193], v52 offset:49152\n"
    "v_cvt_pk_bf16_f32 v116, v84, v85\n"
    "v_mfma_f32_16x16x32_bf16 v[76:79], a[232:235], %[q5], v[76:79]\n"
    "ds_read_b64_tr_b16 a[194:195], v52 offset:57344\n"
    "v_cvt_pk_bf16_f32 v117, v86, v87\n"
    "v_mfma_f32_16x16x32_bf16 v[72:75], a[236:239], %[q6], v[72:75]\n"
    "ds_read_b64_tr_b16 a[196:197], v53 offset:49152\n"
    "v_cvt_pk_bf16_f32 v118, v92, v93\n"
    "v_mfma_f32_16x16x32_bf16 v[76:79], a[236:239], %[q7], v[76:79]\n"
    "ds_read_b64_tr_b16 a[198:199], v53 offset:57344\n"
    "v_cvt_pk_bf16_f32 v119, v94, v95\n"
    "s_waitcnt lgkmcnt(4)\n"
    "v_mfma_f32_16x16x32_bf16 v[72:75], a[240:243], %[q8], v[72:75]\n"
    "ds_read_b64_tr_b16 a[200:201], v54 offset:49152\n"
    "v_add_f32 v26, v26, v80\n"
    "v_mfma_f32_16x16x32_bf16 v[76:79], a[240:243], %[q9], v[76:79]\n"
    "ds_read_b64_tr_b16 a[202:203], v54 offset:57344\n"
    "v_add_f32 v27, v27, v81\n"
    "v_mfma_f32_16x16x32_bf16 v[72:75], a[244:247], %[q10], v[72:75]\n"
    "ds_read_b64_tr_b16 a[204:205], v55 offset:49152\n"
    "v_add_f32 v26, v26, v82\n"
    "v_mfma_f32_16x16x32_bf16 v[76:79], a[244:247], %[q11], v[76:79]\n"
    "ds_read_b64_tr_b16 a[206:207], v55 offset:57344\n"
    "v_add_f32 v27, v27, v83\n"
    "v_mfma_f32_16x16x32_bf16 v[72:75], a[248:251], %[q12], v[72:75]\n"
    "ds_read_b64_tr_b16 a[208:209], v56 offset:49152\n"
    "v_add_f32 v26, v26, v88\n"
    "v_mfma_f32_16x16x32_bf16 v[76:79], a[248:251], %[q13], v[76:79]\n"
    "ds_read_b64_tr_b16 a[210:211], v56 offset:57344\n"
    "v_add_f32 v27, v27, v89\n"
    "v_mfma_f32_16x16x32_bf16 v[72:75], a[252:255], %[q14], v[72:75]\n"
    "ds_read_b64_tr_b16 a[212:213], v57 offset:49152\n"
    "v_add_f32 v26, v26, v90\n"
    "v_mfma_f32_16x16x32_bf16 v[76:79], a[252:255], %[q15], v[76:79]\n"
    "ds_read_b64_tr_b16 a[214:215], v57 offset:57344\n"
    "v_add_f32 v27, v27, v91\n"
    "s_waitcnt lgkmcnt(8)\n"
    "v_mfma_f32_16x16x32_bf16 v[128:131], a[192:195], v[112:115], v[128:131]\n"
    "ds_read_b64_tr_b16 a[216:217], v58 offset:49152\n"
    "v_add_f32 v28, v28, v84\n"
    "v_mfma_f32_16x16x32_bf16 v[132:135], a[192:195], v[116:119], v[132:135]\n"
    "ds_read_b64_tr_b16 a[218:219], v58 offset:57344\n"
    "v_add_f32 v29, v29, v85\n"
    "v_mfma_f32_16x16x32_bf16 v[136:139], a[196:199], v[112:115], v[136:139]\n"
    "ds_read_b64_tr_b16 a[220:221], v59 offset:49152\n"
    "v_add_f32 v28, v28, v86\n"
    "v_mfma_f32_16x16x32_bf16 v[140:143], a[196:199], v[116:119], v[140:143]\n"
    "ds_read_b64_tr_b16 a[222:223], v59 offset:57344\n"
    "v_add_f32 v29, v29, v87\n"
    "s_waitcnt lgkmcnt(8)\n"
    "v_mfma_f32_16x16x32_bf16 v[144:147], a[200:203], v[112:115], v[144:147]\n"
    "ds_read_b64_tr_b16 a[224:225], v52 offset:49408\n"
    "v_add_f32 v28, v28, v92\n"
    "v_mfma_f32_16x16x32_bf16 v[148:151], a[200:203], v[116:119], v[148:151]\n"
    "ds_read_b64_tr_b16 a[226:227], v52 offset:57600\n"
    "v_add_f32 v29, v29, v93\n"
    "v_mfma_f32_16x16x32_bf16 v[152:155], a[204:207], v[112:115], v[152:155]\n"
    "ds_read_b64_tr_b16 a[228:229], v53 offset:49408\n"
    "v_add_f32 v28, v28, v94\n"
    "v_mfma_f32_16x16x32_bf16 v[156:159], a[204:207], v[116:119], v[156:159]\n"
    "ds_read_b64_tr_b16 a[230:231], v53 offset:57600\n"
    "v_add_f32 v29, v29, v95\n"
    "s_waitcnt lgkmcnt(8)\n"
    "v_mfma_f32_16x16x32_bf16 v[160:163], a[208:211], v[112:115], v[160:163]\n"
    "ds_read_b64_tr_b16 a[232:233], v54 offset:49408\n"
    "s_waitcnt vmcnt(12)\n"
    "s_barrier\n"
    "s_add_u32 s48, s48, 16384\n"
    "v_mfma_f32_16x16x32_bf16 v[164:167], a[208:211], v[116:119], v[164:167]\n"
    "ds_read_b64_tr_b16 a[234:235], v54 offset:57600\n"
    "v_max3_f32 v110, v64, v65, v66\n"
    "v_mfma_f32_16x16x32_bf16 v[168:171], a[212:215], v[112:115], v[168:171]\n"
    "ds_read_b64_tr_b16 a[236:237], v55 offset:49408\n"
    "s_add_u32 m0, s50, 65536\n"
    "s_add_u32 s49, %[rot], 1\n"
    "s_and_b32 s49, s49, 15\n"
    "s_mul_i32 s49, s49, 14336\n"
    "buffer_load_dwordx4 v34, s[56:59], s49 offen offset:0 sc1 lds\n"
    "v_mfma_f32_16x16x32_bf16 v[172:175], a[212:215], v[116:119], v[172:175]\n"
    "ds_read_b64_tr_b16 a[238:239], v55 offset:57600\n"
    "v_max3_f32 v110, v110, v67, v68\n"
    "s_waitcnt lgkmcnt(8)\n"
    "v_mfma_f32_16x16x32_bf16 v[176:179], a[216:219], v[112:115], v[176:179]\n"
    "ds_read_b64_tr_b16 a[240:241], v56 offset:49408\n"
    "v_max3_f32 v110, v110, v69, v70\n"
    "v_mfma_f32_16x16x32_bf16 v[180:183], a[216:219], v[116:119], v[180:183]\n"
    "ds_read_b64_tr_b16 a[242:243], v56 offset:57600\n"
    "buffer_load_dwordx4 v35, s[56:59], s49 offen offset:1024 sc1 lds\n"
    "v_mfma_f32_16x16x32_bf16 v[184:187], a[220:223], v[112:115], v[184:187]\n"
    "ds_read_b64_tr_b16 a[244:245], v57 offset:49408\n"
    "v_max3_f32 v110, v110, v71, v72\n"
    "v_mfma_f32_16x16x32_bf16 v[188:191], a[220:223], v[116:119], v[188:191]\n"
    "ds_read_b64_tr_b16 a[246:247], v57 offset:57600\n"
    "v_max3_f32 v110, v110, v73, v74\n"
    "s_waitcnt lgkmcnt(8)\n"
    "v_mfma_f32_16x16x32_bf16 v[192:195], a[224:227], v[112:115], v[192:195]\n"
    "ds_read_b64_tr_b16 a[248:249], v58 offset:49408\n"
    "buffer_load_dwordx4 v36, s[56:59], s49 offen offset:2048 sc1 lds\n"
    "v_mfma_f32_16x16x32_bf16 v[196:199], a[224:227], v[116:119], v[196:199]\n"
    "ds_read_b64_tr_b16 a[250:251], v58 offset:57600\n"
    "v_max3_f32 v110, v110, v75, v76\n"
    "v_mfma_f32_16x16x32_bf16 v[200:203], a[228:231], v[112:115], v[200:203]\n"
    "ds_read_b64_tr_b16 a[252:253], v59 offset:49408\n"
    "v_max3_f32 v110, v110, v77, v78\n"
    "v_mfma_f32_16x16x32_bf16 v[204:207], a[228:231], v[116:119], v[204:207]\n"
    "ds_read_b64_tr_b16 a[254:255], v59 offset:57600\n"
    "buffer_load_dwordx4 v37, s[56:59], s49 offen offset:3072 sc1 lds\n"
    "s_waitcnt lgkmcnt(8)\n"
    "v_mfma_f32_16x16x32_bf16 v[208:211], a[232:235], v[112:115], v[208:211]\n"
    "ds_read_b128 a[192:195], v48 offset:16384\n"
    "v_max_f32 v110, v110, v79\n"
    "v_mfma_f32_16x16x32_bf16 v[212:215], a[232:235], v[116:119], v[212:215]\n"
    "ds_read_b128 a[196:199], v49 offset:16384\n"
    "v_mfma_f32_16x16x32_bf16 v[216:219], a[236:239], v[112:115], v[216:219]\n"
    "ds_read_b128 a[200:203], v50 offset:16384\n"
    "s_add_u32 m0, s50, 81920\n"
    "s_add_u32 s77, %[rot], 1\n"
    "s_and_b32 s77, s77, 15\n"
    "s_lshl_b32 s77, s77, 7\n"
    "buffer_load_dwordx4 v38, s[60:63], s77 offen offset:0 lds\n"
    "v_mfma_f32_16x16x32_bf16 v[220:223], a[236:239], v[116:119], v[220:223]\n"
    "ds_read_b128 a[204:207], v51 offset:16384\n"
    "v_cmp_lt_f32 vcc, s79, v110\n"
    "s_waitcnt lgkmcnt(8)\n"
    "v_mfma_f32_16x16x32_bf16 v[224:227], a[240:243], v[112:115], v[224:227]\n"
    "ds_read_b128 a[208:211], v48 offset:16640\n"
    "v_mfma_f32_16x16x32_bf16 v[228:231], a[240:243], v[116:119], v[228:231]\n"
    "ds_read_b128 a[212:215], v49 offset:16640\n"
    "buffer_load_dwordx4 v39, s[60:63], s77 offen offset:1024 lds\n"
    "v_mfma_f32_16x16x32_bf16 v[232:235], a[244:247], v[112:115], v[232:235]\n"
    "ds_read_b128 a[216:219], v50 offset:16640\n"
    "v_mfma_f32_16x16x32_bf16 v[236:239], a[244:247], v[116:119], v[236:239]\n"
    "ds_read_b128 a[220:223], v51 offset:16640\n"
    "s_waitcnt lgkmcnt(8)\n"
    "v_mfma_f32_16x16x32_bf16 v[240:243], a[248:251], v[112:115], v[240:243]\n"
    "ds_read_b128 a[224:227], v48 offset:24576\n"
    "buffer_load_dwordx4 v40, s[60:63], s77 offen offset:2048 lds\n"
    "v_mfma_f32_16x16x32_bf16 v[244:247], a[248:251], v[116:119], v[244:247]\n"
    "ds_read_b128 a[228:231], v49 offset:24576\n"
    "v_mfma_f32_16x16x32_bf16 v[248:251], a[252:255], v[112:115], v[248:251]\n"
    "ds_read_b128 a[232:235], v50 offset:24576\n"
    "v_mfma_f32_16x16x32_bf16 v[252:255], a[252:255], v[116:119], v[252:255]\n"
    "ds_read_b128 a[236:239], v51 offset:24576\n"
    "buffer_load_dwordx4 v41, s[60:63], s77 offen offset:3072 lds\n"
    "s_cbranch_vccnz .LrareP0T27_%=\n"
    ".LretP0T27_%=:\n"
    "s_waitcnt lgkmcnt(8)\n"
    "v_mfma_f32_16x16x32_bf16 v[80:83], a[192:195], %[q0], v[96:99]\n"
    "v_exp_f32 v64, v64\n"
    "v_mfma_f32_16x16x32_bf16 v[84:87], a[192:195], %[q1], v[100:103]\n"
    "v_exp_f32 v65, v65\n"
    "v_mfma_f32_16x16x32_bf16 v[80:83], a[196:199], %[q2], v[80:83]\n"
    "v_exp_f32 v66, v66\n"
    "v_mfma_f32_16x16x32_bf16 v[84:87], a[196:199], %[q3], v[84:87]\n"
    "v_exp_f32 v67, v67\n"
    "v_mfma_f32_16x16x32_bf16 v[80:83], a[200:203], %[q4], v[80:83]\n"
    "v_exp_f32 v72, v72\n"
    "v_mfma_f32_16x16x32_bf16 v[84:87], a[200:203], %[q5], v[84:87]\n"
    "v_exp_f32 v73, v73\n"
    "v_mfma_f32_16x16x32_bf16 v[80:83], a[204:207], %[q6], v[80:83]\n"
    "v_exp_f32 v74, v74\n"
    "v_mfma_f32_16x16x32_bf16 v[84:87], a[204:207], %[q7], v[84:87]\n"
    "v_exp_f32 v75, v75\n"
    "s_waitcnt lgkmcnt(4)\n"
    "v_mfma_f32_16x16x32_bf16 v[80:83], a[208:211], %[q8], v[80:83]\n"
    "v_exp_f32 v68, v68\n"
    "v_mfma_f32_16x16x32_bf16 v[84:87], a[208:211], %[q9], v[84:87]\n"
    "v_exp_f32 v69, v69\n"
    "v_mfma_f32_16x16x32_bf16 v[80:83], a[212:215], %[q10], v[80:83]\n"
    "v_exp_f32 v70, v70\n"
    "v_mfma_f32_16x16x32_bf16 v[84:87], a[212:215], %[q11], v[84:87]\n"
    "v_exp_f32 v71, v71\n"
    "v_mfma_f32_16x16x32_bf16 v[80:83], a[216:219], %[q12], v[80:83]\n"
    "v_exp_f32 v76, v76\n"
    "v_mfma_f32_16x16x32_bf16 v[84:87], a[216:219], %[q13], v[84:87]\n"
    "v_exp_f32 v77, v77\n"
    "v_mfma_f32_16x16x32_bf16 v[80:83], a[220:223], %[q14], v[80:83]\n"
    "v_exp_f32 v78, v78\n"
    "v_mfma_f32_16x16x32_bf16 v[84:87], a[220:223], %[q15], v[84:87]\n"
    "v_exp_f32 v79, v79\n"
    "s_waitcnt lgkmcnt(0)\n"
    "v_mfma_f32_16x16x32_bf16 v[88:91], a[224:227], %[q0], v[96:99]\n"
    "ds_read_b128 a[240:243], v48 offset:24832\n"
    "v_cvt_pk_bf16_f32 v112, v64, v65\n"
    "v_mfma_f32_16x16x32_bf16 v[92:95], a[224:227], %[q1], v[100:103]\n"
    "ds_read_b128 a[244:247], v49 offset:24832\n"
    "v_cvt_pk_bf16_f32 v113, v66, v67\n"
    "v_mfma_f32_16x16x32_bf16 v[88:91], a[228:231], %[q2], v[88:91]\n"
    "ds_read_b128 a[248:251], v50 offset:24832\n"
    "v_cvt_pk_bf16_f32 v114, v72, v73\n"
    "v_mfma_f32_16x16x32_bf16 v[92:95], a[228:231], %[q3], v[92:95]\n"
    "ds_read_b128 a[252:255], v51 offset:24832\n"
    "v_cvt_pk_bf16_f32 v115, v74, v75\n"
    "v_mfma_f32_16x16x32_bf16 v[88:91], a[232:235], %[q4], v[88:91]\n"
    "ds_read_b64_tr_b16 a[192:193], v52 offset:0\n"
    "v_cvt_pk_bf16_f32 v116, v68, v69\n"
    "v_mfma_f32_16x16x32_bf16 v[92:95], a[232:235], %[q5], v[92:95]\n"
    "ds_read_b64_tr_b16 a[194:195], v52 offset:8192\n"
    "v_cvt_pk_bf16_f32 v117, v70, v71\n"
    "v_mfma_f32_16x16x32_bf16 v[88:91], a[236:239], %[q6], v[88:91]\n"
    "ds_read_b64_tr_b16 a[196:197], v53 offset:0\n"
    "v_cvt_pk_bf16_f32 v118, v76, v77\n"
    "v_mfma_f32_16x16x32_bf16 v[92:95], a[236:239], %[q7], v[92:95]\n"
    "ds_read_b64_tr_b16 a[198:199], v53 offset:8192\n"
    "v_cvt_pk_bf16_f32 v119, v78, v79\n"
    "s_waitcnt lgkmcnt(4)\n"
    "v_mfma_f32_16x16x32_bf16 v[88:91], a[240:243], %[q8], v[88:91]\n"
    "ds_read_b64_tr_b16 a[200:201], v54 offset:0\n"
    "v_add_f32 v26, v26, v64\n"
    "v_mfma_f32_16x16x32_bf16 v[92:95], a[240:243], %[q9], v[92:95]\n"
    "ds_read_b64_tr_b16 a[202:203], v54 offset:8192\n"
    "v_add_f32 v27, v27, v65\n"
    "v_mfma_f32_16x16x32_bf16 v[88:91], a[244:247], %[q10], v[88:91]\n"
    "ds_read_b64_tr_b16 a[204:205], v55 offset:0\n"
    "v_add_f32 v26, v26, v66\n"
    "v_mfma_f32_16x16x32_bf16 v[92:95], a[244:247], %[q11], v[92:95]\n"
    "ds_read_b64_tr_b16 a[206:207], v55 offset:8192\n"
    "v_add_f32 v27, v27, v67\n"
    "v_mfma_f32_16x16x32_bf16 v[88:91], a[248:251], %[q12], v[88:91]\n"
    "ds_read_b64_tr_b16 a[208:209], v56 offset:0\n"
    "v_add_f32 v26, v26, v72\n"
    "v_mfma_f32_16x16x32_bf16 v[92:95], a[248:251], %[q13], v[92:95]\n"
    "ds_read_b64_tr_b16 a[210:211], v56 offset:8192\n"
    "v_add_f32 v27, v27, v73\n"
    "v_mfma_f32_16x16x32_bf16 v[88:91], a[252:255], %[q14], v[88:91]\n"
    "ds_read_b64_tr_b16 a[212:213], v57 offset:0\n"
    "v_add_f32 v26, v26, v74\n"
    "v_mfma_f32_16x16x32_bf16 v[92:95], a[252:255], %[q15], v[92:95]\n"
    "ds_read_b64_tr_b16 a[214:215], v57 offset:8192\n"
    "v_add_f32 v27, v27, v75\n"
    "s_waitcnt lgkmcnt(8)\n"
    "v_mfma_f32_16x16x32_bf16 v[128:131], a[192:195], v[112:115], v[128:131]\n"
    "ds_read_b64_tr_b16 a[216:217], v58 offset:0\n"
    "v_add_f32 v28, v28, v68\n"
    "v_mfma_f32_16x16x32_bf16 v[132:135], a[192:195], v[116:119], v[132:135]\n"
    "ds_read_b64_tr_b16 a[218:219], v58 offset:8192\n"
    "v_add_f32 v29, v29, v69\n"
    "v_mfma_f32_16x16x32_bf16 v[136:139], a[196:199], v[112:115], v[136:139]\n"
    "ds_read_b64_tr_b16 a[220:221], v59 offset:0\n"
    "v_add_f32 v28, v28, v70\n"
    "v_mfma_f32_16x16x32_bf16 v[140:143], a[196:199], v[116:119], v[140:143]\n"
    "ds_read_b64_tr_b16 a[222:223], v59 offset:8192\n"
    "v_add_f32 v29, v29, v71\n"
    "s_waitcnt lgkmcnt(8)\n"
    "v_mfma_f32_16x16x32_bf16 v[144:147], a[200:203], v[112:115], v[144:147]\n"
    "ds_read_b64_tr_b16 a[224:225], v52 offset:256\n"
    "v_add_f32 v28, v28, v76\n"
    "v_mfma_f32_16x16x32_bf16 v[148:151], a[200:203], v[116:119], v[148:151]\n"
    "ds_read_b64_tr_b16 a[226:227], v52 offset:8448\n"
    "v_add_f32 v29, v29, v77\n"
    "v_mfma_f32_16x16x32_bf16 v[152:155], a[204:207], v[112:115], v[152:155]\n"
    "ds_read_b64_tr_b16 a[228:229], v53 offset:256\n"
    "v_add_f32 v28, v28, v78\n"
    "v_mfma_f32_16x16x32_bf16 v[156:159], a[204:207], v[116:119], v[156:159]\n"
    "ds_read_b64_tr_b16 a[230:231], v53 offset:8448\n"
    "v_add_f32 v29, v29, v79\n"
    "s_waitcnt lgkmcnt(8)\n"
    "v_mfma_f32_16x16x32_bf16 v[160:163], a[208:211], v[112:115], v[160:163]\n"
    "ds_read_b64_tr_b16 a[232:233], v54 offset:256\n"
    "s_waitcnt vmcnt(12)\n"
    "s_barrier\n"
    "s_add_u32 s48, s48, 16384\n"
    "v_mfma_f32_16x16x32_bf16 v[164:167], a[208:211], v[116:119], v[164:167]\n"
    "ds_read_b64_tr_b16 a[234:235], v54 offset:8448\n"
    "v_max3_f32 v110, v80, v81, v82\n"
    "v_mfma_f32_16x16x32_bf16 v[168:171], a[212:215], v[112:115], v[168:171]\n"
    "ds_read_b64_tr_b16 a[236:237], v55 offset:256\n"
    "s_add_u32 m0, s50, 32768\n"
    "s_add_u32 s49, %[rot], 2\n"
    "s_and_b32 s49, s49, 15\n"
    "s_mul_i32 s49, s49, 14336\n"
    "buffer_load_dwordx4 v34, s[56:59], s49 offen offset:0 sc1 lds\n"
    "v_mfma_f32_16x16x32_bf16 v[172:175], a[212:215], v[116:119], v[172:175]\n"
    "ds_read_b64_tr_b16 a[238:239], v55 offset:8448\n"
    "v_max3_f32 v110, v110, v83, v84\n"
    "s_waitcnt lgkmcnt(8)\n"
    "v_mfma_f32_16x16x32_bf16 v[176:179], a[216:219], v[112:115], v[176:179]\n"
    "ds_read_b64_tr_b16 a[240:241], v56 offset:256\n"
    "v_max3_f32 v110, v110, v85, v86\n"
    "v_mfma_f32_16x16x32_bf16 v[180:183], a[216:219], v[116:119], v[180:183]\n"
    "ds_read_b64_tr_b16 a[242:243], v56 offset:8448\n"
    "buffer_load_dwordx4 v35, s[56:59], s49 offen offset:1024 sc1 lds\n"
    "v_mfma_f32_16x16x32_bf16 v[184:187], a[220:223], v[112:115], v[184:187]\n"
    "ds_read_b64_tr_b16 a[244:245], v57 offset:256\n"
    "v_max3_f32 v110, v110, v87, v88\n"
    "v_mfma_f32_16x16x32_bf16 v[188:191], a[220:223], v[116:119], v[188:191]\n"
    "ds_read_b64_tr_b16 a[246:247], v57 offset:8448\n"
    "v_max3_f32 v110, v110, v89, v90\n"
    "s_waitcnt lgkmcnt(8)\n"
    "v_mfma_f32_16x16x32_bf16 v[192:195], a[224:227], v[112:115], v[192:195]\n"
    "ds_read_b64_tr_b16 a[248:249], v58 offset:256\n"
    "buffer_load_dwordx4 v36, s[56:59], s49 offen offset:2048 sc1 lds\n"
    "v_mfma_f32_16x16x32_bf16 v[196:199], a[224:227], v[116:119], v[196:199]\n"
    "ds_read_b64_tr_b16 a[250:251], v58 offset:8448\n"
    "v_max3_f32 v110, v110, v91, v92\n"
    "v_mfma_f32_16x16x32_bf16 v[200:203], a[228:231], v[112:115], v[200:203]\n"
    "ds_read_b64_tr_b16 a[252:253], v59 offset:256\n"
    "v_max3_f32 v110, v110, v93, v94\n"
    "v_mfma_f32_16x16x32_bf16 v[204:207], a[228:231], v[116:119], v[204:207]\n"
    "ds_read_b64_tr_b16 a[254:255], v59 offset:8448\n"
    "buffer_load_dwordx4 v37, s[56:59], s49 offen offset:3072 sc1 lds\n"
    "s_waitcnt lgkmcnt(8)\n"
    "v_mfma_f32_16x16x32_bf16 v[208:211], a[232:235], v[112:115], v[208:211]\n"
    "v_max_f32 v110, v110, v95\n"
    "v_mfma_f32_16x16x32_bf16 v[212:215], a[232:235], v[116:119], v[212:215]\n"
    "v_mfma_f32_16x16x32_bf16 v[216:219], a[236:239], v[112:115], v[216:219]\n"
    "s_add_u32 m0, s50, 131072\n"
    "s_add_u32 s49, %[rot], 3\n"
    "s_and_b32 s49, s49, 15\n"
    "s_mul_i32 s49, s49, 14336\n"
    "buffer_load_dwordx4 v34, s[56:59], s49 offen offset:0 sc1 lds\n"
    "v_mfma_f32_16x16x32_bf16 v[220:223], a[236:239], v[116:119], v[220:223]\n"
    "v_cmp_lt_f32 vcc, s79, v110\n"
    "s_waitcnt lgkmcnt(4)\n"
    "v_mfma_f32_16x16x32_bf16 v[224:227], a[240:243], v[112:115], v[224:227]\n"
    "v_mfma_f32_16x16x32_bf16 v[228:231], a[240:243], v[116:119], v[228:231]\n"
    "buffer_load_dwordx4 v35, s[56:59], s49 offen offset:1024 sc1 lds\n"
    "v_mfma_f32_16x16x32_bf16 v[232:235], a[244:247], v[112:115], v[232:235]\n"
    "v_mfma_f32_16x16x32_bf16 v[236:239], a[244:247], v[116:119], v[236:239]\n"
    "s_waitcnt lgkmcnt(0)\n"
    "v_mfma_f32_16x16x32_bf16 v[240:243], a[248:251], v[112:115], v[240:243]\n"
    "buffer_load_dwordx4 v36, s[56:59], s49 offen offset:2048 sc1 lds\n"
    "v_mfma_f32_16x16x32_bf16 v[244:247], a[248:251], v[116:119], v[244:247]\n"
    "v_mfma_f32_16x16x32_bf16 v[248:251], a[252:255], v[112:115], v[248:251]\n"
    "v_mfma_f32_16x16x32_bf16 v[252:255], a[252:255], v[116:119], v[252:255]\n"
    "buffer_load_dwordx4 v37, s[56:59], s49 offen offset:3072 sc1 lds\n"
    "s_cbranch_vccnz .LrareP0T28_%=\n"
    ".LretP0T28_%=:\n"
    "v_exp_f32 v80, v80\n"
    "v_exp_f32 v81, v81\n"
    "v_exp_f32 v82, v82\n"
    "v_exp_f32 v83, v83\n"
    "v_exp_f32 v88, v88\n"
    "v_exp_f32 v89, v89\n"
    "v_exp_f32 v90, v90\n"
    "v_exp_f32 v91, v91\n"
    "v_exp_f32 v84, v84\n"
    "v_exp_f32 v85, v85\n"
    "v_exp_f32 v86, v86\n"
    "v_exp_f32 v87, v87\n"
    "v_exp_f32 v92, v92\n"
    "v_exp_f32 v93, v93\n"
    "v_exp_f32 v94, v94\n"
    "v_exp_f32 v95, v95\n"
    "v_cvt_pk_bf16_f32 v112, v80, v81\n"
    "v_cvt_pk_bf16_f32 v113, v82, v83\n"
    "v_cvt_pk_bf16_f32 v114, v88, v89\n"
    "v_cvt_pk_bf16_f32 v115, v90, v91\n"
    "ds_read_b64_tr_b16 a[192:193], v52 offset:16384\n"
    "v_cvt_pk_bf16_f32 v116, v84, v85\n"
    "ds_read_b64_tr_b16 a[194:195], v52 offset:24576\n"
    "v_cvt_pk_bf16_f32 v117, v86, v87\n"
    "ds_read_b64_tr_b16 a[196:197], v53 offset:16384\n"
    "v_cvt_pk_bf16_f32 v118, v92, v93\n"
    "ds_read_b64_tr_b16 a[198:199], v53 offset:24576\n"
    "v_cvt_pk_bf16_f32 v119, v94, v95\n"
    "ds_read_b64_tr_b16 a[200:201], v54 offset:16384\n"
    "v_add_f32 v26, v26, v80\n"
    "ds_read_b64_tr_b16 a[202:203], v54 offset:24576\n"
    "v_add_f32 v27, v27, v81\n"
    "ds_read_b64_tr_b16 a[204:205], v55 offset:16384\n"
    "v_add_f32 v26, v26, v82\n"
    "ds_read_b64_tr_b16 a[206:207], v55 offset:24576\n"
    "v_add_f32 v27, v27, v83\n"
    "ds_read_b64_tr_b16 a[208:209], v56 offset:16384\n"
    "v_add_f32 v26, v26, v88\n"
    "ds_read_b64_tr_b16 a[210:211], v56 offset:24576\n"
    "v_add_f32 v27, v27, v89\n"
    "ds_read_b64_tr_b16 a[212:213], v57 offset:16384\n"
    "v_add_f32 v26, v26, v90\n"
    "ds_read_b64_tr_b16 a[214:215], v57 offset:24576\n"
    "v_add_f32 v27, v27, v91\n"
    "s_waitcnt lgkmcnt(8)\n"
    "v_mfma_f32_16x16x32_bf16 v[128:131], a[192:195], v[112:115], v[128:131]\n"
    "ds_read_b64_tr_b16 a[216:217], v58 offset:16384\n"
    "v_add_f32 v28, v28, v84\n"
    "v_mfma_f32_16x16x32_bf16 v[132:135], a[192:195], v[116:119], v[132:135]\n"
    "ds_read_b64_tr_b16 a[218:219], v58 offset:24576\n"
    "v_add_f32 v29, v29, v85\n"
    "v_mfma_f32_16x16x32_bf16 v[136:139], a[196:199], v[112:115], v[136:139]\n"
    "ds_read_b64_tr_b16 a[220:221], v59 offset:16384\n"
    "v_add_f32 v28, v28, v86\n"
    "v_mfma_f32_16x16x32_bf16 v[140:143], a[196:199], v[116:119], v[140:143]\n"
    "ds_read_b64_tr_b16 a[222:223], v59 offset:24576\n"
    "v_add_f32 v29, v29, v87\n"
    "s_waitcnt lgkmcnt(8)\n"
    "v_mfma_f32_16x16x32_bf16 v[144:147], a[200:203], v[112:115], v[144:147]\n"
    "ds_read_b64_tr_b16 a[224:225], v52 offset:16640\n"
    "v_add_f32 v28, v28, v92\n"
    "v_mfma_f32_16x16x32_bf16 v[148:151], a[200:203], v[116:119], v[148:151]\n"
    "ds_read_b64_tr_b16 a[226:227], v52 offset:24832\n"
    "v_add_f32 v29, v29, v93\n"
    "v_mfma_f32_16x16x32_bf16 v[152:155], a[204:207], v[112:115], v[152:155]\n"
    "ds_read_b64_tr_b16 a[228:229], v53 offset:16640\n"
    "v_add_f32 v28, v28, v94\n"
    "v_mfma_f32_16x16x32_bf16 v[156:159], a[204:207], v[116:119], v[156:159]\n"
    "ds_read_b64_tr_b16 a[230:231], v53 offset:24832\n"
    "v_add_f32 v29, v29, v95\n"
    "s_waitcnt lgkmcnt(8)\n"
    "v_mfma_f32_16x16x32_bf16 v[160:163], a[208:211], v[112:115], v[160:163]\n"
    "ds_read_b64_tr_b16 a[232:233], v54 offset:16640\n"
    "s_add_u32 s48, s48, 16384\n"
    "v_mfma_f32_16x16x32_bf16 v[164:167], a[208:211], v[116:119], v[164:167]\n"
    "ds_read_b64_tr_b16 a[234:235], v54 offset:24832\n"
    "v_mfma_f32_16x16x32_bf16 v[168:171], a[212:215], v[112:115], v[168:171]\n"
    "ds_read_b64_tr_b16 a[236:237], v55 offset:16640\n"
    "s_add_u32 m0, s50, 49152\n"
    "s_add_u32 s77, %[rot], 2\n"
    "s_and_b32 s77, s77, 15\n"
    "s_lshl_b32 s77, s77, 7\n"
    "buffer_load_dwordx4 v38, s[60:63], s77 offen offset:0 lds\n"
    "v_mfma_f32_16x16x32_bf16 v[172:175], a[212:215], v[116:119], v[172:175]\n"
    "ds_read_b64_tr_b16 a[238:239], v55 offset:24832\n"
    "s_waitcnt lgkmcnt(8)\n"
    "v_mfma_f32_16x16x32_bf16 v[176:179], a[216:219], v[112:115], v[176:179]\n"
    "ds_read_b64_tr_b16 a[240:241], v56 offset:16640\n"
    "v_mfma_f32_16x16x32_bf16 v[180:183], a[216:219], v[116:119], v[180:183]\n"
    "ds_read_b64_tr_b16 a[242:243], v56 offset:24832\n"
    "buffer_load_dwordx4 v39, s[60:63], s77 offen offset:1024 lds\n"
    "v_mfma_f32_16x16x32_bf16 v[184:187], a[220:223], v[112:115], v[184:187]\n"
    "ds_read_b64_tr_b16 a[244:245], v57 offset:16640\n"
    "v_mfma_f32_16x16x32_bf16 v[188:191], a[220:223], v[116:119], v[188:191]\n"
    "ds_read_b64_tr_b16 a[246:247], v57 offset:24832\n"
    "s_waitcnt lgkmcnt(8)\n"
    "v_mfma_f32_16x16x32_bf16 v[192:195], a[224:227], v[112:115], v[192:195]\n"
    "ds_read_b64_tr_b16 a[248:249], v58 offset:16640\n"
    "buffer_load_dwordx4 v40, s[60:63], s77 offen offset:2048 lds\n"
    "v_mfma_f32_16x16x32_bf16 v[196:199], a[224:227], v[116:119], v[196:199]\n"
    "ds_read_b64_tr_b16 a[250:251], v58 offset:24832\n"
    "v_mfma_f32_16x16x32_bf16 v[200:203], a[228:231], v[112:115], v[200:203]\n"
    "ds_read_b64_tr_b16 a[252:253], v59 offset:16640\n"
    "v_mfma_f32_16x16x32_bf16 v[204:207], a[228:231], v[116:119], v[204:207]\n"
    "ds_read_b64_tr_b16 a[254:255], v59 offset:24832\n"
    "buffer_load_dwordx4 v41, s[60:63], s77 offen offset:3072 lds\n"
    "s_waitcnt lgkmcnt(8)\n"
    "v_mfma_f32_16x16x32_bf16 v[208:211], a[232:235], v[112:115], v[208:211]\n"
    "v_mfma_f32_16x16x32_bf16 v[212:215], a[232:235], v[116:119], v[212:215]\n"
    "v_mfma_f32_16x16x32_bf16 v[216:219], a[236:239], v[112:115], v[216:219]\n"
    "s_add_u32 m0, s50, 147456\n"
    "s_add_u32 s77, %[rot], 3\n"
    "s_and_b32 s77, s77, 15\n"
    "s_lshl_b32 s77, s77, 7\n"
    "buffer_load_dwordx4 v38, s[60:63], s77 offen offset:0 lds\n"
    "v_mfma_f32_16x16x32_bf16 v[220:223], a[236:239], v[116:119], v[220:223]\n"
    "s_waitcnt lgkmcnt(4)\n"
    "v_mfma_f32_16x16x32_bf16 v[224:227], a[240:243], v[112:115], v[224:227]\n"
    "v_mfma_f32_16x16x32_bf16 v[228:231], a[240:243], v[116:119], v[228:231]\n"
    "buffer_load_dwordx4 v39, s[60:63], s77 offen offset:1024 lds\n"
    "v_mfma_f32_16x16x32_bf16 v[232:235], a[244:247], v[112:115], v[232:235]\n"
    "v_mfma_f32_16x16x32_bf16 v[236:239], a[244:247], v[116:119], v[236:239]\n"
    "s_waitcnt lgkmcnt(0)\n"
    "v_mfma_f32_16x16x32_bf16 v[240:243], a[248:251], v[112:115], v[240:243]\n"
    "buffer_load_dwordx4 v40, s[60:63], s77 offen offset:2048 lds\n"
    "v_mfma_f32_16x16x32_bf16 v[244:247], a[248:251], v[116:119], v[244:247]\n"
    "v_mfma_f32_16x16x32_bf16 v[248:251], a[252:255], v[112:115], v[248:251]\n"
    "v_mfma_f32_16x16x32_bf16 v[252:255], a[252:255], v[116:119], v[252:255]\n"
    "buffer_load_dwordx4 v41, s[60:63], s77 offen offset:3072 lds\n"
    "s_waitcnt lgkmcnt(0)\n"
    "s_barrier\n"
    "s_or_b32 s52, %[tp], %[fs]\n"
    "s_or_b32 s52, s52, %[kh]\n"
    "s_or_b32 s52, s52, %[w]\n"
    "s_cmp_lg_u32 s52, 0\n"
    "s_cbranch_scc1 .Lt3_%=\n"
    "s_mov_b64 exec, 1\n"
    "buffer_store_dwordx2 v[20:21], off, s[80:83], 0 sc1\n"
    "s_mov_b64 exec, -1\n"
    ".Lt3_%=:\n"
    "s_add_u32 m0, s50, 98304\n"
    "s_add_u32 s49, %[rot], 4\n"
    "s_and_b32 s49, s49, 15\n"
    "s_mul_i32 s49, s49, 14336\n"
    "buffer_load_dwordx4 v34, s[56:59], s49 offen offset:0 sc1 lds\n"
    "buffer_load_dwordx4 v35, s[56:59], s49 offen offset:1024 sc1 lds\n"
    "buffer_load_dwordx4 v36, s[56:59], s49 offen offset:2048 sc1 lds\n"
    "buffer_load_dwordx4 v37, s[56:59], s49 offen offset:3072 sc1 lds\n"
    "s_add_u32 m0, s50, 114688\n"
    "s_add_u32 s77, %[rot], 4\n"
    "s_and_b32 s77, s77, 15\n"
    "s_lshl_b32 s77, s77, 7\n"
    "buffer_load_dwordx4 v38, s[60:63], s77 offen offset:0 lds\n"
    "buffer_load_dwordx4 v39, s[60:63], s77 offen offset:1024 lds\n"
    "buffer_load_dwordx4 v40, s[60:63], s77 offen offset:2048 lds\n"
    "buffer_load_dwordx4 v41, s[60:63], s77 offen offset:3072 lds\n"
    "s_lshl_b32 s52, %[tp], 3\n"
    "s_lshl_b32 s53, %[w], 1\n"
    "s_add_u32 s78, s52, s53\n"
    "s_lshl_b32 s52, s78, 5\n"
    "s_add_u32 s68, %[bslo], s52\n"
    "s_addc_u32 s69, %[bshi], 0\n"
    "s_and_b32 s69, s69, 0xffff\n"
    "s_mov_b32 s70, 64\n"
    "s_mov_b32 s71, 0x20000\n"
    "s_cmp_eq_u32 %[kh], 0\n"
    "s_cselect_b32 s70, 64, 0\n"
    "v_lshlrev_b32 v23, 1, v31\n"
    "buffer_load_ushort v92, v23, s[68:71], 0 offen\n"
    "buffer_load_ushort v96, v23, s[68:71], 0 offen offset:32\n"
    "v_lshrrev_b32 v100, 2, v31\n"
    "v_and_b32 v101, 3, v30\n"
    "v_lshrrev_b32 v102, 1, v101\n"
    "v_lshlrev_b32 v102, 4, v102\n"
    "v_and_b32 v101, 1, v101\n"
    "v_lshl_add_u32 v102, v101, 3, v102\n"
    "v_add_u32 v102, %[ldsb], v102\n"
    "v_lshl_add_u32 v101, v32, 3, v100\n"
    "v_and_b32 v103, 1, v32\n"
    "v_lshl_or_b32 v103, v103, 2, v100\n"
    "v_lshlrev_b32 v101, 8, v101\n"
    "v_lshl_add_u32 v101, v103, 5, v101\n"
    "v_add_u32 v101, v101, v102\n"
    "v_xor_b32 v64, 0, v101\n"
    "v_xor_b32 v65, 32, v101\n"
    "v_xor_b32 v66, 64, v101\n"
    "v_xor_b32 v67, 96, v101\n"
    "v_xor_b32 v68, 128, v101\n"
    "v_xor_b32 v69, 160, v101\n"
    "v_xor_b32 v70, 192, v101\n"
    "v_xor_b32 v71, 0, v101\n"
    "v_add_u32 v71, 65536, v71\n"
    "v_xor_b32 v72, 32, v101\n"
    "v_add_u32 v72, 65536, v72\n"
    "v_xor_b32 v73, 64, v101\n"
    "v_add_u32 v73, 65536, v73\n"
    "v_xor_b32 v74, 96, v101\n"
    "v_add_u32 v74, 65536, v74\n"
    "v_xor_b32 v75, 128, v101\n"
    "v_add_u32 v75, 65536, v75\n"
    "v_xor_b32 v76, 160, v101\n"
    "v_add_u32 v76, 65536, v76\n"
    "v_xor_b32 v77, 192, v101\n"
    "v_add_u32 v77, 65536, v77\n"
    "v_xor_b32 v78, 0, v101\n"
    "v_add_u32 v78, 131072, v78\n"
    "v_xor_b32 v79, 32, v101\n"
    "v_add_u32 v79, 131072, v79\n"
    "v_xor_b32 v80, 64, v101\n"
    "v_add_u32 v80, 131072, v80\n"
    "v_xor_b32 v81, 96, v101\n"
    "v_add_u32 v81, 131072, v81\n"
    "v_xor_b32 v82, 128, v101\n"
    "v_add_u32 v82, 131072, v82\n"
    "v_xor_b32 v83, 160, v101\n"
    "v_add_u32 v83, 131072, v83\n"
    "v_xor_b32 v84, 192, v101\n"
    "v_add_u32 v84, 131072, v84\n"
    "v_lshrrev_b32 v100, 1, v31\n"
    "v_xor_b32 v100, v100, v32\n"
    "v_lshlrev_b32 v100, 4, v100\n"
    "v_lshl_add_u32 v100, v31, 7, v100\n"
    "v_add_u32 v85, %[m0b], v100\n"
    "v_xor_b32 v86, 64, v85\n"
    "v_add_u32 v87, 65536, v85\n"
    "v_add_u32 v88, 65536, v86\n"
    "v_add_u32 v89, 131072, v85\n"
    "v_add_u32 v90, 131072, v86\n"
    "s_waitcnt vmcnt(0)\n"
    "v_lshlrev_b32 v92, 16, v92\n"
    "v_accvgpr_write_b32 a56, v92\n"
    "v_accvgpr_write_b32 a57, v92\n"
    "v_accvgpr_write_b32 a58, v92\n"
    "v_accvgpr_write_b32 a59, v92\n"
    "v_lshlrev_b32 v96, 16, v96\n"
    "v_accvgpr_write_b32 a60, v96\n"
    "v_accvgpr_write_b32 a61, v96\n"
    "v_accvgpr_write_b32 a62, v96\n"
    "v_accvgpr_write_b32 a63, v96\n"
    "s_barrier\n"
    "ds_read_b128 a[64:67], v85 offset:16384\n"
    "ds_read_b128 a[68:71], v85 offset:18432\n"
    "ds_read_b128 a[72:75], v86 offset:16384\n"
    "ds_read_b128 a[76:79], v86 offset:18432\n"
    "ds_read_b64_tr_b16 a[224:225], v64 offset:0\n"
    "ds_read_b64_tr_b16 a[226:227], v64 offset:1024\n"
    "ds_read_b64_tr_b16 a[228:229], v65 offset:0\n"
    "ds_read_b64_tr_b16 a[230:231], v65 offset:1024\n"
    "ds_read_b64_tr_b16 a[232:233], v66 offset:0\n"
    "ds_read_b64_tr_b16 a[234:235], v66 offset:1024\n"
    "ds_read_b64_tr_b16 a[236:237], v67 offset:0\n"
    "ds_read_b64_tr_b16 a[238:239], v67 offset:1024\n"
    "ds_read_b64_tr_b16 a[240:241], v68 offset:0\n"
    "ds_read_b64_tr_b16 a[242:243], v68 offset:1024\n"
    "ds_read_b64_tr_b16 a[244:245], v69 offset:0\n"
    "ds_read_b64_tr_b16 a[246:247], v69 offset:1024\n"
    "s_waitcnt lgkmcnt(12)\n"
    "s_waitcnt lgkmcnt(8)\n"
    "v_mfma_f32_16x16x32_bf16 a[0:3], a[224:227], a[64:67], a[56:59]\n"
    "ds_read_b64_tr_b16 a[248:249], v70 offset:0\n"
    "v_mfma_f32_16x16x32_bf16 a[4:7], a[224:227], a[68:71], a[60:63]\n"
    "ds_read_b64_tr_b16 a[250:251], v70 offset:1024\n"
    "v_mfma_f32_16x16x32_bf16 a[8:11], a[228:231], a[64:67], a[56:59]\n"
    "ds_read_b64_tr_b16 a[252:253], v64 offset:8192\n"
    "v_mfma_f32_16x16x32_bf16 a[12:15], a[228:231], a[68:71], a[60:63]\n"
    "ds_read_b64_tr_b16 a[254:255], v64 offset:9216\n"
    "s_waitcnt lgkmcnt(8)\n"
    "v_mfma_f32_16x16x32_bf16 a[16:19], a[232:235], a[64:67], a[56:59]\n"
    "ds_read_b64_tr_b16 a[224:225], v65 offset:8192\n"
    "v_mfma_f32_16x16x32_bf16 a[20:23], a[232:235], a[68:71], a[60:63]\n"
    "ds_read_b64_tr_b16 a[226:227], v65 offset:9216\n"
    "v_mfma_f32_16x16x32_bf16 a[24:27], a[236:239], a[64:67], a[56:59]\n"
    "ds_read_b64_tr_b16 a[228:229], v66 offset:8192\n"
    "v_mfma_f32_16x16x32_bf16 a[28:31], a[236:239], a[68:71], a[60:63]\n"
    "ds_read_b64_tr_b16 a[230:231], v66 offset:9216\n"
    "s_waitcnt lgkmcnt(8)\n"
    "v_mfma_f32_16x16x32_bf16 a[32:35], a[240:243], a[64:67], a[56:59]\n"
    "ds_read_b64_tr_b16 a[232:233], v67 offset:8192\n"
    "v_mfma_f32_16x16x32_bf16 a[36:39], a[240:243], a[68:71], a[60:63]\n"
    "ds_read_b64_tr_b16 a[234:235], v67 offset:9216\n"
    "v_mfma_f32_16x16x32_bf16 a[40:43], a[244:247], a[64:67], a[56:59]\n"
    "ds_read_b64_tr_b16 a[236:237], v68 offset:8192\n"
    "v_mfma_f32_16x16x32_bf16 a[44:47], a[244:247], a[68:71], a[60:63]\n"
    "ds_read_b64_tr_b16 a[238:239], v68 offset:9216\n"
    "s_waitcnt lgkmcnt(8)\n"
    "v_mfma_f32_16x16x32_bf16 a[48:51], a[248:251], a[64:67], a[56:59]\n"
    "ds_read_b64_tr_b16 a[240:241], v69 offset:8192\n"
    "v_mfma_f32_16x16x32_bf16 a[52:55], a[248:251], a[68:71], a[60:63]\n"
    "ds_read_b64_tr_b16 a[242:243], v69 offset:9216\n"
    "v_mfma_f32_16x16x32_bf16 a[0:3], a[252:255], a[72:75], a[0:3]\n"
    "ds_read_b64_tr_b16 a[244:245], v70 offset:8192\n"
    "v_mfma_f32_16x16x32_bf16 a[4:7], a[252:255], a[76:79], a[4:7]\n"
    "ds_read_b64_tr_b16 a[246:247], v70 offset:9216\n"
    "s_waitcnt lgkmcnt(8)\n"
    "s_barrier\n"
    "ds_read_b128 a[80:83], v87 offset:16384\n"
    "ds_read_b128 a[84:87], v87 offset:18432\n"
    "ds_read_b128 a[88:91], v88 offset:16384\n"
    "ds_read_b128 a[92:95], v88 offset:18432\n"
    "v_mfma_f32_16x16x32_bf16 a[8:11], a[224:227], a[72:75], a[8:11]\n"
    "ds_read_b64_tr_b16 a[248:249], v71 offset:0\n"
    "v_mfma_f32_16x16x32_bf16 a[12:15], a[224:227], a[76:79], a[12:15]\n"
    "ds_read_b64_tr_b16 a[250:251], v71 offset:1024\n"
    "v_mfma_f32_16x16x32_bf16 a[16:19], a[228:231], a[72:75], a[16:19]\n"
    "ds_read_b64_tr_b16 a[252:253], v72 offset:0\n"
    "v_mfma_f32_16x16x32_bf16 a[20:23], a[228:231], a[76:79], a[20:23]\n"
    "ds_read_b64_tr_b16 a[254:255], v72 offset:1024\n"
    "s_waitcnt lgkmcnt(12)\n"
    "v_mfma_f32_16x16x32_bf16 a[24:27], a[232:235], a[72:75], a[24:27]\n"
    "ds_read_b64_tr_b16 a[224:225], v73 offset:0\n"
    "v_mfma_f32_16x16x32_bf16 a[28:31], a[232:235], a[76:79], a[28:31]\n"
    "ds_read_b64_tr_b16 a[226:227], v73 offset:1024\n"
    "v_mfma_f32_16x16x32_bf16 a[32:35], a[236:239], a[72:75], a[32:35]\n"
    "ds_read_b64_tr_b16 a[228:229], v74 offset:0\n"
    "v_mfma_f32_16x16x32_bf16 a[36:39], a[236:239], a[76:79], a[36:39]\n"
    "ds_read_b64_tr_b16 a[230:231], v74 offset:1024\n"
    "s_waitcnt lgkmcnt(12)\n"
    "v_mfma_f32_16x16x32_bf16 a[40:43], a[240:243], a[72:75], a[40:43]\n"
    "ds_read_b64_tr_b16 a[232:233], v75 offset:0\n"
    "v_mfma_f32_16x16x32_bf16 a[44:47], a[240:243], a[76:79], a[44:47]\n"
    "ds_read_b64_tr_b16 a[234:235], v75 offset:1024\n"
    "v_mfma_f32_16x16x32_bf16 a[48:51], a[244:247], a[72:75], a[48:51]\n"
    "ds_read_b64_tr_b16 a[236:237], v76 offset:0\n"
    "v_mfma_f32_16x16x32_bf16 a[52:55], a[244:247], a[76:79], a[52:55]\n"
    "ds_read_b64_tr_b16 a[238:239], v76 offset:1024\n"
    "s_waitcnt lgkmcnt(12)\n"
    "s_waitcnt lgkmcnt(8)\n"
    "v_mfma_f32_16x16x32_bf16 a[0:3], a[248:251], a[80:83], a[0:3]\n"
    "ds_read_b64_tr_b16 a[240:241], v77 offset:0\n"
    "v_mfma_f32_16x16x32_bf16 a[4:7], a[248:251], a[84:87], a[4:7]\n"
    "ds_read_b64_tr_b16 a[242:243], v77 offset:1024\n"
    "v_mfma_f32_16x16x32_bf16 a[8:11], a[252:255], a[80:83], a[8:11]\n"
    "ds_read_b64_tr_b16 a[244:245], v71 offset:8192\n"
    "v_mfma_f32_16x16x32_bf16 a[12:15], a[252:255], a[84:87], a[12:15]\n"
    "ds_read_b64_tr_b16 a[246:247], v71 offset:9216\n"
    "s_waitcnt lgkmcnt(8)\n"
    "v_mfma_f32_16x16x32_bf16 a[16:19], a[224:227], a[80:83], a[16:19]\n"
    "ds_read_b64_tr_b16 a[248:249], v72 offset:8192\n"
    "v_mfma_f32_16x16x32_bf16 a[20:23], a[224:227], a[84:87], a[20:23]\n"
    "ds_read_b64_tr_b16 a[250:251], v72 offset:9216\n"
    "v_mfma_f32_16x16x32_bf16 a[24:27], a[228:231], a[80:83], a[24:27]\n"
    "ds_read_b64_tr_b16 a[252:253], v73 offset:8192\n"
    "v_mfma_f32_16x16x32_bf16 a[28:31], a[228:231], a[84:87], a[28:31]\n"
    "ds_read_b64_tr_b16 a[254:255], v73 offset:9216\n"
    "s_waitcnt lgkmcnt(8)\n"
    "v_mfma_f32_16x16x32_bf16 a[32:35], a[232:235], a[80:83], a[32:35]\n"
    "ds_read_b64_tr_b16 a[224:225], v74 offset:8192\n"
    "v_mfma_f32_16x16x32_bf16 a[36:39], a[232:235], a[84:87], a[36:39]\n"
    "ds_read_b64_tr_b16 a[226:227], v74 offset:9216\n"
    "v_mfma_f32_16x16x32_bf16 a[40:43], a[236:239], a[80:83], a[40:43]\n"
    "ds_read_b64_tr_b16 a[228:229], v75 offset:8192\n"
    "v_mfma_f32_16x16x32_bf16 a[44:47], a[236:239], a[84:87], a[44:47]\n"
    "ds_read_b64_tr_b16 a[230:231], v75 offset:9216\n"
    "s_waitcnt lgkmcnt(8)\n"
    "v_mfma_f32_16x16x32_bf16 a[48:51], a[240:243], a[80:83], a[48:51]\n"
    "ds_read_b64_tr_b16 a[232:233], v76 offset:8192\n"
    "v_mfma_f32_16x16x32_bf16 a[52:55], a[240:243], a[84:87], a[52:55]\n"
    "ds_read_b64_tr_b16 a[234:235], v76 offset:9216\n"
    "v_mfma_f32_16x16x32_bf16 a[0:3], a[244:247], a[88:91], a[0:3]\n"
    "ds_read_b64_tr_b16 a[236:237], v77 offset:8192\n"
    "v_mfma_f32_16x16x32_bf16 a[4:7], a[244:247], a[92:95], a[4:7]\n"
    "ds_read_b64_tr_b16 a[238:239], v77 offset:9216\n"
    "s_waitcnt lgkmcnt(8)\n"
    "s_barrier\n"
    "ds_read_b128 a[64:67], v85 offset:49152\n"
    "ds_read_b128 a[68:71], v85 offset:51200\n"
    "ds_read_b128 a[72:75], v86 offset:49152\n"
    "ds_read_b128 a[76:79], v86 offset:51200\n"
    "v_mfma_f32_16x16x32_bf16 a[8:11], a[248:251], a[88:91], a[8:11]\n"
    "ds_read_b64_tr_b16 a[240:241], v64 offset:32768\n"
    "v_mfma_f32_16x16x32_bf16 a[12:15], a[248:251], a[92:95], a[12:15]\n"
    "ds_read_b64_tr_b16 a[242:243], v64 offset:33792\n"
    "s_add_u32 m0, s50, 0\n"
    "s_add_u32 s49, %[rot], 5\n"
    "s_and_b32 s49, s49, 15\n"
    "s_mul_i32 s49, s49, 14336\n"
    "buffer_load_dwordx4 v34, s[56:59], s49 offen offset:0 sc1 lds\n"
    "v_mfma_f32_16x16x32_bf16 a[16:19], a[252:255], a[88:91], a[16:19]\n"
    "ds_read_b64_tr_b16 a[244:245], v65 offset:32768\n"
    "v_mfma_f32_16x16x32_bf16 a[20:23], a[252:255], a[92:95], a[20:23]\n"
    "ds_read_b64_tr_b16 a[246:247], v65 offset:33792\n"
    "buffer_load_dwordx4 v35, s[56:59], s49 offen offset:1024 sc1 lds\n"
    "s_waitcnt lgkmcnt(12)\n"
    "v_mfma_f32_16x16x32_bf16 a[24:27], a[224:227], a[88:91], a[24:27]\n"
    "ds_read_b64_tr_b16 a[248:249], v66 offset:32768\n"
    "v_mfma_f32_16x16x32_bf16 a[28:31], a[224:227], a[92:95], a[28:31]\n"
    "ds_read_b64_tr_b16 a[250:251], v66 offset:33792\n"
    "buffer_load_dwordx4 v36, s[56:59], s49 offen offset:2048 sc1 lds\n"
    "v_mfma_f32_16x16x32_bf16 a[32:35], a[228:231], a[88:91], a[32:35]\n"
    "ds_read_b64_tr_b16 a[252:253], v67 offset:32768\n"
    "v_mfma_f32_16x16x32_bf16 a[36:39], a[228:231], a[92:95], a[36:39]\n"
    "ds_read_b64_tr_b16 a[254:255], v67 offset:33792\n"
    "buffer_load_dwordx4 v37, s[56:59], s49 offen offset:3072 sc1 lds\n"
    "s_waitcnt lgkmcnt(12)\n"
    "v_mfma_f32_16x16x32_bf16 a[40:43], a[232:235], a[88:91], a[40:43]\n"
    "ds_read_b64_tr_b16 a[224:225], v68 offset:32768\n"
    "v_mfma_f32_16x16x32_bf16 a[44:47], a[232:235], a[92:95], a[44:47]\n"
    "ds_read_b64_tr_b16 a[226:227], v68 offset:33792\n"
    "s_add_u32 m0, s50, 16384\n"
    "s_add_u32 s77, %[rot], 5\n"
    "s_and_b32 s77, s77, 15\n"
    "s_lshl_b32 s77, s77, 7\n"
    "buffer_load_dwordx4 v38, s[60:63], s77 offen offset:0 lds\n"
    "v_mfma_f32_16x16x32_bf16 a[48:51], a[236:239], a[88:91], a[48:51]\n"
    "ds_read_b64_tr_b16 a[228:229], v69 offset:32768\n"
    "v_mfma_f32_16x16x32_bf16 a[52:55], a[236:239], a[92:95], a[52:55]\n"
    "ds_read_b64_tr_b16 a[230:231], v69 offset:33792\n"
    "buffer_load_dwordx4 v39, s[60:63], s77 offen offset:1024 lds\n"
    "s_waitcnt lgkmcnt(12)\n"
    "s_waitcnt lgkmcnt(8)\n"
    "v_mfma_f32_16x16x32_bf16 a[0:3], a[240:243], a[64:67], a[0:3]\n"
    "ds_read_b64_tr_b16 a[232:233], v70 offset:32768\n"
    "v_mfma_f32_16x16x32_bf16 a[4:7], a[240:243], a[68:71], a[4:7]\n"
    "ds_read_b64_tr_b16 a[234:235], v70 offset:33792\n"
    "buffer_load_dwordx4 v40, s[60:63], s77 offen offset:2048 lds\n"
    "v_mfma_f32_16x16x32_bf16 a[8:11], a[244:247], a[64:67], a[8:11]\n"
    "ds_read_b64_tr_b16 a[236:237], v64 offset:40960\n"
    "v_mfma_f32_16x16x32_bf16 a[12:15], a[244:247], a[68:71], a[12:15]\n"
    "ds_read_b64_tr_b16 a[238:239], v64 offset:41984\n"
    "buffer_load_dwordx4 v41, s[60:63], s77 offen offset:3072 lds\n"
    "s_waitcnt lgkmcnt(8)\n"
    "v_mfma_f32_16x16x32_bf16 a[16:19], a[248:251], a[64:67], a[16:19]\n"
    "ds_read_b64_tr_b16 a[240:241], v65 offset:40960\n"
    "v_mfma_f32_16x16x32_bf16 a[20:23], a[248:251], a[68:71], a[20:23]\n"
    "ds_read_b64_tr_b16 a[242:243], v65 offset:41984\n"
    "v_mfma_f32_16x16x32_bf16 a[24:27], a[252:255], a[64:67], a[24:27]\n"
    "ds_read_b64_tr_b16 a[244:245], v66 offset:40960\n"
    "v_mfma_f32_16x16x32_bf16 a[28:31], a[252:255], a[68:71], a[28:31]\n"
    "ds_read_b64_tr_b16 a[246:247], v66 offset:41984\n"
    "s_waitcnt lgkmcnt(8)\n"
    "v_mfma_f32_16x16x32_bf16 a[32:35], a[224:227], a[64:67], a[32:35]\n"
    "ds_read_b64_tr_b16 a[248:249], v67 offset:40960\n"
    "v_mfma_f32_16x16x32_bf16 a[36:39], a[224:227], a[68:71], a[36:39]\n"
    "ds_read_b64_tr_b16 a[250:251], v67 offset:41984\n"
    "v_mfma_f32_16x16x32_bf16 a[40:43], a[228:231], a[64:67], a[40:43]\n"
    "ds_read_b64_tr_b16 a[252:253], v68 offset:40960\n"
    "v_mfma_f32_16x16x32_bf16 a[44:47], a[228:231], a[68:71], a[44:47]\n"
    "ds_read_b64_tr_b16 a[254:255], v68 offset:41984\n"
    "s_waitcnt lgkmcnt(8)\n"
    "v_mfma_f32_16x16x32_bf16 a[48:51], a[232:235], a[64:67], a[48:51]\n"
    "ds_read_b64_tr_b16 a[224:225], v69 offset:40960\n"
    "v_mfma_f32_16x16x32_bf16 a[52:55], a[232:235], a[68:71], a[52:55]\n"
    "ds_read_b64_tr_b16 a[226:227], v69 offset:41984\n"
    "v_mfma_f32_16x16x32_bf16 a[0:3], a[236:239], a[72:75], a[0:3]\n"
    "ds_read_b64_tr_b16 a[228:229], v70 offset:40960\n"
    "v_mfma_f32_16x16x32_bf16 a[4:7], a[236:239], a[76:79], a[4:7]\n"
    "ds_read_b64_tr_b16 a[230:231], v70 offset:41984\n"
    "s_waitcnt lgkmcnt(8)\n"
    "s_barrier\n"
    "ds_read_b128 a[80:83], v89 offset:16384\n"
    "ds_read_b128 a[84:87], v89 offset:18432\n"
    "ds_read_b128 a[88:91], v90 offset:16384\n"
    "ds_read_b128 a[92:95], v90 offset:18432\n"
    "v_mfma_f32_16x16x32_bf16 a[8:11], a[240:243], a[72:75], a[8:11]\n"
    "ds_read_b64_tr_b16 a[232:233], v78 offset:0\n"
    "v_mfma_f32_16x16x32_bf16 a[12:15], a[240:243], a[76:79], a[12:15]\n"
    "ds_read_b64_tr_b16 a[234:235], v78 offset:1024\n"
    "s_add_u32 m0, s50, 65536\n"
    "s_add_u32 s49, %[rot], 6\n"
    "s_and_b32 s49, s49, 15\n"
    "s_mul_i32 s49, s49, 14336\n"
    "buffer_load_dwordx4 v34, s[56:59], s49 offen offset:0 sc1 lds\n"
    "v_mfma_f32_16x16x32_bf16 a[16:19], a[244:247], a[72:75], a[16:19]\n"
    "ds_read_b64_tr_b16 a[236:237], v79 offset:0\n"
    "v_mfma_f32_16x16x32_bf16 a[20:23], a[244:247], a[76:79], a[20:23]\n"
    "ds_read_b64_tr_b16 a[238:239], v79 offset:1024\n"
    "buffer_load_dwordx4 v35, s[56:59], s49 offen offset:1024 sc1 lds\n"
    "s_waitcnt lgkmcnt(12)\n"
    "v_mfma_f32_16x16x32_bf16 a[24:27], a[248:251], a[72:75], a[24:27]\n"
    "ds_read_b64_tr_b16 a[240:241], v80 offset:0\n"
    "v_mfma_f32_16x16x32_bf16 a[28:31], a[248:251], a[76:79], a[28:31]\n"
    "ds_read_b64_tr_b16 a[242:243], v80 offset:1024\n"
    "buffer_load_dwordx4 v36, s[56:59], s49 offen offset:2048 sc1 lds\n"
    "v_mfma_f32_16x16x32_bf16 a[32:35], a[252:255], a[72:75], a[32:35]\n"
    "ds_read_b64_tr_b16 a[244:245], v81 offset:0\n"
    "v_mfma_f32_16x16x32_bf16 a[36:39], a[252:255], a[76:79], a[36:39]\n"
    "ds_read_b64_tr_b16 a[246:247], v81 offset:1024\n"
    "buffer_load_dwordx4 v37, s[56:59], s49 offen offset:3072 sc1 lds\n"
    "s_waitcnt lgkmcnt(12)\n"
    "v_mfma_f32_16x16x32_bf16 a[40:43], a[224:227], a[72:75], a[40:43]\n"
    "ds_read_b64_tr_b16 a[248:249], v82 offset:0\n"
    "v_mfma_f32_16x16x32_bf16 a[44:47], a[224:227], a[76:79], a[44:47]\n"
    "ds_read_b64_tr_b16 a[250:251], v82 offset:1024\n"
    "s_add_u32 m0, s50, 81920\n"
    "s_add_u32 s77, %[rot], 6\n"
    "s_and_b32 s77, s77, 15\n"
    "s_lshl_b32 s77, s77, 7\n"
    "buffer_load_dwordx4 v38, s[60:63], s77 offen offset:0 lds\n"
    "v_mfma_f32_16x16x32_bf16 a[48:51], a[228:231], a[72:75], a[48:51]\n"
    "ds_read_b64_tr_b16 a[252:253], v83 offset:0\n"
    "v_mfma_f32_16x16x32_bf16 a[52:55], a[228:231], a[76:79], a[52:55]\n"
    "ds_read_b64_tr_b16 a[254:255], v83 offset:1024\n"
    "buffer_load_dwordx4 v39, s[60:63], s77 offen offset:1024 lds\n"
    "s_waitcnt lgkmcnt(12)\n"
    "s_waitcnt lgkmcnt(8)\n"
    "v_mfma_f32_16x16x32_bf16 a[0:3], a[232:235], a[80:83], a[0:3]\n"
    "ds_read_b64_tr_b16 a[224:225], v84 offset:0\n"
    "v_mfma_f32_16x16x32_bf16 a[4:7], a[232:235], a[84:87], a[4:7]\n"
    "ds_read_b64_tr_b16 a[226:227], v84 offset:1024\n"
    "buffer_load_dwordx4 v40, s[60:63], s77 offen offset:2048 lds\n"
    "v_mfma_f32_16x16x32_bf16 a[8:11], a[236:239], a[80:83], a[8:11]\n"
    "ds_read_b64_tr_b16 a[228:229], v78 offset:8192\n"
    "v_mfma_f32_16x16x32_bf16 a[12:15], a[236:239], a[84:87], a[12:15]\n"
    "ds_read_b64_tr_b16 a[230:231], v78 offset:9216\n"
    "buffer_load_dwordx4 v41, s[60:63], s77 offen offset:3072 lds\n"
    "s_waitcnt lgkmcnt(8)\n"
    "v_mfma_f32_16x16x32_bf16 a[16:19], a[240:243], a[80:83], a[16:19]\n"
    "ds_read_b64_tr_b16 a[232:233], v79 offset:8192\n"
    "v_mfma_f32_16x16x32_bf16 a[20:23], a[240:243], a[84:87], a[20:23]\n"
    "ds_read_b64_tr_b16 a[234:235], v79 offset:9216\n"
    "v_mfma_f32_16x16x32_bf16 a[24:27], a[244:247], a[80:83], a[24:27]\n"
    "ds_read_b64_tr_b16 a[236:237], v80 offset:8192\n"
    "v_mfma_f32_16x16x32_bf16 a[28:31], a[244:247], a[84:87], a[28:31]\n"
    "ds_read_b64_tr_b16 a[238:239], v80 offset:9216\n"
    "s_waitcnt lgkmcnt(8)\n"
    "v_mfma_f32_16x16x32_bf16 a[32:35], a[248:251], a[80:83], a[32:35]\n"
    "ds_read_b64_tr_b16 a[240:241], v81 offset:8192\n"
    "v_mfma_f32_16x16x32_bf16 a[36:39], a[248:251], a[84:87], a[36:39]\n"
    "ds_read_b64_tr_b16 a[242:243], v81 offset:9216\n"
    "v_mfma_f32_16x16x32_bf16 a[40:43], a[252:255], a[80:83], a[40:43]\n"
    "ds_read_b64_tr_b16 a[244:245], v82 offset:8192\n"
    "v_mfma_f32_16x16x32_bf16 a[44:47], a[252:255], a[84:87], a[44:47]\n"
    "ds_read_b64_tr_b16 a[246:247], v82 offset:9216\n"
    "s_waitcnt lgkmcnt(8)\n"
    "v_mfma_f32_16x16x32_bf16 a[48:51], a[224:227], a[80:83], a[48:51]\n"
    "ds_read_b64_tr_b16 a[248:249], v83 offset:8192\n"
    "v_mfma_f32_16x16x32_bf16 a[52:55], a[224:227], a[84:87], a[52:55]\n"
    "ds_read_b64_tr_b16 a[250:251], v83 offset:9216\n"
    "v_mfma_f32_16x16x32_bf16 a[0:3], a[228:231], a[88:91], a[0:3]\n"
    "ds_read_b64_tr_b16 a[252:253], v84 offset:8192\n"
    "v_mfma_f32_16x16x32_bf16 a[4:7], a[228:231], a[92:95], a[4:7]\n"
    "ds_read_b64_tr_b16 a[254:255], v84 offset:9216\n"
    "s_waitcnt lgkmcnt(8)\n"
    "s_barrier\n"
    "ds_read_b128 a[64:67], v87 offset:49152\n"
    "ds_read_b128 a[68:71], v87 offset:51200\n"
    "ds_read_b128 a[72:75], v88 offset:49152\n"
    "ds_read_b128 a[76:79], v88 offset:51200\n"
    "v_mfma_f32_16x16x32_bf16 a[8:11], a[232:235], a[88:91], a[8:11]\n"
    "ds_read_b64_tr_b16 a[224:225], v71 offset:32768\n"
    "v_mfma_f32_16x16x32_bf16 a[12:15], a[232:235], a[92:95], a[12:15]\n"
    "ds_read_b64_tr_b16 a[226:227], v71 offset:33792\n"
    "s_add_u32 m0, s50, 32768\n"
    "s_add_u32 s49, %[rot], 7\n"
    "s_and_b32 s49, s49, 15\n"
    "s_mul_i32 s49, s49, 14336\n"
    "buffer_load_dwordx4 v34, s[56:59], s49 offen offset:0 sc1 lds\n"
    "v_mfma_f32_16x16x32_bf16 a[16:19], a[236:239], a[88:91], a[16:19]\n"
    "ds_read_b64_tr_b16 a[228:229], v72 offset:32768\n"
    "v_mfma_f32_16x16x32_bf16 a[20:23], a[236:239], a[92:95], a[20:23]\n"
    "ds_read_b64_tr_b16 a[230:231], v72 offset:33792\n"
    "buffer_load_dwordx4 v35, s[56:59], s49 offen offset:1024 sc1 lds\n"
    "s_waitcnt lgkmcnt(12)\n"
    "v_mfma_f32_16x16x32_bf16 a[24:27], a[240:243], a[88:91], a[24:27]\n"
    "ds_read_b64_tr_b16 a[232:233], v73 offset:32768\n"
    "v_mfma_f32_16x16x32_bf16 a[28:31], a[240:243], a[92:95], a[28:31]\n"
    "ds_read_b64_tr_b16 a[234:235], v73 offset:33792\n"
    "buffer_load_dwordx4 v36, s[56:59], s49 offen offset:2048 sc1 lds\n"
    "v_mfma_f32_16x16x32_bf16 a[32:35], a[244:247], a[88:91], a[32:35]\n"
    "ds_read_b64_tr_b16 a[236:237], v74 offset:32768\n"
    "v_mfma_f32_16x16x32_bf16 a[36:39], a[244:247], a[92:95], a[36:39]\n"
    "ds_read_b64_tr_b16 a[238:239], v74 offset:33792\n"
    "buffer_load_dwordx4 v37, s[56:59], s49 offen offset:3072 sc1 lds\n"
    "s_waitcnt lgkmcnt(12)\n"
    "v_mfma_f32_16x16x32_bf16 a[40:43], a[248:251], a[88:91], a[40:43]\n"
    "ds_read_b64_tr_b16 a[240:241], v75 offset:32768\n"
    "v_mfma_f32_16x16x32_bf16 a[44:47], a[248:251], a[92:95], a[44:47]\n"
    "ds_read_b64_tr_b16 a[242:243], v75 offset:33792\n"
    "s_add_u32 m0, s50, 49152\n"
    "s_add_u32 s77, %[rot], 7\n"
    "s_and_b32 s77, s77, 15\n"
    "s_lshl_b32 s77, s77, 7\n"
    "buffer_load_dwordx4 v38, s[60:63], s77 offen offset:0 lds\n"
    "v_mfma_f32_16x16x32_bf16 a[48:51], a[252:255], a[88:91], a[48:51]\n"
    "ds_read_b64_tr_b16 a[244:245], v76 offset:32768\n"
    "v_mfma_f32_16x16x32_bf16 a[52:55], a[252:255], a[92:95], a[52:55]\n"
    "ds_read_b64_tr_b16 a[246:247], v76 offset:33792\n"
    "buffer_load_dwordx4 v39, s[60:63], s77 offen offset:1024 lds\n"
    "s_waitcnt lgkmcnt(12)\n"
    "s_waitcnt lgkmcnt(8)\n"
    "v_mfma_f32_16x16x32_bf16 a[0:3], a[224:227], a[64:67], a[0:3]\n"
    "ds_read_b64_tr_b16 a[248:249], v77 offset:32768\n"
    "v_mfma_f32_16x16x32_bf16 a[4:7], a[224:227], a[68:71], a[4:7]\n"
    "ds_read_b64_tr_b16 a[250:251], v77 offset:33792\n"
    "buffer_load_dwordx4 v40, s[60:63], s77 offen offset:2048 lds\n"
    "v_mfma_f32_16x16x32_bf16 a[8:11], a[228:231], a[64:67], a[8:11]\n"
    "ds_read_b64_tr_b16 a[252:253], v71 offset:40960\n"
    "v_mfma_f32_16x16x32_bf16 a[12:15], a[228:231], a[68:71], a[12:15]\n"
    "ds_read_b64_tr_b16 a[254:255], v71 offset:41984\n"
    "buffer_load_dwordx4 v41, s[60:63], s77 offen offset:3072 lds\n"
    "s_waitcnt lgkmcnt(8)\n"
    "v_mfma_f32_16x16x32_bf16 a[16:19], a[232:235], a[64:67], a[16:19]\n"
    "ds_read_b64_tr_b16 a[224:225], v72 offset:40960\n"
    "v_mfma_f32_16x16x32_bf16 a[20:23], a[232:235], a[68:71], a[20:23]\n"
    "ds_read_b64_tr_b16 a[226:227], v72 offset:41984\n"
    "v_mfma_f32_16x16x32_bf16 a[24:27], a[236:239], a[64:67], a[24:27]\n"
    "ds_read_b64_tr_b16 a[228:229], v73 offset:40960\n"
    "v_mfma_f32_16x16x32_bf16 a[28:31], a[236:239], a[68:71], a[28:31]\n"
    "ds_read_b64_tr_b16 a[230:231], v73 offset:41984\n"
    "s_waitcnt lgkmcnt(8)\n"
    "v_mfma_f32_16x16x32_bf16 a[32:35], a[240:243], a[64:67], a[32:35]\n"
    "ds_read_b64_tr_b16 a[232:233], v74 offset:40960\n"
    "v_mfma_f32_16x16x32_bf16 a[36:39], a[240:243], a[68:71], a[36:39]\n"
    "ds_read_b64_tr_b16 a[234:235], v74 offset:41984\n"
    "v_mfma_f32_16x16x32_bf16 a[40:43], a[244:247], a[64:67], a[40:43]\n"
    "ds_read_b64_tr_b16 a[236:237], v75 offset:40960\n"
    "v_mfma_f32_16x16x32_bf16 a[44:47], a[244:247], a[68:71], a[44:47]\n"
    "ds_read_b64_tr_b16 a[238:239], v75 offset:41984\n"
    "s_waitcnt lgkmcnt(8)\n"
    "v_mfma_f32_16x16x32_bf16 a[48:51], a[248:251], a[64:67], a[48:51]\n"
    "ds_read_b64_tr_b16 a[240:241], v76 offset:40960\n"
    "v_mfma_f32_16x16x32_bf16 a[52:55], a[248:251], a[68:71], a[52:55]\n"
    "ds_read_b64_tr_b16 a[242:243], v76 offset:41984\n"
    "v_mfma_f32_16x16x32_bf16 a[0:3], a[252:255], a[72:75], a[0:3]\n"
    "ds_read_b64_tr_b16 a[244:245], v77 offset:40960\n"
    "v_mfma_f32_16x16x32_bf16 a[4:7], a[252:255], a[76:79], a[4:7]\n"
    "ds_read_b64_tr_b16 a[246:247], v77 offset:41984\n"
    "s_waitcnt lgkmcnt(8)\n"
    "s_waitcnt vmcnt(16)\n"
    "s_barrier\n"
    "ds_read_b128 a[80:83], v85 offset:16384\n"
    "ds_read_b128 a[84:87], v85 offset:18432\n"
    "ds_read_b128 a[88:91], v86 offset:16384\n"
    "ds_read_b128 a[92:95], v86 offset:18432\n"
    "v_mfma_f32_16x16x32_bf16 a[8:11], a[224:227], a[72:75], a[8:11]\n"
    "ds_read_b64_tr_b16 a[248:249], v64 offset:0\n"
    "v_mfma_f32_16x16x32_bf16 a[12:15], a[224:227], a[76:79], a[12:15]\n"
    "ds_read_b64_tr_b16 a[250:251], v64 offset:1024\n"
    "s_add_u32 m0, s50, 131072\n"
    "s_add_u32 s49, %[rot], 8\n"
    "s_and_b32 s49, s49, 15\n"
    "s_mul_i32 s49, s49, 14336\n"
    "buffer_load_dwordx4 v34, s[56:59], s49 offen offset:0 sc1 lds\n"
    "v_mfma_f32_16x16x32_bf16 a[16:19], a[228:231], a[72:75], a[16:19]\n"
    "ds_read_b64_tr_b16 a[252:253], v65 offset:0\n"
    "v_mfma_f32_16x16x32_bf16 a[20:23], a[228:231], a[76:79], a[20:23]\n"
    "ds_read_b64_tr_b16 a[254:255], v65 offset:1024\n"
    "buffer_load_dwordx4 v35, s[56:59], s49 offen offset:1024 sc1 lds\n"
    "s_waitcnt lgkmcnt(12)\n"
    "v_mfma_f32_16x16x32_bf16 a[24:27], a[232:235], a[72:75], a[24:27]\n"
    "ds_read_b64_tr_b16 a[224:225], v66 offset:0\n"
    "v_mfma_f32_16x16x32_bf16 a[28:31], a[232:235], a[76:79], a[28:31]\n"
    "ds_read_b64_tr_b16 a[226:227], v66 offset:1024\n"
    "buffer_load_dwordx4 v36, s[56:59], s49 offen offset:2048 sc1 lds\n"
    "v_mfma_f32_16x16x32_bf16 a[32:35], a[236:239], a[72:75], a[32:35]\n"
    "ds_read_b64_tr_b16 a[228:229], v67 offset:0\n"
    "v_mfma_f32_16x16x32_bf16 a[36:39], a[236:239], a[76:79], a[36:39]\n"
    "ds_read_b64_tr_b16 a[230:231], v67 offset:1024\n"
    "buffer_load_dwordx4 v37, s[56:59], s49 offen offset:3072 sc1 lds\n"
    "s_waitcnt lgkmcnt(12)\n"
    "v_mfma_f32_16x16x32_bf16 a[40:43], a[240:243], a[72:75], a[40:43]\n"
    "ds_read_b64_tr_b16 a[232:233], v68 offset:0\n"
    "v_mfma_f32_16x16x32_bf16 a[44:47], a[240:243], a[76:79], a[44:47]\n"
    "ds_read_b64_tr_b16 a[234:235], v68 offset:1024\n"
    "s_add_u32 m0, s50, 147456\n"
    "s_add_u32 s77, %[rot], 8\n"
    "s_and_b32 s77, s77, 15\n"
    "s_lshl_b32 s77, s77, 7\n"
    "buffer_load_dwordx4 v38, s[60:63], s77 offen offset:0 lds\n"
    "v_mfma_f32_16x16x32_bf16 a[48:51], a[244:247], a[72:75], a[48:51]\n"
    "ds_read_b64_tr_b16 a[236:237], v69 offset:0\n"
    "v_mfma_f32_16x16x32_bf16 a[52:55], a[244:247], a[76:79], a[52:55]\n"
    "ds_read_b64_tr_b16 a[238:239], v69 offset:1024\n"
    "buffer_load_dwordx4 v39, s[60:63], s77 offen offset:1024 lds\n"
    "s_waitcnt lgkmcnt(12)\n"
    "s_waitcnt lgkmcnt(8)\n"
    "v_mfma_f32_16x16x32_bf16 a[0:3], a[248:251], a[80:83], a[0:3]\n"
    "ds_read_b64_tr_b16 a[240:241], v70 offset:0\n"
    "v_mfma_f32_16x16x32_bf16 a[4:7], a[248:251], a[84:87], a[4:7]\n"
    "ds_read_b64_tr_b16 a[242:243], v70 offset:1024\n"
    "buffer_load_dwordx4 v40, s[60:63], s77 offen offset:2048 lds\n"
    "v_mfma_f32_16x16x32_bf16 a[8:11], a[252:255], a[80:83], a[8:11]\n"
    "ds_read_b64_tr_b16 a[244:245], v64 offset:8192\n"
    "v_mfma_f32_16x16x32_bf16 a[12:15], a[252:255], a[84:87], a[12:15]\n"
    "ds_read_b64_tr_b16 a[246:247], v64 offset:9216\n"
    "buffer_load_dwordx4 v41, s[60:63], s77 offen offset:3072 lds\n"
    "s_waitcnt lgkmcnt(8)\n"
    "v_mfma_f32_16x16x32_bf16 a[16:19], a[224:227], a[80:83], a[16:19]\n"
    "ds_read_b64_tr_b16 a[248:249], v65 offset:8192\n"
    "v_mfma_f32_16x16x32_bf16 a[20:23], a[224:227], a[84:87], a[20:23]\n"
    "ds_read_b64_tr_b16 a[250:251], v65 offset:9216\n"
    "v_mfma_f32_16x16x32_bf16 a[24:27], a[228:231], a[80:83], a[24:27]\n"
    "ds_read_b64_tr_b16 a[252:253], v66 offset:8192\n"
    "v_mfma_f32_16x16x32_bf16 a[28:31], a[228:231], a[84:87], a[28:31]\n"
    "ds_read_b64_tr_b16 a[254:255], v66 offset:9216\n"
    "s_waitcnt lgkmcnt(8)\n"
    "v_mfma_f32_16x16x32_bf16 a[32:35], a[232:235], a[80:83], a[32:35]\n"
    "ds_read_b64_tr_b16 a[224:225], v67 offset:8192\n"
    "v_mfma_f32_16x16x32_bf16 a[36:39], a[232:235], a[84:87], a[36:39]\n"
    "ds_read_b64_tr_b16 a[226:227], v67 offset:9216\n"
    "v_mfma_f32_16x16x32_bf16 a[40:43], a[236:239], a[80:83], a[40:43]\n"
    "ds_read_b64_tr_b16 a[228:229], v68 offset:8192\n"
    "v_mfma_f32_16x16x32_bf16 a[44:47], a[236:239], a[84:87], a[44:47]\n"
    "ds_read_b64_tr_b16 a[230:231], v68 offset:9216\n"
    "s_waitcnt lgkmcnt(8)\n"
    "v_mfma_f32_16x16x32_bf16 a[48:51], a[240:243], a[80:83], a[48:51]\n"
    "ds_read_b64_tr_b16 a[232:233], v69 offset:8192\n"
    "v_mfma_f32_16x16x32_bf16 a[52:55], a[240:243], a[84:87], a[52:55]\n"
    "ds_read_b64_tr_b16 a[234:235], v69 offset:9216\n"
    "v_mfma_f32_16x16x32_bf16 a[0:3], a[244:247], a[88:91], a[0:3]\n"
    "ds_read_b64_tr_b16 a[236:237], v70 offset:8192\n"
    "v_mfma_f32_16x16x32_bf16 a[4:7], a[244:247], a[92:95], a[4:7]\n"
    "ds_read_b64_tr_b16 a[238:239], v70 offset:9216\n"
    "s_waitcnt lgkmcnt(8)\n"
    "s_waitcnt vmcnt(16)\n"
    "s_barrier\n"
    "ds_read_b128 a[64:67], v87 offset:16384\n"
    "ds_read_b128 a[68:71], v87 offset:18432\n"
    "ds_read_b128 a[72:75], v88 offset:16384\n"
    "ds_read_b128 a[76:79], v88 offset:18432\n"
    "v_mfma_f32_16x16x32_bf16 a[8:11], a[248:251], a[88:91], a[8:11]\n"
    "ds_read_b64_tr_b16 a[240:241], v71 offset:0\n"
    "v_mfma_f32_16x16x32_bf16 a[12:15], a[248:251], a[92:95], a[12:15]\n"
    "ds_read_b64_tr_b16 a[242:243], v71 offset:1024\n"
    "s_add_u32 m0, s50, 98304\n"
    "s_add_u32 s49, %[rot], 9\n"
    "s_and_b32 s49, s49, 15\n"
    "s_mul_i32 s49, s49, 14336\n"
    "buffer_load_dwordx4 v34, s[56:59], s49 offen offset:0 sc1 lds\n"
    "v_mfma_f32_16x16x32_bf16 a[16:19], a[252:255], a[88:91], a[16:19]\n"
    "ds_read_b64_tr_b16 a[244:245], v72 offset:0\n"
    "v_mfma_f32_16x16x32_bf16 a[20:23], a[252:255], a[92:95], a[20:23]\n"
    "ds_read_b64_tr_b16 a[246:247], v72 offset:1024\n"
    "buffer_load_dwordx4 v35, s[56:59], s49 offen offset:1024 sc1 lds\n"
    "s_waitcnt lgkmcnt(12)\n"
    "v_mfma_f32_16x16x32_bf16 a[24:27], a[224:227], a[88:91], a[24:27]\n"
    "ds_read_b64_tr_b16 a[248:249], v73 offset:0\n"
    "v_mfma_f32_16x16x32_bf16 a[28:31], a[224:227], a[92:95], a[28:31]\n"
    "ds_read_b64_tr_b16 a[250:251], v73 offset:1024\n"
    "buffer_load_dwordx4 v36, s[56:59], s49 offen offset:2048 sc1 lds\n"
    "v_mfma_f32_16x16x32_bf16 a[32:35], a[228:231], a[88:91], a[32:35]\n"
    "ds_read_b64_tr_b16 a[252:253], v74 offset:0\n"
    "v_mfma_f32_16x16x32_bf16 a[36:39], a[228:231], a[92:95], a[36:39]\n"
    "ds_read_b64_tr_b16 a[254:255], v74 offset:1024\n"
    "buffer_load_dwordx4 v37, s[56:59], s49 offen offset:3072 sc1 lds\n"
    "s_waitcnt lgkmcnt(12)\n"
    "v_mfma_f32_16x16x32_bf16 a[40:43], a[232:235], a[88:91], a[40:43]\n"
    "ds_read_b64_tr_b16 a[224:225], v75 offset:0\n"
    "v_mfma_f32_16x16x32_bf16 a[44:47], a[232:235], a[92:95], a[44:47]\n"
    "ds_read_b64_tr_b16 a[226:227], v75 offset:1024\n"
    "s_add_u32 m0, s50, 114688\n"
    "s_add_u32 s77, %[rot], 9\n"
    "s_and_b32 s77, s77, 15\n"
    "s_lshl_b32 s77, s77, 7\n"
    "buffer_load_dwordx4 v38, s[60:63], s77 offen offset:0 lds\n"
    "v_mfma_f32_16x16x32_bf16 a[48:51], a[236:239], a[88:91], a[48:51]\n"
    "ds_read_b64_tr_b16 a[228:229], v76 offset:0\n"
    "v_mfma_f32_16x16x32_bf16 a[52:55], a[236:239], a[92:95], a[52:55]\n"
    "ds_read_b64_tr_b16 a[230:231], v76 offset:1024\n"
    "buffer_load_dwordx4 v39, s[60:63], s77 offen offset:1024 lds\n"
    "s_waitcnt lgkmcnt(12)\n"
    "s_waitcnt lgkmcnt(8)\n"
    "v_mfma_f32_16x16x32_bf16 a[0:3], a[240:243], a[64:67], a[0:3]\n"
    "ds_read_b64_tr_b16 a[232:233], v77 offset:0\n"
    "v_mfma_f32_16x16x32_bf16 a[4:7], a[240:243], a[68:71], a[4:7]\n"
    "ds_read_b64_tr_b16 a[234:235], v77 offset:1024\n"
    "buffer_load_dwordx4 v40, s[60:63], s77 offen offset:2048 lds\n"
    "v_mfma_f32_16x16x32_bf16 a[8:11], a[244:247], a[64:67], a[8:11]\n"
    "ds_read_b64_tr_b16 a[236:237], v71 offset:8192\n"
    "v_mfma_f32_16x16x32_bf16 a[12:15], a[244:247], a[68:71], a[12:15]\n"
    "ds_read_b64_tr_b16 a[238:239], v71 offset:9216\n"
    "buffer_load_dwordx4 v41, s[60:63], s77 offen offset:3072 lds\n"
    "s_waitcnt lgkmcnt(8)\n"
    "v_mfma_f32_16x16x32_bf16 a[16:19], a[248:251], a[64:67], a[16:19]\n"
    "ds_read_b64_tr_b16 a[240:241], v72 offset:8192\n"
    "v_mfma_f32_16x16x32_bf16 a[20:23], a[248:251], a[68:71], a[20:23]\n"
    "ds_read_b64_tr_b16 a[242:243], v72 offset:9216\n"
    "v_mfma_f32_16x16x32_bf16 a[24:27], a[252:255], a[64:67], a[24:27]\n"
    "ds_read_b64_tr_b16 a[244:245], v73 offset:8192\n"
    "v_mfma_f32_16x16x32_bf16 a[28:31], a[252:255], a[68:71], a[28:31]\n"
    "ds_read_b64_tr_b16 a[246:247], v73 offset:9216\n"
    "s_waitcnt lgkmcnt(8)\n"
    "v_mfma_f32_16x16x32_bf16 a[32:35], a[224:227], a[64:67], a[32:35]\n"
    "ds_read_b64_tr_b16 a[248:249], v74 offset:8192\n"
    "v_mfma_f32_16x16x32_bf16 a[36:39], a[224:227], a[68:71], a[36:39]\n"
    "ds_read_b64_tr_b16 a[250:251], v74 offset:9216\n"
    "v_mfma_f32_16x16x32_bf16 a[40:43], a[228:231], a[64:67], a[40:43]\n"
    "ds_read_b64_tr_b16 a[252:253], v75 offset:8192\n"
    "v_mfma_f32_16x16x32_bf16 a[44:47], a[228:231], a[68:71], a[44:47]\n"
    "ds_read_b64_tr_b16 a[254:255], v75 offset:9216\n"
    "s_waitcnt lgkmcnt(8)\n"
    "v_mfma_f32_16x16x32_bf16 a[48:51], a[232:235], a[64:67], a[48:51]\n"
    "ds_read_b64_tr_b16 a[224:225], v76 offset:8192\n"
    "v_mfma_f32_16x16x32_bf16 a[52:55], a[232:235], a[68:71], a[52:55]\n"
    "ds_read_b64_tr_b16 a[226:227], v76 offset:9216\n"
    "v_mfma_f32_16x16x32_bf16 a[0:3], a[236:239], a[72:75], a[0:3]\n"
    "ds_read_b64_tr_b16 a[228:229], v77 offset:8192\n"
    "v_mfma_f32_16x16x32_bf16 a[4:7], a[236:239], a[76:79], a[4:7]\n"
    "ds_read_b64_tr_b16 a[230:231], v77 offset:9216\n"
    "s_waitcnt lgkmcnt(8)\n"
    "s_waitcnt vmcnt(16)\n"
    "s_barrier\n"
    "ds_read_b128 a[80:83], v85 offset:49152\n"
    "ds_read_b128 a[84:87], v85 offset:51200\n"
    "ds_read_b128 a[88:91], v86 offset:49152\n"
    "ds_read_b128 a[92:95], v86 offset:51200\n"
    "v_mfma_f32_16x16x32_bf16 a[8:11], a[240:243], a[72:75], a[8:11]\n"
    "ds_read_b64_tr_b16 a[232:233], v64 offset:32768\n"
    "v_mfma_f32_16x16x32_bf16 a[12:15], a[240:243], a[76:79], a[12:15]\n"
    "ds_read_b64_tr_b16 a[234:235], v64 offset:33792\n"
    "s_add_u32 m0, s50, 0\n"
    "s_add_u32 s49, %[rot], 10\n"
    "s_and_b32 s49, s49, 15\n"
    "s_mul_i32 s49, s49, 14336\n"
    "buffer_load_dwordx4 v34, s[56:59], s49 offen offset:0 sc1 lds\n"
    "v_mfma_f32_16x16x32_bf16 a[16:19], a[244:247], a[72:75], a[16:19]\n"
    "ds_read_b64_tr_b16 a[236:237], v65 offset:32768\n"
    "v_mfma_f32_16x16x32_bf16 a[20:23], a[244:247], a[76:79], a[20:23]\n"
    "ds_read_b64_tr_b16 a[238:239], v65 offset:33792\n"
    "buffer_load_dwordx4 v35, s[56:59], s49 offen offset:1024 sc1 lds\n"
    "s_waitcnt lgkmcnt(12)\n"
    "v_mfma_f32_16x16x32_bf16 a[24:27], a[248:251], a[72:75], a[24:27]\n"
    "ds_read_b64_tr_b16 a[240:241], v66 offset:32768\n"
    "v_mfma_f32_16x16x32_bf16 a[28:31], a[248:251], a[76:79], a[28:31]\n"
    "ds_read_b64_tr_b16 a[242:243], v66 offset:33792\n"
    "buffer_load_dwordx4 v36, s[56:59], s49 offen offset:2048 sc1 lds\n"
    "v_mfma_f32_16x16x32_bf16 a[32:35], a[252:255], a[72:75], a[32:35]\n"
    "ds_read_b64_tr_b16 a[244:245], v67 offset:32768\n"
    "v_mfma_f32_16x16x32_bf16 a[36:39], a[252:255], a[76:79], a[36:39]\n"
    "ds_read_b64_tr_b16 a[246:247], v67 offset:33792\n"
    "buffer_load_dwordx4 v37, s[56:59], s49 offen offset:3072 sc1 lds\n"
    "s_waitcnt lgkmcnt(12)\n"
    "v_mfma_f32_16x16x32_bf16 a[40:43], a[224:227], a[72:75], a[40:43]\n"
    "ds_read_b64_tr_b16 a[248:249], v68 offset:32768\n"
    "v_mfma_f32_16x16x32_bf16 a[44:47], a[224:227], a[76:79], a[44:47]\n"
    "ds_read_b64_tr_b16 a[250:251], v68 offset:33792\n"
    "s_add_u32 m0, s50, 16384\n"
    "s_add_u32 s77, %[rot], 10\n"
    "s_and_b32 s77, s77, 15\n"
    "s_lshl_b32 s77, s77, 7\n"
    "buffer_load_dwordx4 v38, s[60:63], s77 offen offset:0 lds\n"
    "v_mfma_f32_16x16x32_bf16 a[48:51], a[228:231], a[72:75], a[48:51]\n"
    "ds_read_b64_tr_b16 a[252:253], v69 offset:32768\n"
    "v_mfma_f32_16x16x32_bf16 a[52:55], a[228:231], a[76:79], a[52:55]\n"
    "ds_read_b64_tr_b16 a[254:255], v69 offset:33792\n"
    "buffer_load_dwordx4 v39, s[60:63], s77 offen offset:1024 lds\n"
    "s_waitcnt lgkmcnt(12)\n"
    "s_waitcnt lgkmcnt(8)\n"
    "v_mfma_f32_16x16x32_bf16 a[0:3], a[232:235], a[80:83], a[0:3]\n"
    "ds_read_b64_tr_b16 a[224:225], v70 offset:32768\n"
    "v_mfma_f32_16x16x32_bf16 a[4:7], a[232:235], a[84:87], a[4:7]\n"
    "ds_read_b64_tr_b16 a[226:227], v70 offset:33792\n"
    "buffer_load_dwordx4 v40, s[60:63], s77 offen offset:2048 lds\n"
    "v_mfma_f32_16x16x32_bf16 a[8:11], a[236:239], a[80:83], a[8:11]\n"
    "ds_read_b64_tr_b16 a[228:229], v64 offset:40960\n"
    "v_mfma_f32_16x16x32_bf16 a[12:15], a[236:239], a[84:87], a[12:15]\n"
    "ds_read_b64_tr_b16 a[230:231], v64 offset:41984\n"
    "buffer_load_dwordx4 v41, s[60:63], s77 offen offset:3072 lds\n"
    "s_waitcnt lgkmcnt(8)\n"
    "v_mfma_f32_16x16x32_bf16 a[16:19], a[240:243], a[80:83], a[16:19]\n"
    "ds_read_b64_tr_b16 a[232:233], v65 offset:40960\n"
    "v_mfma_f32_16x16x32_bf16 a[20:23], a[240:243], a[84:87], a[20:23]\n"
    "ds_read_b64_tr_b16 a[234:235], v65 offset:41984\n"
    "v_mfma_f32_16x16x32_bf16 a[24:27], a[244:247], a[80:83], a[24:27]\n"
    "ds_read_b64_tr_b16 a[236:237], v66 offset:40960\n"
    "v_mfma_f32_16x16x32_bf16 a[28:31], a[244:247], a[84:87], a[28:31]\n"
    "ds_read_b64_tr_b16 a[238:239], v66 offset:41984\n"
    "s_waitcnt lgkmcnt(8)\n"
    "v_mfma_f32_16x16x32_bf16 a[32:35], a[248:251], a[80:83], a[32:35]\n"
    "ds_read_b64_tr_b16 a[240:241], v67 offset:40960\n"
    "v_mfma_f32_16x16x32_bf16 a[36:39], a[248:251], a[84:87], a[36:39]\n"
    "ds_read_b64_tr_b16 a[242:243], v67 offset:41984\n"
    "v_mfma_f32_16x16x32_bf16 a[40:43], a[252:255], a[80:83], a[40:43]\n"
    "ds_read_b64_tr_b16 a[244:245], v68 offset:40960\n"
    "v_mfma_f32_16x16x32_bf16 a[44:47], a[252:255], a[84:87], a[44:47]\n"
    "ds_read_b64_tr_b16 a[246:247], v68 offset:41984\n"
    "s_waitcnt lgkmcnt(8)\n"
    "v_mfma_f32_16x16x32_bf16 a[48:51], a[224:227], a[80:83], a[48:51]\n"
    "ds_read_b64_tr_b16 a[248:249], v69 offset:40960\n"
    "v_mfma_f32_16x16x32_bf16 a[52:55], a[224:227], a[84:87], a[52:55]\n"
    "ds_read_b64_tr_b16 a[250:251], v69 offset:41984\n"
    "v_mfma_f32_16x16x32_bf16 a[0:3], a[228:231], a[88:91], a[0:3]\n"
    "ds_read_b64_tr_b16 a[252:253], v70 offset:40960\n"
    "v_mfma_f32_16x16x32_bf16 a[4:7], a[228:231], a[92:95], a[4:7]\n"
    "ds_read_b64_tr_b16 a[254:255], v70 offset:41984\n"
    "s_waitcnt lgkmcnt(8)\n"
    "s_waitcnt vmcnt(16)\n"
    "s_barrier\n"
    "ds_read_b128 a[64:67], v89 offset:16384\n"
    "ds_read_b128 a[68:71], v89 offset:18432\n"
    "ds_read_b128 a[72:75], v90 offset:16384\n"
    "ds_read_b128 a[76:79], v90 offset:18432\n"
    "v_mfma_f32_16x16x32_bf16 a[8:11], a[232:235], a[88:91], a[8:11]\n"
    "ds_read_b64_tr_b16 a[224:225], v78 offset:0\n"
    "v_mfma_f32_16x16x32_bf16 a[12:15], a[232:235], a[92:95], a[12:15]\n"
    "ds_read_b64_tr_b16 a[226:227], v78 offset:1024\n"
    "s_add_u32 m0, s50, 65536\n"
    "s_add_u32 s49, %[rot], 11\n"
    "s_and_b32 s49, s49, 15\n"
    "s_mul_i32 s49, s49, 14336\n"
    "buffer_load_dwordx4 v34, s[56:59], s49 offen offset:0 sc1 lds\n"
    "v_mfma_f32_16x16x32_bf16 a[16:19], a[236:239], a[88:91], a[16:19]\n"
    "ds_read_b64_tr_b16 a[228:229], v79 offset:0\n"
    "v_mfma_f32_16x16x32_bf16 a[20:23], a[236:239], a[92:95], a[20:23]\n"
    "ds_read_b64_tr_b16 a[230:231], v79 offset:1024\n"
    "buffer_load_dwordx4 v35, s[56:59], s49 offen offset:1024 sc1 lds\n"
    "s_waitcnt lgkmcnt(12)\n"
    "v_mfma_f32_16x16x32_bf16 a[24:27], a[240:243], a[88:91], a[24:27]\n"
    "ds_read_b64_tr_b16 a[232:233], v80 offset:0\n"
    "v_mfma_f32_16x16x32_bf16 a[28:31], a[240:243], a[92:95], a[28:31]\n"
    "ds_read_b64_tr_b16 a[234:235], v80 offset:1024\n"
    "buffer_load_dwordx4 v36, s[56:59], s49 offen offset:2048 sc1 lds\n"
    "v_mfma_f32_16x16x32_bf16 a[32:35], a[244:247], a[88:91], a[32:35]\n"
    "ds_read_b64_tr_b16 a[236:237], v81 offset:0\n"
    "v_mfma_f32_16x16x32_bf16 a[36:39], a[244:247], a[92:95], a[36:39]\n"
    "ds_read_b64_tr_b16 a[238:239], v81 offset:1024\n"
    "buffer_load_dwordx4 v37, s[56:59], s49 offen offset:3072 sc1 lds\n"
    "s_waitcnt lgkmcnt(12)\n"
    "v_mfma_f32_16x16x32_bf16 a[40:43], a[248:251], a[88:91], a[40:43]\n"
    "ds_read_b64_tr_b16 a[240:241], v82 offset:0\n"
    "v_mfma_f32_16x16x32_bf16 a[44:47], a[248:251], a[92:95], a[44:47]\n"
    "ds_read_b64_tr_b16 a[242:243], v82 offset:1024\n"
    "s_add_u32 m0, s50, 81920\n"
    "s_add_u32 s77, %[rot], 11\n"
    "s_and_b32 s77, s77, 15\n"
    "s_lshl_b32 s77, s77, 7\n"
    "buffer_load_dwordx4 v38, s[60:63], s77 offen offset:0 lds\n"
    "v_mfma_f32_16x16x32_bf16 a[48:51], a[252:255], a[88:91], a[48:51]\n"
    "ds_read_b64_tr_b16 a[244:245], v83 offset:0\n"
    "v_mfma_f32_16x16x32_bf16 a[52:55], a[252:255], a[92:95], a[52:55]\n"
    "ds_read_b64_tr_b16 a[246:247], v83 offset:1024\n"
    "buffer_load_dwordx4 v39, s[60:63], s77 offen offset:1024 lds\n"
    "s_waitcnt lgkmcnt(12)\n"
    "s_waitcnt lgkmcnt(8)\n"
    "s_mul_i32 s52, s78, 28672\n"
    "s_mul_i32 s53, %[fs], 3584\n"
    "s_add_u32 s52, s52, s53\n"
    "s_add_u32 s52, s52, 25165824\n"
    "s_add_u32 s64, %[wslo], s52\n"
    "s_addc_u32 s65, %[wshi], 0\n"
    "s_and_b32 s65, s65, 0xffff\n"
    "s_mov_b32 s66, 32256\n"
    "s_mov_b32 s67, 0x20000\n"
    "v_lshlrev_b32 v23, 3, v30\n"
    "s_mov_b32 s52, 28672\n"
    "buffer_load_dwordx2 v[100:101], v23, s[64:67], 0 offen offset:0 sc1\n"
    "buffer_load_dwordx2 v[102:103], v23, s[64:67], 0 offen offset:512 sc1\n"
    "buffer_load_dwordx2 v[104:105], v23, s[64:67], 0 offen offset:1024 sc1\n"
    "buffer_load_dwordx2 v[106:107], v23, s[64:67], 0 offen offset:1536 sc1\n"
    "buffer_load_dwordx2 v[108:109], v23, s[64:67], 0 offen offset:2048 sc1\n"
    "buffer_load_dwordx2 v[110:111], v23, s[64:67], 0 offen offset:2560 sc1\n"
    "buffer_load_dwordx2 v[112:113], v23, s[64:67], 0 offen offset:3072 sc1\n"
    "buffer_load_dwordx2 v[114:115], v23, s[64:67], s52 offen offset:0 sc1\n"
    "buffer_load_dwordx2 v[116:117], v23, s[64:67], s52 offen offset:512 sc1\n"
    "buffer_load_dwordx2 v[118:119], v23, s[64:67], s52 offen offset:1024 sc1\n"
    "buffer_load_dwordx2 v[120:121], v23, s[64:67], s52 offen offset:1536 sc1\n"
    "buffer_load_dwordx2 v[122:123], v23, s[64:67], s52 offen offset:2048 sc1\n"
    "buffer_load_dwordx2 v[124:125], v23, s[64:67], s52 offen offset:2560 sc1\n"
    "buffer_load_dwordx2 v[126:127], v23, s[64:67], s52 offen offset:3072 sc1\n"
    "v_mfma_f32_16x16x32_bf16 a[0:3], a[224:227], a[64:67], a[0:3]\n"
    "ds_read_b64_tr_b16 a[248:249], v84 offset:0\n"
    "v_mfma_f32_16x16x32_bf16 a[4:7], a[224:227], a[68:71], a[4:7]\n"
    "ds_read_b64_tr_b16 a[250:251], v84 offset:1024\n"
    "buffer_load_dwordx4 v40, s[60:63], s77 offen offset:2048 lds\n"
    "v_mfma_f32_16x16x32_bf16 a[8:11], a[228:231], a[64:67], a[8:11]\n"
    "ds_read_b64_tr_b16 a[252:253], v78 offset:8192\n"
    "v_mfma_f32_16x16x32_bf16 a[12:15], a[228:231], a[68:71], a[12:15]\n"
    "ds_read_b64_tr_b16 a[254:255], v78 offset:9216\n"
    "buffer_load_dwordx4 v41, s[60:63], s77 offen offset:3072 lds\n"
    "s_waitcnt lgkmcnt(8)\n"
    "v_mfma_f32_16x16x32_bf16 a[16:19], a[232:235], a[64:67], a[16:19]\n"
    "ds_read_b64_tr_b16 a[224:225], v79 offset:8192\n"
    "v_mfma_f32_16x16x32_bf16 a[20:23], a[232:235], a[68:71], a[20:23]\n"
    "ds_read_b64_tr_b16 a[226:227], v79 offset:9216\n"
    "v_mfma_f32_16x16x32_bf16 a[24:27], a[236:239], a[64:67], a[24:27]\n"
    "ds_read_b64_tr_b16 a[228:229], v80 offset:8192\n"
    "v_mfma_f32_16x16x32_bf16 a[28:31], a[236:239], a[68:71], a[28:31]\n"
    "ds_read_b64_tr_b16 a[230:231], v80 offset:9216\n"
    "s_waitcnt lgkmcnt(8)\n"
    "v_mfma_f32_16x16x32_bf16 a[32:35], a[240:243], a[64:67], a[32:35]\n"
    "ds_read_b64_tr_b16 a[232:233], v81 offset:8192\n"
    "v_mfma_f32_16x16x32_bf16 a[36:39], a[240:243], a[68:71], a[36:39]\n"
    "ds_read_b64_tr_b16 a[234:235], v81 offset:9216\n"
    "v_mfma_f32_16x16x32_bf16 a[40:43], a[244:247], a[64:67], a[40:43]\n"
    "ds_read_b64_tr_b16 a[236:237], v82 offset:8192\n"
    "v_mfma_f32_16x16x32_bf16 a[44:47], a[244:247], a[68:71], a[44:47]\n"
    "ds_read_b64_tr_b16 a[238:239], v82 offset:9216\n"
    "s_waitcnt lgkmcnt(8)\n"
    "v_mfma_f32_16x16x32_bf16 a[48:51], a[248:251], a[64:67], a[48:51]\n"
    "ds_read_b64_tr_b16 a[240:241], v83 offset:8192\n"
    "v_mfma_f32_16x16x32_bf16 a[52:55], a[248:251], a[68:71], a[52:55]\n"
    "ds_read_b64_tr_b16 a[242:243], v83 offset:9216\n"
    "v_mfma_f32_16x16x32_bf16 a[0:3], a[252:255], a[72:75], a[0:3]\n"
    "ds_read_b64_tr_b16 a[244:245], v84 offset:8192\n"
    "v_mfma_f32_16x16x32_bf16 a[4:7], a[252:255], a[76:79], a[4:7]\n"
    "ds_read_b64_tr_b16 a[246:247], v84 offset:9216\n"
    "s_waitcnt lgkmcnt(8)\n"
    "s_waitcnt vmcnt(30)\n"
    "s_barrier\n"
    "ds_read_b128 a[80:83], v87 offset:49152\n"
    "ds_read_b128 a[84:87], v87 offset:51200\n"
    "ds_read_b128 a[88:91], v88 offset:49152\n"
    "ds_read_b128 a[92:95], v88 offset:51200\n"
    "v_mfma_f32_16x16x32_bf16 a[8:11], a[224:227], a[72:75], a[8:11]\n"
    "ds_read_b64_tr_b16 a[248:249], v71 offset:32768\n"
    "v_mfma_f32_16x16x32_bf16 a[12:15], a[224:227], a[76:79], a[12:15]\n"
    "ds_read_b64_tr_b16 a[250:251], v71 offset:33792\n"
    "s_add_u32 m0, s50, 32768\n"
    "s_add_u32 s49, %[rot], 12\n"
    "s_and_b32 s49, s49, 15\n"
    "s_mul_i32 s49, s49, 14336\n"
    "buffer_load_dwordx4 v34, s[56:59], s49 offen offset:0 sc1 lds\n"
    "v_mfma_f32_16x16x32_bf16 a[16:19], a[228:231], a[72:75], a[16:19]\n"
    "ds_read_b64_tr_b16 a[252:253], v72 offset:32768\n"
    "v_mfma_f32_16x16x32_bf16 a[20:23], a[228:231], a[76:79], a[20:23]\n"
    "ds_read_b64_tr_b16 a[254:255], v72 offset:33792\n"
    "buffer_load_dwordx4 v35, s[56:59], s49 offen offset:1024 sc1 lds\n"
    "s_waitcnt lgkmcnt(12)\n"
    "v_mfma_f32_16x16x32_bf16 a[24:27], a[232:235], a[72:75], a[24:27]\n"
    "ds_read_b64_tr_b16 a[224:225], v73 offset:32768\n"
    "v_mfma_f32_16x16x32_bf16 a[28:31], a[232:235], a[76:79], a[28:31]\n"
    "ds_read_b64_tr_b16 a[226:227], v73 offset:33792\n"
    "buffer_load_dwordx4 v36, s[56:59], s49 offen offset:2048 sc1 lds\n"
    "v_mfma_f32_16x16x32_bf16 a[32:35], a[236:239], a[72:75], a[32:35]\n"
    "ds_read_b64_tr_b16 a[228:229], v74 offset:32768\n"
    "v_mfma_f32_16x16x32_bf16 a[36:39], a[236:239], a[76:79], a[36:39]\n"
    "ds_read_b64_tr_b16 a[230:231], v74 offset:33792\n"
    "buffer_load_dwordx4 v37, s[56:59], s49 offen offset:3072 sc1 lds\n"
    "s_waitcnt lgkmcnt(12)\n"
    "v_mfma_f32_16x16x32_bf16 a[40:43], a[240:243], a[72:75], a[40:43]\n"
    "ds_read_b64_tr_b16 a[232:233], v75 offset:32768\n"
    "v_mfma_f32_16x16x32_bf16 a[44:47], a[240:243], a[76:79], a[44:47]\n"
    "ds_read_b64_tr_b16 a[234:235], v75 offset:33792\n"
    "s_add_u32 m0, s50, 49152\n"
    "s_add_u32 s77, %[rot], 12\n"
    "s_and_b32 s77, s77, 15\n"
    "s_lshl_b32 s77, s77, 7\n"
    "buffer_load_dwordx4 v38, s[60:63], s77 offen offset:0 lds\n"
    "v_mfma_f32_16x16x32_bf16 a[48:51], a[244:247], a[72:75], a[48:51]\n"
    "ds_read_b64_tr_b16 a[236:237], v76 offset:32768\n"
    "v_mfma_f32_16x16x32_bf16 a[52:55], a[244:247], a[76:79], a[52:55]\n"
    "ds_read_b64_tr_b16 a[238:239], v76 offset:33792\n"
    "buffer_load_dwordx4 v39, s[60:63], s77 offen offset:1024 lds\n"
    "s_waitcnt lgkmcnt(12)\n"
    "s_waitcnt lgkmcnt(8)\n"
    "v_mfma_f32_16x16x32_bf16 a[0:3], a[248:251], a[80:83], a[0:3]\n"
    "ds_read_b64_tr_b16 a[240:241], v77 offset:32768\n"
    "v_mfma_f32_16x16x32_bf16 a[4:7], a[248:251], a[84:87], a[4:7]\n"
    "ds_read_b64_tr_b16 a[242:243], v77 offset:33792\n"
    "buffer_load_dwordx4 v40, s[60:63], s77 offen offset:2048 lds\n"
    "v_mfma_f32_16x16x32_bf16 a[8:11], a[252:255], a[80:83], a[8:11]\n"
    "ds_read_b64_tr_b16 a[244:245], v71 offset:40960\n"
    "v_mfma_f32_16x16x32_bf16 a[12:15], a[252:255], a[84:87], a[12:15]\n"
    "ds_read_b64_tr_b16 a[246:247], v71 offset:41984\n"
    "buffer_load_dwordx4 v41, s[60:63], s77 offen offset:3072 lds\n"
    "s_waitcnt lgkmcnt(8)\n"
    "v_mfma_f32_16x16x32_bf16 a[16:19], a[224:227], a[80:83], a[16:19]\n"
    "ds_read_b64_tr_b16 a[248:249], v72 offset:40960\n"
    "v_mfma_f32_16x16x32_bf16 a[20:23], a[224:227], a[84:87], a[20:23]\n"
    "ds_read_b64_tr_b16 a[250:251], v72 offset:41984\n"
    "v_mfma_f32_16x16x32_bf16 a[24:27], a[228:231], a[80:83], a[24:27]\n"
    "ds_read_b64_tr_b16 a[252:253], v73 offset:40960\n"
    "v_mfma_f32_16x16x32_bf16 a[28:31], a[228:231], a[84:87], a[28:31]\n"
    "ds_read_b64_tr_b16 a[254:255], v73 offset:41984\n"
    "s_waitcnt lgkmcnt(8)\n"
    "v_mfma_f32_16x16x32_bf16 a[32:35], a[232:235], a[80:83], a[32:35]\n"
    "ds_read_b64_tr_b16 a[224:225], v74 offset:40960\n"
    "v_mfma_f32_16x16x32_bf16 a[36:39], a[232:235], a[84:87], a[36:39]\n"
    "ds_read_b64_tr_b16 a[226:227], v74 offset:41984\n"
    "v_mfma_f32_16x16x32_bf16 a[40:43], a[236:239], a[80:83], a[40:43]\n"
    "ds_read_b64_tr_b16 a[228:229], v75 offset:40960\n"
    "v_mfma_f32_16x16x32_bf16 a[44:47], a[236:239], a[84:87], a[44:47]\n"
    "ds_read_b64_tr_b16 a[230:231], v75 offset:41984\n"
    "s_waitcnt lgkmcnt(8)\n"
    "v_mfma_f32_16x16x32_bf16 a[48:51], a[240:243], a[80:83], a[48:51]\n"
    "ds_read_b64_tr_b16 a[232:233], v76 offset:40960\n"
    "v_mfma_f32_16x16x32_bf16 a[52:55], a[240:243], a[84:87], a[52:55]\n"
    "ds_read_b64_tr_b16 a[234:235], v76 offset:41984\n"
    "v_mfma_f32_16x16x32_bf16 a[0:3], a[244:247], a[88:91], a[0:3]\n"
    "ds_read_b64_tr_b16 a[236:237], v77 offset:40960\n"
    "v_mfma_f32_16x16x32_bf16 a[4:7], a[244:247], a[92:95], a[4:7]\n"
    "ds_read_b64_tr_b16 a[238:239], v77 offset:41984\n"
    "s_waitcnt lgkmcnt(8)\n"
    "s_waitcnt vmcnt(30)\n"
    "s_barrier\n"
    "ds_read_b128 a[64:67], v85 offset:16384\n"
    "ds_read_b128 a[68:71], v85 offset:18432\n"
    "ds_read_b128 a[72:75], v86 offset:16384\n"
    "ds_read_b128 a[76:79], v86 offset:18432\n"
    "v_mfma_f32_16x16x32_bf16 a[8:11], a[248:251], a[88:91], a[8:11]\n"
    "ds_read_b64_tr_b16 a[240:241], v64 offset:0\n"
    "v_mfma_f32_16x16x32_bf16 a[12:15], a[248:251], a[92:95], a[12:15]\n"
    "ds_read_b64_tr_b16 a[242:243], v64 offset:1024\n"
    "s_add_u32 m0, s50, 131072\n"
    "s_add_u32 s49, %[rot], 13\n"
    "s_and_b32 s49, s49, 15\n"
    "s_mul_i32 s49, s49, 14336\n"
    "buffer_load_dwordx4 v34, s[56:59], s49 offen offset:0 sc1 lds\n"
    "v_mfma_f32_16x16x32_bf16 a[16:19], a[252:255], a[88:91], a[16:19]\n"
    "ds_read_b64_tr_b16 a[244:245], v65 offset:0\n"
    "v_mfma_f32_16x16x32_bf16 a[20:23], a[252:255], a[92:95], a[20:23]\n"
    "ds_read_b64_tr_b16 a[246:247], v65 offset:1024\n"
    "buffer_load_dwordx4 v35, s[56:59], s49 offen offset:1024 sc1 lds\n"
    "s_waitcnt lgkmcnt(12)\n"
    "v_mfma_f32_16x16x32_bf16 a[24:27], a[224:227], a[88:91], a[24:27]\n"
    "ds_read_b64_tr_b16 a[248:249], v66 offset:0\n"
    "v_mfma_f32_16x16x32_bf16 a[28:31], a[224:227], a[92:95], a[28:31]\n"
    "ds_read_b64_tr_b16 a[250:251], v66 offset:1024\n"
    "buffer_load_dwordx4 v36, s[56:59], s49 offen offset:2048 sc1 lds\n"
    "v_mfma_f32_16x16x32_bf16 a[32:35], a[228:231], a[88:91], a[32:35]\n"
    "ds_read_b64_tr_b16 a[252:253], v67 offset:0\n"
    "v_mfma_f32_16x16x32_bf16 a[36:39], a[228:231], a[92:95], a[36:39]\n"
    "ds_read_b64_tr_b16 a[254:255], v67 offset:1024\n"
    "buffer_load_dwordx4 v37, s[56:59], s49 offen offset:3072 sc1 lds\n"
    "s_waitcnt lgkmcnt(12)\n"
    "v_mfma_f32_16x16x32_bf16 a[40:43], a[232:235], a[88:91], a[40:43]\n"
    "ds_read_b64_tr_b16 a[224:225], v68 offset:0\n"
    "v_mfma_f32_16x16x32_bf16 a[44:47], a[232:235], a[92:95], a[44:47]\n"
    "ds_read_b64_tr_b16 a[226:227], v68 offset:1024\n"
    "s_add_u32 m0, s50, 147456\n"
    "s_add_u32 s77, %[rot], 13\n"
    "s_and_b32 s77, s77, 15\n"
    "s_lshl_b32 s77, s77, 7\n"
    "buffer_load_dwordx4 v38, s[60:63], s77 offen offset:0 lds\n"
    "v_mfma_f32_16x16x32_bf16 a[48:51], a[236:239], a[88:91], a[48:51]\n"
    "ds_read_b64_tr_b16 a[228:229], v69 offset:0\n"
    "v_mfma_f32_16x16x32_bf16 a[52:55], a[236:239], a[92:95], a[52:55]\n"
    "ds_read_b64_tr_b16 a[230:231], v69 offset:1024\n"
    "buffer_load_dwordx4 v39, s[60:63], s77 offen offset:1024 lds\n"
    "s_waitcnt lgkmcnt(12)\n"
    "s_waitcnt lgkmcnt(8)\n"
    "v_mfma_f32_16x16x32_bf16 a[0:3], a[240:243], a[64:67], a[0:3]\n"
    "ds_read_b64_tr_b16 a[232:233], v70 offset:0\n"
    "v_mfma_f32_16x16x32_bf16 a[4:7], a[240:243], a[68:71], a[4:7]\n"
    "ds_read_b64_tr_b16 a[234:235], v70 offset:1024\n"
    "buffer_load_dwordx4 v40, s[60:63], s77 offen offset:2048 lds\n"
    "v_mfma_f32_16x16x32_bf16 a[8:11], a[244:247], a[64:67], a[8:11]\n"
    "ds_read_b64_tr_b16 a[236:237], v64 offset:8192\n"
    "v_mfma_f32_16x16x32_bf16 a[12:15], a[244:247], a[68:71], a[12:15]\n"
    "ds_read_b64_tr_b16 a[238:239], v64 offset:9216\n"
    "buffer_load_dwordx4 v41, s[60:63], s77 offen offset:3072 lds\n"
    "s_waitcnt lgkmcnt(8)\n"
    "v_mfma_f32_16x16x32_bf16 a[16:19], a[248:251], a[64:67], a[16:19]\n"
    "ds_read_b64_tr_b16 a[240:241], v65 offset:8192\n"
    "v_mfma_f32_16x16x32_bf16 a[20:23], a[248:251], a[68:71], a[20:23]\n"
    "ds_read_b64_tr_b16 a[242:243], v65 offset:9216\n"
    "v_mfma_f32_16x16x32_bf16 a[24:27], a[252:255], a[64:67], a[24:27]\n"
    "ds_read_b64_tr_b16 a[244:245], v66 offset:8192\n"
    "v_mfma_f32_16x16x32_bf16 a[28:31], a[252:255], a[68:71], a[28:31]\n"
    "ds_read_b64_tr_b16 a[246:247], v66 offset:9216\n"
    "s_waitcnt lgkmcnt(8)\n"
    "v_mfma_f32_16x16x32_bf16 a[32:35], a[224:227], a[64:67], a[32:35]\n"
    "ds_read_b64_tr_b16 a[248:249], v67 offset:8192\n"
    "v_mfma_f32_16x16x32_bf16 a[36:39], a[224:227], a[68:71], a[36:39]\n"
    "ds_read_b64_tr_b16 a[250:251], v67 offset:9216\n"
    "v_mfma_f32_16x16x32_bf16 a[40:43], a[228:231], a[64:67], a[40:43]\n"
    "ds_read_b64_tr_b16 a[252:253], v68 offset:8192\n"
    "v_mfma_f32_16x16x32_bf16 a[44:47], a[228:231], a[68:71], a[44:47]\n"
    "ds_read_b64_tr_b16 a[254:255], v68 offset:9216\n"
    "s_waitcnt lgkmcnt(8)\n"
    "v_mfma_f32_16x16x32_bf16 a[48:51], a[232:235], a[64:67], a[48:51]\n"
    "ds_read_b64_tr_b16 a[224:225], v69 offset:8192\n"
    "v_mfma_f32_16x16x32_bf16 a[52:55], a[232:235], a[68:71], a[52:55]\n"
    "ds_read_b64_tr_b16 a[226:227], v69 offset:9216\n"
    "v_mfma_f32_16x16x32_bf16 a[0:3], a[236:239], a[72:75], a[0:3]\n"
    "ds_read_b64_tr_b16 a[228:229], v70 offset:8192\n"
    "v_mfma_f32_16x16x32_bf16 a[4:7], a[236:239], a[76:79], a[4:7]\n"
    "ds_read_b64_tr_b16 a[230:231], v70 offset:9216\n"
    "s_waitcnt lgkmcnt(8)\n"
    "s_waitcnt vmcnt(16)\n"
    "s_barrier\n"
    "ds_read_b128 a[80:83], v87 offset:16384\n"
    "ds_read_b128 a[84:87], v87 offset:18432\n"
    "ds_read_b128 a[88:91], v88 offset:16384\n"
    "ds_read_b128 a[92:95], v88 offset:18432\n"
    "v_mfma_f32_16x16x32_bf16 a[8:11], a[240:243], a[72:75], a[8:11]\n"
    "ds_read_b64_tr_b16 a[232:233], v71 offset:0\n"
    "v_mfma_f32_16x16x32_bf16 a[12:15], a[240:243], a[76:79], a[12:15]\n"
    "ds_read_b64_tr_b16 a[234:235], v71 offset:1024\n"
    "s_add_u32 m0, s50, 98304\n"
    "s_add_u32 s49, %[rot], 14\n"
    "s_and_b32 s49, s49, 15\n"
    "s_mul_i32 s49, s49, 14336\n"
    "buffer_load_dwordx4 v34, s[56:59], s49 offen offset:0 sc1 lds\n"
    "v_mfma_f32_16x16x32_bf16 a[16:19], a[244:247], a[72:75], a[16:19]\n"
    "ds_read_b64_tr_b16 a[236:237], v72 offset:0\n"
    "v_mfma_f32_16x16x32_bf16 a[20:23], a[244:247], a[76:79], a[20:23]\n"
    "ds_read_b64_tr_b16 a[238:239], v72 offset:1024\n"
    "buffer_load_dwordx4 v35, s[56:59], s49 offen offset:1024 sc1 lds\n"
    "s_waitcnt lgkmcnt(12)\n"
    "v_mfma_f32_16x16x32_bf16 a[24:27], a[248:251], a[72:75], a[24:27]\n"
    "ds_read_b64_tr_b16 a[240:241], v73 offset:0\n"
    "v_mfma_f32_16x16x32_bf16 a[28:31], a[248:251], a[76:79], a[28:31]\n"
    "ds_read_b64_tr_b16 a[242:243], v73 offset:1024\n"
    "buffer_load_dwordx4 v36, s[56:59], s49 offen offset:2048 sc1 lds\n"
    "v_mfma_f32_16x16x32_bf16 a[32:35], a[252:255], a[72:75], a[32:35]\n"
    "ds_read_b64_tr_b16 a[244:245], v74 offset:0\n"
    "v_mfma_f32_16x16x32_bf16 a[36:39], a[252:255], a[76:79], a[36:39]\n"
    "ds_read_b64_tr_b16 a[246:247], v74 offset:1024\n"
    "buffer_load_dwordx4 v37, s[56:59], s49 offen offset:3072 sc1 lds\n"
    "s_waitcnt lgkmcnt(12)\n"
    "v_mfma_f32_16x16x32_bf16 a[40:43], a[224:227], a[72:75], a[40:43]\n"
    "ds_read_b64_tr_b16 a[248:249], v75 offset:0\n"
    "v_mfma_f32_16x16x32_bf16 a[44:47], a[224:227], a[76:79], a[44:47]\n"
    "ds_read_b64_tr_b16 a[250:251], v75 offset:1024\n"
    "s_add_u32 m0, s50, 114688\n"
    "s_add_u32 s77, %[rot], 14\n"
    "s_and_b32 s77, s77, 15\n"
    "s_lshl_b32 s77, s77, 7\n"
    "buffer_load_dwordx4 v38, s[60:63], s77 offen offset:0 lds\n"
    "v_mfma_f32_16x16x32_bf16 a[48:51], a[228:231], a[72:75], a[48:51]\n"
    "ds_read_b64_tr_b16 a[252:253], v76 offset:0\n"
    "v_mfma_f32_16x16x32_bf16 a[52:55], a[228:231], a[76:79], a[52:55]\n"
    "ds_read_b64_tr_b16 a[254:255], v76 offset:1024\n"
    "buffer_load_dwordx4 v39, s[60:63], s77 offen offset:1024 lds\n"
    "s_waitcnt lgkmcnt(12)\n"
    "s_waitcnt lgkmcnt(8)\n"
    "v_mfma_f32_16x16x32_bf16 a[0:3], a[232:235], a[80:83], a[0:3]\n"
    "ds_read_b64_tr_b16 a[224:225], v77 offset:0\n"
    "v_mfma_f32_16x16x32_bf16 a[4:7], a[232:235], a[84:87], a[4:7]\n"
    "ds_read_b64_tr_b16 a[226:227], v77 offset:1024\n"
    "buffer_load_dwordx4 v40, s[60:63], s77 offen offset:2048 lds\n"
    "v_mfma_f32_16x16x32_bf16 a[8:11], a[236:239], a[80:83], a[8:11]\n"
    "ds_read_b64_tr_b16 a[228:229], v71 offset:8192\n"
    "v_mfma_f32_16x16x32_bf16 a[12:15], a[236:239], a[84:87], a[12:15]\n"
    "ds_read_b64_tr_b16 a[230:231], v71 offset:9216\n"
    "buffer_load_dwordx4 v41, s[60:63], s77 offen offset:3072 lds\n"
    "s_waitcnt lgkmcnt(8)\n"
    "v_mfma_f32_16x16x32_bf16 a[16:19], a[240:243], a[80:83], a[16:19]\n"
    "ds_read_b64_tr_b16 a[232:233], v72 offset:8192\n"
    "v_mfma_f32_16x16x32_bf16 a[20:23], a[240:243], a[84:87], a[20:23]\n"
    "ds_read_b64_tr_b16 a[234:235], v72 offset:9216\n"
    "v_mfma_f32_16x16x32_bf16 a[24:27], a[244:247], a[80:83], a[24:27]\n"
    "ds_read_b64_tr_b16 a[236:237], v73 offset:8192\n"
    "v_mfma_f32_16x16x32_bf16 a[28:31], a[244:247], a[84:87], a[28:31]\n"
    "ds_read_b64_tr_b16 a[238:239], v73 offset:9216\n"
    "s_waitcnt lgkmcnt(8)\n"
    "v_mfma_f32_16x16x32_bf16 a[32:35], a[248:251], a[80:83], a[32:35]\n"
    "ds_read_b64_tr_b16 a[240:241], v74 offset:8192\n"
    "v_mfma_f32_16x16x32_bf16 a[36:39], a[248:251], a[84:87], a[36:39]\n"
    "ds_read_b64_tr_b16 a[242:243], v74 offset:9216\n"
    "v_mfma_f32_16x16x32_bf16 a[40:43], a[252:255], a[80:83], a[40:43]\n"
    "ds_read_b64_tr_b16 a[244:245], v75 offset:8192\n"
    "v_mfma_f32_16x16x32_bf16 a[44:47], a[252:255], a[84:87], a[44:47]\n"
    "ds_read_b64_tr_b16 a[246:247], v75 offset:9216\n"
    "s_waitcnt lgkmcnt(8)\n"
    "v_mfma_f32_16x16x32_bf16 a[48:51], a[224:227], a[80:83], a[48:51]\n"
    "ds_read_b64_tr_b16 a[248:249], v76 offset:8192\n"
    "v_mfma_f32_16x16x32_bf16 a[52:55], a[224:227], a[84:87], a[52:55]\n"
    "ds_read_b64_tr_b16 a[250:251], v76 offset:9216\n"
    "v_mfma_f32_16x16x32_bf16 a[0:3], a[228:231], a[88:91], a[0:3]\n"
    "ds_read_b64_tr_b16 a[252:253], v77 offset:8192\n"
    "v_mfma_f32_16x16x32_bf16 a[4:7], a[228:231], a[92:95], a[4:7]\n"
    "ds_read_b64_tr_b16 a[254:255], v77 offset:9216\n"
    "s_waitcnt lgkmcnt(8)\n"
    "s_waitcnt vmcnt(16)\n"
    "s_barrier\n"
    "ds_read_b128 a[64:67], v85 offset:49152\n"
    "ds_read_b128 a[68:71], v85 offset:51200\n"
    "ds_read_b128 a[72:75], v86 offset:49152\n"
    "ds_read_b128 a[76:79], v86 offset:51200\n"
    "v_mfma_f32_16x16x32_bf16 a[8:11], a[232:235], a[88:91], a[8:11]\n"
    "ds_read_b64_tr_b16 a[224:225], v64 offset:32768\n"
    "v_mfma_f32_16x16x32_bf16 a[12:15], a[232:235], a[92:95], a[12:15]\n"
    "ds_read_b64_tr_b16 a[226:227], v64 offset:33792\n"
    "s_add_u32 m0, s50, 0\n"
    "s_add_u32 s49, %[rot], 15\n"
    "s_and_b32 s49, s49, 15\n"
    "s_mul_i32 s49, s49, 14336\n"
    "buffer_load_dwordx4 v34, s[56:59], s49 offen offset:0 sc1 lds\n"
    "v_mfma_f32_16x16x32_bf16 a[16:19], a[236:239], a[88:91], a[16:19]\n"
    "ds_read_b64_tr_b16 a[228:229], v65 offset:32768\n"
    "v_mfma_f32_16x16x32_bf16 a[20:23], a[236:239], a[92:95], a[20:23]\n"
    "ds_read_b64_tr_b16 a[230:231], v65 offset:33792\n"
    "buffer_load_dwordx4 v35, s[56:59], s49 offen offset:1024 sc1 lds\n"
    "s_waitcnt lgkmcnt(12)\n"
    "v_mfma_f32_16x16x32_bf16 a[24:27], a[240:243], a[88:91], a[24:27]\n"
    "ds_read_b64_tr_b16 a[232:233], v66 offset:32768\n"
    "v_mfma_f32_16x16x32_bf16 a[28:31], a[240:243], a[92:95], a[28:31]\n"
    "ds_read_b64_tr_b16 a[234:235], v66 offset:33792\n"
    "buffer_load_dwordx4 v36, s[56:59], s49 offen offset:2048 sc1 lds\n"
    "v_mfma_f32_16x16x32_bf16 a[32:35], a[244:247], a[88:91], a[32:35]\n"
    "ds_read_b64_tr_b16 a[236:237], v67 offset:32768\n"
    "v_mfma_f32_16x16x32_bf16 a[36:39], a[244:247], a[92:95], a[36:39]\n"
    "ds_read_b64_tr_b16 a[238:239], v67 offset:33792\n"
    "buffer_load_dwordx4 v37, s[56:59], s49 offen offset:3072 sc1 lds\n"
    "s_waitcnt lgkmcnt(12)\n"
    "v_mfma_f32_16x16x32_bf16 a[40:43], a[248:251], a[88:91], a[40:43]\n"
    "ds_read_b64_tr_b16 a[240:241], v68 offset:32768\n"
    "v_mfma_f32_16x16x32_bf16 a[44:47], a[248:251], a[92:95], a[44:47]\n"
    "ds_read_b64_tr_b16 a[242:243], v68 offset:33792\n"
    "s_add_u32 m0, s50, 16384\n"
    "s_add_u32 s77, %[rot], 15\n"
    "s_and_b32 s77, s77, 15\n"
    "s_lshl_b32 s77, s77, 7\n"
    "buffer_load_dwordx4 v38, s[60:63], s77 offen offset:0 lds\n"
    "v_mfma_f32_16x16x32_bf16 a[48:51], a[252:255], a[88:91], a[48:51]\n"
    "ds_read_b64_tr_b16 a[244:245], v69 offset:32768\n"
    "v_mfma_f32_16x16x32_bf16 a[52:55], a[252:255], a[92:95], a[52:55]\n"
    "ds_read_b64_tr_b16 a[246:247], v69 offset:33792\n"
    "buffer_load_dwordx4 v39, s[60:63], s77 offen offset:1024 lds\n"
    "s_waitcnt lgkmcnt(12)\n"
    "s_waitcnt lgkmcnt(8)\n"
    "v_mfma_f32_16x16x32_bf16 a[0:3], a[224:227], a[64:67], a[0:3]\n"
    "ds_read_b64_tr_b16 a[248:249], v70 offset:32768\n"
    "v_mfma_f32_16x16x32_bf16 a[4:7], a[224:227], a[68:71], a[4:7]\n"
    "ds_read_b64_tr_b16 a[250:251], v70 offset:33792\n"
    "buffer_load_dwordx4 v40, s[60:63], s77 offen offset:2048 lds\n"
    "v_mfma_f32_16x16x32_bf16 a[8:11], a[228:231], a[64:67], a[8:11]\n"
    "ds_read_b64_tr_b16 a[252:253], v64 offset:40960\n"
    "v_mfma_f32_16x16x32_bf16 a[12:15], a[228:231], a[68:71], a[12:15]\n"
    "ds_read_b64_tr_b16 a[254:255], v64 offset:41984\n"
    "buffer_load_dwordx4 v41, s[60:63], s77 offen offset:3072 lds\n"
    "s_waitcnt lgkmcnt(8)\n"
    "v_mfma_f32_16x16x32_bf16 a[16:19], a[232:235], a[64:67], a[16:19]\n"
    "ds_read_b64_tr_b16 a[224:225], v65 offset:40960\n"
    "v_mfma_f32_16x16x32_bf16 a[20:23], a[232:235], a[68:71], a[20:23]\n"
    "ds_read_b64_tr_b16 a[226:227], v65 offset:41984\n"
    "v_mfma_f32_16x16x32_bf16 a[24:27], a[236:239], a[64:67], a[24:27]\n"
    "ds_read_b64_tr_b16 a[228:229], v66 offset:40960\n"
    "v_mfma_f32_16x16x32_bf16 a[28:31], a[236:239], a[68:71], a[28:31]\n"
    "ds_read_b64_tr_b16 a[230:231], v66 offset:41984\n"
    "s_waitcnt lgkmcnt(8)\n"
    "v_mfma_f32_16x16x32_bf16 a[32:35], a[240:243], a[64:67], a[32:35]\n"
    "ds_read_b64_tr_b16 a[232:233], v67 offset:40960\n"
    "v_mfma_f32_16x16x32_bf16 a[36:39], a[240:243], a[68:71], a[36:39]\n"
    "ds_read_b64_tr_b16 a[234:235], v67 offset:41984\n"
    "v_mfma_f32_16x16x32_bf16 a[40:43], a[244:247], a[64:67], a[40:43]\n"
    "ds_read_b64_tr_b16 a[236:237], v68 offset:40960\n"
    "v_mfma_f32_16x16x32_bf16 a[44:47], a[244:247], a[68:71], a[44:47]\n"
    "ds_read_b64_tr_b16 a[238:239], v68 offset:41984\n"
    "s_waitcnt lgkmcnt(8)\n"
    "v_mfma_f32_16x16x32_bf16 a[48:51], a[248:251], a[64:67], a[48:51]\n"
    "ds_read_b64_tr_b16 a[240:241], v69 offset:40960\n"
    "v_mfma_f32_16x16x32_bf16 a[52:55], a[248:251], a[68:71], a[52:55]\n"
    "ds_read_b64_tr_b16 a[242:243], v69 offset:41984\n"
    "v_mfma_f32_16x16x32_bf16 a[0:3], a[252:255], a[72:75], a[0:3]\n"
    "ds_read_b64_tr_b16 a[244:245], v70 offset:40960\n"
    "v_mfma_f32_16x16x32_bf16 a[4:7], a[252:255], a[76:79], a[4:7]\n"
    "ds_read_b64_tr_b16 a[246:247], v70 offset:41984\n"
    "s_waitcnt lgkmcnt(8)\n"
    "s_waitcnt vmcnt(16)\n"
    "s_barrier\n"
    "ds_read_b128 a[80:83], v89 offset:16384\n"
    "ds_read_b128 a[84:87], v89 offset:18432\n"
    "ds_read_b128 a[88:91], v90 offset:16384\n"
    "ds_read_b128 a[92:95], v90 offset:18432\n"
    "v_mfma_f32_16x16x32_bf16 a[8:11], a[224:227], a[72:75], a[8:11]\n"
    "ds_read_b64_tr_b16 a[248:249], v78 offset:0\n"
    "v_mfma_f32_16x16x32_bf16 a[12:15], a[224:227], a[76:79], a[12:15]\n"
    "ds_read_b64_tr_b16 a[250:251], v78 offset:1024\n"
    "s_add_u32 s48, s39, 0\n"
    "s_add_u32 m0, s50, 65536\n"
    "s_nop 0\n"
    "buffer_load_dwordx4 v60, s[40:43], s48 offen offset:0 nt lds\n"
    "v_mfma_f32_16x16x32_bf16 a[16:19], a[228:231], a[72:75], a[16:19]\n"
    "ds_read_b64_tr_b16 a[252:253], v79 offset:0\n"
    "v_mfma_f32_16x16x32_bf16 a[20:23], a[228:231], a[76:79], a[20:23]\n"
    "ds_read_b64_tr_b16 a[254:255], v79 offset:1024\n"
    "buffer_load_dwordx4 v61, s[40:43], s48 offen offset:1024 nt lds\n"
    "s_waitcnt lgkmcnt(12)\n"
    "v_mfma_f32_16x16x32_bf16 a[24:27], a[232:235], a[72:75], a[24:27]\n"
    "ds_read_b64_tr_b16 a[224:225], v80 offset:0\n"
    "v_mfma_f32_16x16x32_bf16 a[28:31], a[232:235], a[76:79], a[28:31]\n"
    "ds_read_b64_tr_b16 a[226:227], v80 offset:1024\n"
    "buffer_load_dwordx4 v62, s[40:43], s48 offen offset:2048 nt lds\n"
    "v_mfma_f32_16x16x32_bf16 a[32:35], a[236:239], a[72:75], a[32:35]\n"
    "ds_read_b64_tr_b16 a[228:229], v81 offset:0\n"
    "v_mfma_f32_16x16x32_bf16 a[36:39], a[236:239], a[76:79], a[36:39]\n"
    "ds_read_b64_tr_b16 a[230:231], v81 offset:1024\n"
    "buffer_load_dwordx4 v63, s[40:43], s48 offen offset:3072 nt lds\n"
    "s_waitcnt lgkmcnt(12)\n"
    "v_mfma_f32_16x16x32_bf16 a[40:43], a[240:243], a[72:75], a[40:43]\n"
    "ds_read_b64_tr_b16 a[232:233], v82 offset:0\n"
    "v_mfma_f32_16x16x32_bf16 a[44:47], a[240:243], a[76:79], a[44:47]\n"
    "ds_read_b64_tr_b16 a[234:235], v82 offset:1024\n"
    "s_add_u32 s48, s39, 16384\n"
    "s_add_u32 m0, s50, 81920\n"
    "s_nop 0\n"
    "buffer_load_dwordx4 v60, s[40:43], s48 offen offset:0 nt lds\n"
    "v_mfma_f32_16x16x32_bf16 a[48:51], a[244:247], a[72:75], a[48:51]\n"
    "ds_read_b64_tr_b16 a[236:237], v83 offset:0\n"
    "v_mfma_f32_16x16x32_bf16 a[52:55], a[244:247], a[76:79], a[52:55]\n"
    "ds_read_b64_tr_b16 a[238:239], v83 offset:1024\n"
    "buffer_load_dwordx4 v61, s[40:43], s48 offen offset:1024 nt lds\n"
    "s_waitcnt lgkmcnt(12)\n"
    "s_waitcnt lgkmcnt(8)\n"
    "v_mfma_f32_16x16x32_bf16 a[0:3], a[248:251], a[80:83], a[0:3]\n"
    "ds_read_b64_tr_b16 a[240:241], v84 offset:0\n"
    "v_mfma_f32_16x16x32_bf16 a[4:7], a[248:251], a[84:87], a[4:7]\n"
    "ds_read_b64_tr_b16 a[242:243], v84 offset:1024\n"
    "buffer_load_dwordx4 v62, s[40:43], s48 offen offset:2048 nt lds\n"
    "v_mfma_f32_16x16x32_bf16 a[8:11], a[252:255], a[80:83], a[8:11]\n"
    "ds_read_b64_tr_b16 a[244:245], v78 offset:8192\n"
    "v_mfma_f32_16x16x32_bf16 a[12:15], a[252:255], a[84:87], a[12:15]\n"
    "ds_read_b64_tr_b16 a[246:247], v78 offset:9216\n"
    "buffer_load_dwordx4 v63, s[40:43], s48 offen offset:3072 nt lds\n"
    "s_waitcnt lgkmcnt(8)\n"
    "v_mfma_f32_16x16x32_bf16 a[16:19], a[224:227], a[80:83], a[16:19]\n"
    "ds_read_b64_tr_b16 a[248:249], v79 offset:8192\n"
    "v_mfma_f32_16x16x32_bf16 a[20:23], a[224:227], a[84:87], a[20:23]\n"
    "ds_read_b64_tr_b16 a[250:251], v79 offset:9216\n"
    "v_mfma_f32_16x16x32_bf16 a[24:27], a[228:231], a[80:83], a[24:27]\n"
    "ds_read_b64_tr_b16 a[252:253], v80 offset:8192\n"
    "v_mfma_f32_16x16x32_bf16 a[28:31], a[228:231], a[84:87], a[28:31]\n"
    "ds_read_b64_tr_b16 a[254:255], v80 offset:9216\n"
    "s_waitcnt lgkmcnt(8)\n"
    "v_mfma_f32_16x16x32_bf16 a[32:35], a[232:235], a[80:83], a[32:35]\n"
    "ds_read_b64_tr_b16 a[224:225], v81 offset:8192\n"
    "v_mfma_f32_16x16x32_bf16 a[36:39], a[232:235], a[84:87], a[36:39]\n"
    "ds_read_b64_tr_b16 a[226:227], v81 offset:9216\n"
    "v_mfma_f32_16x16x32_bf16 a[40:43], a[236:239], a[80:83], a[40:43]\n"
    "ds_read_b64_tr_b16 a[228:229], v82 offset:8192\n"
    "v_mfma_f32_16x16x32_bf16 a[44:47], a[236:239], a[84:87], a[44:47]\n"
    "ds_read_b64_tr_b16 a[230:231], v82 offset:9216\n"
    "s_waitcnt lgkmcnt(8)\n"
    "v_mfma_f32_16x16x32_bf16 a[48:51], a[240:243], a[80:83], a[48:51]\n"
    "ds_read_b64_tr_b16 a[232:233], v83 offset:8192\n"
    "v_mfma_f32_16x16x32_bf16 a[52:55], a[240:243], a[84:87], a[52:55]\n"
    "ds_read_b64_tr_b16 a[234:235], v83 offset:9216\n"
    "v_mfma_f32_16x16x32_bf16 a[0:3], a[244:247], a[88:91], a[0:3]\n"
    "ds_read_b64_tr_b16 a[236:237], v84 offset:8192\n"
    "v_mfma_f32_16x16x32_bf16 a[4:7], a[244:247], a[92:95], a[4:7]\n"
    "ds_read_b64_tr_b16 a[238:239], v84 offset:9216\n"
    "s_waitcnt lgkmcnt(8)\n"
    "s_waitcnt vmcnt(16)\n"
    "s_barrier\n"
    "ds_read_b128 a[64:67], v87 offset:49152\n"
    "ds_read_b128 a[68:71], v87 offset:51200\n"
    "ds_read_b128 a[72:75], v88 offset:49152\n"
    "ds_read_b128 a[76:79], v88 offset:51200\n"
    "v_mfma_f32_16x16x32_bf16 a[8:11], a[248:251], a[88:91], a[8:11]\n"
    "ds_read_b64_tr_b16 a[240:241], v71 offset:32768\n"
    "v_mfma_f32_16x16x32_bf16 a[12:15], a[248:251], a[92:95], a[12:15]\n"
    "ds_read_b64_tr_b16 a[242:243], v71 offset:33792\n"
    "s_sub_u32 s48, s39, 32768\n"
    "s_add_u32 m0, s50, 32768\n"
    "s_nop 0\n"
    "buffer_load_dwordx4 v60, s[40:43], s48 offen offset:0 nt lds\n"
    "v_mfma_f32_16x16x32_bf16 a[16:19], a[252:255], a[88:91], a[16:19]\n"
    "ds_read_b64_tr_b16 a[244:245], v72 offset:32768\n"
    "v_mfma_f32_16x16x32_bf16 a[20:23], a[252:255], a[92:95], a[20:23]\n"
    "ds_read_b64_tr_b16 a[246:247], v72 offset:33792\n"
    "buffer_load_dwordx4 v61, s[40:43], s48 offen offset:1024 nt lds\n"
    "s_waitcnt lgkmcnt(12)\n"
    "v_mfma_f32_16x16x32_bf16 a[24:27], a[224:227], a[88:91], a[24:27]\n"
    "ds_read_b64_tr_b16 a[248:249], v73 offset:32768\n"
    "v_mfma_f32_16x16x32_bf16 a[28:31], a[224:227], a[92:95], a[28:31]\n"
    "ds_read_b64_tr_b16 a[250:251], v73 offset:33792\n"
    "buffer_load_dwordx4 v62, s[40:43], s48 offen offset:2048 nt lds\n"
    "v_mfma_f32_16x16x32_bf16 a[32:35], a[228:231], a[88:91], a[32:35]\n"
    "ds_read_b64_tr_b16 a[252:253], v74 offset:32768\n"
    "v_mfma_f32_16x16x32_bf16 a[36:39], a[228:231], a[92:95], a[36:39]\n"
    "ds_read_b64_tr_b16 a[254:255], v74 offset:33792\n"
    "buffer_load_dwordx4 v63, s[40:43], s48 offen offset:3072 nt lds\n"
    "s_waitcnt lgkmcnt(12)\n"
    "v_mfma_f32_16x16x32_bf16 a[40:43], a[232:235], a[88:91], a[40:43]\n"
    "ds_read_b64_tr_b16 a[224:225], v75 offset:32768\n"
    "v_mfma_f32_16x16x32_bf16 a[44:47], a[232:235], a[92:95], a[44:47]\n"
    "ds_read_b64_tr_b16 a[226:227], v75 offset:33792\n"
    "s_sub_u32 s48, s39, 16384\n"
    "s_add_u32 m0, s50, 49152\n"
    "s_nop 0\n"
    "buffer_load_dwordx4 v60, s[40:43], s48 offen offset:0 nt lds\n"
    "v_mfma_f32_16x16x32_bf16 a[48:51], a[236:239], a[88:91], a[48:51]\n"
    "ds_read_b64_tr_b16 a[228:229], v76 offset:32768\n"
    "v_mfma_f32_16x16x32_bf16 a[52:55], a[236:239], a[92:95], a[52:55]\n"
    "ds_read_b64_tr_b16 a[230:231], v76 offset:33792\n"
    "buffer_load_dwordx4 v61, s[40:43], s48 offen offset:1024 nt lds\n"
    "s_waitcnt lgkmcnt(12)\n"
    "s_waitcnt lgkmcnt(8)\n"
    "v_mfma_f32_16x16x32_bf16 a[0:3], a[240:243], a[64:67], a[0:3]\n"
    "ds_read_b64_tr_b16 a[232:233], v77 offset:32768\n"
    "v_mfma_f32_16x16x32_bf16 a[4:7], a[240:243], a[68:71], a[4:7]\n"
    "ds_read_b64_tr_b16 a[234:235], v77 offset:33792\n"
    "buffer_load_dwordx4 v62, s[40:43], s48 offen offset:2048 nt lds\n"
    "v_mfma_f32_16x16x32_bf16 a[8:11], a[244:247], a[64:67], a[8:11]\n"
    "ds_read_b64_tr_b16 a[236:237], v71 offset:40960\n"
    "v_mfma_f32_16x16x32_bf16 a[12:15], a[244:247], a[68:71], a[12:15]\n"
    "ds_read_b64_tr_b16 a[238:239], v71 offset:41984\n"
    "buffer_load_dwordx4 v63, s[40:43], s48 offen offset:3072 nt lds\n"
    "s_waitcnt lgkmcnt(8)\n"
    "v_mfma_f32_16x16x32_bf16 a[16:19], a[248:251], a[64:67], a[16:19]\n"
    "ds_read_b64_tr_b16 a[240:241], v72 offset:40960\n"
    "v_mfma_f32_16x16x32_bf16 a[20:23], a[248:251], a[68:71], a[20:23]\n"
    "ds_read_b64_tr_b16 a[242:243], v72 offset:41984\n"
    "v_mfma_f32_16x16x32_bf16 a[24:27], a[252:255], a[64:67], a[24:27]\n"
    "ds_read_b64_tr_b16 a[244:245], v73 offset:40960\n"
    "v_mfma_f32_16x16x32_bf16 a[28:31], a[252:255], a[68:71], a[28:31]\n"
    "ds_read_b64_tr_b16 a[246:247], v73 offset:41984\n"
    "s_waitcnt lgkmcnt(8)\n"
    "v_mfma_f32_16x16x32_bf16 a[32:35], a[224:227], a[64:67], a[32:35]\n"
    "ds_read_b64_tr_b16 a[248:249], v74 offset:40960\n"
    "v_mfma_f32_16x16x32_bf16 a[36:39], a[224:227], a[68:71], a[36:39]\n"
    "ds_read_b64_tr_b16 a[250:251], v74 offset:41984\n"
    "v_mfma_f32_16x16x32_bf16 a[40:43], a[228:231], a[64:67], a[40:43]\n"
    "ds_read_b64_tr_b16 a[252:253], v75 offset:40960\n"
    "v_mfma_f32_16x16x32_bf16 a[44:47], a[228:231], a[68:71], a[44:47]\n"
    "ds_read_b64_tr_b16 a[254:255], v75 offset:41984\n"
    "s_waitcnt lgkmcnt(8)\n"
    "v_mfma_f32_16x16x32_bf16 a[48:51], a[232:235], a[64:67], a[48:51]\n"
    "ds_read_b64_tr_b16 a[224:225], v76 offset:40960\n"
    "v_mfma_f32_16x16x32_bf16 a[52:55], a[232:235], a[68:71], a[52:55]\n"
    "ds_read_b64_tr_b16 a[226:227], v76 offset:41984\n"
    "v_mfma_f32_16x16x32_bf16 a[0:3], a[236:239], a[72:75], a[0:3]\n"
    "ds_read_b64_tr_b16 a[228:229], v77 offset:40960\n"
    "v_mfma_f32_16x16x32_bf16 a[4:7], a[236:239], a[76:79], a[4:7]\n"
    "ds_read_b64_tr_b16 a[230:231], v77 offset:41984\n"
    "s_waitcnt lgkmcnt(8)\n"
    "s_waitcnt vmcnt(16)\n"
    "s_barrier\n"
    "ds_read_b128 a[80:83], v85 offset:16384\n"
    "ds_read_b128 a[84:87], v85 offset:18432\n"
    "ds_read_b128 a[88:91], v86 offset:16384\n"
    "ds_read_b128 a[92:95], v86 offset:18432\n"
    "v_mfma_f32_16x16x32_bf16 a[8:11], a[240:243], a[72:75], a[8:11]\n"
    "ds_read_b64_tr_b16 a[232:233], v64 offset:0\n"
    "v_mfma_f32_16x16x32_bf16 a[12:15], a[240:243], a[76:79], a[12:15]\n"
    "ds_read_b64_tr_b16 a[234:235], v64 offset:1024\n"
    "v_mfma_f32_16x16x32_bf16 a[16:19], a[244:247], a[72:75], a[16:19]\n"
    "ds_read_b64_tr_b16 a[236:237], v65 offset:0\n"
    "v_mfma_f32_16x16x32_bf16 a[20:23], a[244:247], a[76:79], a[20:23]\n"
    "ds_read_b64_tr_b16 a[238:239], v65 offset:1024\n"
    "s_waitcnt lgkmcnt(12)\n"
    "v_mfma_f32_16x16x32_bf16 a[24:27], a[248:251], a[72:75], a[24:27]\n"
    "ds_read_b64_tr_b16 a[240:241], v66 offset:0\n"
    "v_mfma_f32_16x16x32_bf16 a[28:31], a[248:251], a[76:79], a[28:31]\n"
    "ds_read_b64_tr_b16 a[242:243], v66 offset:1024\n"
    "v_mfma_f32_16x16x32_bf16 a[32:35], a[252:255], a[72:75], a[32:35]\n"
    "ds_read_b64_tr_b16 a[244:245], v67 offset:0\n"
    "v_mfma_f32_16x16x32_bf16 a[36:39], a[252:255], a[76:79], a[36:39]\n"
    "ds_read_b64_tr_b16 a[246:247], v67 offset:1024\n"
    "s_waitcnt lgkmcnt(12)\n"
    "v_mfma_f32_16x16x32_bf16 a[40:43], a[224:227], a[72:75], a[40:43]\n"
    "ds_read_b64_tr_b16 a[248:249], v68 offset:0\n"
    "v_mfma_f32_16x16x32_bf16 a[44:47], a[224:227], a[76:79], a[44:47]\n"
    "ds_read_b64_tr_b16 a[250:251], v68 offset:1024\n"
    "v_mfma_f32_16x16x32_bf16 a[48:51], a[228:231], a[72:75], a[48:51]\n"
    "ds_read_b64_tr_b16 a[252:253], v69 offset:0\n"
    "v_mfma_f32_16x16x32_bf16 a[52:55], a[228:231], a[76:79], a[52:55]\n"
    "ds_read_b64_tr_b16 a[254:255], v69 offset:1024\n"
    "s_waitcnt lgkmcnt(12)\n"
    "s_waitcnt lgkmcnt(8)\n"
    "v_mfma_f32_16x16x32_bf16 a[0:3], a[232:235], a[80:83], a[0:3]\n"
    "ds_read_b64_tr_b16 a[224:225], v70 offset:0\n"
    "v_mfma_f32_16x16x32_bf16 a[4:7], a[232:235], a[84:87], a[4:7]\n"
    "ds_read_b64_tr_b16 a[226:227], v70 offset:1024\n"
    "v_mfma_f32_16x16x32_bf16 a[8:11], a[236:239], a[80:83], a[8:11]\n"
    "ds_read_b64_tr_b16 a[228:229], v64 offset:8192\n"
    "v_mfma_f32_16x16x32_bf16 a[12:15], a[236:239], a[84:87], a[12:15]\n"
    "ds_read_b64_tr_b16 a[230:231], v64 offset:9216\n"
    "s_waitcnt lgkmcnt(8)\n"
    "v_mfma_f32_16x16x32_bf16 a[16:19], a[240:243], a[80:83], a[16:19]\n"
    "ds_read_b64_tr_b16 a[232:233], v65 offset:8192\n"
    "v_mfma_f32_16x16x32_bf16 a[20:23], a[240:243], a[84:87], a[20:23]\n"
    "ds_read_b64_tr_b16 a[234:235], v65 offset:9216\n"
    "v_mfma_f32_16x16x32_bf16 a[24:27], a[244:247], a[80:83], a[24:27]\n"
    "ds_read_b64_tr_b16 a[236:237], v66 offset:8192\n"
    "v_mfma_f32_16x16x32_bf16 a[28:31], a[244:247], a[84:87], a[28:31]\n"
    "ds_read_b64_tr_b16 a[238:239], v66 offset:9216\n"
    "s_waitcnt lgkmcnt(8)\n"
    "v_mfma_f32_16x16x32_bf16 a[32:35], a[248:251], a[80:83], a[32:35]\n"
    "ds_read_b64_tr_b16 a[240:241], v67 offset:8192\n"
    "v_mfma_f32_16x16x32_bf16 a[36:39], a[248:251], a[84:87], a[36:39]\n"
    "ds_read_b64_tr_b16 a[242:243], v67 offset:9216\n"
    "v_mfma_f32_16x16x32_bf16 a[40:43], a[252:255], a[80:83], a[40:43]\n"
    "ds_read_b64_tr_b16 a[244:245], v68 offset:8192\n"
    "v_mfma_f32_16x16x32_bf16 a[44:47], a[252:255], a[84:87], a[44:47]\n"
    "ds_read_b64_tr_b16 a[246:247], v68 offset:9216\n"
    "s_waitcnt lgkmcnt(8)\n"
    "v_mfma_f32_16x16x32_bf16 a[48:51], a[224:227], a[80:83], a[48:51]\n"
    "ds_read_b64_tr_b16 a[248:249], v69 offset:8192\n"
    "v_mfma_f32_16x16x32_bf16 a[52:55], a[224:227], a[84:87], a[52:55]\n"
    "ds_read_b64_tr_b16 a[250:251], v69 offset:9216\n"
    "v_mfma_f32_16x16x32_bf16 a[0:3], a[228:231], a[88:91], a[0:3]\n"
    "ds_read_b64_tr_b16 a[252:253], v70 offset:8192\n"
    "v_mfma_f32_16x16x32_bf16 a[4:7], a[228:231], a[92:95], a[4:7]\n"
    "ds_read_b64_tr_b16 a[254:255], v70 offset:9216\n"
    "s_waitcnt lgkmcnt(8)\n"
    "v_mfma_f32_16x16x32_bf16 a[8:11], a[232:235], a[88:91], a[8:11]\n"
    "v_mfma_f32_16x16x32_bf16 a[12:15], a[232:235], a[92:95], a[12:15]\n"
    "v_mfma_f32_16x16x32_bf16 a[16:19], a[236:239], a[88:91], a[16:19]\n"
    "v_mfma_f32_16x16x32_bf16 a[20:23], a[236:239], a[92:95], a[20:23]\n"
    "s_waitcnt lgkmcnt(4)\n"
    "v_mfma_f32_16x16x32_bf16 a[24:27], a[240:243], a[88:91], a[24:27]\n"
    "v_mfma_f32_16x16x32_bf16 a[28:31], a[240:243], a[92:95], a[28:31]\n"
    "v_mfma_f32_16x16x32_bf16 a[32:35], a[244:247], a[88:91], a[32:35]\n"
    "v_mfma_f32_16x16x32_bf16 a[36:39], a[244:247], a[92:95], a[36:39]\n"
    "s_waitcnt lgkmcnt(0)\n"
    "v_mfma_f32_16x16x32_bf16 a[40:43], a[248:251], a[88:91], a[40:43]\n"
    "v_mfma_f32_16x16x32_bf16 a[44:47], a[248:251], a[92:95], a[44:47]\n"
    "v_mfma_f32_16x16x32_bf16 a[48:51], a[252:255], a[88:91], a[48:51]\n"
    "v_mfma_f32_16x16x32_bf16 a[52:55], a[252:255], a[92:95], a[52:55]\n"
    "s_waitcnt lgkmcnt(0)\n"
    "s_barrier\n"
    "s_add_u32 s48, s39, 0\n"
    "s_add_u32 m0, s50, 98304\n"
    "s_nop 0\n"
    "buffer_load_dwordx4 %[vov0], s[44:47], s48 offen offset:0 nt lds\n"
    "buffer_load_dwordx4 %[vov1], s[44:47], s48 offen offset:1024 nt lds\n"
    "buffer_load_dwordx4 %[vov2], s[44:47], s48 offen offset:2048 nt lds\n"
    "buffer_load_dwordx4 %[vov3], s[44:47], s48 offen offset:3072 nt lds\n"
    "s_add_u32 s48, s39, 16384\n"
    "s_add_u32 m0, s50, 114688\n"
    "s_nop 0\n"
    "buffer_load_dwordx4 %[vov0], s[44:47], s48 offen offset:0 nt lds\n"
    "buffer_load_dwordx4 %[vov1], s[44:47], s48 offen offset:1024 nt lds\n"
    "buffer_load_dwordx4 %[vov2], s[44:47], s48 offen offset:2048 nt lds\n"
    "buffer_load_dwordx4 %[vov3], s[44:47], s48 offen offset:3072 nt lds\n"
    "s_mul_i32 s52, s78, 28672\n"
    "s_mul_i32 s53, %[kh], 3670016\n"
    "s_add_u32 s52, s52, s53\n"
    "s_add_u32 s52, s52, 33554432\n"
    "s_lshr_b32 s53, %[fs], 1\n"
    "s_mul_i32 s53, s53, 7168\n"
    "s_add_u32 s52, s52, s53\n"
    "s_add_u32 s64, %[wslo], s52\n"
    "s_addc_u32 s65, %[wshi], 0\n"
    "s_and_b32 s65, s65, 0xffff\n"
    "s_mov_b32 s66, 35840\n"
    "s_mov_b32 s67, 0x20000\n"
    "s_and_b32 s53, %[fs], 1\n"
    "s_mul_i32 s76, s53, 3072\n"
    "s_lshl_b32 s53, s53, 3\n"
    "s_add_u32 s77, s53, 6144\n"
    "v_add_u32 v23, 28672, v33\n"
    "v_accvgpr_read_b32 v64, a0\n"
    "v_accvgpr_read_b32 v65, a1\n"
    "v_accvgpr_read_b32 v66, a2\n"
    "v_accvgpr_read_b32 v67, a3\n"
    "v_lshlrev_b32 v68, 16, v100\n"
    "v_and_b32 v69, 0xffff0000, v100\n"
    "v_lshlrev_b32 v70, 16, v101\n"
    "v_and_b32 v71, 0xffff0000, v101\n"
    "v_pk_mul_f32 v[64:65], v[64:65], v[68:69]\n"
    "v_pk_mul_f32 v[66:67], v[66:67], v[70:71]\n"
    "v_cvt_pk_bf16_f32 v100, v64, v65\n"
    "v_cvt_pk_bf16_f32 v101, v66, v67\n"
    "v_accvgpr_read_b32 v72, a8\n"
    "v_accvgpr_read_b32 v73, a9\n"
    "v_accvgpr_read_b32 v74, a10\n"
    "v_accvgpr_read_b32 v75, a11\n"
    "v_lshlrev_b32 v76, 16, v102\n"
    "v_and_b32 v77, 0xffff0000, v102\n"
    "v_lshlrev_b32 v78, 16, v103\n"
    "v_and_b32 v79, 0xffff0000, v103\n"
    "v_pk_mul_f32 v[72:73], v[72:73], v[76:77]\n"
    "v_pk_mul_f32 v[74:75], v[74:75], v[78:79]\n"
    "v_cvt_pk_bf16_f32 v102, v72, v73\n"
    "v_cvt_pk_bf16_f32 v103, v74, v75\n"
    "buffer_store_dwordx4 v[100:103], v33, s[64:67], s76 offen offset:0 sc1\n"
    "v_accvgpr_read_b32 v64, a16\n"
    "v_accvgpr_read_b32 v65, a17\n"
    "v_accvgpr_read_b32 v66, a18\n"
    "v_accvgpr_read_b32 v67, a19\n"
    "v_lshlrev_b32 v68, 16, v104\n"
    "v_and_b32 v69, 0xffff0000, v104\n"
    "v_lshlrev_b32 v70, 16, v105\n"
    "v_and_b32 v71, 0xffff0000, v105\n"
    "v_pk_mul_f32 v[64:65], v[64:65], v[68:69]\n"
    "v_pk_mul_f32 v[66:67], v[66:67], v[70:71]\n"
    "v_cvt_pk_bf16_f32 v104, v64, v65\n"
    "v_cvt_pk_bf16_f32 v105, v66, v67\n"
    "v_accvgpr_read_b32 v72, a24\n"
    "v_accvgpr_read_b32 v73, a25\n"
    "v_accvgpr_read_b32 v74, a26\n"
    "v_accvgpr_read_b32 v75, a27\n"
    "v_lshlrev_b32 v76, 16, v106\n"
    "v_and_b32 v77, 0xffff0000, v106\n"
    "v_lshlrev_b32 v78, 16, v107\n"
    "v_and_b32 v79, 0xffff0000, v107\n"
    "v_pk_mul_f32 v[72:73], v[72:73], v[76:77]\n"
    "v_pk_mul_f32 v[74:75], v[74:75], v[78:79]\n"
    "v_cvt_pk_bf16_f32 v106, v72, v73\n"
    "v_cvt_pk_bf16_f32 v107, v74, v75\n"
    "buffer_store_dwordx4 v[104:107], v33, s[64:67], s76 offen offset:1024 sc1\n"
    "v_accvgpr_read_b32 v64, a32\n"
    "v_accvgpr_read_b32 v65, a33\n"
    "v_accvgpr_read_b32 v66, a34\n"
    "v_accvgpr_read_b32 v67, a35\n"
    "v_lshlrev_b32 v68, 16, v108\n"
    "v_and_b32 v69, 0xffff0000, v108\n"
    "v_lshlrev_b32 v70, 16, v109\n"
    "v_and_b32 v71, 0xffff0000, v109\n"
    "v_pk_mul_f32 v[64:65], v[64:65], v[68:69]\n"
    "v_pk_mul_f32 v[66:67], v[66:67], v[70:71]\n"
    "v_cvt_pk_bf16_f32 v108, v64, v65\n"
    "v_cvt_pk_bf16_f32 v109, v66, v67\n"
    "v_accvgpr_read_b32 v72, a40\n"
    "v_accvgpr_read_b32 v73, a41\n"
    "v_accvgpr_read_b32 v74, a42\n"
    "v_accvgpr_read_b32 v75, a43\n"
    "v_lshlrev_b32 v76, 16, v110\n"
    "v_and_b32 v77, 0xffff0000, v110\n"
    "v_lshlrev_b32 v78, 16, v111\n"
    "v_and_b32 v79, 0xffff0000, v111\n"
    "v_pk_mul_f32 v[72:73], v[72:73], v[76:77]\n"
    "v_pk_mul_f32 v[74:75], v[74:75], v[78:79]\n"
    "v_cvt_pk_bf16_f32 v110, v72, v73\n"
    "v_cvt_pk_bf16_f32 v111, v74, v75\n"
    "buffer_store_dwordx4 v[108:111], v33, s[64:67], s76 offen offset:2048 sc1\n"
    "v_accvgpr_read_b32 v64, a48\n"
    "v_accvgpr_read_b32 v65, a49\n"
    "v_accvgpr_read_b32 v66, a50\n"
    "v_accvgpr_read_b32 v67, a51\n"
    "v_lshlrev_b32 v68, 16, v112\n"
    "v_and_b32 v69, 0xffff0000, v112\n"
    "v_lshlrev_b32 v70, 16, v113\n"
    "v_and_b32 v71, 0xffff0000, v113\n"
    "v_pk_mul_f32 v[64:65], v[64:65], v[68:69]\n"
    "v_pk_mul_f32 v[66:67], v[66:67], v[70:71]\n"
    "v_cvt_pk_bf16_f32 v112, v64, v65\n"
    "v_cvt_pk_bf16_f32 v113, v66, v67\n"
    "buffer_store_dwordx2 v[112:113], v33, s[64:67], s77 offen sc1\n"
    "v_accvgpr_read_b32 v64, a4\n"
    "v_accvgpr_read_b32 v65, a5\n"
    "v_accvgpr_read_b32 v66, a6\n"
    "v_accvgpr_read_b32 v67, a7\n"
    "v_lshlrev_b32 v68, 16, v114\n"
    "v_and_b32 v69, 0xffff0000, v114\n"
    "v_lshlrev_b32 v70, 16, v115\n"
    "v_and_b32 v71, 0xffff0000, v115\n"
    "v_pk_mul_f32 v[64:65], v[64:65], v[68:69]\n"
    "v_pk_mul_f32 v[66:67], v[66:67], v[70:71]\n"
    "v_cvt_pk_bf16_f32 v114, v64, v65\n"
    "v_cvt_pk_bf16_f32 v115, v66, v67\n"
    "v_accvgpr_read_b32 v72, a12\n"
    "v_accvgpr_read_b32 v73, a13\n"
    "v_accvgpr_read_b32 v74, a14\n"
    "v_accvgpr_read_b32 v75, a15\n"
    "v_lshlrev_b32 v76, 16, v116\n"
    "v_and_b32 v77, 0xffff0000, v116\n"
    "v_lshlrev_b32 v78, 16, v117\n"
    "v_and_b32 v79, 0xffff0000, v117\n"
    "v_pk_mul_f32 v[72:73], v[72:73], v[76:77]\n"
    "v_pk_mul_f32 v[74:75], v[74:75], v[78:79]\n"
    "v_cvt_pk_bf16_f32 v116, v72, v73\n"
    "v_cvt_pk_bf16_f32 v117, v74, v75\n"
    "buffer_store_dwordx4 v[114:117], v23, s[64:67], s76 offen offset:0 sc1\n"
    "v_accvgpr_read_b32 v64, a20\n"
    "v_accvgpr_read_b32 v65, a21\n"
    "v_accvgpr_read_b32 v66, a22\n"
    "v_accvgpr_read_b32 v67, a23\n"
    "v_lshlrev_b32 v68, 16, v118\n"
    "v_and_b32 v69, 0xffff0000, v118\n"
    "v_lshlrev_b32 v70, 16, v119\n"
    "v_and_b32 v71, 0xffff0000, v119\n"
    "v_pk_mul_f32 v[64:65], v[64:65], v[68:69]\n"
    "v_pk_mul_f32 v[66:67], v[66:67], v[70:71]\n"
    "v_cvt_pk_bf16_f32 v118, v64, v65\n"
    "v_cvt_pk_bf16_f32 v119, v66, v67\n"
    "v_accvgpr_read_b32 v72, a28\n"
    "v_accvgpr_read_b32 v73, a29\n"
    "v_accvgpr_read_b32 v74, a30\n"
    "v_accvgpr_read_b32 v75, a31\n"
    "v_lshlrev_b32 v76, 16, v120\n"
    "v_and_b32 v77, 0xffff0000, v120\n"
    "v_lshlrev_b32 v78, 16, v121\n"
    "v_and_b32 v79, 0xffff0000, v121\n"
    "v_pk_mul_f32 v[72:73], v[72:73], v[76:77]\n"
    "v_pk_mul_f32 v[74:75], v[74:75], v[78:79]\n"
    "v_cvt_pk_bf16_f32 v120, v72, v73\n"
    "v_cvt_pk_bf16_f32 v121, v74, v75\n"
    "buffer_store_dwordx4 v[118:121], v23, s[64:67], s76 offen offset:1024 sc1\n"
    "v_accvgpr_read_b32 v64, a36\n"
    "v_accvgpr_read_b32 v65, a37\n"
    "v_accvgpr_read_b32 v66, a38\n"
    "v_accvgpr_read_b32 v67, a39\n"
    "v_lshlrev_b32 v68, 16, v122\n"
    "v_and_b32 v69, 0xffff0000, v122\n"
    "v_lshlrev_b32 v70, 16, v123\n"
    "v_and_b32 v71, 0xffff0000, v123\n"
    "v_pk_mul_f32 v[64:65], v[64:65], v[68:69]\n"
    "v_pk_mul_f32 v[66:67], v[66:67], v[70:71]\n"
    "v_cvt_pk_bf16_f32 v122, v64, v65\n"
    "v_cvt_pk_bf16_f32 v123, v66, v67\n"
    "v_accvgpr_read_b32 v72, a44\n"
    "v_accvgpr_read_b32 v73, a45\n"
    "v_accvgpr_read_b32 v74, a46\n"
    "v_accvgpr_read_b32 v75, a47\n"
    "v_lshlrev_b32 v76, 16, v124\n"
    "v_and_b32 v77, 0xffff0000, v124\n"
    "v_lshlrev_b32 v78, 16, v125\n"
    "v_and_b32 v79, 0xffff0000, v125\n"
    "v_pk_mul_f32 v[72:73], v[72:73], v[76:77]\n"
    "v_pk_mul_f32 v[74:75], v[74:75], v[78:79]\n"
    "v_cvt_pk_bf16_f32 v124, v72, v73\n"
    "v_cvt_pk_bf16_f32 v125, v74, v75\n"
    "buffer_store_dwordx4 v[122:125], v23, s[64:67], s76 offen offset:2048 sc1\n"
    "v_accvgpr_read_b32 v64, a52\n"
    "v_accvgpr_read_b32 v65, a53\n"
    "v_accvgpr_read_b32 v66, a54\n"
    "v_accvgpr_read_b32 v67, a55\n"
    "v_lshlrev_b32 v68, 16, v126\n"
    "v_and_b32 v69, 0xffff0000, v126\n"
    "v_lshlrev_b32 v70, 16, v127\n"
    "v_and_b32 v71, 0xffff0000, v127\n"
    "v_pk_mul_f32 v[64:65], v[64:65], v[68:69]\n"
    "v_pk_mul_f32 v[66:67], v[66:67], v[70:71]\n"
    "v_cvt_pk_bf16_f32 v126, v64, v65\n"
    "v_cvt_pk_bf16_f32 v127, v66, v67\n"
    "buffer_store_dwordx2 v[126:127], v23, s[64:67], s77 offen sc1\n"
    "v_sub_f32 v96, 0, v24\n"
    "v_sub_f32 v97, 0, v24\n"
    "v_sub_f32 v98, 0, v24\n"
    "v_sub_f32 v99, 0, v24\n"
    "v_sub_f32 v100, 0, v25\n"
    "v_sub_f32 v101, 0, v25\n"
    "v_sub_f32 v102, 0, v25\n"
    "v_sub_f32 v103, 0, v25\n"
    "s_waitcnt vmcnt(20)\n"
    "s_barrier\n"
    "s_add_u32 s48, s39, 32768\n"
    "s_add_u32 m0, s50, 131072\n"
    "s_nop 0\n"
    "buffer_load_dwordx4 %[vov0], s[44:47], s48 offen offset:0 nt lds\n"
    "buffer_load_dwordx4 %[vov1], s[44:47], s48 offen offset:1024 nt lds\n"
    "buffer_load_dwordx4 %[vov2], s[44:47], s48 offen offset:2048 nt lds\n"
    "buffer_load_dwordx4 %[vov3], s[44:47], s48 offen offset:3072 nt lds\n"
    "ds_read_b128 a[192:195], v48 offset:0\n"
    "ds_read_b128 a[196:199], v49 offset:0\n"
    "ds_read_b128 a[200:203], v50 offset:0\n"
    "ds_read_b128 a[204:207], v51 offset:0\n"
    "ds_read_b128 a[208:211], v48 offset:256\n"
    "ds_read_b128 a[212:215], v49 offset:256\n"
    "ds_read_b128 a[216:219], v50 offset:256\n"
    "ds_read_b128 a[220:223], v51 offset:256\n"
    "ds_read_b128 a[224:227], v48 offset:8192\n"
    "ds_read_b128 a[228:231], v49 offset:8192\n"
    "ds_read_b128 a[232:235], v50 offset:8192\n"
    "ds_read_b128 a[236:239], v51 offset:8192\n"
    "ds_read_b128 a[240:243], v48 offset:8448\n"
    "ds_read_b128 a[244:247], v49 offset:8448\n"
    "ds_read_b128 a[248:251], v50 offset:8448\n"
    "ds_read_b128 a[252:255], v51 offset:8448\n"
    "s_waitcnt lgkmcnt(15)\n"
    "v_mfma_f32_16x16x32_bf16 v[64:67], a[192:195], %[q0], v[96:99]\n"
    "v_mfma_f32_16x16x32_bf16 v[68:71], a[192:195], %[q1], v[100:103]\n"
    "s_waitcnt lgkmcnt(14)\n"
    "v_mfma_f32_16x16x32_bf16 v[64:67], a[196:199], %[q2], v[64:67]\n"
    "v_mfma_f32_16x16x32_bf16 v[68:71], a[196:199], %[q3], v[68:71]\n"
    "s_waitcnt lgkmcnt(13)\n"
    "v_mfma_f32_16x16x32_bf16 v[64:67], a[200:203], %[q4], v[64:67]\n"
    "v_mfma_f32_16x16x32_bf16 v[68:71], a[200:203], %[q5], v[68:71]\n"
    "s_waitcnt lgkmcnt(12)\n"
    "v_mfma_f32_16x16x32_bf16 v[64:67], a[204:207], %[q6], v[64:67]\n"
    "v_mfma_f32_16x16x32_bf16 v[68:71], a[204:207], %[q7], v[68:71]\n"
    "s_waitcnt lgkmcnt(11)\n"
    "v_mfma_f32_16x16x32_bf16 v[64:67], a[208:211], %[q8], v[64:67]\n"
    "v_mfma_f32_16x16x32_bf16 v[68:71], a[208:211], %[q9], v[68:71]\n"
    "s_waitcnt lgkmcnt(10)\n"
    "v_mfma_f32_16x16x32_bf16 v[64:67], a[212:215], %[q10], v[64:67]\n"
    "v_mfma_f32_16x16x32_bf16 v[68:71], a[212:215], %[q11], v[68:71]\n"
    "s_waitcnt lgkmcnt(9)\n"
    "v_mfma_f32_16x16x32_bf16 v[64:67], a[216:219], %[q12], v[64:67]\n"
    "v_mfma_f32_16x16x32_bf16 v[68:71], a[216:219], %[q13], v[68:71]\n"
    "s_waitcnt lgkmcnt(8)\n"
    "v_mfma_f32_16x16x32_bf16 v[64:67], a[220:223], %[q14], v[64:67]\n"
    "v_mfma_f32_16x16x32_bf16 v[68:71], a[220:223], %[q15], v[68:71]\n"
    "s_waitcnt lgkmcnt(7)\n"
    "v_mfma_f32_16x16x32_bf16 v[72:75], a[224:227], %[q0], v[96:99]\n"
    "v_mfma_f32_16x16x32_bf16 v[76:79], a[224:227], %[q1], v[100:103]\n"
    "s_waitcnt lgkmcnt(6)\n"
    "v_mfma_f32_16x16x32_bf16 v[72:75], a[228:231], %[q2], v[72:75]\n"
    "v_mfma_f32_16x16x32_bf16 v[76:79], a[228:231], %[q3], v[76:79]\n"
    "s_waitcnt lgkmcnt(5)\n"
    "v_mfma_f32_16x16x32_bf16 v[72:75], a[232:235], %[q4], v[72:75]\n"
    "v_mfma_f32_16x16x32_bf16 v[76:79], a[232:235], %[q5], v[76:79]\n"
    "s_waitcnt lgkmcnt(4)\n"
    "v_mfma_f32_16x16x32_bf16 v[72:75], a[236:239], %[q6], v[72:75]\n"
    "v_mfma_f32_16x16x32_bf16 v[76:79], a[236:239], %[q7], v[76:79]\n"
    "s_waitcnt lgkmcnt(3)\n"
    "v_mfma_f32_16x16x32_bf16 v[72:75], a[240:243], %[q8], v[72:75]\n"
    "v_mfma_f32_16x16x32_bf16 v[76:79], a[240:243], %[q9], v[76:79]\n"
    "s_waitcnt lgkmcnt(2)\n"
    "v_mfma_f32_16x16x32_bf16 v[72:75], a[244:247], %[q10], v[72:75]\n"
    "v_mfma_f32_16x16x32_bf16 v[76:79], a[244:247], %[q11], v[76:79]\n"
    "s_waitcnt lgkmcnt(1)\n"
    "v_mfma_f32_16x16x32_bf16 v[72:75], a[248:251], %[q12], v[72:75]\n"
    "v_mfma_f32_16x16x32_bf16 v[76:79], a[248:251], %[q13], v[76:79]\n"
    "s_waitcnt lgkmcnt(0)\n"
    "v_mfma_f32_16x16x32_bf16 v[72:75], a[252:255], %[q14], v[72:75]\n"
    "v_mfma_f32_16x16x32_bf16 v[76:79], a[252:255], %[q15], v[76:79]\n"
    "s_waitcnt vmcnt(16)\n"
    "s_barrier\n"
    "s_add_u32 m0, s50, 32768\n"
    "s_nop 0\n"
    "buffer_load_dwordx4 v60, s[40:43], s48 offen offset:0 nt lds\n"
    "buffer_load_dwordx4 v61, s[40:43], s48 offen offset:1024 nt lds\n"
    "buffer_load_dwordx4 v62, s[40:43], s48 offen offset:2048 nt lds\n"
    "buffer_load_dwordx4 v63, s[40:43], s48 offen offset:3072 nt lds\n"
    "ds_read_b128 a[192:195], v48 offset:16384\n"
    "ds_read_b128 a[196:199], v49 offset:16384\n"
    "ds_read_b128 a[200:203], v50 offset:16384\n"
    "ds_read_b128 a[204:207], v51 offset:16384\n"
    "ds_read_b128 a[208:211], v48 offset:16640\n"
    "ds_read_b128 a[212:215], v49 offset:16640\n"
    "ds_read_b128 a[216:219], v50 offset:16640\n"
    "ds_read_b128 a[220:223], v51 offset:16640\n"
    "ds_read_b128 a[224:227], v48 offset:24576\n"
    "ds_read_b128 a[228:231], v49 offset:24576\n"
    "ds_read_b128 a[232:235], v50 offset:24576\n"
    "ds_read_b128 a[236:239], v51 offset:24576\n"
    "s_nop 7\n"
    "v_max3_f32 v110, v64, v65, v66\n"
    "v_max3_f32 v110, v110, v67, v68\n"
    "v_max3_f32 v110, v110, v69, v70\n"
    "v_max3_f32 v110, v110, v71, v72\n"
    "v_max3_f32 v110, v110, v73, v74\n"
    "v_max3_f32 v110, v110, v75, v76\n"
    "v_max3_f32 v110, v110, v77, v78\n"
    "v_max_f32 v110, v110, v79\n"
    "v_cmp_lt_f32 vcc, s79, v110\n"
    "s_cbranch_vccnz .LrareS_%=\n"
    ".LretS_%=:\n"
    "s_nop 1\n"
    "s_sub_u32 s51, 13, %[n0]\n"
    "s_mov_b32 s73, 0\n"
    "s_mov_b32 s74, 1\n"
    "s_branch .Lloop_%=\n"
    ".Lpass1tail_%=:\n"
    "s_lshl_b32 s75, %[tp2], 7\n"
    "s_add_u32 s75, s75, 8192\n"
    "buffer_load_dwordx2 v[46:47], v22, s[80:83], s75 offen sc1\n"
    "s_mul_i32 s52, %[nq], 200704\n"
    "s_add_u32 s52, s52, 62910464\n"
    "s_add_u32 s56, %[wslo], s52\n"
    "s_addc_u32 s57, %[wshi], 0\n"
    "s_and_b32 s57, s57, 0xffff\n"
    "s_mov_b32 s58, 204800\n"
    "s_mov_b32 s59, 0x20000\n"
    "s_mul_i32 s52, %[nq], 917504\n"
    "s_mul_i32 s53, %[kq], 229376\n"
    "s_add_u32 s52, s52, s53\n"
    "s_add_u32 s52, s52, 58716160\n"
    "s_add_u32 s60, %[wslo], s52\n"
    "s_addc_u32 s61, %[wshi], 0\n"
    "s_and_b32 s61, s61, 0xffff\n"
    "s_mov_b32 s62, 233472\n"
    "s_mov_b32 s63, 0x20000\n"
    "s_lshl_b32 s52, %[tp2], 3\n"
    "s_lshl_b32 s53, %[kq], 1\n"
    "s_add_u32 s52, s52, s53\n"
    "s_and_b32 s53, %[w], 1\n"
    "s_add_u32 s52, s52, s53\n"
    "s_lshr_b32 s53, %[w], 1\n"
    "s_lshl_b32 s53, s53, 7\n"
    "s_add_u32 s52, s52, s53\n"
    "s_mul_i32 s52, s52, 28672\n"
    "s_add_u32 s52, s52, 33554432\n"
    "s_add_u32 s68, %[wslo], s52\n"
    "s_addc_u32 s69, %[wshi], 0\n"
    "s_and_b32 s69, s69, 0xffff\n"
    "s_mov_b32 s70, 28672\n"
    "s_mov_b32 s71, 0x20000\n"
    "s_waitcnt lgkmcnt(8)\n"
    "v_mfma_f32_16x16x32_bf16 v[80:83], a[192:195], %[q0], v[96:99]\n"
    "v_exp_f32 v64, v64\n"
    "v_mfma_f32_16x16x32_bf16 v[84:87], a[192:195], %[q1], v[100:103]\n"
    "v_exp_f32 v65, v65\n"
    "v_mfma_f32_16x16x32_bf16 v[80:83], a[196:199], %[q2], v[80:83]\n"
    "v_exp_f32 v66, v66\n"
    "v_mfma_f32_16x16x32_bf16 v[84:87], a[196:199], %[q3], v[84:87]\n"
    "v_exp_f32 v67, v67\n"
    "v_mfma_f32_16x16x32_bf16 v[80:83], a[200:203], %[q4], v[80:83]\n"
    "v_exp_f32 v72, v72\n"
    "v_mfma_f32_16x16x32_bf16 v[84:87], a[200:203], %[q5], v[84:87]\n"
    "v_exp_f32 v73, v73\n"
    "v_mfma_f32_16x16x32_bf16 v[80:83], a[204:207], %[q6], v[80:83]\n"
    "v_exp_f32 v74, v74\n"
    "v_mfma_f32_16x16x32_bf16 v[84:87], a[204:207], %[q7], v[84:87]\n"
    "v_exp_f32 v75, v75\n"
    "s_waitcnt lgkmcnt(4)\n"
    "v_mfma_f32_16x16x32_bf16 v[80:83], a[208:211], %[q8], v[80:83]\n"
    "v_exp_f32 v68, v68\n"
    "v_mfma_f32_16x16x32_bf16 v[84:87], a[208:211], %[q9], v[84:87]\n"
    "v_exp_f32 v69, v69\n"
    "v_mfma_f32_16x16x32_bf16 v[80:83], a[212:215], %[q10], v[80:83]\n"
    "v_exp_f32 v70, v70\n"
    "v_mfma_f32_16x16x32_bf16 v[84:87], a[212:215], %[q11], v[84:87]\n"
    "v_exp_f32 v71, v71\n"
    "v_mfma_f32_16x16x32_bf16 v[80:83], a[216:219], %[q12], v[80:83]\n"
    "v_exp_f32 v76, v76\n"
    "v_mfma_f32_16x16x32_bf16 v[84:87], a[216:219], %[q13], v[84:87]\n"
    "v_exp_f32 v77, v77\n"
    "v_mfma_f32_16x16x32_bf16 v[80:83], a[220:223], %[q14], v[80:83]\n"
    "v_exp_f32 v78, v78\n"
    "v_mfma_f32_16x16x32_bf16 v[84:87], a[220:223], %[q15], v[84:87]\n"
    "v_exp_f32 v79, v79\n"
    "s_waitcnt lgkmcnt(0)\n"
    "v_mfma_f32_16x16x32_bf16 v[88:91], a[224:227], %[q0], v[96:99]\n"
    "ds_read_b128 a[240:243], v48 offset:24832\n"
    "v_cvt_pk_bf16_f32 v112, v64, v65\n"
    "v_mfma_f32_16x16x32_bf16 v[92:95], a[224:227], %[q1], v[100:103]\n"
    "ds_read_b128 a[244:247], v49 offset:24832\n"
    "v_cvt_pk_bf16_f32 v113, v66, v67\n"
    "v_mfma_f32_16x16x32_bf16 v[88:91], a[228:231], %[q2], v[88:91]\n"
    "ds_read_b128 a[248:251], v50 offset:24832\n"
    "v_cvt_pk_bf16_f32 v114, v72, v73\n"
    "v_mfma_f32_16x16x32_bf16 v[92:95], a[228:231], %[q3], v[92:95]\n"
    "ds_read_b128 a[252:255], v51 offset:24832\n"
    "v_cvt_pk_bf16_f32 v115, v74, v75\n"
    "v_mfma_f32_16x16x32_bf16 v[88:91], a[232:235], %[q4], v[88:91]\n"
    "ds_read_b64_tr_b16 a[192:193], v52 offset:0\n"
    "v_cvt_pk_bf16_f32 v116, v68, v69\n"
    "v_mfma_f32_16x16x32_bf16 v[92:95], a[232:235], %[q5], v[92:95]\n"
    "ds_read_b64_tr_b16 a[194:195], v52 offset:8192\n"
    "v_cvt_pk_bf16_f32 v117, v70, v71\n"
    "v_mfma_f32_16x16x32_bf16 v[88:91], a[236:239], %[q6], v[88:91]\n"
    "ds_read_b64_tr_b16 a[196:197], v53 offset:0\n"
    "v_cvt_pk_bf16_f32 v118, v76, v77\n"
    "v_mfma_f32_16x16x32_bf16 v[92:95], a[236:239], %[q7], v[92:95]\n"
    "ds_read_b64_tr_b16 a[198:199], v53 offset:8192\n"
    "v_cvt_pk_bf16_f32 v119, v78, v79\n"
    "s_waitcnt lgkmcnt(4)\n"
    "v_mfma_f32_16x16x32_bf16 v[88:91], a[240:243], %[q8], v[88:91]\n"
    "ds_read_b64_tr_b16 a[200:201], v54 offset:0\n"
    "v_add_f32 v26, v26, v64\n"
    "v_mfma_f32_16x16x32_bf16 v[92:95], a[240:243], %[q9], v[92:95]\n"
    "ds_read_b64_tr_b16 a[202:203], v54 offset:8192\n"
    "v_add_f32 v27, v27, v65\n"
    "v_mfma_f32_16x16x32_bf16 v[88:91], a[244:247], %[q10], v[88:91]\n"
    "ds_read_b64_tr_b16 a[204:205], v55 offset:0\n"
    "v_add_f32 v26, v26, v66\n"
    "v_mfma_f32_16x16x32_bf16 v[92:95], a[244:247], %[q11], v[92:95]\n"
    "ds_read_b64_tr_b16 a[206:207], v55 offset:8192\n"
    "v_add_f32 v27, v27, v67\n"
    "v_mfma_f32_16x16x32_bf16 v[88:91], a[248:251], %[q12], v[88:91]\n"
    "ds_read_b64_tr_b16 a[208:209], v56 offset:0\n"
    "v_add_f32 v26, v26, v72\n"
    "v_mfma_f32_16x16x32_bf16 v[92:95], a[248:251], %[q13], v[92:95]\n"
    "ds_read_b64_tr_b16 a[210:211], v56 offset:8192\n"
    "v_add_f32 v27, v27, v73\n"
    "v_mfma_f32_16x16x32_bf16 v[88:91], a[252:255], %[q14], v[88:91]\n"
    "ds_read_b64_tr_b16 a[212:213], v57 offset:0\n"
    "v_add_f32 v26, v26, v74\n"
    "v_mfma_f32_16x16x32_bf16 v[92:95], a[252:255], %[q15], v[92:95]\n"
    "ds_read_b64_tr_b16 a[214:215], v57 offset:8192\n"
    "v_add_f32 v27, v27, v75\n"
    "s_waitcnt lgkmcnt(8)\n"
    "v_mfma_f32_16x16x32_bf16 v[128:131], a[192:195], v[112:115], v[128:131]\n"
    "ds_read_b64_tr_b16 a[216:217], v58 offset:0\n"
    "v_add_f32 v28, v28, v68\n"
    "v_mfma_f32_16x16x32_bf16 v[132:135], a[192:195], v[116:119], v[132:135]\n"
    "ds_read_b64_tr_b16 a[218:219], v58 offset:8192\n"
    "v_add_f32 v29, v29, v69\n"
    "v_mfma_f32_16x16x32_bf16 v[136:139], a[196:199], v[112:115], v[136:139]\n"
    "ds_read_b64_tr_b16 a[220:221], v59 offset:0\n"
    "v_add_f32 v28, v28, v70\n"
    "v_mfma_f32_16x16x32_bf16 v[140:143], a[196:199], v[116:119], v[140:143]\n"
    "ds_read_b64_tr_b16 a[222:223], v59 offset:8192\n"
    "v_add_f32 v29, v29, v71\n"
    "s_waitcnt lgkmcnt(8)\n"
    "v_mfma_f32_16x16x32_bf16 v[144:147], a[200:203], v[112:115], v[144:147]\n"
    "ds_read_b64_tr_b16 a[224:225], v52 offset:256\n"
    "v_add_f32 v28, v28, v76\n"
    "v_mfma_f32_16x16x32_bf16 v[148:151], a[200:203], v[116:119], v[148:151]\n"
    "ds_read_b64_tr_b16 a[226:227], v52 offset:8448\n"
    "v_add_f32 v29, v29, v77\n"
    "v_mfma_f32_16x16x32_bf16 v[152:155], a[204:207], v[112:115], v[152:155]\n"
    "ds_read_b64_tr_b16 a[228:229], v53 offset:256\n"
    "v_add_f32 v28, v28, v78\n"
    "v_mfma_f32_16x16x32_bf16 v[156:159], a[204:207], v[116:119], v[156:159]\n"
    "ds_read_b64_tr_b16 a[230:231], v53 offset:8448\n"
    "v_add_f32 v29, v29, v79\n"
    "s_waitcnt lgkmcnt(8)\n"
    "v_mfma_f32_16x16x32_bf16 v[160:163], a[208:211], v[112:115], v[160:163]\n"
    "ds_read_b64_tr_b16 a[232:233], v54 offset:256\n"
    "s_waitcnt vmcnt(13)\n"
    "s_barrier\n"
    "s_add_u32 s48, s48, 16384\n"
    "v_mfma_f32_16x16x32_bf16 v[164:167], a[208:211], v[116:119], v[164:167]\n"
    "ds_read_b64_tr_b16 a[234:235], v54 offset:8448\n"
    "v_max3_f32 v110, v80, v81, v82\n"
    "s_add_u32 m0, s50, 147456\n"
    "v_mfma_f32_16x16x32_bf16 v[168:171], a[212:215], v[112:115], v[168:171]\n"
    "ds_read_b64_tr_b16 a[236:237], v55 offset:256\n"
    "buffer_load_dwordx4 %[vov0], s[44:47], s48 offen offset:0 nt lds\n"
    "v_mfma_f32_16x16x32_bf16 v[172:175], a[212:215], v[116:119], v[172:175]\n"
    "ds_read_b64_tr_b16 a[238:239], v55 offset:8448\n"
    "v_max3_f32 v110, v110, v83, v84\n"
    "s_waitcnt lgkmcnt(8)\n"
    "v_mfma_f32_16x16x32_bf16 v[176:179], a[216:219], v[112:115], v[176:179]\n"
    "ds_read_b64_tr_b16 a[240:241], v56 offset:256\n"
    "v_max3_f32 v110, v110, v85, v86\n"
    "v_mfma_f32_16x16x32_bf16 v[180:183], a[216:219], v[116:119], v[180:183]\n"
    "ds_read_b64_tr_b16 a[242:243], v56 offset:8448\n"
    "buffer_load_dwordx4 %[vov1], s[44:47], s48 offen offset:1024 nt lds\n"
    "v_mfma_f32_16x16x32_bf16 v[184:187], a[220:223], v[112:115], v[184:187]\n"
    "ds_read_b64_tr_b16 a[244:245], v57 offset:256\n"
    "v_max3_f32 v110, v110, v87, v88\n"
    "v_mfma_f32_16x16x32_bf16 v[188:191], a[220:223], v[116:119], v[188:191]\n"
    "ds_read_b64_tr_b16 a[246:247], v57 offset:8448\n"
    "v_max3_f32 v110, v110, v89, v90\n"
    "s_waitcnt lgkmcnt(8)\n"
    "v_mfma_f32_16x16x32_bf16 v[192:195], a[224:227], v[112:115], v[192:195]\n"
    "ds_read_b64_tr_b16 a[248:249], v58 offset:256\n"
    "buffer_load_dwordx4 %[vov2], s[44:47], s48 offen offset:2048 nt lds\n"
    "v_mfma_f32_16x16x32_bf16 v[196:199], a[224:227], v[116:119], v[196:199]\n"
    "ds_read_b64_tr_b16 a[250:251], v58 offset:8448\n"
    "v_max3_f32 v110, v110, v91, v92\n"
    "v_mfma_f32_16x16x32_bf16 v[200:203], a[228:231], v[112:115], v[200:203]\n"
    "ds_read_b64_tr_b16 a[252:253], v59 offset:256\n"
    "v_max3_f32 v110, v110, v93, v94\n"
    "v_mfma_f32_16x16x32_bf16 v[204:207], a[228:231], v[116:119], v[204:207]\n"
    "ds_read_b64_tr_b16 a[254:255], v59 offset:8448\n"
    "buffer_load_dwordx4 %[vov3], s[44:47], s48 offen offset:3072 nt lds\n"
    "s_waitcnt lgkmcnt(8)\n"
    "v_mfma_f32_16x16x32_bf16 v[208:211], a[232:235], v[112:115], v[208:211]\n"
    "ds_read_b128 a[192:195], v48 offset:32768\n"
    "v_max_f32 v110, v110, v95\n"
    "v_mfma_f32_16x16x32_bf16 v[212:215], a[232:235], v[116:119], v[212:215]\n"
    "ds_read_b128 a[196:199], v49 offset:32768\n"
    "s_add_u32 m0, s50, 49152\n"
    "v_mfma_f32_16x16x32_bf16 v[216:219], a[236:239], v[112:115], v[216:219]\n"
    "ds_read_b128 a[200:203], v50 offset:32768\n"
    "buffer_load_dwordx4 v60, s[40:43], s48 offen offset:0 nt lds\n"
    "v_mfma_f32_16x16x32_bf16 v[220:223], a[236:239], v[116:119], v[220:223]\n"
    "ds_read_b128 a[204:207], v51 offset:32768\n"
    "v_cmp_lt_f32 vcc, s79, v110\n"
    "s_waitcnt lgkmcnt(8)\n"
    "v_mfma_f32_16x16x32_bf16 v[224:227], a[240:243], v[112:115], v[224:227]\n"
    "ds_read_b128 a[208:211], v48 offset:33024\n"
    "v_mfma_f32_16x16x32_bf16 v[228:231], a[240:243], v[116:119], v[228:231]\n"
    "ds_read_b128 a[212:215], v49 offset:33024\n"
    "buffer_load_dwordx4 v61, s[40:43], s48 offen offset:1024 nt lds\n"
    "v_mfma_f32_16x16x32_bf16 v[232:235], a[244:247], v[112:115], v[232:235]\n"
    "ds_read_b128 a[216:219], v50 offset:33024\n"
    "v_mfma_f32_16x16x32_bf16 v[236:239], a[244:247], v[116:119], v[236:239]\n"
    "ds_read_b128 a[220:223], v51 offset:33024\n"
    "s_waitcnt lgkmcnt(8)\n"
    "v_mfma_f32_16x16x32_bf16 v[240:243], a[248:251], v[112:115], v[240:243]\n"
    "ds_read_b128 a[224:227], v48 offset:40960\n"
    "buffer_load_dwordx4 v62, s[40:43], s48 offen offset:2048 nt lds\n"
    "v_mfma_f32_16x16x32_bf16 v[244:247], a[248:251], v[116:119], v[244:247]\n"
    "ds_read_b128 a[228:231], v49 offset:40960\n"
    "v_mfma_f32_16x16x32_bf16 v[248:251], a[252:255], v[112:115], v[248:251]\n"
    "ds_read_b128 a[232:235], v50 offset:40960\n"
    "v_mfma_f32_16x16x32_bf16 v[252:255], a[252:255], v[116:119], v[252:255]\n"
    "ds_read_b128 a[236:239], v51 offset:40960\n"
    "buffer_load_dwordx4 v63, s[40:43], s48 offen offset:3072 nt lds\n"
    "s_cbranch_vccnz .LrareP1T28_%=\n"
    ".LretP1T28_%=:\n"
    "s_waitcnt lgkmcnt(8)\n"
    "v_mfma_f32_16x16x32_bf16 v[64:67], a[192:195], %[q0], v[96:99]\n"
    "v_exp_f32 v80, v80\n"
    "v_mfma_f32_16x16x32_bf16 v[68:71], a[192:195], %[q1], v[100:103]\n"
    "v_exp_f32 v81, v81\n"
    "v_mfma_f32_16x16x32_bf16 v[64:67], a[196:199], %[q2], v[64:67]\n"
    "v_exp_f32 v82, v82\n"
    "v_mfma_f32_16x16x32_bf16 v[68:71], a[196:199], %[q3], v[68:71]\n"
    "v_exp_f32 v83, v83\n"
    "v_mfma_f32_16x16x32_bf16 v[64:67], a[200:203], %[q4], v[64:67]\n"
    "v_exp_f32 v88, v88\n"
    "v_mfma_f32_16x16x32_bf16 v[68:71], a[200:203], %[q5], v[68:71]\n"
    "v_exp_f32 v89, v89\n"
    "v_mfma_f32_16x16x32_bf16 v[64:67], a[204:207], %[q6], v[64:67]\n"
    "v_exp_f32 v90, v90\n"
    "v_mfma_f32_16x16x32_bf16 v[68:71], a[204:207], %[q7], v[68:71]\n"
    "v_exp_f32 v91, v91\n"
    "s_waitcnt lgkmcnt(4)\n"
    "v_mfma_f32_16x16x32_bf16 v[64:67], a[208:211], %[q8], v[64:67]\n"
    "v_exp_f32 v84, v84\n"
    "v_mfma_f32_16x16x32_bf16 v[68:71], a[208:211], %[q9], v[68:71]\n"
    "v_exp_f32 v85, v85\n"
    "v_mfma_f32_16x16x32_bf16 v[64:67], a[212:215], %[q10], v[64:67]\n"
    "v_exp_f32 v86, v86\n"
    "v_mfma_f32_16x16x32_bf16 v[68:71], a[212:215], %[q11], v[68:71]\n"
    "v_exp_f32 v87, v87\n"
    "v_mfma_f32_16x16x32_bf16 v[64:67], a[216:219], %[q12], v[64:67]\n"
    "v_exp_f32 v92, v92\n"
    "v_mfma_f32_16x16x32_bf16 v[68:71], a[216:219], %[q13], v[68:71]\n"
    "v_exp_f32 v93, v93\n"
    "v_mfma_f32_16x16x32_bf16 v[64:67], a[220:223], %[q14], v[64:67]\n"
    "v_exp_f32 v94, v94\n"
    "v_mfma_f32_16x16x32_bf16 v[68:71], a[220:223], %[q15], v[68:71]\n"
    "v_exp_f32 v95, v95\n"
    "s_waitcnt lgkmcnt(0)\n"
    "v_mfma_f32_16x16x32_bf16 v[72:75], a[224:227], %[q0], v[96:99]\n"
    "ds_read_b128 a[240:243], v48 offset:41216\n"
    "v_cvt_pk_bf16_f32 v112, v80, v81\n"
    "v_mfma_f32_16x16x32_bf16 v[76:79], a[224:227], %[q1], v[100:103]\n"
    "ds_read_b128 a[244:247], v49 offset:41216\n"
    "v_cvt_pk_bf16_f32 v113, v82, v83\n"
    "v_mfma_f32_16x16x32_bf16 v[72:75], a[228:231], %[q2], v[72:75]\n"
    "ds_read_b128 a[248:251], v50 offset:41216\n"
    "v_cvt_pk_bf16_f32 v114, v88, v89\n"
    "v_mfma_f32_16x16x32_bf16 v[76:79], a[228:231], %[q3], v[76:79]\n"
    "ds_read_b128 a[252:255], v51 offset:41216\n"
    "v_cvt_pk_bf16_f32 v115, v90, v91\n"
    "v_mfma_f32_16x16x32_bf16 v[72:75], a[232:235], %[q4], v[72:75]\n"
    "ds_read_b64_tr_b16 a[192:193], v52 offset:16384\n"
    "v_cvt_pk_bf16_f32 v116, v84, v85\n"
    "v_mfma_f32_16x16x32_bf16 v[76:79], a[232:235], %[q5], v[76:79]\n"
    "ds_read_b64_tr_b16 a[194:195], v52 offset:24576\n"
    "v_cvt_pk_bf16_f32 v117, v86, v87\n"
    "v_mfma_f32_16x16x32_bf16 v[72:75], a[236:239], %[q6], v[72:75]\n"
    "ds_read_b64_tr_b16 a[196:197], v53 offset:16384\n"
    "v_cvt_pk_bf16_f32 v118, v92, v93\n"
    "v_mfma_f32_16x16x32_bf16 v[76:79], a[236:239], %[q7], v[76:79]\n"
    "ds_read_b64_tr_b16 a[198:199], v53 offset:24576\n"
    "v_cvt_pk_bf16_f32 v119, v94, v95\n"
    "s_waitcnt lgkmcnt(4)\n"
    "v_mfma_f32_16x16x32_bf16 v[72:75], a[240:243], %[q8], v[72:75]\n"
    "ds_read_b64_tr_b16 a[200:201], v54 offset:16384\n"
    "v_add_f32 v26, v26, v80\n"
    "v_mfma_f32_16x16x32_bf16 v[76:79], a[240:243], %[q9], v[76:79]\n"
    "ds_read_b64_tr_b16 a[202:203], v54 offset:24576\n"
    "v_add_f32 v27, v27, v81\n"
    "v_mfma_f32_16x16x32_bf16 v[72:75], a[244:247], %[q10], v[72:75]\n"
    "ds_read_b64_tr_b16 a[204:205], v55 offset:16384\n"
    "v_add_f32 v26, v26, v82\n"
    "v_mfma_f32_16x16x32_bf16 v[76:79], a[244:247], %[q11], v[76:79]\n"
    "ds_read_b64_tr_b16 a[206:207], v55 offset:24576\n"
    "v_add_f32 v27, v27, v83\n"
    "v_mfma_f32_16x16x32_bf16 v[72:75], a[248:251], %[q12], v[72:75]\n"
    "ds_read_b64_tr_b16 a[208:209], v56 offset:16384\n"
    "v_add_f32 v26, v26, v88\n"
    "v_mfma_f32_16x16x32_bf16 v[76:79], a[248:251], %[q13], v[76:79]\n"
    "ds_read_b64_tr_b16 a[210:211], v56 offset:24576\n"
    "v_add_f32 v27, v27, v89\n"
    "v_mfma_f32_16x16x32_bf16 v[72:75], a[252:255], %[q14], v[72:75]\n"
    "ds_read_b64_tr_b16 a[212:213], v57 offset:16384\n"
    "v_add_f32 v26, v26, v90\n"
    "v_mfma_f32_16x16x32_bf16 v[76:79], a[252:255], %[q15], v[76:79]\n"
    "ds_read_b64_tr_b16 a[214:215], v57 offset:24576\n"
    "v_add_f32 v27, v27, v91\n"
    "s_waitcnt lgkmcnt(8)\n"
    "v_mfma_f32_16x16x32_bf16 v[128:131], a[192:195], v[112:115], v[128:131]\n"
    "ds_read_b64_tr_b16 a[216:217], v58 offset:16384\n"
    "v_add_f32 v28, v28, v84\n"
    "v_mfma_f32_16x16x32_bf16 v[132:135], a[192:195], v[116:119], v[132:135]\n"
    "ds_read_b64_tr_b16 a[218:219], v58 offset:24576\n"
    "v_add_f32 v29, v29, v85\n"
    "v_mfma_f32_16x16x32_bf16 v[136:139], a[196:199], v[112:115], v[136:139]\n"
    "ds_read_b64_tr_b16 a[220:221], v59 offset:16384\n"
    "v_add_f32 v28, v28, v86\n"
    "v_mfma_f32_16x16x32_bf16 v[140:143], a[196:199], v[116:119], v[140:143]\n"
    "ds_read_b64_tr_b16 a[222:223], v59 offset:24576\n"
    "v_add_f32 v29, v29, v87\n"
    "s_waitcnt lgkmcnt(8)\n"
    "v_mfma_f32_16x16x32_bf16 v[144:147], a[200:203], v[112:115], v[144:147]\n"
    "ds_read_b64_tr_b16 a[224:225], v52 offset:16640\n"
    "v_add_f32 v28, v28, v92\n"
    "v_mfma_f32_16x16x32_bf16 v[148:151], a[200:203], v[116:119], v[148:151]\n"
    "ds_read_b64_tr_b16 a[226:227], v52 offset:24832\n"
    "v_add_f32 v29, v29, v93\n"
    "v_mfma_f32_16x16x32_bf16 v[152:155], a[204:207], v[112:115], v[152:155]\n"
    "ds_read_b64_tr_b16 a[228:229], v53 offset:16640\n"
    "v_add_f32 v28, v28, v94\n"
    "v_mfma_f32_16x16x32_bf16 v[156:159], a[204:207], v[116:119], v[156:159]\n"
    "ds_read_b64_tr_b16 a[230:231], v53 offset:24832\n"
    "v_add_f32 v29, v29, v95\n"
    "s_waitcnt lgkmcnt(8)\n"
    "v_mfma_f32_16x16x32_bf16 v[160:163], a[208:211], v[112:115], v[160:163]\n"
    "ds_read_b64_tr_b16 a[232:233], v54 offset:16640\n"
    "s_waitcnt vmcnt(13)\n"
    "s_barrier\n"
    "s_add_u32 s48, s48, 16384\n"
    "v_mfma_f32_16x16x32_bf16 v[164:167], a[208:211], v[116:119], v[164:167]\n"
    "ds_read_b64_tr_b16 a[234:235], v54 offset:24832\n"
    "v_max3_f32 v110, v64, v65, v66\n"
    "s_add_u32 m0, s50, 98304\n"
    "v_mfma_f32_16x16x32_bf16 v[168:171], a[212:215], v[112:115], v[168:171]\n"
    "ds_read_b64_tr_b16 a[236:237], v55 offset:16640\n"
    "buffer_load_dwordx4 %[vov0], s[44:47], s48 offen offset:0 nt lds\n"
    "v_mfma_f32_16x16x32_bf16 v[172:175], a[212:215], v[116:119], v[172:175]\n"
    "ds_read_b64_tr_b16 a[238:239], v55 offset:24832\n"
    "v_max3_f32 v110, v110, v67, v68\n"
    "s_waitcnt lgkmcnt(8)\n"
    "v_mfma_f32_16x16x32_bf16 v[176:179], a[216:219], v[112:115], v[176:179]\n"
    "ds_read_b64_tr_b16 a[240:241], v56 offset:16640\n"
    "v_max3_f32 v110, v110, v69, v70\n"
    "s_waitcnt vmcnt(9)\n"
    "v_cmp_eq_u64 s[52:53], v[46:47], v[20:21]\n"
    "s_or_b32 s52, s52, 4294901760\n"
    "s_mov_b32 s53, -1\n"
    "s_cmp_eq_u64 s[52:53], -1\n"
    "s_cbranch_scc1 .Lt4_%=\n"
    "s_mov_b32 s38, 0\n"
    ".Lt5_%=:\n"
    "s_sleep 2\n"
    "buffer_load_dwordx2 v[46:47], v22, s[80:83], s75 offen sc1\n"
    "s_waitcnt vmcnt(0)\n"
    "v_cmp_eq_u64 s[52:53], v[46:47], v[20:21]\n"
    "s_or_b32 s52, s52, 4294901760\n"
    "s_mov_b32 s53, -1\n"
    "s_cmp_eq_u64 s[52:53], -1\n"
    "s_cbranch_scc1 .Lt4_%=\n"
    "s_add_u32 s38, s38, 1\n"
    "s_cmp_lt_u32 s38, 0x200000\n"
    "s_cbranch_scc1 .Lt5_%=\n"
    ".Lt4_%=:\n"
    "v_mfma_f32_16x16x32_bf16 v[180:183], a[216:219], v[116:119], v[180:183]\n"
    "ds_read_b64_tr_b16 a[242:243], v56 offset:24832\n"
    "buffer_load_dwordx4 %[vov1], s[44:47], s48 offen offset:1024 nt lds\n"
    "v_mfma_f32_16x16x32_bf16 v[184:187], a[220:223], v[112:115], v[184:187]\n"
    "ds_read_b64_tr_b16 a[244:245], v57 offset:16640\n"
    "v_max3_f32 v110, v110, v71, v72\n"
    "v_mfma_f32_16x16x32_bf16 v[188:191], a[220:223], v[116:119], v[188:191]\n"
    "ds_read_b64_tr_b16 a[246:247], v57 offset:24832\n"
    "v_max3_f32 v110, v110, v73, v74\n"
    "s_waitcnt lgkmcnt(8)\n"
    "v_mfma_f32_16x16x32_bf16 v[192:195], a[224:227], v[112:115], v[192:195]\n"
    "ds_read_b64_tr_b16 a[248:249], v58 offset:16640\n"
    "buffer_load_dwordx4 %[vov2], s[44:47], s48 offen offset:2048 nt lds\n"
    "v_mfma_f32_16x16x32_bf16 v[196:199], a[224:227], v[116:119], v[196:199]\n"
    "ds_read_b64_tr_b16 a[250:251], v58 offset:24832\n"
    "v_max3_f32 v110, v110, v75, v76\n"
    "v_mfma_f32_16x16x32_bf16 v[200:203], a[228:231], v[112:115], v[200:203]\n"
    "ds_read_b64_tr_b16 a[252:253], v59 offset:16640\n"
    "v_max3_f32 v110, v110, v77, v78\n"
    "v_mfma_f32_16x16x32_bf16 v[204:207], a[228:231], v[116:119], v[204:207]\n"
    "ds_read_b64_tr_b16 a[254:255], v59 offset:24832\n"
    "buffer_load_dwordx4 %[vov3], s[44:47], s48 offen offset:3072 nt lds\n"
    "s_waitcnt lgkmcnt(8)\n"
    "v_mfma_f32_16x16x32_bf16 v[208:211], a[232:235], v[112:115], v[208:211]\n"
    "ds_read_b128 a[192:195], v48 offset:49152\n"
    "v_max_f32 v110, v110, v79\n"
    "v_mfma_f32_16x16x32_bf16 v[212:215], a[232:235], v[116:119], v[212:215]\n"
    "ds_read_b128 a[196:199], v49 offset:49152\n"
    "v_mfma_f32_16x16x32_bf16 v[216:219], a[236:239], v[112:115], v[216:219]\n"
    "ds_read_b128 a[200:203], v50 offset:49152\n"
    "s_add_u32 m0, s50, 0\n"
    "s_mov_b32 s49, 0\n"
    "buffer_load_dwordx4 v42, s[56:59], s49 offen offset:0 sc1 lds\n"
    "v_mfma_f32_16x16x32_bf16 v[220:223], a[236:239], v[116:119], v[220:223]\n"
    "ds_read_b128 a[204:207], v51 offset:49152\n"
    "v_cmp_lt_f32 vcc, s79, v110\n"
    "s_waitcnt lgkmcnt(8)\n"
    "v_mfma_f32_16x16x32_bf16 v[224:227], a[240:243], v[112:115], v[224:227]\n"
    "ds_read_b128 a[208:211], v48 offset:49408\n"
    "s_mov_b32 s76, 0\n"
    "buffer_load_dwordx4 a[0:3], v33, s[68:71], s76 offen offset:0 sc1\n"
    "v_mfma_f32_16x16x32_bf16 v[228:231], a[240:243], v[116:119], v[228:231]\n"
    "ds_read_b128 a[212:215], v49 offset:49408\n"
    "buffer_load_dwordx4 v43, s[56:59], s49 offen offset:1024 sc1 lds\n"
    "v_mfma_f32_16x16x32_bf16 v[232:235], a[244:247], v[112:115], v[232:235]\n"
    "ds_read_b128 a[216:219], v50 offset:49408\n"
    "v_mfma_f32_16x16x32_bf16 v[236:239], a[244:247], v[116:119], v[236:239]\n"
    "ds_read_b128 a[220:223], v51 offset:49408\n"
    "s_mov_b32 s76, 0\n"
    "buffer_load_dwordx4 a[4:7], v33, s[68:71], s76 offen offset:1024 sc1\n"
    "s_waitcnt lgkmcnt(8)\n"
    "v_mfma_f32_16x16x32_bf16 v[240:243], a[248:251], v[112:115], v[240:243]\n"
    "ds_read_b128 a[224:227], v48 offset:57344\n"
    "buffer_load_dwordx4 v44, s[56:59], s49 offen offset:2048 sc1 lds\n"
    "v_mfma_f32_16x16x32_bf16 v[244:247], a[248:251], v[116:119], v[244:247]\n"
    "ds_read_b128 a[228:231], v49 offset:57344\n"
    "v_mfma_f32_16x16x32_bf16 v[248:251], a[252:255], v[112:115], v[248:251]\n"
    "ds_read_b128 a[232:235], v50 offset:57344\n"
    "s_mov_b32 s76, 0\n"
    "buffer_load_dwordx4 a[8:11], v33, s[68:71], s76 offen offset:2048 sc1\n"
    "v_mfma_f32_16x16x32_bf16 v[252:255], a[252:255], v[116:119], v[252:255]\n"
    "ds_read_b128 a[236:239], v51 offset:57344\n"
    "buffer_load_dwordx4 v45, s[56:59], s49 offen offset:3072 sc1 lds\n"
    "s_cbranch_vccnz .LrareP1T29_%=\n"
    ".LretP1T29_%=:\n"
    "s_waitcnt lgkmcnt(8)\n"
    "v_mfma_f32_16x16x32_bf16 v[80:83], a[192:195], %[q0], v[96:99]\n"
    "v_exp_f32 v64, v64\n"
    "v_mfma_f32_16x16x32_bf16 v[84:87], a[192:195], %[q1], v[100:103]\n"
    "v_exp_f32 v65, v65\n"
    "v_mfma_f32_16x16x32_bf16 v[80:83], a[196:199], %[q2], v[80:83]\n"
    "v_exp_f32 v66, v66\n"
    "v_mfma_f32_16x16x32_bf16 v[84:87], a[196:199], %[q3], v[84:87]\n"
    "v_exp_f32 v67, v67\n"
    "v_mfma_f32_16x16x32_bf16 v[80:83], a[200:203], %[q4], v[80:83]\n"
    "v_exp_f32 v72, v72\n"
    "v_mfma_f32_16x16x32_bf16 v[84:87], a[200:203], %[q5], v[84:87]\n"
    "v_exp_f32 v73, v73\n"
    "v_mfma_f32_16x16x32_bf16 v[80:83], a[204:207], %[q6], v[80:83]\n"
    "v_exp_f32 v74, v74\n"
    "v_mfma_f32_16x16x32_bf16 v[84:87], a[204:207], %[q7], v[84:87]\n"
    "v_exp_f32 v75, v75\n"
    "s_waitcnt lgkmcnt(4)\n"
    "v_mfma_f32_16x16x32_bf16 v[80:83], a[208:211], %[q8], v[80:83]\n"
    "v_exp_f32 v68, v68\n"
    "v_mfma_f32_16x16x32_bf16 v[84:87], a[208:211], %[q9], v[84:87]\n"
    "v_exp_f32 v69, v69\n"
    "v_mfma_f32_16x16x32_bf16 v[80:83], a[212:215], %[q10], v[80:83]\n"
    "v_exp_f32 v70, v70\n"
    "v_mfma_f32_16x16x32_bf16 v[84:87], a[212:215], %[q11], v[84:87]\n"
    "v_exp_f32 v71, v71\n"
    "v_mfma_f32_16x16x32_bf16 v[80:83], a[216:219], %[q12], v[80:83]\n"
    "v_exp_f32 v76, v76\n"
    "v_mfma_f32_16x16x32_bf16 v[84:87], a[216:219], %[q13], v[84:87]\n"
    "v_exp_f32 v77, v77\n"
    "v_mfma_f32_16x16x32_bf16 v[80:83], a[220:223], %[q14], v[80:83]\n"
    "v_exp_f32 v78, v78\n"
    "v_mfma_f32_16x16x32_bf16 v[84:87], a[220:223], %[q15], v[84:87]\n"
    "v_exp_f32 v79, v79\n"
    "s_waitcnt lgkmcnt(0)\n"
    "v_mfma_f32_16x16x32_bf16 v[88:91], a[224:227], %[q0], v[96:99]\n"
    "ds_read_b128 a[240:243], v48 offset:57600\n"
    "v_cvt_pk_bf16_f32 v112, v64, v65\n"
    "v_mfma_f32_16x16x32_bf16 v[92:95], a[224:227], %[q1], v[100:103]\n"
    "ds_read_b128 a[244:247], v49 offset:57600\n"
    "v_cvt_pk_bf16_f32 v113, v66, v67\n"
    "v_mfma_f32_16x16x32_bf16 v[88:91], a[228:231], %[q2], v[88:91]\n"
    "ds_read_b128 a[248:251], v50 offset:57600\n"
    "v_cvt_pk_bf16_f32 v114, v72, v73\n"
    "v_mfma_f32_16x16x32_bf16 v[92:95], a[228:231], %[q3], v[92:95]\n"
    "ds_read_b128 a[252:255], v51 offset:57600\n"
    "v_cvt_pk_bf16_f32 v115, v74, v75\n"
    "v_mfma_f32_16x16x32_bf16 v[88:91], a[232:235], %[q4], v[88:91]\n"
    "ds_read_b64_tr_b16 a[192:193], v52 offset:32768\n"
    "v_cvt_pk_bf16_f32 v116, v68, v69\n"
    "v_mfma_f32_16x16x32_bf16 v[92:95], a[232:235], %[q5], v[92:95]\n"
    "ds_read_b64_tr_b16 a[194:195], v52 offset:40960\n"
    "v_cvt_pk_bf16_f32 v117, v70, v71\n"
    "v_mfma_f32_16x16x32_bf16 v[88:91], a[236:239], %[q6], v[88:91]\n"
    "ds_read_b64_tr_b16 a[196:197], v53 offset:32768\n"
    "v_cvt_pk_bf16_f32 v118, v76, v77\n"
    "v_mfma_f32_16x16x32_bf16 v[92:95], a[236:239], %[q7], v[92:95]\n"
    "ds_read_b64_tr_b16 a[198:199], v53 offset:40960\n"
    "v_cvt_pk_bf16_f32 v119, v78, v79\n"
    "s_waitcnt lgkmcnt(4)\n"
    "v_mfma_f32_16x16x32_bf16 v[88:91], a[240:243], %[q8], v[88:91]\n"
    "ds_read_b64_tr_b16 a[200:201], v54 offset:32768\n"
    "v_add_f32 v26, v26, v64\n"
    "v_mfma_f32_16x16x32_bf16 v[92:95], a[240:243], %[q9], v[92:95]\n"
    "ds_read_b64_tr_b16 a[202:203], v54 offset:40960\n"
    "v_add_f32 v27, v27, v65\n"
    "v_mfma_f32_16x16x32_bf16 v[88:91], a[244:247], %[q10], v[88:91]\n"
    "ds_read_b64_tr_b16 a[204:205], v55 offset:32768\n"
    "v_add_f32 v26, v26, v66\n"
    "v_mfma_f32_16x16x32_bf16 v[92:95], a[244:247], %[q11], v[92:95]\n"
    "ds_read_b64_tr_b16 a[206:207], v55 offset:40960\n"
    "v_add_f32 v27, v27, v67\n"
    "v_mfma_f32_16x16x32_bf16 v[88:91], a[248:251], %[q12], v[88:91]\n"
    "ds_read_b64_tr_b16 a[208:209], v56 offset:32768\n"
    "v_add_f32 v26, v26, v72\n"
    "v_mfma_f32_16x16x32_bf16 v[92:95], a[248:251], %[q13], v[92:95]\n"
    "ds_read_b64_tr_b16 a[210:211], v56 offset:40960\n"
    "v_add_f32 v27, v27, v73\n"
    "v_mfma_f32_16x16x32_bf16 v[88:91], a[252:255], %[q14], v[88:91]\n"
    "ds_read_b64_tr_b16 a[212:213], v57 offset:32768\n"
    "v_add_f32 v26, v26, v74\n"
    "v_mfma_f32_16x16x32_bf16 v[92:95], a[252:255], %[q15], v[92:95]\n"
    "ds_read_b64_tr_b16 a[214:215], v57 offset:40960\n"
    "v_add_f32 v27, v27, v75\n"
    "s_waitcnt lgkmcnt(8)\n"
    "v_mfma_f32_16x16x32_bf16 v[128:131], a[192:195], v[112:115], v[128:131]\n"
    "ds_read_b64_tr_b16 a[216:217], v58 offset:32768\n"
    "v_add_f32 v28, v28, v68\n"
    "v_mfma_f32_16x16x32_bf16 v[132:135], a[192:195], v[116:119], v[132:135]\n"
    "ds_read_b64_tr_b16 a[218:219], v58 offset:40960\n"
    "v_add_f32 v29, v29, v69\n"
    "v_mfma_f32_16x16x32_bf16 v[136:139], a[196:199], v[112:115], v[136:139]\n"
    "ds_read_b64_tr_b16 a[220:221], v59 offset:32768\n"
    "v_add_f32 v28, v28, v70\n"
    "v_mfma_f32_16x16x32_bf16 v[140:143], a[196:199], v[116:119], v[140:143]\n"
    "ds_read_b64_tr_b16 a[222:223], v59 offset:40960\n"
    "v_add_f32 v29, v29, v71\n"
    "s_waitcnt lgkmcnt(8)\n"
    "v_mfma_f32_16x16x32_bf16 v[144:147], a[200:203], v[112:115], v[144:147]\n"
    "ds_read_b64_tr_b16 a[224:225], v52 offset:33024\n"
    "v_add_f32 v28, v28, v76\n"
    "v_mfma_f32_16x16x32_bf16 v[148:151], a[200:203], v[116:119], v[148:151]\n"
    "ds_read_b64_tr_b16 a[226:227], v52 offset:41216\n"
    "v_add_f32 v29, v29, v77\n"
    "v_mfma_f32_16x16x32_bf16 v[152:155], a[204:207], v[112:115], v[152:155]\n"
    "ds_read_b64_tr_b16 a[228:229], v53 offset:33024\n"
    "v_add_f32 v28, v28, v78\n"
    "v_mfma_f32_16x16x32_bf16 v[156:159], a[204:207], v[116:119], v[156:159]\n"
    "ds_read_b64_tr_b16 a[230:231], v53 offset:41216\n"
    "v_add_f32 v29, v29, v79\n"
    "s_waitcnt lgkmcnt(8)\n"
    "v_mfma_f32_16x16x32_bf16 v[160:163], a[208:211], v[112:115], v[160:163]\n"
    "ds_read_b64_tr_b16 a[232:233], v54 offset:33024\n"
    "s_waitcnt vmcnt(15)\n"
    "s_barrier\n"
    "s_add_u32 s48, s48, 16384\n"
    "v_mfma_f32_16x16x32_bf16 v[164:167], a[208:211], v[116:119], v[164:167]\n"
    "ds_read_b64_tr_b16 a[234:235], v54 offset:41216\n"
    "v_max3_f32 v110, v80, v81, v82\n"
    "s_add_u32 m0, s50, 114688\n"
    "v_mfma_f32_16x16x32_bf16 v[168:171], a[212:215], v[112:115], v[168:171]\n"
    "ds_read_b64_tr_b16 a[236:237], v55 offset:33024\n"
    "buffer_load_dwordx4 %[vov0], s[44:47], s48 offen offset:0 nt lds\n"
    "v_mfma_f32_16x16x32_bf16 v[172:175], a[212:215], v[116:119], v[172:175]\n"
    "ds_read_b64_tr_b16 a[238:239], v55 offset:41216\n"
    "v_max3_f32 v110, v110, v83, v84\n"
    "s_waitcnt lgkmcnt(8)\n"
    "v_mfma_f32_16x16x32_bf16 v[176:179], a[216:219], v[112:115], v[176:179]\n"
    "ds_read_b64_tr_b16 a[240:241], v56 offset:33024\n"
    "v_max3_f32 v110, v110, v85, v86\n"
    "v_mfma_f32_16x16x32_bf16 v[180:183], a[216:219], v[116:119], v[180:183]\n"
    "ds_read_b64_tr_b16 a[242:243], v56 offset:41216\n"
    "buffer_load_dwordx4 %[vov1], s[44:47], s48 offen offset:1024 nt lds\n"
    "v_mfma_f32_16x16x32_bf16 v[184:187], a[220:223], v[112:115], v[184:187]\n"
    "ds_read_b64_tr_b16 a[244:245], v57 offset:33024\n"
    "v_max3_f32 v110, v110, v87, v88\n"
    "v_mfma_f32_16x16x32_bf16 v[188:191], a[220:223], v[116:119], v[188:191]\n"
    "ds_read_b64_tr_b16 a[246:247], v57 offset:41216\n"
    "v_max3_f32 v110, v110, v89, v90\n"
    "s_waitcnt lgkmcnt(8)\n"
    "v_mfma_f32_16x16x32_bf16 v[192:195], a[224:227], v[112:115], v[192:195]\n"
    "ds_read_b64_tr_b16 a[248:249], v58 offset:33024\n"
    "buffer_load_dwordx4 %[vov2], s[44:47], s48 offen offset:2048 nt lds\n"
    "v_mfma_f32_16x16x32_bf16 v[196:199], a[224:227], v[116:119], v[196:199]\n"
    "ds_read_b64_tr_b16 a[250:251], v58 offset:41216\n"
    "v_max3_f32 v110, v110, v91, v92\n"
    "v_mfma_f32_16x16x32_bf16 v[200:203], a[228:231], v[112:115], v[200:203]\n"
    "ds_read_b64_tr_b16 a[252:253], v59 offset:33024\n"
    "v_max3_f32 v110, v110, v93, v94\n"
    "v_mfma_f32_16x16x32_bf16 v[204:207], a[228:231], v[116:119], v[204:207]\n"
    "ds_read_b64_tr_b16 a[254:255], v59 offset:41216\n"
    "buffer_load_dwordx4 %[vov3], s[44:47], s48 offen offset:3072 nt lds\n"
    "s_waitcnt lgkmcnt(8)\n"
    "v_mfma_f32_16x16x32_bf16 v[208:211], a[232:235], v[112:115], v[208:211]\n"
    "ds_read_b128 a[192:195], v48 offset:0\n"
    "v_max_f32 v110, v110, v95\n"
    "v_mfma_f32_16x16x32_bf16 v[212:215], a[232:235], v[116:119], v[212:215]\n"
    "ds_read_b128 a[196:199], v49 offset:0\n"
    "v_mfma_f32_16x16x32_bf16 v[216:219], a[236:239], v[112:115], v[216:219]\n"
    "ds_read_b128 a[200:203], v50 offset:0\n"
    "s_add_u32 m0, s50, 16384\n"
    "s_mov_b32 s49, 14336\n"
    "buffer_load_dwordx4 v42, s[56:59], s49 offen offset:0 sc1 lds\n"
    "v_mfma_f32_16x16x32_bf16 v[220:223], a[236:239], v[116:119], v[220:223]\n"
    "ds_read_b128 a[204:207], v51 offset:0\n"
    "v_cmp_lt_f32 vcc, s79, v110\n"
    "s_waitcnt lgkmcnt(8)\n"
    "v_mfma_f32_16x16x32_bf16 v[224:227], a[240:243], v[112:115], v[224:227]\n"
    "ds_read_b128 a[208:211], v48 offset:256\n"
    "s_mov_b32 s76, 0\n"
    "buffer_load_dwordx4 a[12:15], v33, s[68:71], s76 offen offset:3072 sc1\n"
    "v_mfma_f32_16x16x32_bf16 v[228:231], a[240:243], v[116:119], v[228:231]\n"
    "ds_read_b128 a[212:215], v49 offset:256\n"
    "buffer_load_dwordx4 v43, s[56:59], s49 offen offset:1024 sc1 lds\n"
    "v_mfma_f32_16x16x32_bf16 v[232:235], a[244:247], v[112:115], v[232:235]\n"
    "ds_read_b128 a[216:219], v50 offset:256\n"
    "v_mfma_f32_16x16x32_bf16 v[236:239], a[244:247], v[116:119], v[236:239]\n"
    "ds_read_b128 a[220:223], v51 offset:256\n"
    "s_mov_b32 s76, 4096\n"
    "buffer_load_dwordx4 a[16:19], v33, s[68:71], s76 offen offset:0 sc1\n"
    "s_waitcnt lgkmcnt(8)\n"
    "v_mfma_f32_16x16x32_bf16 v[240:243], a[248:251], v[112:115], v[240:243]\n"
    "ds_read_b128 a[224:227], v48 offset:8192\n"
    "buffer_load_dwordx4 v44, s[56:59], s49 offen offset:2048 sc1 lds\n"
    "v_mfma_f32_16x16x32_bf16 v[244:247], a[248:251], v[116:119], v[244:247]\n"
    "ds_read_b128 a[228:231], v49 offset:8192\n"
    "v_mfma_f32_16x16x32_bf16 v[248:251], a[252:255], v[112:115], v[248:251]\n"
    "ds_read_b128 a[232:235], v50 offset:8192\n"
    "s_mov_b32 s76, 4096\n"
    "buffer_load_dwordx4 a[20:23], v33, s[68:71], s76 offen offset:1024 sc1\n"
    "v_mfma_f32_16x16x32_bf16 v[252:255], a[252:255], v[116:119], v[252:255]\n"
    "ds_read_b128 a[236:239], v51 offset:8192\n"
    "buffer_load_dwordx4 v45, s[56:59], s49 offen offset:3072 sc1 lds\n"
    "s_cbranch_vccnz .LrareP1T30_%=\n"
    ".LretP1T30_%=:\n"
    "s_waitcnt lgkmcnt(8)\n"
    "v_mfma_f32_16x16x32_bf16 v[64:67], a[192:195], %[q0], v[96:99]\n"
    "v_exp_f32 v80, v80\n"
    "v_mfma_f32_16x16x32_bf16 v[68:71], a[192:195], %[q1], v[100:103]\n"
    "v_exp_f32 v81, v81\n"
    "v_mfma_f32_16x16x32_bf16 v[64:67], a[196:199], %[q2], v[64:67]\n"
    "v_exp_f32 v82, v82\n"
    "v_mfma_f32_16x16x32_bf16 v[68:71], a[196:199], %[q3], v[68:71]\n"
    "v_exp_f32 v83, v83\n"
    "v_mfma_f32_16x16x32_bf16 v[64:67], a[200:203], %[q4], v[64:67]\n"
    "v_exp_f32 v88, v88\n"
    "v_mfma_f32_16x16x32_bf16 v[68:71], a[200:203], %[q5], v[68:71]\n"
    "v_exp_f32 v89, v89\n"
    "v_mfma_f32_16x16x32_bf16 v[64:67], a[204:207], %[q6], v[64:67]\n"
    "v_exp_f32 v90, v90\n"
    "v_mfma_f32_16x16x32_bf16 v[68:71], a[204:207], %[q7], v[68:71]\n"
    "v_exp_f32 v91, v91\n"
    "s_waitcnt lgkmcnt(4)\n"
    "v_mfma_f32_16x16x32_bf16 v[64:67], a[208:211], %[q8], v[64:67]\n"
    "v_exp_f32 v84, v84\n"
    "v_mfma_f32_16x16x32_bf16 v[68:71], a[208:211], %[q9], v[68:71]\n"
    "v_exp_f32 v85, v85\n"
    "v_mfma_f32_16x16x32_bf16 v[64:67], a[212:215], %[q10], v[64:67]\n"
    "v_exp_f32 v86, v86\n"
    "v_mfma_f32_16x16x32_bf16 v[68:71], a[212:215], %[q11], v[68:71]\n"
    "v_exp_f32 v87, v87\n"
    "v_mfma_f32_16x16x32_bf16 v[64:67], a[216:219], %[q12], v[64:67]\n"
    "v_exp_f32 v92, v92\n"
    "v_mfma_f32_16x16x32_bf16 v[68:71], a[216:219], %[q13], v[68:71]\n"
    "v_exp_f32 v93, v93\n"
    "v_mfma_f32_16x16x32_bf16 v[64:67], a[220:223], %[q14], v[64:67]\n"
    "v_exp_f32 v94, v94\n"
    "v_mfma_f32_16x16x32_bf16 v[68:71], a[220:223], %[q15], v[68:71]\n"
    "v_exp_f32 v95, v95\n"
    "s_waitcnt lgkmcnt(0)\n"
    "v_mfma_f32_16x16x32_bf16 v[72:75], a[224:227], %[q0], v[96:99]\n"
    "ds_read_b128 a[240:243], v48 offset:8448\n"
    "v_cvt_pk_bf16_f32 v112, v80, v81\n"
    "v_mfma_f32_16x16x32_bf16 v[76:79], a[224:227], %[q1], v[100:103]\n"
    "ds_read_b128 a[244:247], v49 offset:8448\n"
    "v_cvt_pk_bf16_f32 v113, v82, v83\n"
    "v_mfma_f32_16x16x32_bf16 v[72:75], a[228:231], %[q2], v[72:75]\n"
    "ds_read_b128 a[248:251], v50 offset:8448\n"
    "v_cvt_pk_bf16_f32 v114, v88, v89\n"
    "v_mfma_f32_16x16x32_bf16 v[76:79], a[228:231], %[q3], v[76:79]\n"
    "ds_read_b128 a[252:255], v51 offset:8448\n"
    "v_cvt_pk_bf16_f32 v115, v90, v91\n"
    "v_mfma_f32_16x16x32_bf16 v[72:75], a[232:235], %[q4], v[72:75]\n"
    "ds_read_b64_tr_b16 a[192:193], v52 offset:49152\n"
    "v_cvt_pk_bf16_f32 v116, v84, v85\n"
    "v_mfma_f32_16x16x32_bf16 v[76:79], a[232:235], %[q5], v[76:79]\n"
    "ds_read_b64_tr_b16 a[194:195], v52 offset:57344\n"
    "v_cvt_pk_bf16_f32 v117, v86, v87\n"
    "v_mfma_f32_16x16x32_bf16 v[72:75], a[236:239], %[q6], v[72:75]\n"
    "ds_read_b64_tr_b16 a[196:197], v53 offset:49152\n"
    "v_cvt_pk_bf16_f32 v118, v92, v93\n"
    "v_mfma_f32_16x16x32_bf16 v[76:79], a[236:239], %[q7], v[76:79]\n"
    "ds_read_b64_tr_b16 a[198:199], v53 offset:57344\n"
    "v_cvt_pk_bf16_f32 v119, v94, v95\n"
    "s_waitcnt lgkmcnt(4)\n"
    "v_mfma_f32_16x16x32_bf16 v[72:75], a[240:243], %[q8], v[72:75]\n"
    "ds_read_b64_tr_b16 a[200:201], v54 offset:49152\n"
    "v_add_f32 v26, v26, v80\n"
    "v_mfma_f32_16x16x32_bf16 v[76:79], a[240:243], %[q9], v[76:79]\n"
    "ds_read_b64_tr_b16 a[202:203], v54 offset:57344\n"
    "v_add_f32 v27, v27, v81\n"
    "v_mfma_f32_16x16x32_bf16 v[72:75], a[244:247], %[q10], v[72:75]\n"
    "ds_read_b64_tr_b16 a[204:205], v55 offset:49152\n"
    "v_add_f32 v26, v26, v82\n"
    "v_mfma_f32_16x16x32_bf16 v[76:79], a[244:247], %[q11], v[76:79]\n"
    "ds_read_b64_tr_b16 a[206:207], v55 offset:57344\n"
    "v_add_f32 v27, v27, v83\n"
    "v_mfma_f32_16x16x32_bf16 v[72:75], a[248:251], %[q12], v[72:75]\n"
    "ds_read_b64_tr_b16 a[208:209], v56 offset:49152\n"
    "v_add_f32 v26, v26, v88\n"
    "v_mfma_f32_16x16x32_bf16 v[76:79], a[248:251], %[q13], v[76:79]\n"
    "ds_read_b64_tr_b16 a[210:211], v56 offset:57344\n"
    "v_add_f32 v27, v27, v89\n"
    "v_mfma_f32_16x16x32_bf16 v[72:75], a[252:255], %[q14], v[72:75]\n"
    "ds_read_b64_tr_b16 a[212:213], v57 offset:49152\n"
    "v_add_f32 v26, v26, v90\n"
    "v_mfma_f32_16x16x32_bf16 v[76:79], a[252:255], %[q15], v[76:79]\n"
    "ds_read_b64_tr_b16 a[214:215], v57 offset:57344\n"
    "v_add_f32 v27, v27, v91\n"
    "s_waitcnt lgkmcnt(8)\n"
    "v_mfma_f32_16x16x32_bf16 v[128:131], a[192:195], v[112:115], v[128:131]\n"
    "ds_read_b64_tr_b16 a[216:217], v58 offset:49152\n"
    "v_add_f32 v28, v28, v84\n"
    "v_mfma_f32_16x16x32_bf16 v[132:135], a[192:195], v[116:119], v[132:135]\n"
    "ds_read_b64_tr_b16 a[218:219], v58 offset:57344\n"
    "v_add_f32 v29, v29, v85\n"
    "v_mfma_f32_16x16x32_bf16 v[136:139], a[196:199], v[112:115], v[136:139]\n"
    "ds_read_b64_tr_b16 a[220:221], v59 offset:49152\n"
    "v_add_f32 v28, v28, v86\n"
    "v_mfma_f32_16x16x32_bf16 v[140:143], a[196:199], v[116:119], v[140:143]\n"
    "ds_read_b64_tr_b16 a[222:223], v59 offset:57344\n"
    "v_add_f32 v29, v29, v87\n"
    "s_waitcnt lgkmcnt(8)\n"
    "v_mfma_f32_16x16x32_bf16 v[144:147], a[200:203], v[112:115], v[144:147]\n"
    "ds_read_b64_tr_b16 a[224:225], v52 offset:49408\n"
    "v_add_f32 v28, v28, v92\n"
    "v_mfma_f32_16x16x32_bf16 v[148:151], a[200:203], v[116:119], v[148:151]\n"
    "ds_read_b64_tr_b16 a[226:227], v52 offset:57600\n"
    "v_add_f32 v29, v29, v93\n"
    "v_mfma_f32_16x16x32_bf16 v[152:155], a[204:207], v[112:115], v[152:155]\n"
    "ds_read_b64_tr_b16 a[228:229], v53 offset:49408\n"
    "v_add_f32 v28, v28, v94\n"
    "v_mfma_f32_16x16x32_bf16 v[156:159], a[204:207], v[116:119], v[156:159]\n"
    "ds_read_b64_tr_b16 a[230:231], v53 offset:57600\n"
    "v_add_f32 v29, v29, v95\n"
    "s_waitcnt lgkmcnt(8)\n"
    "v_mfma_f32_16x16x32_bf16 v[160:163], a[208:211], v[112:115], v[160:163]\n"
    "ds_read_b64_tr_b16 a[232:233], v54 offset:49408\n"
    "s_waitcnt vmcnt(18)\n"
    "s_barrier\n"
    "s_add_u32 s48, s48, 16384\n"
    "v_mfma_f32_16x16x32_bf16 v[164:167], a[208:211], v[116:119], v[164:167]\n"
    "ds_read_b64_tr_b16 a[234:235], v54 offset:57600\n"
    "v_max3_f32 v110, v64, v65, v66\n"
    "v_mfma_f32_16x16x32_bf16 v[168:171], a[212:215], v[112:115], v[168:171]\n"
    "ds_read_b64_tr_b16 a[236:237], v55 offset:49408\n"
    "s_add_u32 m0, s50, 65536\n"
    "s_mov_b32 s49, 28672\n"
    "buffer_load_dwordx4 v42, s[56:59], s49 offen offset:0 sc1 lds\n"
    "v_mfma_f32_16x16x32_bf16 v[172:175], a[212:215], v[116:119], v[172:175]\n"
    "ds_read_b64_tr_b16 a[238:239], v55 offset:57600\n"
    "v_max3_f32 v110, v110, v67, v68\n"
    "s_waitcnt lgkmcnt(8)\n"
    "v_mfma_f32_16x16x32_bf16 v[176:179], a[216:219], v[112:115], v[176:179]\n"
    "ds_read_b64_tr_b16 a[240:241], v56 offset:49408\n"
    "v_max3_f32 v110, v110, v69, v70\n"
    "v_mfma_f32_16x16x32_bf16 v[180:183], a[216:219], v[116:119], v[180:183]\n"
    "ds_read_b64_tr_b16 a[242:243], v56 offset:57600\n"
    "buffer_load_dwordx4 v43, s[56:59], s49 offen offset:1024 sc1 lds\n"
    "v_mfma_f32_16x16x32_bf16 v[184:187], a[220:223], v[112:115], v[184:187]\n"
    "ds_read_b64_tr_b16 a[244:245], v57 offset:49408\n"
    "v_max3_f32 v110, v110, v71, v72\n"
    "v_mfma_f32_16x16x32_bf16 v[188:191], a[220:223], v[116:119], v[188:191]\n"
    "ds_read_b64_tr_b16 a[246:247], v57 offset:57600\n"
    "v_max3_f32 v110, v110, v73, v74\n"
    "s_waitcnt lgkmcnt(8)\n"
    "v_mfma_f32_16x16x32_bf16 v[192:195], a[224:227], v[112:115], v[192:195]\n"
    "ds_read_b64_tr_b16 a[248:249], v58 offset:49408\n"
    "buffer_load_dwordx4 v44, s[56:59], s49 offen offset:2048 sc1 lds\n"
    "v_mfma_f32_16x16x32_bf16 v[196:199], a[224:227], v[116:119], v[196:199]\n"
    "ds_read_b64_tr_b16 a[250:251], v58 offset:57600\n"
    "v_max3_f32 v110, v110, v75, v76\n"
    "v_mfma_f32_16x16x32_bf16 v[200:203], a[228:231], v[112:115], v[200:203]\n"
    "ds_read_b64_tr_b16 a[252:253], v59 offset:49408\n"
    "v_max3_f32 v110, v110, v77, v78\n"
    "v_mfma_f32_16x16x32_bf16 v[204:207], a[228:231], v[116:119], v[204:207]\n"
    "ds_read_b64_tr_b16 a[254:255], v59 offset:57600\n"
    "buffer_load_dwordx4 v45, s[56:59], s49 offen offset:3072 sc1 lds\n"
    "s_waitcnt lgkmcnt(8)\n"
    "v_mfma_f32_16x16x32_bf16 v[208:211], a[232:235], v[112:115], v[208:211]\n"
    "ds_read_b128 a[192:195], v48 offset:16384\n"
    "v_max_f32 v110, v110, v79\n"
    "v_mfma_f32_16x16x32_bf16 v[212:215], a[232:235], v[116:119], v[212:215]\n"
    "ds_read_b128 a[196:199], v49 offset:16384\n"
    "v_mfma_f32_16x16x32_bf16 v[216:219], a[236:239], v[112:115], v[216:219]\n"
    "ds_read_b128 a[200:203], v50 offset:16384\n"
    "s_add_u32 m0, s50, 81920\n"
    "s_mov_b32 s49, 43008\n"
    "buffer_load_dwordx4 v42, s[56:59], s49 offen offset:0 sc1 lds\n"
    "v_mfma_f32_16x16x32_bf16 v[220:223], a[236:239], v[116:119], v[220:223]\n"
    "ds_read_b128 a[204:207], v51 offset:16384\n"
    "v_cmp_lt_f32 vcc, s79, v110\n"
    "s_waitcnt lgkmcnt(8)\n"
    "v_mfma_f32_16x16x32_bf16 v[224:227], a[240:243], v[112:115], v[224:227]\n"
    "ds_read_b128 a[208:211], v48 offset:16640\n"
    "s_mov_b32 s76, 4096\n"
    "buffer_load_dwordx4 a[24:27], v33, s[68:71], s76 offen offset:2048 sc1\n"
    "v_mfma_f32_16x16x32_bf16 v[228:231], a[240:243], v[116:119], v[228:231]\n"
    "ds_read_b128 a[212:215], v49 offset:16640\n"
    "buffer_load_dwordx4 v43, s[56:59], s49 offen offset:1024 sc1 lds\n"
    "v_mfma_f32_16x16x32_bf16 v[232:235], a[244:247], v[112:115], v[232:235]\n"
    "ds_read_b128 a[216:219], v50 offset:16640\n"
    "v_mfma_f32_16x16x32_bf16 v[236:239], a[244:247], v[116:119], v[236:239]\n"
    "ds_read_b128 a[220:223], v51 offset:16640\n"
    "s_mov_b32 s76, 4096\n"
    "buffer_load_dwordx4 a[28:31], v33, s[68:71], s76 offen offset:3072 sc1\n"
    "s_waitcnt lgkmcnt(8)\n"
    "v_mfma_f32_16x16x32_bf16 v[240:243], a[248:251], v[112:115], v[240:243]\n"
    "ds_read_b128 a[224:227], v48 offset:24576\n"
    "buffer_load_dwordx4 v44, s[56:59], s49 offen offset:2048 sc1 lds\n"
    "v_mfma_f32_16x16x32_bf16 v[244:247], a[248:251], v[116:119], v[244:247]\n"
    "ds_read_b128 a[228:231], v49 offset:24576\n"
    "v_mfma_f32_16x16x32_bf16 v[248:251], a[252:255], v[112:115], v[248:251]\n"
    "ds_read_b128 a[232:235], v50 offset:24576\n"
    "s_mov_b32 s76, 8192\n"
    "buffer_load_dwordx4 a[32:35], v33, s[68:71], s76 offen offset:0 sc1\n"
    "v_mfma_f32_16x16x32_bf16 v[252:255], a[252:255], v[116:119], v[252:255]\n"
    "ds_read_b128 a[236:239], v51 offset:24576\n"
    "buffer_load_dwordx4 v45, s[56:59], s49 offen offset:3072 sc1 lds\n"
    "s_cbranch_vccnz .LrareP1T31_%=\n"
    ".LretP1T31_%=:\n"
    "s_waitcnt lgkmcnt(8)\n"
    "v_mfma_f32_16x16x32_bf16 v[80:83], a[192:195], %[q0], v[96:99]\n"
    "v_exp_f32 v64, v64\n"
    "v_mfma_f32_16x16x32_bf16 v[84:87], a[192:195], %[q1], v[100:103]\n"
    "v_exp_f32 v65, v65\n"
    "v_mfma_f32_16x16x32_bf16 v[80:83], a[196:199], %[q2], v[80:83]\n"
    "v_exp_f32 v66, v66\n"
    "v_mfma_f32_16x16x32_bf16 v[84:87], a[196:199], %[q3], v[84:87]\n"
    "v_exp_f32 v67, v67\n"
    "v_mfma_f32_16x16x32_bf16 v[80:83], a[200:203], %[q4], v[80:83]\n"
    "v_exp_f32 v72, v72\n"
    "v_mfma_f32_16x16x32_bf16 v[84:87], a[200:203], %[q5], v[84:87]\n"
    "v_exp_f32 v73, v73\n"
    "v_mfma_f32_16x16x32_bf16 v[80:83], a[204:207], %[q6], v[80:83]\n"
    "v_exp_f32 v74, v74\n"
    "v_mfma_f32_16x16x32_bf16 v[84:87], a[204:207], %[q7], v[84:87]\n"
    "v_exp_f32 v75, v75\n"
    "s_waitcnt lgkmcnt(4)\n"
    "v_mfma_f32_16x16x32_bf16 v[80:83], a[208:211], %[q8], v[80:83]\n"
    "v_exp_f32 v68, v68\n"
    "v_mfma_f32_16x16x32_bf16 v[84:87], a[208:211], %[q9], v[84:87]\n"
    "v_exp_f32 v69, v69\n"
    "v_mfma_f32_16x16x32_bf16 v[80:83], a[212:215], %[q10], v[80:83]\n"
    "v_exp_f32 v70, v70\n"
    "v_mfma_f32_16x16x32_bf16 v[84:87], a[212:215], %[q11], v[84:87]\n"
    "v_exp_f32 v71, v71\n"
    "v_mfma_f32_16x16x32_bf16 v[80:83], a[216:219], %[q12], v[80:83]\n"
    "v_exp_f32 v76, v76\n"
    "v_mfma_f32_16x16x32_bf16 v[84:87], a[216:219], %[q13], v[84:87]\n"
    "v_exp_f32 v77, v77\n"
    "v_mfma_f32_16x16x32_bf16 v[80:83], a[220:223], %[q14], v[80:83]\n"
    "v_exp_f32 v78, v78\n"
    "v_mfma_f32_16x16x32_bf16 v[84:87], a[220:223], %[q15], v[84:87]\n"
    "v_exp_f32 v79, v79\n"
    "s_waitcnt lgkmcnt(0)\n"
    "v_mfma_f32_16x16x32_bf16 v[88:91], a[224:227], %[q0], v[96:99]\n"
    "ds_read_b128 a[240:243], v48 offset:24832\n"
    "v_cvt_pk_bf16_f32 v112, v64, v65\n"
    "v_mfma_f32_16x16x32_bf16 v[92:95], a[224:227], %[q1], v[100:103]\n"
    "ds_read_b128 a[244:247], v49 offset:24832\n"
    "v_cvt_pk_bf16_f32 v113, v66, v67\n"
    "v_mfma_f32_16x16x32_bf16 v[88:91], a[228:231], %[q2], v[88:91]\n"
    "ds_read_b128 a[248:251], v50 offset:24832\n"
    "v_cvt_pk_bf16_f32 v114, v72, v73\n"
    "v_mfma_f32_16x16x32_bf16 v[92:95], a[228:231], %[q3], v[92:95]\n"
    "ds_read_b128 a[252:255], v51 offset:24832\n"
    "v_cvt_pk_bf16_f32 v115, v74, v75\n"
    "v_mfma_f32_16x16x32_bf16 v[88:91], a[232:235], %[q4], v[88:91]\n"
    "ds_read_b64_tr_b16 a[192:193], v52 offset:0\n"
    "v_cvt_pk_bf16_f32 v116, v68, v69\n"
    "v_mfma_f32_16x16x32_bf16 v[92:95], a[232:235], %[q5], v[92:95]\n"
    "ds_read_b64_tr_b16 a[194:195], v52 offset:8192\n"
    "v_cvt_pk_bf16_f32 v117, v70, v71\n"
    "v_mfma_f32_16x16x32_bf16 v[88:91], a[236:239], %[q6], v[88:91]\n"
    "ds_read_b64_tr_b16 a[196:197], v53 offset:0\n"
    "v_cvt_pk_bf16_f32 v118, v76, v77\n"
    "v_mfma_f32_16x16x32_bf16 v[92:95], a[236:239], %[q7], v[92:95]\n"
    "ds_read_b64_tr_b16 a[198:199], v53 offset:8192\n"
    "v_cvt_pk_bf16_f32 v119, v78, v79\n"
    "s_waitcnt lgkmcnt(4)\n"
    "v_mfma_f32_16x16x32_bf16 v[88:91], a[240:243], %[q8], v[88:91]\n"
    "ds_read_b64_tr_b16 a[200:201], v54 offset:0\n"
    "v_add_f32 v26, v26, v64\n"
    "v_mfma_f32_16x16x32_bf16 v[92:95], a[240:243], %[q9], v[92:95]\n"
    "ds_read_b64_tr_b16 a[202:203], v54 offset:8192\n"
    "v_add_f32 v27, v27, v65\n"
    "v_mfma_f32_16x16x32_bf16 v[88:91], a[244:247], %[q10], v[88:91]\n"
    "ds_read_b64_tr_b16 a[204:205], v55 offset:0\n"
    "v_add_f32 v26, v26, v66\n"
    "v_mfma_f32_16x16x32_bf16 v[92:95], a[244:247], %[q11], v[92:95]\n"
    "ds_read_b64_tr_b16 a[206:207], v55 offset:8192\n"
    "v_add_f32 v27, v27, v67\n"
    "v_mfma_f32_16x16x32_bf16 v[88:91], a[248:251], %[q12], v[88:91]\n"
    "ds_read_b64_tr_b16 a[208:209], v56 offset:0\n"
    "v_add_f32 v26, v26, v72\n"
    "v_mfma_f32_16x16x32_bf16 v[92:95], a[248:251], %[q13], v[92:95]\n"
    "ds_read_b64_tr_b16 a[210:211], v56 offset:8192\n"
    "v_add_f32 v27, v27, v73\n"
    "v_mfma_f32_16x16x32_bf16 v[88:91], a[252:255], %[q14], v[88:91]\n"
    "ds_read_b64_tr_b16 a[212:213], v57 offset:0\n"
    "v_add_f32 v26, v26, v74\n"
    "v_mfma_f32_16x16x32_bf16 v[92:95], a[252:255], %[q15], v[92:95]\n"
    "ds_read_b64_tr_b16 a[214:215], v57 offset:8192\n"
    "v_add_f32 v27, v27, v75\n"
    "s_waitcnt lgkmcnt(8)\n"
    "v_mfma_f32_16x16x32_bf16 v[128:131], a[192:195], v[112:115], v[128:131]\n"
    "ds_read_b64_tr_b16 a[216:217], v58 offset:0\n"
    "v_add_f32 v28, v28, v68\n"
    "v_mfma_f32_16x16x32_bf16 v[132:135], a[192:195], v[116:119], v[132:135]\n"
    "ds_read_b64_tr_b16 a[218:219], v58 offset:8192\n"
    "v_add_f32 v29, v29, v69\n"
    "v_mfma_f32_16x16x32_bf16 v[136:139], a[196:199], v[112:115], v[136:139]\n"
    "ds_read_b64_tr_b16 a[220:221], v59 offset:0\n"
    "v_add_f32 v28, v28, v70\n"
    "v_mfma_f32_16x16x32_bf16 v[140:143], a[196:199], v[116:119], v[140:143]\n"
    "ds_read_b64_tr_b16 a[222:223], v59 offset:8192\n"
    "v_add_f32 v29, v29, v71\n"
    "s_waitcnt lgkmcnt(8)\n"
    "v_mfma_f32_16x16x32_bf16 v[144:147], a[200:203], v[112:115], v[144:147]\n"
    "ds_read_b64_tr_b16 a[224:225], v52 offset:256\n"
    "v_add_f32 v28, v28, v76\n"
    "v_mfma_f32_16x16x32_bf16 v[148:151], a[200:203], v[116:119], v[148:151]\n"
    "ds_read_b64_tr_b16 a[226:227], v52 offset:8448\n"
    "v_add_f32 v29, v29, v77\n"
    "v_mfma_f32_16x16x32_bf16 v[152:155], a[204:207], v[112:115], v[152:155]\n"
    "ds_read_b64_tr_b16 a[228:229], v53 offset:256\n"
    "v_add_f32 v28, v28, v78\n"
    "v_mfma_f32_16x16x32_bf16 v[156:159], a[204:207], v[116:119], v[156:159]\n"
    "ds_read_b64_tr_b16 a[230:231], v53 offset:8448\n"
    "v_add_f32 v29, v29, v79\n"
    "s_waitcnt lgkmcnt(8)\n"
    "v_mfma_f32_16x16x32_bf16 v[160:163], a[208:211], v[112:115], v[160:163]\n"
    "ds_read_b64_tr_b16 a[232:233], v54 offset:256\n"
    "s_waitcnt vmcnt(18)\n"
    "s_barrier\n"
    "s_add_u32 s48, s48, 16384\n"
    "v_mfma_f32_16x16x32_bf16 v[164:167], a[208:211], v[116:119], v[164:167]\n"
    "ds_read_b64_tr_b16 a[234:235], v54 offset:8448\n"
    "v_max3_f32 v110, v80, v81, v82\n"
    "v_mfma_f32_16x16x32_bf16 v[168:171], a[212:215], v[112:115], v[168:171]\n"
    "ds_read_b64_tr_b16 a[236:237], v55 offset:256\n"
    "s_add_u32 m0, s50, 32768\n"
    "s_mov_b32 s49, 57344\n"
    "buffer_load_dwordx4 v42, s[56:59], s49 offen offset:0 sc1 lds\n"
    "v_mfma_f32_16x16x32_bf16 v[172:175], a[212:215], v[116:119], v[172:175]\n"
    "ds_read_b64_tr_b16 a[238:239], v55 offset:8448\n"
    "v_max3_f32 v110, v110, v83, v84\n"
    "s_waitcnt lgkmcnt(8)\n"
    "v_mfma_f32_16x16x32_bf16 v[176:179], a[216:219], v[112:115], v[176:179]\n"
    "ds_read_b64_tr_b16 a[240:241], v56 offset:256\n"
    "v_max3_f32 v110, v110, v85, v86\n"
    "v_mfma_f32_16x16x32_bf16 v[180:183], a[216:219], v[116:119], v[180:183]\n"
    "ds_read_b64_tr_b16 a[242:243], v56 offset:8448\n"
    "buffer_load_dwordx4 v43, s[56:59], s49 offen offset:1024 sc1 lds\n"
    "v_mfma_f32_16x16x32_bf16 v[184:187], a[220:223], v[112:115], v[184:187]\n"
    "ds_read_b64_tr_b16 a[244:245], v57 offset:256\n"
    "v_max3_f32 v110, v110, v87, v88\n"
    "v_mfma_f32_16x16x32_bf16 v[188:191], a[220:223], v[116:119], v[188:191]\n"
    "ds_read_b64_tr_b16 a[246:247], v57 offset:8448\n"
    "v_max3_f32 v110, v110, v89, v90\n"
    "s_waitcnt lgkmcnt(8)\n"
    "v_mfma_f32_16x16x32_bf16 v[192:195], a[224:227], v[112:115], v[192:195]\n"
    "ds_read_b64_tr_b16 a[248:249], v58 offset:256\n"
    "buffer_load_dwordx4 v44, s[56:59], s49 offen offset:2048 sc1 lds\n"
    "v_mfma_f32_16x16x32_bf16 v[196:199], a[224:227], v[116:119], v[196:199]\n"
    "ds_read_b64_tr_b16 a[250:251], v58 offset:8448\n"
    "v_max3_f32 v110, v110, v91, v92\n"
    "v_mfma_f32_16x16x32_bf16 v[200:203], a[228:231], v[112:115], v[200:203]\n"
    "ds_read_b64_tr_b16 a[252:253], v59 offset:256\n"
    "v_max3_f32 v110, v110, v93, v94\n"
    "v_mfma_f32_16x16x32_bf16 v[204:207], a[228:231], v[116:119], v[204:207]\n"
    "ds_read_b64_tr_b16 a[254:255], v59 offset:8448\n"
    "buffer_load_dwordx4 v45, s[56:59], s49 offen offset:3072 sc1 lds\n"
    "s_waitcnt lgkmcnt(8)\n"
    "v_mfma_f32_16x16x32_bf16 v[208:211], a[232:235], v[112:115], v[208:211]\n"
    "v_max_f32 v110, v110, v95\n"
    "v_mfma_f32_16x16x32_bf16 v[212:215], a[232:235], v[116:119], v[212:215]\n"
    "v_mfma_f32_16x16x32_bf16 v[216:219], a[236:239], v[112:115], v[216:219]\n"
    "s_add_u32 m0, s50, 131072\n"
    "s_mov_b32 s49, 71680\n"
    "buffer_load_dwordx4 v42, s[56:59], s49 offen offset:0 sc1 lds\n"
    "v_mfma_f32_16x16x32_bf16 v[220:223], a[236:239], v[116:119], v[220:223]\n"
    "v_cmp_lt_f32 vcc, s79, v110\n"
    "s_waitcnt lgkmcnt(4)\n"
    "v_mfma_f32_16x16x32_bf16 v[224:227], a[240:243], v[112:115], v[224:227]\n"
    "s_mov_b32 s76, 8192\n"
    "buffer_load_dwordx4 a[36:39], v33, s[68:71], s76 offen offset:1024 sc1\n"
    "v_mfma_f32_16x16x32_bf16 v[228:231], a[240:243], v[116:119], v[228:231]\n"
    "buffer_load_dwordx4 v43, s[56:59], s49 offen offset:1024 sc1 lds\n"
    "v_mfma_f32_16x16x32_bf16 v[232:235], a[244:247], v[112:115], v[232:235]\n"
    "v_mfma_f32_16x16x32_bf16 v[236:239], a[244:247], v[116:119], v[236:239]\n"
    "s_mov_b32 s76, 8192\n"
    "buffer_load_dwordx4 a[40:43], v33, s[68:71], s76 offen offset:2048 sc1\n"
    "s_waitcnt lgkmcnt(0)\n"
    "v_mfma_f32_16x16x32_bf16 v[240:243], a[248:251], v[112:115], v[240:243]\n"
    "buffer_load_dwordx4 v44, s[56:59], s49 offen offset:2048 sc1 lds\n"
    "v_mfma_f32_16x16x32_bf16 v[244:247], a[248:251], v[116:119], v[244:247]\n"
    "v_mfma_f32_16x16x32_bf16 v[248:251], a[252:255], v[112:115], v[248:251]\n"
    "s_mov_b32 s76, 8192\n"
    "buffer_load_dwordx4 a[44:47], v33, s[68:71], s76 offen offset:3072 sc1\n"
    "v_mfma_f32_16x16x32_bf16 v[252:255], a[252:255], v[116:119], v[252:255]\n"
    "buffer_load_dwordx4 v45, s[56:59], s49 offen offset:3072 sc1 lds\n"
    "s_cbranch_vccnz .LrareP1T32_%=\n"
    ".LretP1T32_%=:\n"
    "v_exp_f32 v80, v80\n"
    "v_exp_f32 v81, v81\n"
    "v_exp_f32 v82, v82\n"
    "v_exp_f32 v83, v83\n"
    "v_exp_f32 v88, v88\n"
    "v_exp_f32 v89, v89\n"
    "v_exp_f32 v90, v90\n"
    "v_exp_f32 v91, v91\n"
    "v_exp_f32 v84, v84\n"
    "v_exp_f32 v85, v85\n"
    "v_exp_f32 v86, v86\n"
    "v_exp_f32 v87, v87\n"
    "v_exp_f32 v92, v92\n"
    "v_exp_f32 v93, v93\n"
    "v_exp_f32 v94, v94\n"
    "v_exp_f32 v95, v95\n"
    "v_cvt_pk_bf16_f32 v112, v80, v81\n"
    "v_cvt_pk_bf16_f32 v113, v82, v83\n"
    "s_mov_b32 s76, 12288\n"
    "buffer_load_dwordx4 a[48:51], v33, s[68:71], s76 offen offset:0 sc1\n"
    "v_cvt_pk_bf16_f32 v114, v88, v89\n"
    "v_cvt_pk_bf16_f32 v115, v90, v91\n"
    "s_mov_b32 s76, 12288\n"
    "buffer_load_dwordx4 a[52:55], v33, s[68:71], s76 offen offset:1024 sc1\n"
    "ds_read_b64_tr_b16 a[192:193], v52 offset:16384\n"
    "v_cvt_pk_bf16_f32 v116, v84, v85\n"
    "ds_read_b64_tr_b16 a[194:195], v52 offset:24576\n"
    "v_cvt_pk_bf16_f32 v117, v86, v87\n"
    "s_mov_b32 s76, 12288\n"
    "buffer_load_dwordx4 a[56:59], v33, s[68:71], s76 offen offset:2048 sc1\n"
    "ds_read_b64_tr_b16 a[196:197], v53 offset:16384\n"
    "v_cvt_pk_bf16_f32 v118, v92, v93\n"
    "ds_read_b64_tr_b16 a[198:199], v53 offset:24576\n"
    "v_cvt_pk_bf16_f32 v119, v94, v95\n"
    "s_mov_b32 s76, 12288\n"
    "buffer_load_dwordx4 a[60:63], v33, s[68:71], s76 offen offset:3072 sc1\n"
    "ds_read_b64_tr_b16 a[200:201], v54 offset:16384\n"
    "v_add_f32 v26, v26, v80\n"
    "ds_read_b64_tr_b16 a[202:203], v54 offset:24576\n"
    "v_add_f32 v27, v27, v81\n"
    "s_mov_b32 s76, 16384\n"
    "buffer_load_dwordx4 a[64:67], v33, s[68:71], s76 offen offset:0 sc1\n"
    "ds_read_b64_tr_b16 a[204:205], v55 offset:16384\n"
    "v_add_f32 v26, v26, v82\n"
    "ds_read_b64_tr_b16 a[206:207], v55 offset:24576\n"
    "v_add_f32 v27, v27, v83\n"
    "s_mov_b32 s76, 16384\n"
    "buffer_load_dwordx4 a[68:71], v33, s[68:71], s76 offen offset:1024 sc1\n"
    "ds_read_b64_tr_b16 a[208:209], v56 offset:16384\n"
    "v_add_f32 v26, v26, v88\n"
    "ds_read_b64_tr_b16 a[210:211], v56 offset:24576\n"
    "v_add_f32 v27, v27, v89\n"
    "s_mov_b32 s76, 16384\n"
    "buffer_load_dwordx4 a[72:75], v33, s[68:71], s76 offen offset:2048 sc1\n"
    "ds_read_b64_tr_b16 a[212:213], v57 offset:16384\n"
    "v_add_f32 v26, v26, v90\n"
    "ds_read_b64_tr_b16 a[214:215], v57 offset:24576\n"
    "v_add_f32 v27, v27, v91\n"
    "s_mov_b32 s76, 16384\n"
    "buffer_load_dwordx4 a[76:79], v33, s[68:71], s76 offen offset:3072 sc1\n"
    "s_waitcnt lgkmcnt(8)\n"
    "v_mfma_f32_16x16x32_bf16 v[128:131], a[192:195], v[112:115], v[128:131]\n"
    "ds_read_b64_tr_b16 a[216:217], v58 offset:16384\n"
    "v_add_f32 v28, v28, v84\n"
    "v_mfma_f32_16x16x32_bf16 v[132:135], a[192:195], v[116:119], v[132:135]\n"
    "ds_read_b64_tr_b16 a[218:219], v58 offset:24576\n"
    "v_add_f32 v29, v29, v85\n"
    "s_mov_b32 s76, 20480\n"
    "buffer_load_dwordx4 a[80:83], v33, s[68:71], s76 offen offset:0 sc1\n"
    "v_mfma_f32_16x16x32_bf16 v[136:139], a[196:199], v[112:115], v[136:139]\n"
    "ds_read_b64_tr_b16 a[220:221], v59 offset:16384\n"
    "v_add_f32 v28, v28, v86\n"
    "v_mfma_f32_16x16x32_bf16 v[140:143], a[196:199], v[116:119], v[140:143]\n"
    "ds_read_b64_tr_b16 a[222:223], v59 offset:24576\n"
    "v_add_f32 v29, v29, v87\n"
    "s_mov_b32 s76, 20480\n"
    "buffer_load_dwordx4 a[84:87], v33, s[68:71], s76 offen offset:1024 sc1\n"
    "s_waitcnt lgkmcnt(8)\n"
    "v_mfma_f32_16x16x32_bf16 v[144:147], a[200:203], v[112:115], v[144:147]\n"
    "ds_read_b64_tr_b16 a[224:225], v52 offset:16640\n"
    "v_add_f32 v28, v28, v92\n"
    "v_mfma_f32_16x16x32_bf16 v[148:151], a[200:203], v[116:119], v[148:151]\n"
    "ds_read_b64_tr_b16 a[226:227], v52 offset:24832\n"
    "v_add_f32 v29, v29, v93\n"
    "s_mov_b32 s76, 20480\n"
    "buffer_load_dwordx4 a[88:91], v33, s[68:71], s76 offen offset:2048 sc1\n"
    "v_mfma_f32_16x16x32_bf16 v[152:155], a[204:207], v[112:115], v[152:155]\n"
    "ds_read_b64_tr_b16 a[228:229], v53 offset:16640\n"
    "v_add_f32 v28, v28, v94\n"
    "v_mfma_f32_16x16x32_bf16 v[156:159], a[204:207], v[116:119], v[156:159]\n"
    "ds_read_b64_tr_b16 a[230:231], v53 offset:24832\n"
    "v_add_f32 v29, v29, v95\n"
    "s_mov_b32 s76, 20480\n"
    "buffer_load_dwordx4 a[92:95], v33, s[68:71], s76 offen offset:3072 sc1\n"
    "s_waitcnt lgkmcnt(8)\n"
    "v_mfma_f32_16x16x32_bf16 v[160:163], a[208:211], v[112:115], v[160:163]\n"
    "ds_read_b64_tr_b16 a[232:233], v54 offset:16640\n"
    "s_add_u32 s48, s48, 16384\n"
    "v_mfma_f32_16x16x32_bf16 v[164:167], a[208:211], v[116:119], v[164:167]\n"
    "ds_read_b64_tr_b16 a[234:235], v54 offset:24832\n"
    "v_mfma_f32_16x16x32_bf16 v[168:171], a[212:215], v[112:115], v[168:171]\n"
    "ds_read_b64_tr_b16 a[236:237], v55 offset:16640\n"
    "s_add_u32 m0, s50, 49152\n"
    "s_mov_b32 s49, 86016\n"
    "buffer_load_dwordx4 v42, s[56:59], s49 offen offset:0 sc1 lds\n"
    "v_mfma_f32_16x16x32_bf16 v[172:175], a[212:215], v[116:119], v[172:175]\n"
    "ds_read_b64_tr_b16 a[238:239], v55 offset:24832\n"
    "s_waitcnt lgkmcnt(8)\n"
    "v_mfma_f32_16x16x32_bf16 v[176:179], a[216:219], v[112:115], v[176:179]\n"
    "ds_read_b64_tr_b16 a[240:241], v56 offset:16640\n"
    "v_mfma_f32_16x16x32_bf16 v[180:183], a[216:219], v[116:119], v[180:183]\n"
    "ds_read_b64_tr_b16 a[242:243], v56 offset:24832\n"
    "buffer_load_dwordx4 v43, s[56:59], s49 offen offset:1024 sc1 lds\n"
    "v_mfma_f32_16x16x32_bf16 v[184:187], a[220:223], v[112:115], v[184:187]\n"
    "ds_read_b64_tr_b16 a[244:245], v57 offset:16640\n"
    "v_mfma_f32_16x16x32_bf16 v[188:191], a[220:223], v[116:119], v[188:191]\n"
    "ds_read_b64_tr_b16 a[246:247], v57 offset:24832\n"
    "s_waitcnt lgkmcnt(8)\n"
    "v_mfma_f32_16x16x32_bf16 v[192:195], a[224:227], v[112:115], v[192:195]\n"
    "ds_read_b64_tr_b16 a[248:249], v58 offset:16640\n"
    "buffer_load_dwordx4 v44, s[56:59], s49 offen offset:2048 sc1 lds\n"
    "v_mfma_f32_16x16x32_bf16 v[196:199], a[224:227], v[116:119], v[196:199]\n"
    "ds_read_b64_tr_b16 a[250:251], v58 offset:24832\n"
    "v_mfma_f32_16x16x32_bf16 v[200:203], a[228:231], v[112:115], v[200:203]\n"
    "ds_read_b64_tr_b16 a[252:253], v59 offset:16640\n"
    "v_mfma_f32_16x16x32_bf16 v[204:207], a[228:231], v[116:119], v[204:207]\n"
    "ds_read_b64_tr_b16 a[254:255], v59 offset:24832\n"
    "buffer_load_dwordx4 v45, s[56:59], s49 offen offset:3072 sc1 lds\n"
    "s_waitcnt lgkmcnt(8)\n"
    "v_mfma_f32_16x16x32_bf16 v[208:211], a[232:235], v[112:115], v[208:211]\n"
    "v_mfma_f32_16x16x32_bf16 v[212:215], a[232:235], v[116:119], v[212:215]\n"
    "s_mov_b32 s76, 24576\n"
    "buffer_load_dwordx4 a[96:99], v33, s[68:71], s76 offen offset:0 sc1\n"
    "v_mfma_f32_16x16x32_bf16 v[216:219], a[236:239], v[112:115], v[216:219]\n"
    "s_add_u32 m0, s50, 147456\n"
    "s_mov_b32 s49, 100352\n"
    "buffer_load_dwordx4 v42, s[56:59], s49 offen offset:0 sc1 lds\n"
    "v_mfma_f32_16x16x32_bf16 v[220:223], a[236:239], v[116:119], v[220:223]\n"
    "s_waitcnt lgkmcnt(4)\n"
    "v_mfma_f32_16x16x32_bf16 v[224:227], a[240:243], v[112:115], v[224:227]\n"
    "s_mov_b32 s76, 24576\n"
    "buffer_load_dwordx4 a[100:103], v33, s[68:71], s76 offen offset:1024 sc1\n"
    "v_mfma_f32_16x16x32_bf16 v[228:231], a[240:243], v[116:119], v[228:231]\n"
    "buffer_load_dwordx4 v43, s[56:59], s49 offen offset:1024 sc1 lds\n"
    "v_mfma_f32_16x16x32_bf16 v[232:235], a[244:247], v[112:115], v[232:235]\n"
    "v_mfma_f32_16x16x32_bf16 v[236:239], a[244:247], v[116:119], v[236:239]\n"
    "s_mov_b32 s76, 24576\n"
    "buffer_load_dwordx4 a[104:107], v33, s[68:71], s76 offen offset:2048 sc1\n"
    "s_waitcnt lgkmcnt(0)\n"
    "v_mfma_f32_16x16x32_bf16 v[240:243], a[248:251], v[112:115], v[240:243]\n"
    "buffer_load_dwordx4 v44, s[56:59], s49 offen offset:2048 sc1 lds\n"
    "v_mfma_f32_16x16x32_bf16 v[244:247], a[248:251], v[116:119], v[244:247]\n"
    "v_mfma_f32_16x16x32_bf16 v[248:251], a[252:255], v[112:115], v[248:251]\n"
    "s_mov_b32 s76, 24576\n"
    "buffer_load_dwordx4 a[108:111], v33, s[68:71], s76 offen offset:3072 sc1\n"
    "v_mfma_f32_16x16x32_bf16 v[252:255], a[252:255], v[116:119], v[252:255]\n"
    "buffer_load_dwordx4 v45, s[56:59], s49 offen offset:3072 sc1 lds\n"
    "s_nop 7\n"
    "s_nop 7\n"
    "v_add_f32 v26, v26, v27\n"
    "v_mov_b32 v120, v26\n"
    "s_nop 1\n"
    "v_permlane32_swap_b32 v26, v120\n"
    "s_nop 1\n"
    "v_add_f32 v26, v26, v120\n"
    "v_mov_b32 v120, v26\n"
    "s_nop 1\n"
    "v_permlane16_swap_b32 v26, v120\n"
    "s_nop 1\n"
    "v_add_f32 v26, v26, v120\n"
    "v_rcp_f32 v124, v26\n"
    "v_add_f32 v28, v28, v29\n"
    "v_mov_b32 v120, v28\n"
    "s_nop 1\n"
    "v_permlane32_swap_b32 v28, v120\n"
    "s_nop 1\n"
    "v_add_f32 v28, v28, v120\n"
    "v_mov_b32 v120, v28\n"
    "s_nop 1\n"
    "v_permlane16_swap_b32 v28, v120\n"
    "s_nop 1\n"
    "v_add_f32 v28, v28, v120\n"
    "v_rcp_f32 v125, v28\n"
    "v_mov_b32 v104, v33\n"
    "v_add_u32 v105, 0x20000, v33\n"
    "s_lshl_b32 s52, %[qb], 20\n"
    "s_lshl_b32 s53, %[w], 18\n"
    "s_add_u32 s52, s52, s53\n"
    "s_lshl_b32 s53, %[head], 13\n"
    "s_add_u32 s52, s52, s53\n"
    "s_add_u32 s52, s52, 4194304\n"
    "s_add_u32 s64, %[wslo], s52\n"
    "s_addc_u32 s65, %[wshi], 0\n"
    "s_and_b32 s65, s65, 0xffff\n"
    "s_mov_b32 s66, 139264\n"
    "s_mov_b32 s67, 0x20000\n"
    "s_mov_b32 s72, 4096\n"
    "v_mov_b32 v126, v125\n"
    "v_mov_b32 v127, v125\n"
    "v_mov_b32 v125, v124\n"
    "v_pk_mul_f32 v[128:129], v[128:129], v[124:125]\n"
    "v_pk_mul_f32 v[130:131], v[130:131], v[124:125]\n"
    "v_cvt_pk_bf16_f32 v108, v128, v129\n"
    "v_cvt_pk_bf16_f32 v109, v130, v131\n"
    "v_pk_mul_f32 v[136:137], v[136:137], v[124:125]\n"
    "v_pk_mul_f32 v[138:139], v[138:139], v[124:125]\n"
    "v_cvt_pk_bf16_f32 v110, v136, v137\n"
    "v_cvt_pk_bf16_f32 v111, v138, v139\n"
    "buffer_store_dwordx4 v[108:111], v104, s[64:67], 0 offen offset:0 sc1\n"
    "v_pk_mul_f32 v[132:133], v[132:133], v[126:127]\n"
    "v_pk_mul_f32 v[134:135], v[134:135], v[126:127]\n"
    "v_cvt_pk_bf16_f32 v112, v132, v133\n"
    "v_cvt_pk_bf16_f32 v113, v134, v135\n"
    "v_pk_mul_f32 v[140:141], v[140:141], v[126:127]\n"
    "v_pk_mul_f32 v[142:143], v[142:143], v[126:127]\n"
    "v_cvt_pk_bf16_f32 v114, v140, v141\n"
    "v_cvt_pk_bf16_f32 v115, v142, v143\n"
    "buffer_store_dwordx4 v[112:115], v105, s[64:67], 0 offen offset:0 sc1\n"
    "v_pk_mul_f32 v[144:145], v[144:145], v[124:125]\n"
    "v_pk_mul_f32 v[146:147], v[146:147], v[124:125]\n"
    "v_cvt_pk_bf16_f32 v116, v144, v145\n"
    "v_cvt_pk_bf16_f32 v117, v146, v147\n"
    "v_pk_mul_f32 v[152:153], v[152:153], v[124:125]\n"
    "v_pk_mul_f32 v[154:155], v[154:155], v[124:125]\n"
    "v_cvt_pk_bf16_f32 v118, v152, v153\n"
    "v_cvt_pk_bf16_f32 v119, v154, v155\n"
    "buffer_store_dwordx4 v[116:119], v104, s[64:67], 0 offen offset:1024 sc1\n"
    "v_pk_mul_f32 v[148:149], v[148:149], v[126:127]\n"
    "v_pk_mul_f32 v[150:151], v[150:151], v[126:127]\n"
    "v_cvt_pk_bf16_f32 v120, v148, v149\n"
    "v_cvt_pk_bf16_f32 v121, v150, v151\n"
    "v_pk_mul_f32 v[156:157], v[156:157], v[126:127]\n"
    "v_pk_mul_f32 v[158:159], v[158:159], v[126:127]\n"
    "v_cvt_pk_bf16_f32 v122, v156, v157\n"
    "v_cvt_pk_bf16_f32 v123, v158, v159\n"
    "buffer_store_dwordx4 v[120:123], v105, s[64:67], 0 offen offset:1024 sc1\n"
    "v_pk_mul_f32 v[160:161], v[160:161], v[124:125]\n"
    "v_pk_mul_f32 v[162:163], v[162:163], v[124:125]\n"
    "v_cvt_pk_bf16_f32 v108, v160, v161\n"
    "v_cvt_pk_bf16_f32 v109, v162, v163\n"
    "v_pk_mul_f32 v[168:169], v[168:169], v[124:125]\n"
    "v_pk_mul_f32 v[170:171], v[170:171], v[124:125]\n"
    "v_cvt_pk_bf16_f32 v110, v168, v169\n"
    "v_cvt_pk_bf16_f32 v111, v170, v171\n"
    "buffer_store_dwordx4 v[108:111], v104, s[64:67], 0 offen offset:2048 sc1\n"
    "v_pk_mul_f32 v[164:165], v[164:165], v[126:127]\n"
    "v_pk_mul_f32 v[166:167], v[166:167], v[126:127]\n"
    "v_cvt_pk_bf16_f32 v112, v164, v165\n"
    "v_cvt_pk_bf16_f32 v113, v166, v167\n"
    "v_pk_mul_f32 v[172:173], v[172:173], v[126:127]\n"
    "v_pk_mul_f32 v[174:175], v[174:175], v[126:127]\n"
    "v_cvt_pk_bf16_f32 v114, v172, v173\n"
    "v_cvt_pk_bf16_f32 v115, v174, v175\n"
    "buffer_store_dwordx4 v[112:115], v105, s[64:67], 0 offen offset:2048 sc1\n"
    "v_pk_mul_f32 v[176:177], v[176:177], v[124:125]\n"
    "v_pk_mul_f32 v[178:179], v[178:179], v[124:125]\n"
    "v_cvt_pk_bf16_f32 v116, v176, v177\n"
    "v_cvt_pk_bf16_f32 v117, v178, v179\n"
    "v_pk_mul_f32 v[184:185], v[184:185], v[124:125]\n"
    "v_pk_mul_f32 v[186:187], v[186:187], v[124:125]\n"
    "v_cvt_pk_bf16_f32 v118, v184, v185\n"
    "v_cvt_pk_bf16_f32 v119, v186, v187\n"
    "buffer_store_dwordx4 v[116:119], v104, s[64:67], 0 offen offset:3072 sc1\n"
    "v_pk_mul_f32 v[180:181], v[180:181], v[126:127]\n"
    "v_pk_mul_f32 v[182:183], v[182:183], v[126:127]\n"
    "v_cvt_pk_bf16_f32 v120, v180, v181\n"
    "v_cvt_pk_bf16_f32 v121, v182, v183\n"
    "v_pk_mul_f32 v[188:189], v[188:189], v[126:127]\n"
    "v_pk_mul_f32 v[190:191], v[190:191], v[126:127]\n"
    "v_cvt_pk_bf16_f32 v122, v188, v189\n"
    "v_cvt_pk_bf16_f32 v123, v190, v191\n"
    "buffer_store_dwordx4 v[120:123], v105, s[64:67], 0 offen offset:3072 sc1\n"
    "v_pk_mul_f32 v[192:193], v[192:193], v[124:125]\n"
    "v_pk_mul_f32 v[194:195], v[194:195], v[124:125]\n"
    "v_cvt_pk_bf16_f32 v108, v192, v193\n"
    "v_cvt_pk_bf16_f32 v109, v194, v195\n"
    "v_pk_mul_f32 v[200:201], v[200:201], v[124:125]\n"
    "v_pk_mul_f32 v[202:203], v[202:203], v[124:125]\n"
    "v_cvt_pk_bf16_f32 v110, v200, v201\n"
    "v_cvt_pk_bf16_f32 v111, v202, v203\n"
    "buffer_store_dwordx4 v[108:111], v104, s[64:67], s72 offen offset:0 sc1\n"
    "v_pk_mul_f32 v[196:197], v[196:197], v[126:127]\n"
    "v_pk_mul_f32 v[198:199], v[198:199], v[126:127]\n"
    "v_cvt_pk_bf16_f32 v112, v196, v197\n"
    "v_cvt_pk_bf16_f32 v113, v198, v199\n"
    "v_pk_mul_f32 v[204:205], v[204:205], v[126:127]\n"
    "v_pk_mul_f32 v[206:207], v[206:207], v[126:127]\n"
    "v_cvt_pk_bf16_f32 v114, v204, v205\n"
    "v_cvt_pk_bf16_f32 v115, v206, v207\n"
    "buffer_store_dwordx4 v[112:115], v105, s[64:67], s72 offen offset:0 sc1\n"
    "v_pk_mul_f32 v[208:209], v[208:209], v[124:125]\n"
    "v_pk_mul_f32 v[210:211], v[210:211], v[124:125]\n"
    "v_cvt_pk_bf16_f32 v116, v208, v209\n"
    "v_cvt_pk_bf16_f32 v117, v210, v211\n"
    "v_pk_mul_f32 v[216:217], v[216:217], v[124:125]\n"
    "v_pk_mul_f32 v[218:219], v[218:219], v[124:125]\n"
    "v_cvt_pk_bf16_f32 v118, v216, v217\n"
    "v_cvt_pk_bf16_f32 v119, v218, v219\n"
    "buffer_store_dwordx4 v[116:119], v104, s[64:67], s72 offen offset:1024 sc1\n"
    "v_pk_mul_f32 v[212:213], v[212:213], v[126:127]\n"
    "v_pk_mul_f32 v[214:215], v[214:215], v[126:127]\n"
    "v_cvt_pk_bf16_f32 v120, v212, v213\n"
    "v_cvt_pk_bf16_f32 v121, v214, v215\n"
    "v_pk_mul_f32 v[220:221], v[220:221], v[126:127]\n"
    "v_pk_mul_f32 v[222:223], v[222:223], v[126:127]\n"
    "v_cvt_pk_bf16_f32 v122, v220, v221\n"
    "v_cvt_pk_bf16_f32 v123, v222, v223\n"
    "buffer_store_dwordx4 v[120:123], v105, s[64:67], s72 offen offset:1024 sc1\n"
    "v_pk_mul_f32 v[224:225], v[224:225], v[124:125]\n"
    "v_pk_mul_f32 v[226:227], v[226:227], v[124:125]\n"
    "v_cvt_pk_bf16_f32 v108, v224, v225\n"
    "v_cvt_pk_bf16_f32 v109, v226, v227\n"
    "v_pk_mul_f32 v[232:233], v[232:233], v[124:125]\n"
    "v_pk_mul_f32 v[234:235], v[234:235], v[124:125]\n"
    "v_cvt_pk_bf16_f32 v110, v232, v233\n"
    "v_cvt_pk_bf16_f32 v111, v234, v235\n"
    "buffer_store_dwordx4 v[108:111], v104, s[64:67], s72 offen offset:2048 sc1\n"
    "v_pk_mul_f32 v[228:229], v[228:229], v[126:127]\n"
    "v_pk_mul_f32 v[230:231], v[230:231], v[126:127]\n"
    "v_cvt_pk_bf16_f32 v112, v228, v229\n"
    "v_cvt_pk_bf16_f32 v113, v230, v231\n"
    "v_pk_mul_f32 v[236:237], v[236:237], v[126:127]\n"
    "v_pk_mul_f32 v[238:239], v[238:239], v[126:127]\n"
    "v_cvt_pk_bf16_f32 v114, v236, v237\n"
    "v_cvt_pk_bf16_f32 v115, v238, v239\n"
    "buffer_store_dwordx4 v[112:115], v105, s[64:67], s72 offen offset:2048 sc1\n"
    "v_pk_mul_f32 v[240:241], v[240:241], v[124:125]\n"
    "v_pk_mul_f32 v[242:243], v[242:243], v[124:125]\n"
    "v_cvt_pk_bf16_f32 v116, v240, v241\n"
    "v_cvt_pk_bf16_f32 v117, v242, v243\n"
    "v_pk_mul_f32 v[248:249], v[248:249], v[124:125]\n"
    "v_pk_mul_f32 v[250:251], v[250:251], v[124:125]\n"
    "v_cvt_pk_bf16_f32 v118, v248, v249\n"
    "v_cvt_pk_bf16_f32 v119, v250, v251\n"
    "buffer_store_dwordx4 v[116:119], v104, s[64:67], s72 offen offset:3072 sc1\n"
    "v_pk_mul_f32 v[244:245], v[244:245], v[126:127]\n"
    "v_pk_mul_f32 v[246:247], v[246:247], v[126:127]\n"
    "v_cvt_pk_bf16_f32 v120, v244, v245\n"
    "v_cvt_pk_bf16_f32 v121, v246, v247\n"
    "v_pk_mul_f32 v[252:253], v[252:253], v[126:127]\n"
    "v_pk_mul_f32 v[254:255], v[254:255], v[126:127]\n"
    "v_cvt_pk_bf16_f32 v122, v252, v253\n"
    "v_cvt_pk_bf16_f32 v123, v254, v255\n"
    "buffer_store_dwordx4 v[120:123], v105, s[64:67], s72 offen offset:3072 sc1\n"
    "v_lshrrev_b32 v86, 2, v31\n"
    "v_and_b32 v87, 3, v30\n"
    "v_lshrrev_b32 v88, 1, v87\n"
    "v_lshlrev_b32 v88, 4, v88\n"
    "v_and_b32 v87, 1, v87\n"
    "v_lshl_add_u32 v88, v87, 3, v88\n"
    "v_add_u32 v88, %[ldsb], v88\n"
    "v_lshl_add_u32 v87, v32, 2, v86\n"
    "v_and_b32 v89, 7, v87\n"
    "v_lshlrev_b32 v87, 8, v87\n"
    "v_lshl_add_u32 v87, v89, 5, v87\n"
    "v_add_u32 v87, v87, v88\n"
    "v_xor_b32 v64, 0, v87\n"
    "v_xor_b32 v65, 32, v87\n"
    "v_xor_b32 v66, 64, v87\n"
    "v_xor_b32 v67, 96, v87\n"
    "v_xor_b32 v68, 128, v87\n"
    "v_xor_b32 v69, 160, v87\n"
    "v_xor_b32 v70, 192, v87\n"
    "v_xor_b32 v71, 0, v87\n"
    "v_add_u32 v71, 65536, v71\n"
    "v_xor_b32 v72, 32, v87\n"
    "v_add_u32 v72, 65536, v72\n"
    "v_xor_b32 v73, 64, v87\n"
    "v_add_u32 v73, 65536, v73\n"
    "v_xor_b32 v74, 96, v87\n"
    "v_add_u32 v74, 65536, v74\n"
    "v_xor_b32 v75, 128, v87\n"
    "v_add_u32 v75, 65536, v75\n"
    "v_xor_b32 v76, 160, v87\n"
    "v_add_u32 v76, 65536, v76\n"
    "v_xor_b32 v77, 192, v87\n"
    "v_add_u32 v77, 65536, v77\n"
    "v_xor_b32 v78, 0, v87\n"
    "v_add_u32 v78, 131072, v78\n"
    "v_xor_b32 v79, 32, v87\n"
    "v_add_u32 v79, 131072, v79\n"
    "v_xor_b32 v80, 64, v87\n"
    "v_add_u32 v80, 131072, v80\n"
    "v_xor_b32 v81, 96, v87\n"
    "v_add_u32 v81, 131072, v81\n"
    "v_xor_b32 v82, 128, v87\n"
    "v_add_u32 v82, 131072, v82\n"
    "v_xor_b32 v83, 160, v87\n"
    "v_add_u32 v83, 131072, v83\n"
    "v_xor_b32 v84, 192, v87\n"
    "v_add_u32 v84, 131072, v84\n"
    "s_lshl_b32 s52, %[tp2], 20\n"
    "s_lshl_b32 s53, %[w], 18\n"
    "s_add_u32 s52, s52, s53\n"
    "s_lshl_b32 s53, %[kq], 15\n"
    "s_add_u32 s52, s52, s53\n"
    "s_add_u32 s52, s52, 4194304\n"
    "s_add_u32 s40, %[wslo], s52\n"
    "s_addc_u32 s41, %[wshi], 0\n"
    "s_and_b32 s41, s41, 0xffff\n"
    "s_mov_b32 s42, 163840\n"
    "s_mov_b32 s43, 0x20000\n"
    "s_barrier\n"
    "ds_read_b64_tr_b16 a[224:225], v64 offset:0\n"
    "ds_read_b64_tr_b16 a[226:227], v64 offset:4096\n"
    "ds_read_b64_tr_b16 a[228:229], v65 offset:0\n"
    "ds_read_b64_tr_b16 a[230:231], v65 offset:4096\n"
    "ds_read_b64_tr_b16 a[232:233], v66 offset:0\n"
    "ds_read_b64_tr_b16 a[234:235], v66 offset:4096\n"
    "ds_read_b64_tr_b16 a[236:237], v67 offset:0\n"
    "ds_read_b64_tr_b16 a[238:239], v67 offset:4096\n"
    "ds_read_b64_tr_b16 a[240:241], v68 offset:0\n"
    "ds_read_b64_tr_b16 a[242:243], v68 offset:4096\n"
    "ds_read_b64_tr_b16 a[244:245], v69 offset:0\n"
    "ds_read_b64_tr_b16 a[246:247], v69 offset:4096\n"
    "s_waitcnt lgkmcnt(8)\n"
    "v_mfma_f32_16x16x32_bf16 v[128:131], a[224:227], a[0:3], 0\n"
    "ds_read_b64_tr_b16 a[248:249], v70 offset:0\n"
    "ds_read_b64_tr_b16 a[250:251], v70 offset:4096\n"
    "v_mfma_f32_16x16x32_bf16 v[132:135], a[228:231], a[0:3], 0\n"
    "ds_read_b64_tr_b16 a[252:253], v64 offset:8192\n"
    "ds_read_b64_tr_b16 a[254:255], v64 offset:12288\n"
    "s_waitcnt lgkmcnt(8)\n"
    "v_mfma_f32_16x16x32_bf16 v[136:139], a[232:235], a[0:3], 0\n"
    "ds_read_b64_tr_b16 a[224:225], v65 offset:8192\n"
    "ds_read_b64_tr_b16 a[226:227], v65 offset:12288\n"
    "v_mfma_f32_16x16x32_bf16 v[140:143], a[236:239], a[0:3], 0\n"
    "ds_read_b64_tr_b16 a[228:229], v66 offset:8192\n"
    "ds_read_b64_tr_b16 a[230:231], v66 offset:12288\n"
    "s_waitcnt lgkmcnt(8)\n"
    "v_mfma_f32_16x16x32_bf16 v[144:147], a[240:243], a[0:3], 0\n"
    "ds_read_b64_tr_b16 a[232:233], v67 offset:8192\n"
    "ds_read_b64_tr_b16 a[234:235], v67 offset:12288\n"
    "v_mfma_f32_16x16x32_bf16 v[148:151], a[244:247], a[0:3], 0\n"
    "ds_read_b64_tr_b16 a[236:237], v68 offset:8192\n"
    "ds_read_b64_tr_b16 a[238:239], v68 offset:12288\n"
    "s_waitcnt lgkmcnt(8)\n"
    "v_mfma_f32_16x16x32_bf16 v[152:155], a[248:251], a[0:3], 0\n"
    "ds_read_b64_tr_b16 a[240:241], v69 offset:8192\n"
    "ds_read_b64_tr_b16 a[242:243], v69 offset:12288\n"
    "v_mfma_f32_16x16x32_bf16 v[128:131], a[252:255], a[4:7], v[128:131]\n"
    "ds_read_b64_tr_b16 a[244:245], v70 offset:8192\n"
    "ds_read_b64_tr_b16 a[246:247], v70 offset:12288\n"
    "s_waitcnt lgkmcnt(8)\n"
    "s_waitcnt vmcnt(62)\n"
    "s_barrier\n"
    "v_mfma_f32_16x16x32_bf16 v[132:135], a[224:227], a[4:7], v[132:135]\n"
    "ds_read_b64_tr_b16 a[248:249], v64 offset:16384\n"
    "ds_read_b64_tr_b16 a[250:251], v64 offset:20480\n"
    "v_mfma_f32_16x16x32_bf16 v[136:139], a[228:231], a[4:7], v[136:139]\n"
    "ds_read_b64_tr_b16 a[252:253], v65 offset:16384\n"
    "ds_read_b64_tr_b16 a[254:255], v65 offset:20480\n"
    "s_waitcnt lgkmcnt(8)\n"
    "v_mfma_f32_16x16x32_bf16 v[140:143], a[232:235], a[4:7], v[140:143]\n"
    "ds_read_b64_tr_b16 a[224:225], v66 offset:16384\n"
    "ds_read_b64_tr_b16 a[226:227], v66 offset:20480\n"
    "v_mfma_f32_16x16x32_bf16 v[144:147], a[236:239], a[4:7], v[144:147]\n"
    "ds_read_b64_tr_b16 a[228:229], v67 offset:16384\n"
    "ds_read_b64_tr_b16 a[230:231], v67 offset:20480\n"
    "s_waitcnt lgkmcnt(8)\n"
    "v_mfma_f32_16x16x32_bf16 v[148:151], a[240:243], a[4:7], v[148:151]\n"
    "ds_read_b64_tr_b16 a[232:233], v68 offset:16384\n"
    "ds_read_b64_tr_b16 a[234:235], v68 offset:20480\n"
    "v_mfma_f32_16x16x32_bf16 v[152:155], a[244:247], a[4:7], v[152:155]\n"
    "ds_read_b64_tr_b16 a[236:237], v69 offset:16384\n"
    "ds_read_b64_tr_b16 a[238:239], v69 offset:20480\n"
    "s_waitcnt lgkmcnt(8)\n"
    "v_mfma_f32_16x16x32_bf16 v[128:131], a[248:251], a[8:11], v[128:131]\n"
    "ds_read_b64_tr_b16 a[240:241], v70 offset:16384\n"
    "ds_read_b64_tr_b16 a[242:243], v70 offset:20480\n"
    "v_mfma_f32_16x16x32_bf16 v[132:135], a[252:255], a[8:11], v[132:135]\n"
    "ds_read_b64_tr_b16 a[244:245], v64 offset:24576\n"
    "ds_read_b64_tr_b16 a[246:247], v64 offset:28672\n"
    "s_waitcnt lgkmcnt(8)\n"
    "v_mfma_f32_16x16x32_bf16 v[136:139], a[224:227], a[8:11], v[136:139]\n"
    "ds_read_b64_tr_b16 a[248:249], v65 offset:24576\n"
    "ds_read_b64_tr_b16 a[250:251], v65 offset:28672\n"
    "v_mfma_f32_16x16x32_bf16 v[140:143], a[228:231], a[8:11], v[140:143]\n"
    "ds_read_b64_tr_b16 a[252:253], v66 offset:24576\n"
    "ds_read_b64_tr_b16 a[254:255], v66 offset:28672\n"
    "s_waitcnt lgkmcnt(8)\n"
    "v_mfma_f32_16x16x32_bf16 v[144:147], a[232:235], a[8:11], v[144:147]\n"
    "ds_read_b64_tr_b16 a[224:225], v67 offset:24576\n"
    "ds_read_b64_tr_b16 a[226:227], v67 offset:28672\n"
    "v_mfma_f32_16x16x32_bf16 v[148:151], a[236:239], a[8:11], v[148:151]\n"
    "ds_read_b64_tr_b16 a[228:229], v68 offset:24576\n"
    "ds_read_b64_tr_b16 a[230:231], v68 offset:28672\n"
    "s_waitcnt lgkmcnt(8)\n"
    "v_mfma_f32_16x16x32_bf16 v[152:155], a[240:243], a[8:11], v[152:155]\n"
    "ds_read_b64_tr_b16 a[232:233], v69 offset:24576\n"
    "ds_read_b64_tr_b16 a[234:235], v69 offset:28672\n"
    "v_mfma_f32_16x16x32_bf16 v[128:131], a[244:247], a[12:15], v[128:131]\n"
    "ds_read_b64_tr_b16 a[236:237], v70 offset:24576\n"
    "ds_read_b64_tr_b16 a[238:239], v70 offset:28672\n"
    "s_waitcnt lgkmcnt(8)\n"
    "s_waitcnt vmcnt(58)\n"
    "s_barrier\n"
    "v_mfma_f32_16x16x32_bf16 v[132:135], a[248:251], a[12:15], v[132:135]\n"
    "ds_read_b64_tr_b16 a[240:241], v71 offset:0\n"
    "ds_read_b64_tr_b16 a[242:243], v71 offset:4096\n"
    "s_add_u32 m0, s50, 0\n"
    "s_mov_b32 s49, 114688\n"
    "buffer_load_dwordx4 v42, s[56:59], s49 offen offset:0 sc1 lds\n"
    "v_mfma_f32_16x16x32_bf16 v[136:139], a[252:255], a[12:15], v[136:139]\n"
    "ds_read_b64_tr_b16 a[244:245], v72 offset:0\n"
    "ds_read_b64_tr_b16 a[246:247], v72 offset:4096\n"
    "buffer_load_dwordx4 v43, s[56:59], s49 offen offset:1024 sc1 lds\n"
    "s_waitcnt lgkmcnt(8)\n"
    "v_mfma_f32_16x16x32_bf16 v[140:143], a[224:227], a[12:15], v[140:143]\n"
    "ds_read_b64_tr_b16 a[248:249], v73 offset:0\n"
    "ds_read_b64_tr_b16 a[250:251], v73 offset:4096\n"
    "buffer_load_dwordx4 v44, s[56:59], s49 offen offset:2048 sc1 lds\n"
    "v_mfma_f32_16x16x32_bf16 v[144:147], a[228:231], a[12:15], v[144:147]\n"
    "ds_read_b64_tr_b16 a[252:253], v74 offset:0\n"
    "ds_read_b64_tr_b16 a[254:255], v74 offset:4096\n"
    "buffer_load_dwordx4 v45, s[56:59], s49 offen offset:3072 sc1 lds\n"
    "s_waitcnt lgkmcnt(8)\n"
    "v_mfma_f32_16x16x32_bf16 v[148:151], a[232:235], a[12:15], v[148:151]\n"
    "ds_read_b64_tr_b16 a[224:225], v75 offset:0\n"
    "ds_read_b64_tr_b16 a[226:227], v75 offset:4096\n"
    "v_mfma_f32_16x16x32_bf16 v[152:155], a[236:239], a[12:15], v[152:155]\n"
    "ds_read_b64_tr_b16 a[228:229], v76 offset:0\n"
    "ds_read_b64_tr_b16 a[230:231], v76 offset:4096\n"
    "s_waitcnt lgkmcnt(8)\n"
    "v_mfma_f32_16x16x32_bf16 v[128:131], a[240:243], a[16:19], v[128:131]\n"
    "ds_read_b64_tr_b16 a[232:233], v77 offset:0\n"
    "ds_read_b64_tr_b16 a[234:235], v77 offset:4096\n"
    "v_mfma_f32_16x16x32_bf16 v[132:135], a[244:247], a[16:19], v[132:135]\n"
    "ds_read_b64_tr_b16 a[236:237], v71 offset:8192\n"
    "ds_read_b64_tr_b16 a[238:239], v71 offset:12288\n"
    "s_waitcnt lgkmcnt(8)\n"
    "v_mfma_f32_16x16x32_bf16 v[136:139], a[248:251], a[16:19], v[136:139]\n"
    "ds_read_b64_tr_b16 a[240:241], v72 offset:8192\n"
    "ds_read_b64_tr_b16 a[242:243], v72 offset:12288\n"
    "v_mfma_f32_16x16x32_bf16 v[140:143], a[252:255], a[16:19], v[140:143]\n"
    "ds_read_b64_tr_b16 a[244:245], v73 offset:8192\n"
    "ds_read_b64_tr_b16 a[246:247], v73 offset:12288\n"
    "s_waitcnt lgkmcnt(8)\n"
    "v_mfma_f32_16x16x32_bf16 v[144:147], a[224:227], a[16:19], v[144:147]\n"
    "ds_read_b64_tr_b16 a[248:249], v74 offset:8192\n"
    "ds_read_b64_tr_b16 a[250:251], v74 offset:12288\n"
    "v_mfma_f32_16x16x32_bf16 v[148:151], a[228:231], a[16:19], v[148:151]\n"
    "ds_read_b64_tr_b16 a[252:253], v75 offset:8192\n"
    "ds_read_b64_tr_b16 a[254:255], v75 offset:12288\n"
    "s_waitcnt lgkmcnt(8)\n"
    "v_mfma_f32_16x16x32_bf16 v[152:155], a[232:235], a[16:19], v[152:155]\n"
    "ds_read_b64_tr_b16 a[224:225], v76 offset:8192\n"
    "ds_read_b64_tr_b16 a[226:227], v76 offset:12288\n"
    "v_mfma_f32_16x16x32_bf16 v[128:131], a[236:239], a[20:23], v[128:131]\n"
    "ds_read_b64_tr_b16 a[228:229], v77 offset:8192\n"
    "ds_read_b64_tr_b16 a[230:231], v77 offset:12288\n"
    "s_waitcnt lgkmcnt(8)\n"
    "s_waitcnt vmcnt(55)\n"
    "s_barrier\n"
    "v_mfma_f32_16x16x32_bf16 v[132:135], a[240:243], a[20:23], v[132:135]\n"
    "ds_read_b64_tr_b16 a[232:233], v71 offset:16384\n"
    "ds_read_b64_tr_b16 a[234:235], v71 offset:20480\n"
    "s_add_u32 m0, s50, 16384\n"
    "s_mov_b32 s49, 129024\n"
    "buffer_load_dwordx4 v42, s[56:59], s49 offen offset:0 sc1 lds\n"
    "v_mfma_f32_16x16x32_bf16 v[136:139], a[244:247], a[20:23], v[136:139]\n"
    "ds_read_b64_tr_b16 a[236:237], v72 offset:16384\n"
    "ds_read_b64_tr_b16 a[238:239], v72 offset:20480\n"
    "buffer_load_dwordx4 v43, s[56:59], s49 offen offset:1024 sc1 lds\n"
    "s_waitcnt lgkmcnt(8)\n"
    "v_mfma_f32_16x16x32_bf16 v[140:143], a[248:251], a[20:23], v[140:143]\n"
    "ds_read_b64_tr_b16 a[240:241], v73 offset:16384\n"
    "ds_read_b64_tr_b16 a[242:243], v73 offset:20480\n"
    "buffer_load_dwordx4 v44, s[56:59], s49 offen offset:2048 sc1 lds\n"
    "v_mfma_f32_16x16x32_bf16 v[144:147], a[252:255], a[20:23], v[144:147]\n"
    "ds_read_b64_tr_b16 a[244:245], v74 offset:16384\n"
    "ds_read_b64_tr_b16 a[246:247], v74 offset:20480\n"
    "buffer_load_dwordx4 v45, s[56:59], s49 offen offset:3072 sc1 lds\n"
    "s_waitcnt lgkmcnt(8)\n"
    "v_mfma_f32_16x16x32_bf16 v[148:151], a[224:227], a[20:23], v[148:151]\n"
    "ds_read_b64_tr_b16 a[248:249], v75 offset:16384\n"
    "ds_read_b64_tr_b16 a[250:251], v75 offset:20480\n"
    "v_mfma_f32_16x16x32_bf16 v[152:155], a[228:231], a[20:23], v[152:155]\n"
    "ds_read_b64_tr_b16 a[252:253], v76 offset:16384\n"
    "ds_read_b64_tr_b16 a[254:255], v76 offset:20480\n"
    "s_waitcnt lgkmcnt(8)\n"
    "v_mfma_f32_16x16x32_bf16 v[128:131], a[232:235], a[24:27], v[128:131]\n"
    "ds_read_b64_tr_b16 a[224:225], v77 offset:16384\n"
    "ds_read_b64_tr_b16 a[226:227], v77 offset:20480\n"
    "v_mfma_f32_16x16x32_bf16 v[132:135], a[236:239], a[24:27], v[132:135]\n"
    "ds_read_b64_tr_b16 a[228:229], v71 offset:24576\n"
    "ds_read_b64_tr_b16 a[230:231], v71 offset:28672\n"
    "s_waitcnt lgkmcnt(8)\n"
    "v_mfma_f32_16x16x32_bf16 v[136:139], a[240:243], a[24:27], v[136:139]\n"
    "ds_read_b64_tr_b16 a[232:233], v72 offset:24576\n"
    "ds_read_b64_tr_b16 a[234:235], v72 offset:28672\n"
    "v_mfma_f32_16x16x32_bf16 v[140:143], a[244:247], a[24:27], v[140:143]\n"
    "ds_read_b64_tr_b16 a[236:237], v73 offset:24576\n"
    "ds_read_b64_tr_b16 a[238:239], v73 offset:28672\n"
    "s_waitcnt lgkmcnt(8)\n"
    "v_mfma_f32_16x16x32_bf16 v[144:147], a[248:251], a[24:27], v[144:147]\n"
    "ds_read_b64_tr_b16 a[240:241], v74 offset:24576\n"
    "ds_read_b64_tr_b16 a[242:243], v74 offset:28672\n"
    "v_mfma_f32_16x16x32_bf16 v[148:151], a[252:255], a[24:27], v[148:151]\n"
    "ds_read_b64_tr_b16 a[244:245], v75 offset:24576\n"
    "ds_read_b64_tr_b16 a[246:247], v75 offset:28672\n"
    "s_waitcnt lgkmcnt(8)\n"
    "v_mfma_f32_16x16x32_bf16 v[152:155], a[224:227], a[24:27], v[152:155]\n"
    "ds_read_b64_tr_b16 a[248:249], v76 offset:24576\n"
    "ds_read_b64_tr_b16 a[250:251], v76 offset:28672\n"
    "v_mfma_f32_16x16x32_bf16 v[128:131], a[228:231], a[28:31], v[128:131]\n"
    "ds_read_b64_tr_b16 a[252:253], v77 offset:24576\n"
    "ds_read_b64_tr_b16 a[254:255], v77 offset:28672\n"
    "s_waitcnt lgkmcnt(8)\n"
    "s_waitcnt vmcnt(55)\n"
    "s_barrier\n"
    "v_mfma_f32_16x16x32_bf16 v[132:135], a[232:235], a[28:31], v[132:135]\n"
    "ds_read_b64_tr_b16 a[224:225], v64 offset:32768\n"
    "ds_read_b64_tr_b16 a[226:227], v64 offset:36864\n"
    "s_add_u32 m0, s50, 65536\n"
    "s_mov_b32 s49, 143360\n"
    "buffer_load_dwordx4 v42, s[56:59], s49 offen offset:0 sc1 lds\n"
    "v_mfma_f32_16x16x32_bf16 v[136:139], a[236:239], a[28:31], v[136:139]\n"
    "ds_read_b64_tr_b16 a[228:229], v65 offset:32768\n"
    "ds_read_b64_tr_b16 a[230:231], v65 offset:36864\n"
    "buffer_load_dwordx4 v43, s[56:59], s49 offen offset:1024 sc1 lds\n"
    "s_waitcnt lgkmcnt(8)\n"
    "v_mfma_f32_16x16x32_bf16 v[140:143], a[240:243], a[28:31], v[140:143]\n"
    "ds_read_b64_tr_b16 a[232:233], v66 offset:32768\n"
    "ds_read_b64_tr_b16 a[234:235], v66 offset:36864\n"
    "buffer_load_dwordx4 v44, s[56:59], s49 offen offset:2048 sc1 lds\n"
    "v_mfma_f32_16x16x32_bf16 v[144:147], a[244:247], a[28:31], v[144:147]\n"
    "ds_read_b64_tr_b16 a[236:237], v67 offset:32768\n"
    "ds_read_b64_tr_b16 a[238:239], v67 offset:36864\n"
    "buffer_load_dwordx4 v45, s[56:59], s49 offen offset:3072 sc1 lds\n"
    "s_waitcnt lgkmcnt(8)\n"
    "v_mfma_f32_16x16x32_bf16 v[148:151], a[248:251], a[28:31], v[148:151]\n"
    "ds_read_b64_tr_b16 a[240:241], v68 offset:32768\n"
    "ds_read_b64_tr_b16 a[242:243], v68 offset:36864\n"
    "v_mfma_f32_16x16x32_bf16 v[152:155], a[252:255], a[28:31], v[152:155]\n"
    "ds_read_b64_tr_b16 a[244:245], v69 offset:32768\n"
    "ds_read_b64_tr_b16 a[246:247], v69 offset:36864\n"
    "s_waitcnt vmcnt(57)\n"
    "s_waitcnt lgkmcnt(8)\n"
    "v_mfma_f32_16x16x32_bf16 v[128:131], a[224:227], a[32:35], v[128:131]\n"
    "ds_read_b64_tr_b16 a[248:249], v70 offset:32768\n"
    "ds_read_b64_tr_b16 a[250:251], v70 offset:36864\n"
    "v_mfma_f32_16x16x32_bf16 v[132:135], a[228:231], a[32:35], v[132:135]\n"
    "ds_read_b64_tr_b16 a[252:253], v64 offset:40960\n"
    "ds_read_b64_tr_b16 a[254:255], v64 offset:45056\n"
    "s_waitcnt lgkmcnt(8)\n"
    "v_mfma_f32_16x16x32_bf16 v[136:139], a[232:235], a[32:35], v[136:139]\n"
    "ds_read_b64_tr_b16 a[224:225], v65 offset:40960\n"
    "ds_read_b64_tr_b16 a[226:227], v65 offset:45056\n"
    "v_mfma_f32_16x16x32_bf16 v[140:143], a[236:239], a[32:35], v[140:143]\n"
    "ds_read_b64_tr_b16 a[228:229], v66 offset:40960\n"
    "ds_read_b64_tr_b16 a[230:231], v66 offset:45056\n"
    "s_waitcnt lgkmcnt(8)\n"
    "v_mfma_f32_16x16x32_bf16 v[144:147], a[240:243], a[32:35], v[144:147]\n"
    "ds_read_b64_tr_b16 a[232:233], v67 offset:40960\n"
    "ds_read_b64_tr_b16 a[234:235], v67 offset:45056\n"
    "v_mfma_f32_16x16x32_bf16 v[148:151], a[244:247], a[32:35], v[148:151]\n"
    "ds_read_b64_tr_b16 a[236:237], v68 offset:40960\n"
    "ds_read_b64_tr_b16 a[238:239], v68 offset:45056\n"
    "s_waitcnt lgkmcnt(8)\n"
    "v_mfma_f32_16x16x32_bf16 v[152:155], a[248:251], a[32:35], v[152:155]\n"
    "ds_read_b64_tr_b16 a[240:241], v69 offset:40960\n"
    "ds_read_b64_tr_b16 a[242:243], v69 offset:45056\n"
    "v_mfma_f32_16x16x32_bf16 v[128:131], a[252:255], a[36:39], v[128:131]\n"
    "ds_read_b64_tr_b16 a[244:245], v70 offset:40960\n"
    "ds_read_b64_tr_b16 a[246:247], v70 offset:45056\n"
    "s_waitcnt lgkmcnt(8)\n"
    "s_waitcnt vmcnt(52)\n"
    "s_barrier\n"
    "v_mfma_f32_16x16x32_bf16 v[132:135], a[224:227], a[36:39], v[132:135]\n"
    "ds_read_b64_tr_b16 a[248:249], v78 offset:0\n"
    "ds_read_b64_tr_b16 a[250:251], v78 offset:4096\n"
    "s_add_u32 m0, s50, 81920\n"
    "s_mov_b32 s49, 157696\n"
    "buffer_load_dwordx4 v42, s[56:59], s49 offen offset:0 sc1 lds\n"
    "v_mfma_f32_16x16x32_bf16 v[136:139], a[228:231], a[36:39], v[136:139]\n"
    "ds_read_b64_tr_b16 a[252:253], v79 offset:0\n"
    "ds_read_b64_tr_b16 a[254:255], v79 offset:4096\n"
    "buffer_load_dwordx4 v43, s[56:59], s49 offen offset:1024 sc1 lds\n"
    "s_waitcnt lgkmcnt(8)\n"
    "v_mfma_f32_16x16x32_bf16 v[140:143], a[232:235], a[36:39], v[140:143]\n"
    "ds_read_b64_tr_b16 a[224:225], v80 offset:0\n"
    "ds_read_b64_tr_b16 a[226:227], v80 offset:4096\n"
    "buffer_load_dwordx4 v44, s[56:59], s49 offen offset:2048 sc1 lds\n"
    "v_mfma_f32_16x16x32_bf16 v[144:147], a[236:239], a[36:39], v[144:147]\n"
    "ds_read_b64_tr_b16 a[228:229], v81 offset:0\n"
    "ds_read_b64_tr_b16 a[230:231], v81 offset:4096\n"
    "buffer_load_dwordx4 v45, s[56:59], s49 offen offset:3072 sc1 lds\n"
    "s_waitcnt lgkmcnt(8)\n"
    "v_mfma_f32_16x16x32_bf16 v[148:151], a[240:243], a[36:39], v[148:151]\n"
    "ds_read_b64_tr_b16 a[232:233], v82 offset:0\n"
    "ds_read_b64_tr_b16 a[234:235], v82 offset:4096\n"
    "v_mfma_f32_16x16x32_bf16 v[152:155], a[244:247], a[36:39], v[152:155]\n"
    "ds_read_b64_tr_b16 a[236:237], v83 offset:0\n"
    "ds_read_b64_tr_b16 a[238:239], v83 offset:4096\n"
    "s_waitcnt lgkmcnt(8)\n"
    "s_waitcnt vmcnt(16)\n"
    "s_lshl_b32 s75, %[qb], 2\n"
    "s_lshr_b32 s52, %[head], 2\n"
    "s_add_u32 s75, s75, s52\n"
    "s_lshl_b32 s75, s75, 2\n"
    "s_add_u32 s75, s75, %[w]\n"
    "s_lshl_b32 s75, s75, 6\n"
    "s_and_b32 s52, %[head], 3\n"
    "s_lshl_b32 s52, s52, 3\n"
    "s_add_u32 s75, s75, s52\n"
    "s_add_u32 s75, s75, 16384\n"
    "s_mov_b64 exec, 1\n"
    "buffer_store_dwordx2 v[20:21], off, s[80:83], s75 sc1\n"
    "s_mov_b64 exec, -1\n"
    "v_mfma_f32_16x16x32_bf16 v[128:131], a[248:251], a[40:43], v[128:131]\n"
    "ds_read_b64_tr_b16 a[240:241], v84 offset:0\n"
    "ds_read_b64_tr_b16 a[242:243], v84 offset:4096\n"
    "v_mfma_f32_16x16x32_bf16 v[132:135], a[252:255], a[40:43], v[132:135]\n"
    "ds_read_b64_tr_b16 a[244:245], v78 offset:8192\n"
    "ds_read_b64_tr_b16 a[246:247], v78 offset:12288\n"
    "s_waitcnt lgkmcnt(8)\n"
    "v_mfma_f32_16x16x32_bf16 v[136:139], a[224:227], a[40:43], v[136:139]\n"
    "ds_read_b64_tr_b16 a[248:249], v79 offset:8192\n"
    "ds_read_b64_tr_b16 a[250:251], v79 offset:12288\n"
    "v_mfma_f32_16x16x32_bf16 v[140:143], a[228:231], a[40:43], v[140:143]\n"
    "ds_read_b64_tr_b16 a[252:253], v80 offset:8192\n"
    "ds_read_b64_tr_b16 a[254:255], v80 offset:12288\n"
    "s_waitcnt lgkmcnt(8)\n"
    "v_mfma_f32_16x16x32_bf16 v[144:147], a[232:235], a[40:43], v[144:147]\n"
    "ds_read_b64_tr_b16 a[224:225], v81 offset:8192\n"
    "ds_read_b64_tr_b16 a[226:227], v81 offset:12288\n"
    "v_mfma_f32_16x16x32_bf16 v[148:151], a[236:239], a[40:43], v[148:151]\n"
    "ds_read_b64_tr_b16 a[228:229], v82 offset:8192\n"
    "ds_read_b64_tr_b16 a[230:231], v82 offset:12288\n"
    "s_waitcnt lgkmcnt(8)\n"
    "v_mfma_f32_16x16x32_bf16 v[152:155], a[240:243], a[40:43], v[152:155]\n"
    "ds_read_b64_tr_b16 a[232:233], v83 offset:8192\n"
    "ds_read_b64_tr_b16 a[234:235], v83 offset:12288\n"
    "v_mfma_f32_16x16x32_bf16 v[128:131], a[244:247], a[44:47], v[128:131]\n"
    "ds_read_b64_tr_b16 a[236:237], v84 offset:8192\n"
    "ds_read_b64_tr_b16 a[238:239], v84 offset:12288\n"
    "s_waitcnt lgkmcnt(8)\n"
    "s_barrier\n"
    "v_mfma_f32_16x16x32_bf16 v[132:135], a[248:251], a[44:47], v[132:135]\n"
    "ds_read_b64_tr_b16 a[240:241], v64 offset:49152\n"
    "ds_read_b64_tr_b16 a[242:243], v64 offset:53248\n"
    "s_add_u32 m0, s50, 32768\n"
    "s_mov_b32 s49, 172032\n"
    "buffer_load_dwordx4 v42, s[56:59], s49 offen offset:0 sc1 lds\n"
    "v_mfma_f32_16x16x32_bf16 v[136:139], a[252:255], a[44:47], v[136:139]\n"
    "ds_read_b64_tr_b16 a[244:245], v65 offset:49152\n"
    "ds_read_b64_tr_b16 a[246:247], v65 offset:53248\n"
    "buffer_load_dwordx4 v43, s[56:59], s49 offen offset:1024 sc1 lds\n"
    "s_waitcnt lgkmcnt(8)\n"
    "v_mfma_f32_16x16x32_bf16 v[140:143], a[224:227], a[44:47], v[140:143]\n"
    "ds_read_b64_tr_b16 a[248:249], v66 offset:49152\n"
    "ds_read_b64_tr_b16 a[250:251], v66 offset:53248\n"
    "buffer_load_dwordx4 v44, s[56:59], s49 offen offset:2048 sc1 lds\n"
    "v_mfma_f32_16x16x32_bf16 v[144:147], a[228:231], a[44:47], v[144:147]\n"
    "ds_read_b64_tr_b16 a[252:253], v67 offset:49152\n"
    "ds_read_b64_tr_b16 a[254:255], v67 offset:53248\n"
    "buffer_load_dwordx4 v45, s[56:59], s49 offen offset:3072 sc1 lds\n"
    "s_waitcnt lgkmcnt(8)\n"
    "v_mfma_f32_16x16x32_bf16 v[148:151], a[232:235], a[44:47], v[148:151]\n"
    "ds_read_b64_tr_b16 a[224:225], v68 offset:49152\n"
    "ds_read_b64_tr_b16 a[226:227], v68 offset:53248\n"
    "v_mfma_f32_16x16x32_bf16 v[152:155], a[236:239], a[44:47], v[152:155]\n"
    "ds_read_b64_tr_b16 a[228:229], v69 offset:49152\n"
    "ds_read_b64_tr_b16 a[230:231], v69 offset:53248\n"
    "s_waitcnt lgkmcnt(8)\n"
    "v_mfma_f32_16x16x32_bf16 v[128:131], a[240:243], a[48:51], v[128:131]\n"
    "ds_read_b64_tr_b16 a[232:233], v70 offset:49152\n"
    "ds_read_b64_tr_b16 a[234:235], v70 offset:53248\n"
    "v_mfma_f32_16x16x32_bf16 v[132:135], a[244:247], a[48:51], v[132:135]\n"
    "ds_read_b64_tr_b16 a[236:237], v64 offset:57344\n"
    "ds_read_b64_tr_b16 a[238:239], v64 offset:61440\n"
    "s_waitcnt lgkmcnt(8)\n"
    "v_mfma_f32_16x16x32_bf16 v[136:139], a[248:251], a[48:51], v[136:139]\n"
    "ds_read_b64_tr_b16 a[240:241], v65 offset:57344\n"
    "ds_read_b64_tr_b16 a[242:243], v65 offset:61440\n"
    "v_mfma_f32_16x16x32_bf16 v[140:143], a[252:255], a[48:51], v[140:143]\n"
    "ds_read_b64_tr_b16 a[244:245], v66 offset:57344\n"
    "ds_read_b64_tr_b16 a[246:247], v66 offset:61440\n"
    "s_waitcnt lgkmcnt(8)\n"
    "v_mfma_f32_16x16x32_bf16 v[144:147], a[224:227], a[48:51], v[144:147]\n"
    "ds_read_b64_tr_b16 a[248:249], v67 offset:57344\n"
    "ds_read_b64_tr_b16 a[250:251], v67 offset:61440\n"
    "v_mfma_f32_16x16x32_bf16 v[148:151], a[228:231], a[48:51], v[148:151]\n"
    "ds_read_b64_tr_b16 a[252:253], v68 offset:57344\n"
    "ds_read_b64_tr_b16 a[254:255], v68 offset:61440\n"
    "s_waitcnt lgkmcnt(8)\n"
    "v_mfma_f32_16x16x32_bf16 v[152:155], a[232:235], a[48:51], v[152:155]\n"
    "ds_read_b64_tr_b16 a[224:225], v69 offset:57344\n"
    "ds_read_b64_tr_b16 a[226:227], v69 offset:61440\n"
    "v_mfma_f32_16x16x32_bf16 v[128:131], a[236:239], a[52:55], v[128:131]\n"
    "ds_read_b64_tr_b16 a[228:229], v70 offset:57344\n"
    "ds_read_b64_tr_b16 a[230:231], v70 offset:61440\n"
    "s_waitcnt lgkmcnt(8)\n"
    "s_barrier\n"
    "v_mfma_f32_16x16x32_bf16 v[132:135], a[240:243], a[52:55], v[132:135]\n"
    "ds_read_b64_tr_b16 a[232:233], v78 offset:16384\n"
    "ds_read_b64_tr_b16 a[234:235], v78 offset:20480\n"
    "s_add_u32 m0, s50, 131072\n"
    "s_mov_b32 s49, 186368\n"
    "buffer_load_dwordx4 v42, s[56:59], s49 offen offset:0 sc1 lds\n"
    "v_mfma_f32_16x16x32_bf16 v[136:139], a[244:247], a[52:55], v[136:139]\n"
    "ds_read_b64_tr_b16 a[236:237], v79 offset:16384\n"
    "ds_read_b64_tr_b16 a[238:239], v79 offset:20480\n"
    "buffer_load_dwordx4 v43, s[56:59], s49 offen offset:1024 sc1 lds\n"
    "s_waitcnt lgkmcnt(8)\n"
    "v_mfma_f32_16x16x32_bf16 v[140:143], a[248:251], a[52:55], v[140:143]\n"
    "ds_read_b64_tr_b16 a[240:241], v80 offset:16384\n"
    "ds_read_b64_tr_b16 a[242:243], v80 offset:20480\n"
    "buffer_load_dwordx4 v44, s[56:59], s49 offen offset:2048 sc1 lds\n"
    "v_mfma_f32_16x16x32_bf16 v[144:147], a[252:255], a[52:55], v[144:147]\n"
    "ds_read_b64_tr_b16 a[244:245], v81 offset:16384\n"
    "ds_read_b64_tr_b16 a[246:247], v81 offset:20480\n"
    "buffer_load_dwordx4 v45, s[56:59], s49 offen offset:3072 sc1 lds\n"
    "s_waitcnt lgkmcnt(8)\n"
    "v_mfma_f32_16x16x32_bf16 v[148:151], a[224:227], a[52:55], v[148:151]\n"
    "ds_read_b64_tr_b16 a[248:249], v82 offset:16384\n"
    "ds_read_b64_tr_b16 a[250:251], v82 offset:20480\n"
    "v_mfma_f32_16x16x32_bf16 v[152:155], a[228:231], a[52:55], v[152:155]\n"
    "ds_read_b64_tr_b16 a[252:253], v83 offset:16384\n"
    "ds_read_b64_tr_b16 a[254:255], v83 offset:20480\n"
    "s_waitcnt lgkmcnt(8)\n"
    "v_mfma_f32_16x16x32_bf16 v[128:131], a[232:235], a[56:59], v[128:131]\n"
    "ds_read_b64_tr_b16 a[224:225], v84 offset:16384\n"
    "ds_read_b64_tr_b16 a[226:227], v84 offset:20480\n"
    "v_mfma_f32_16x16x32_bf16 v[132:135], a[236:239], a[56:59], v[132:135]\n"
    "ds_read_b64_tr_b16 a[228:229], v78 offset:24576\n"
    "ds_read_b64_tr_b16 a[230:231], v78 offset:28672\n"
    "s_waitcnt lgkmcnt(8)\n"
    "v_mfma_f32_16x16x32_bf16 v[136:139], a[240:243], a[56:59], v[136:139]\n"
    "ds_read_b64_tr_b16 a[232:233], v79 offset:24576\n"
    "ds_read_b64_tr_b16 a[234:235], v79 offset:28672\n"
    "v_mfma_f32_16x16x32_bf16 v[140:143], a[244:247], a[56:59], v[140:143]\n"
    "ds_read_b64_tr_b16 a[236:237], v80 offset:24576\n"
    "ds_read_b64_tr_b16 a[238:239], v80 offset:28672\n"
    "s_waitcnt lgkmcnt(8)\n"
    "v_mfma_f32_16x16x32_bf16 v[144:147], a[248:251], a[56:59], v[144:147]\n"
    "ds_read_b64_tr_b16 a[240:241], v81 offset:24576\n"
    "ds_read_b64_tr_b16 a[242:243], v81 offset:28672\n"
    "v_mfma_f32_16x16x32_bf16 v[148:151], a[252:255], a[56:59], v[148:151]\n"
    "ds_read_b64_tr_b16 a[244:245], v82 offset:24576\n"
    "ds_read_b64_tr_b16 a[246:247], v82 offset:28672\n"
    "s_waitcnt lgkmcnt(8)\n"
    "v_mfma_f32_16x16x32_bf16 v[152:155], a[224:227], a[56:59], v[152:155]\n"
    "ds_read_b64_tr_b16 a[248:249], v83 offset:24576\n"
    "ds_read_b64_tr_b16 a[250:251], v83 offset:28672\n"
    "v_mfma_f32_16x16x32_bf16 v[128:131], a[228:231], a[60:63], v[128:131]\n"
    "ds_read_b64_tr_b16 a[252:253], v84 offset:24576\n"
    "ds_read_b64_tr_b16 a[254:255], v84 offset:28672\n"
    "s_waitcnt lgkmcnt(8)\n"
    "s_waitcnt vmcnt(21)\n"
    "s_barrier\n"
    "v_mfma_f32_16x16x32_bf16 v[132:135], a[232:235], a[60:63], v[132:135]\n"
    "ds_read_b64_tr_b16 a[224:225], v64 offset:0\n"
    "ds_read_b64_tr_b16 a[226:227], v64 offset:4096\n"
    "s_add_u32 m0, s50, 49152\n"
    "s_add_u32 s49, %[rotk], 0\n"
    "s_and_b32 s49, s49, 15\n"
    "s_mul_i32 s49, s49, 14336\n"
    "buffer_load_dwordx4 v42, s[60:63], s49 offen offset:0 sc1 lds\n"
    "v_mfma_f32_16x16x32_bf16 v[136:139], a[236:239], a[60:63], v[136:139]\n"
    "ds_read_b64_tr_b16 a[228:229], v65 offset:0\n"
    "ds_read_b64_tr_b16 a[230:231], v65 offset:4096\n"
    "buffer_load_dwordx4 v43, s[60:63], s49 offen offset:1024 sc1 lds\n"
    "s_waitcnt lgkmcnt(8)\n"
    "v_mfma_f32_16x16x32_bf16 v[140:143], a[240:243], a[60:63], v[140:143]\n"
    "ds_read_b64_tr_b16 a[232:233], v66 offset:0\n"
    "ds_read_b64_tr_b16 a[234:235], v66 offset:4096\n"
    "buffer_load_dwordx4 v44, s[60:63], s49 offen offset:2048 sc1 lds\n"
    "v_mfma_f32_16x16x32_bf16 v[144:147], a[244:247], a[60:63], v[144:147]\n"
    "ds_read_b64_tr_b16 a[236:237], v67 offset:0\n"
    "ds_read_b64_tr_b16 a[238:239], v67 offset:4096\n"
    "buffer_load_dwordx4 v45, s[60:63], s49 offen offset:3072 sc1 lds\n"
    "s_waitcnt lgkmcnt(8)\n"
    "v_mfma_f32_16x16x32_bf16 v[148:151], a[248:251], a[60:63], v[148:151]\n"
    "ds_read_b64_tr_b16 a[240:241], v68 offset:0\n"
    "ds_read_b64_tr_b16 a[242:243], v68 offset:4096\n"
    "v_mfma_f32_16x16x32_bf16 v[152:155], a[252:255], a[60:63], v[152:155]\n"
    "ds_read_b64_tr_b16 a[244:245], v69 offset:0\n"
    "ds_read_b64_tr_b16 a[246:247], v69 offset:4096\n"
    "s_waitcnt lgkmcnt(8)\n"
    "v_mfma_f32_16x16x32_bf16 v[128:131], a[224:227], a[64:67], v[128:131]\n"
    "ds_read_b64_tr_b16 a[248:249], v70 offset:0\n"
    "ds_read_b64_tr_b16 a[250:251], v70 offset:4096\n"
    "v_mfma_f32_16x16x32_bf16 v[132:135], a[228:231], a[64:67], v[132:135]\n"
    "ds_read_b64_tr_b16 a[252:253], v64 offset:8192\n"
    "ds_read_b64_tr_b16 a[254:255], v64 offset:12288\n"
    "s_waitcnt lgkmcnt(8)\n"
    "v_mfma_f32_16x16x32_bf16 v[136:139], a[232:235], a[64:67], v[136:139]\n"
    "ds_read_b64_tr_b16 a[224:225], v65 offset:8192\n"
    "ds_read_b64_tr_b16 a[226:227], v65 offset:12288\n"
    "v_mfma_f32_16x16x32_bf16 v[140:143], a[236:239], a[64:67], v[140:143]\n"
    "ds_read_b64_tr_b16 a[228:229], v66 offset:8192\n"
    "ds_read_b64_tr_b16 a[230:231], v66 offset:12288\n"
    "s_waitcnt lgkmcnt(8)\n"
    "v_mfma_f32_16x16x32_bf16 v[144:147], a[240:243], a[64:67], v[144:147]\n"
    "ds_read_b64_tr_b16 a[232:233], v67 offset:8192\n"
    "ds_read_b64_tr_b16 a[234:235], v67 offset:12288\n"
    "v_mfma_f32_16x16x32_bf16 v[148:151], a[244:247], a[64:67], v[148:151]\n"
    "ds_read_b64_tr_b16 a[236:237], v68 offset:8192\n"
    "ds_read_b64_tr_b16 a[238:239], v68 offset:12288\n"
    "s_waitcnt lgkmcnt(8)\n"
    "v_mfma_f32_16x16x32_bf16 v[152:155], a[248:251], a[64:67], v[152:155]\n"
    "ds_read_b64_tr_b16 a[240:241], v69 offset:8192\n"
    "ds_read_b64_tr_b16 a[242:243], v69 offset:12288\n"
    "v_mfma_f32_16x16x32_bf16 v[128:131], a[252:255], a[68:71], v[128:131]\n"
    "ds_read_b64_tr_b16 a[244:245], v70 offset:8192\n"
    "ds_read_b64_tr_b16 a[246:247], v70 offset:12288\n"
    "s_waitcnt lgkmcnt(8)\n"
    "s_waitcnt vmcnt(21)\n"
    "s_barrier\n"
    "v_mfma_f32_16x16x32_bf16 v[132:135], a[224:227], a[68:71], v[132:135]\n"
    "ds_read_b64_tr_b16 a[248:249], v64 offset:16384\n"
    "ds_read_b64_tr_b16 a[250:251], v64 offset:20480\n"
    "s_add_u32 m0, s50, 147456\n"
    "s_add_u32 s49, %[rotk], 1\n"
    "s_and_b32 s49, s49, 15\n"
    "s_mul_i32 s49, s49, 14336\n"
    "buffer_load_dwordx4 v42, s[60:63], s49 offen offset:0 sc1 lds\n"
    "v_mfma_f32_16x16x32_bf16 v[136:139], a[228:231], a[68:71], v[136:139]\n"
    "ds_read_b64_tr_b16 a[252:253], v65 offset:16384\n"
    "ds_read_b64_tr_b16 a[254:255], v65 offset:20480\n"
    "buffer_load_dwordx4 v43, s[60:63], s49 offen offset:1024 sc1 lds\n"
    "s_waitcnt lgkmcnt(8)\n"
    "v_mfma_f32_16x16x32_bf16 v[140:143], a[232:235], a[68:71], v[140:143]\n"
    "ds_read_b64_tr_b16 a[224:225], v66 offset:16384\n"
    "ds_read_b64_tr_b16 a[226:227], v66 offset:20480\n"
    "buffer_load_dwordx4 v44, s[60:63], s49 offen offset:2048 sc1 lds\n"
    "v_mfma_f32_16x16x32_bf16 v[144:147], a[236:239], a[68:71], v[144:147]\n"
    "ds_read_b64_tr_b16 a[228:229], v67 offset:16384\n"
    "ds_read_b64_tr_b16 a[230:231], v67 offset:20480\n"
    "buffer_load_dwordx4 v45, s[60:63], s49 offen offset:3072 sc1 lds\n"
    "s_waitcnt lgkmcnt(8)\n"
    "v_mfma_f32_16x16x32_bf16 v[148:151], a[240:243], a[68:71], v[148:151]\n"
    "ds_read_b64_tr_b16 a[232:233], v68 offset:16384\n"
    "ds_read_b64_tr_b16 a[234:235], v68 offset:20480\n"
    "v_mfma_f32_16x16x32_bf16 v[152:155], a[244:247], a[68:71], v[152:155]\n"
    "ds_read_b64_tr_b16 a[236:237], v69 offset:16384\n"
    "ds_read_b64_tr_b16 a[238:239], v69 offset:20480\n"
    "s_waitcnt lgkmcnt(8)\n"
    "v_mfma_f32_16x16x32_bf16 v[128:131], a[248:251], a[72:75], v[128:131]\n"
    "ds_read_b64_tr_b16 a[240:241], v70 offset:16384\n"
    "ds_read_b64_tr_b16 a[242:243], v70 offset:20480\n"
    "v_mfma_f32_16x16x32_bf16 v[132:135], a[252:255], a[72:75], v[132:135]\n"
    "ds_read_b64_tr_b16 a[244:245], v64 offset:24576\n"
    "ds_read_b64_tr_b16 a[246:247], v64 offset:28672\n"
    "s_waitcnt lgkmcnt(8)\n"
    "v_mfma_f32_16x16x32_bf16 v[136:139], a[224:227], a[72:75], v[136:139]\n"
    "ds_read_b64_tr_b16 a[248:249], v65 offset:24576\n"
    "ds_read_b64_tr_b16 a[250:251], v65 offset:28672\n"
    "v_mfma_f32_16x16x32_bf16 v[140:143], a[228:231], a[72:75], v[140:143]\n"
    "ds_read_b64_tr_b16 a[252:253], v66 offset:24576\n"
    "ds_read_b64_tr_b16 a[254:255], v66 offset:28672\n"
    "s_waitcnt lgkmcnt(8)\n"
    "v_mfma_f32_16x16x32_bf16 v[144:147], a[232:235], a[72:75], v[144:147]\n"
    "ds_read_b64_tr_b16 a[224:225], v67 offset:24576\n"
    "ds_read_b64_tr_b16 a[226:227], v67 offset:28672\n"
    "v_mfma_f32_16x16x32_bf16 v[148:151], a[236:239], a[72:75], v[148:151]\n"
    "ds_read_b64_tr_b16 a[228:229], v68 offset:24576\n"
    "ds_read_b64_tr_b16 a[230:231], v68 offset:28672\n"
    "s_waitcnt lgkmcnt(8)\n"
    "v_mfma_f32_16x16x32_bf16 v[152:155], a[240:243], a[72:75], v[152:155]\n"
    "ds_read_b64_tr_b16 a[232:233], v69 offset:24576\n"
    "ds_read_b64_tr_b16 a[234:235], v69 offset:28672\n"
    "v_mfma_f32_16x16x32_bf16 v[128:131], a[244:247], a[76:79], v[128:131]\n"
    "ds_read_b64_tr_b16 a[236:237], v70 offset:24576\n"
    "ds_read_b64_tr_b16 a[238:239], v70 offset:28672\n"
    "s_waitcnt lgkmcnt(8)\n"
    "s_waitcnt vmcnt(21)\n"
    "s_barrier\n"
    "v_mfma_f32_16x16x32_bf16 v[132:135], a[248:251], a[76:79], v[132:135]\n"
    "ds_read_b64_tr_b16 a[240:241], v71 offset:0\n"
    "ds_read_b64_tr_b16 a[242:243], v71 offset:4096\n"
    "s_add_u32 m0, s50, 0\n"
    "s_add_u32 s49, %[rotk], 2\n"
    "s_and_b32 s49, s49, 15\n"
    "s_mul_i32 s49, s49, 14336\n"
    "buffer_load_dwordx4 v42, s[60:63], s49 offen offset:0 sc1 lds\n"
    "v_mfma_f32_16x16x32_bf16 v[136:139], a[252:255], a[76:79], v[136:139]\n"
    "ds_read_b64_tr_b16 a[244:245], v72 offset:0\n"
    "ds_read_b64_tr_b16 a[246:247], v72 offset:4096\n"
    "buffer_load_dwordx4 v43, s[60:63], s49 offen offset:1024 sc1 lds\n"
    "s_waitcnt lgkmcnt(8)\n"
    "v_mfma_f32_16x16x32_bf16 v[140:143], a[224:227], a[76:79], v[140:143]\n"
    "ds_read_b64_tr_b16 a[248:249], v73 offset:0\n"
    "ds_read_b64_tr_b16 a[250:251], v73 offset:4096\n"
    "buffer_load_dwordx4 v44, s[60:63], s49 offen offset:2048 sc1 lds\n"
    "v_mfma_f32_16x16x32_bf16 v[144:147], a[228:231], a[76:79], v[144:147]\n"
    "ds_read_b64_tr_b16 a[252:253], v74 offset:0\n"
    "ds_read_b64_tr_b16 a[254:255], v74 offset:4096\n"
    "buffer_load_dwordx4 v45, s[60:63], s49 offen offset:3072 sc1 lds\n"
    "s_waitcnt lgkmcnt(8)\n"
    "v_mfma_f32_16x16x32_bf16 v[148:151], a[232:235], a[76:79], v[148:151]\n"
    "ds_read_b64_tr_b16 a[224:225], v75 offset:0\n"
    "ds_read_b64_tr_b16 a[226:227], v75 offset:4096\n"
    "v_mfma_f32_16x16x32_bf16 v[152:155], a[236:239], a[76:79], v[152:155]\n"
    "ds_read_b64_tr_b16 a[228:229], v76 offset:0\n"
    "ds_read_b64_tr_b16 a[230:231], v76 offset:4096\n"
    "s_waitcnt lgkmcnt(8)\n"
    "s_lshl_b32 s75, %[tp2], 2\n"
    "s_add_u32 s75, s75, %[kq]\n"
    "s_lshl_b32 s75, s75, 2\n"
    "s_add_u32 s75, s75, %[w]\n"
    "s_lshl_b32 s75, s75, 6\n"
    "s_add_u32 s75, s75, 16384\n"
    "buffer_load_dwordx2 v[46:47], v22, s[80:83], s75 offen sc1\n"
    "v_mfma_f32_16x16x32_bf16 v[128:131], a[240:243], a[80:83], v[128:131]\n"
    "ds_read_b64_tr_b16 a[232:233], v77 offset:0\n"
    "ds_read_b64_tr_b16 a[234:235], v77 offset:4096\n"
    "v_mfma_f32_16x16x32_bf16 v[132:135], a[244:247], a[80:83], v[132:135]\n"
    "ds_read_b64_tr_b16 a[236:237], v71 offset:8192\n"
    "ds_read_b64_tr_b16 a[238:239], v71 offset:12288\n"
    "s_waitcnt lgkmcnt(8)\n"
    "v_mfma_f32_16x16x32_bf16 v[136:139], a[248:251], a[80:83], v[136:139]\n"
    "ds_read_b64_tr_b16 a[240:241], v72 offset:8192\n"
    "ds_read_b64_tr_b16 a[242:243], v72 offset:12288\n"
    "v_mfma_f32_16x16x32_bf16 v[140:143], a[252:255], a[80:83], v[140:143]\n"
    "ds_read_b64_tr_b16 a[244:245], v73 offset:8192\n"
    "ds_read_b64_tr_b16 a[246:247], v73 offset:12288\n"
    "s_waitcnt lgkmcnt(8)\n"
    "v_mfma_f32_16x16x32_bf16 v[144:147], a[224:227], a[80:83], v[144:147]\n"
    "ds_read_b64_tr_b16 a[248:249], v74 offset:8192\n"
    "ds_read_b64_tr_b16 a[250:251], v74 offset:12288\n"
    "v_mfma_f32_16x16x32_bf16 v[148:151], a[228:231], a[80:83], v[148:151]\n"
    "ds_read_b64_tr_b16 a[252:253], v75 offset:8192\n"
    "ds_read_b64_tr_b16 a[254:255], v75 offset:12288\n"
    "s_waitcnt lgkmcnt(8)\n"
    "v_mfma_f32_16x16x32_bf16 v[152:155], a[232:235], a[80:83], v[152:155]\n"
    "ds_read_b64_tr_b16 a[224:225], v76 offset:8192\n"
    "ds_read_b64_tr_b16 a[226:227], v76 offset:12288\n"
    "v_mfma_f32_16x16x32_bf16 v[128:131], a[236:239], a[84:87], v[128:131]\n"
    "ds_read_b64_tr_b16 a[228:229], v77 offset:8192\n"
    "ds_read_b64_tr_b16 a[230:231], v77 offset:12288\n"
    "s_waitcnt lgkmcnt(8)\n"
    "s_waitcnt vmcnt(22)\n"
    "s_barrier\n"
    "v_mfma_f32_16x16x32_bf16 v[132:135], a[240:243], a[84:87], v[132:135]\n"
    "ds_read_b64_tr_b16 a[232:233], v71 offset:16384\n"
    "ds_read_b64_tr_b16 a[234:235], v71 offset:20480\n"
    "s_add_u32 m0, s50, 16384\n"
    "s_add_u32 s49, %[rotk], 3\n"
    "s_and_b32 s49, s49, 15\n"
    "s_mul_i32 s49, s49, 14336\n"
    "buffer_load_dwordx4 v42, s[60:63], s49 offen offset:0 sc1 lds\n"
    "v_mfma_f32_16x16x32_bf16 v[136:139], a[244:247], a[84:87], v[136:139]\n"
    "ds_read_b64_tr_b16 a[236:237], v72 offset:16384\n"
    "ds_read_b64_tr_b16 a[238:239], v72 offset:20480\n"
    "buffer_load_dwordx4 v43, s[60:63], s49 offen offset:1024 sc1 lds\n"
    "s_waitcnt lgkmcnt(8)\n"
    "v_mfma_f32_16x16x32_bf16 v[140:143], a[248:251], a[84:87], v[140:143]\n"
    "ds_read_b64_tr_b16 a[240:241], v73 offset:16384\n"
    "ds_read_b64_tr_b16 a[242:243], v73 offset:20480\n"
    "buffer_load_dwordx4 v44, s[60:63], s49 offen offset:2048 sc1 lds\n"
    "v_mfma_f32_16x16x32_bf16 v[144:147], a[252:255], a[84:87], v[144:147]\n"
    "ds_read_b64_tr_b16 a[244:245], v74 offset:16384\n"
    "ds_read_b64_tr_b16 a[246:247], v74 offset:20480\n"
    "buffer_load_dwordx4 v45, s[60:63], s49 offen offset:3072 sc1 lds\n"
    "s_waitcnt lgkmcnt(8)\n"
    "v_mfma_f32_16x16x32_bf16 v[148:151], a[224:227], a[84:87], v[148:151]\n"
    "ds_read_b64_tr_b16 a[248:249], v75 offset:16384\n"
    "ds_read_b64_tr_b16 a[250:251], v75 offset:20480\n"
    "v_mfma_f32_16x16x32_bf16 v[152:155], a[228:231], a[84:87], v[152:155]\n"
    "ds_read_b64_tr_b16 a[252:253], v76 offset:16384\n"
    "ds_read_b64_tr_b16 a[254:255], v76 offset:20480\n"
    "s_waitcnt lgkmcnt(8)\n"
    "v_mfma_f32_16x16x32_bf16 v[128:131], a[232:235], a[88:91], v[128:131]\n"
    "ds_read_b64_tr_b16 a[224:225], v77 offset:16384\n"
    "ds_read_b64_tr_b16 a[226:227], v77 offset:20480\n"
    "v_mfma_f32_16x16x32_bf16 v[132:135], a[236:239], a[88:91], v[132:135]\n"
    "ds_read_b64_tr_b16 a[228:229], v71 offset:24576\n"
    "ds_read_b64_tr_b16 a[230:231], v71 offset:28672\n"
    "s_waitcnt lgkmcnt(8)\n"
    "v_mfma_f32_16x16x32_bf16 v[136:139], a[240:243], a[88:91], v[136:139]\n"
    "ds_read_b64_tr_b16 a[232:233], v72 offset:24576\n"
    "ds_read_b64_tr_b16 a[234:235], v72 offset:28672\n"
    "v_mfma_f32_16x16x32_bf16 v[140:143], a[244:247], a[88:91], v[140:143]\n"
    "ds_read_b64_tr_b16 a[236:237], v73 offset:24576\n"
    "ds_read_b64_tr_b16 a[238:239], v73 offset:28672\n"
    "s_waitcnt lgkmcnt(8)\n"
    "v_mfma_f32_16x16x32_bf16 v[144:147], a[248:251], a[88:91], v[144:147]\n"
    "ds_read_b64_tr_b16 a[240:241], v74 offset:24576\n"
    "ds_read_b64_tr_b16 a[242:243], v74 offset:28672\n"
    "v_mfma_f32_16x16x32_bf16 v[148:151], a[252:255], a[88:91], v[148:151]\n"
    "ds_read_b64_tr_b16 a[244:245], v75 offset:24576\n"
    "ds_read_b64_tr_b16 a[246:247], v75 offset:28672\n"
    "s_waitcnt lgkmcnt(8)\n"
    "v_mfma_f32_16x16x32_bf16 v[152:155], a[224:227], a[88:91], v[152:155]\n"
    "ds_read_b64_tr_b16 a[248:249], v76 offset:24576\n"
    "ds_read_b64_tr_b16 a[250:251], v76 offset:28672\n"
    "v_mfma_f32_16x16x32_bf16 v[128:131], a[228:231], a[92:95], v[128:131]\n"
    "ds_read_b64_tr_b16 a[252:253], v77 offset:24576\n"
    "ds_read_b64_tr_b16 a[254:255], v77 offset:28672\n"
    "s_waitcnt lgkmcnt(8)\n"
    "s_waitcnt vmcnt(4)\n"
    "v_cmp_eq_u64 s[52:53], v[46:47], v[20:21]\n"
    "s_or_b32 s52, s52, 4294967280\n"
    "s_mov_b32 s53, -1\n"
    "s_cmp_eq_u64 s[52:53], -1\n"
    "s_cbranch_scc1 .Lt6_%=\n"
    "s_mov_b32 s38, 0\n"
    ".Lt7_%=:\n"
    "s_sleep 2\n"
    "buffer_load_dwordx2 v[46:47], v22, s[80:83], s75 offen sc1\n"
    "s_waitcnt vmcnt(0)\n"
    "v_cmp_eq_u64 s[52:53], v[46:47], v[20:21]\n"
    "s_or_b32 s52, s52, 4294967280\n"
    "s_mov_b32 s53, -1\n"
    "s_cmp_eq_u64 s[52:53], -1\n"
    "s_cbranch_scc1 .Lt6_%=\n"
    "s_add_u32 s38, s38, 1\n"
    "s_cmp_lt_u32 s38, 0x200000\n"
    "s_cbranch_scc1 .Lt7_%=\n"
    ".Lt6_%=:\n"
    "s_barrier\n"
    "v_mfma_f32_16x16x32_bf16 v[132:135], a[232:235], a[92:95], v[132:135]\n"
    "ds_read_b64_tr_b16 a[224:225], v64 offset:32768\n"
    "ds_read_b64_tr_b16 a[226:227], v64 offset:36864\n"
    "s_add_u32 s76, %[rotk], 0\n"
    "s_and_b32 s76, s76, 15\n"
    "s_lshl_b32 s76, s76, 11\n"
    "buffer_load_dwordx4 %[q0], v104, s[40:43], s76 offen offset:0 sc1\n"
    "v_mfma_f32_16x16x32_bf16 v[136:139], a[236:239], a[92:95], v[136:139]\n"
    "ds_read_b64_tr_b16 a[228:229], v65 offset:32768\n"
    "ds_read_b64_tr_b16 a[230:231], v65 offset:36864\n"
    "buffer_load_dwordx4 %[q2], v104, s[40:43], s76 offen offset:1024 sc1\n"
    "s_waitcnt lgkmcnt(8)\n"
    "v_mfma_f32_16x16x32_bf16 v[140:143], a[240:243], a[92:95], v[140:143]\n"
    "ds_read_b64_tr_b16 a[232:233], v66 offset:32768\n"
    "ds_read_b64_tr_b16 a[234:235], v66 offset:36864\n"
    "buffer_load_dwordx4 %[q1], v105, s[40:43], s76 offen offset:0 sc1\n"
    "v_mfma_f32_16x16x32_bf16 v[144:147], a[244:247], a[92:95], v[144:147]\n"
    "ds_read_b64_tr_b16 a[236:237], v67 offset:32768\n"
    "ds_read_b64_tr_b16 a[238:239], v67 offset:36864\n"
    "buffer_load_dwordx4 %[q3], v105, s[40:43], s76 offen offset:1024 sc1\n"
    "s_waitcnt lgkmcnt(8)\n"
    "v_mfma_f32_16x16x32_bf16 v[148:151], a[248:251], a[92:95], v[148:151]\n"
    "ds_read_b64_tr_b16 a[240:241], v68 offset:32768\n"
    "ds_read_b64_tr_b16 a[242:243], v68 offset:36864\n"
    "s_add_u32 m0, s50, 65536\n"
    "s_add_u32 s49, %[rotk], 4\n"
    "s_and_b32 s49, s49, 15\n"
    "s_mul_i32 s49, s49, 14336\n"
    "buffer_load_dwordx4 v42, s[60:63], s49 offen offset:0 sc1 lds\n"
    "v_mfma_f32_16x16x32_bf16 v[152:155], a[252:255], a[92:95], v[152:155]\n"
    "ds_read_b64_tr_b16 a[244:245], v69 offset:32768\n"
    "ds_read_b64_tr_b16 a[246:247], v69 offset:36864\n"
    "buffer_load_dwordx4 v43, s[60:63], s49 offen offset:1024 sc1 lds\n"
    "s_waitcnt lgkmcnt(8)\n"
    "v_mfma_f32_16x16x32_bf16 v[128:131], a[224:227], a[96:99], v[128:131]\n"
    "ds_read_b64_tr_b16 a[248:249], v70 offset:32768\n"
    "ds_read_b64_tr_b16 a[250:251], v70 offset:36864\n"
    "buffer_load_dwordx4 v44, s[60:63], s49 offen offset:2048 sc1 lds\n"
    "v_mfma_f32_16x16x32_bf16 v[132:135], a[228:231], a[96:99], v[132:135]\n"
    "ds_read_b64_tr_b16 a[252:253], v64 offset:40960\n"
    "ds_read_b64_tr_b16 a[254:255], v64 offset:45056\n"
    "buffer_load_dwordx4 v45, s[60:63], s49 offen offset:3072 sc1 lds\n"
    "s_waitcnt lgkmcnt(8)\n"
    "v_mfma_f32_16x16x32_bf16 v[136:139], a[232:235], a[96:99], v[136:139]\n"
    "ds_read_b64_tr_b16 a[224:225], v65 offset:40960\n"
    "ds_read_b64_tr_b16 a[226:227], v65 offset:45056\n"
    "v_mfma_f32_16x16x32_bf16 v[140:143], a[236:239], a[96:99], v[140:143]\n"
    "ds_read_b64_tr_b16 a[228:229], v66 offset:40960\n"
    "ds_read_b64_tr_b16 a[230:231], v66 offset:45056\n"
    "s_waitcnt lgkmcnt(8)\n"
    "v_mfma_f32_16x16x32_bf16 v[144:147], a[240:243], a[96:99], v[144:147]\n"
    "ds_read_b64_tr_b16 a[232:233], v67 offset:40960\n"
    "ds_read_b64_tr_b16 a[234:235], v67 offset:45056\n"
    "v_mfma_f32_16x16x32_bf16 v[148:151], a[244:247], a[96:99], v[148:151]\n"
    "ds_read_b64_tr_b16 a[236:237], v68 offset:40960\n"
    "ds_read_b64_tr_b16 a[238:239], v68 offset:45056\n"
    "s_waitcnt lgkmcnt(8)\n"
    "v_mfma_f32_16x16x32_bf16 v[152:155], a[248:251], a[96:99], v[152:155]\n"
    "ds_read_b64_tr_b16 a[240:241], v69 offset:40960\n"
    "ds_read_b64_tr_b16 a[242:243], v69 offset:45056\n"
    "v_mfma_f32_16x16x32_bf16 v[128:131], a[252:255], a[100:103], v[128:131]\n"
    "ds_read_b64_tr_b16 a[244:245], v70 offset:40960\n"
    "ds_read_b64_tr_b16 a[246:247], v70 offset:45056\n"
    "s_waitcnt lgkmcnt(8)\n"
    "s_barrier\n"
    "v_mfma_f32_16x16x32_bf16 v[132:135], a[224:227], a[100:103], v[132:135]\n"
    "ds_read_b64_tr_b16 a[248:249], v78 offset:0\n"
    "ds_read_b64_tr_b16 a[250:251], v78 offset:4096\n"
    "s_add_u32 s76, %[rotk], 1\n"
    "s_and_b32 s76, s76, 15\n"
    "s_lshl_b32 s76, s76, 11\n"
    "buffer_load_dwordx4 %[q4], v104, s[40:43], s76 offen offset:0 sc1\n"
    "v_mfma_f32_16x16x32_bf16 v[136:139], a[228:231], a[100:103], v[136:139]\n"
    "ds_read_b64_tr_b16 a[252:253], v79 offset:0\n"
    "ds_read_b64_tr_b16 a[254:255], v79 offset:4096\n"
    "buffer_load_dwordx4 %[q6], v104, s[40:43], s76 offen offset:1024 sc1\n"
    "s_waitcnt lgkmcnt(8)\n"
    "v_mfma_f32_16x16x32_bf16 v[140:143], a[232:235], a[100:103], v[140:143]\n"
    "ds_read_b64_tr_b16 a[224:225], v80 offset:0\n"
    "ds_read_b64_tr_b16 a[226:227], v80 offset:4096\n"
    "buffer_load_dwordx4 %[q5], v105, s[40:43], s76 offen offset:0 sc1\n"
    "v_mfma_f32_16x16x32_bf16 v[144:147], a[236:239], a[100:103], v[144:147]\n"
    "ds_read_b64_tr_b16 a[228:229], v81 offset:0\n"
    "ds_read_b64_tr_b16 a[230:231], v81 offset:4096\n"
    "buffer_load_dwordx4 %[q7], v105, s[40:43], s76 offen offset:1024 sc1\n"
    "s_waitcnt lgkmcnt(8)\n"
    "v_mfma_f32_16x16x32_bf16 v[148:151], a[240:243], a[100:103], v[148:151]\n"
    "ds_read_b64_tr_b16 a[232:233], v82 offset:0\n"
    "ds_read_b64_tr_b16 a[234:235], v82 offset:4096\n"
    "s_add_u32 m0, s50, 81920\n"
    "s_add_u32 s49, %[rotk], 5\n"
    "s_and_b32 s49, s49, 15\n"
    "s_mul_i32 s49, s49, 14336\n"
    "buffer_load_dwordx4 v42, s[60:63], s49 offen offset:0 sc1 lds\n"
    "v_mfma_f32_16x16x32_bf16 v[152:155], a[244:247], a[100:103], v[152:155]\n"
    "ds_read_b64_tr_b16 a[236:237], v83 offset:0\n"
    "ds_read_b64_tr_b16 a[238:239], v83 offset:4096\n"
    "buffer_load_dwordx4 v43, s[60:63], s49 offen offset:1024 sc1 lds\n"
    "s_waitcnt lgkmcnt(8)\n"
    "v_mfma_f32_16x16x32_bf16 v[128:131], a[248:251], a[104:107], v[128:131]\n"
    "ds_read_b64_tr_b16 a[240:241], v84 offset:0\n"
    "ds_read_b64_tr_b16 a[242:243], v84 offset:4096\n"
    "buffer_load_dwordx4 v44, s[60:63], s49 offen offset:2048 sc1 lds\n"
    "v_mfma_f32_16x16x32_bf16 v[132:135], a[252:255], a[104:107], v[132:135]\n"
    "ds_read_b64_tr_b16 a[244:245], v78 offset:8192\n"
    "ds_read_b64_tr_b16 a[246:247], v78 offset:12288\n"
    "buffer_load_dwordx4 v45, s[60:63], s49 offen offset:3072 sc1 lds\n"
    "s_waitcnt lgkmcnt(8)\n"
    "v_mfma_f32_16x16x32_bf16 v[136:139], a[224:227], a[104:107], v[136:139]\n"
    "ds_read_b64_tr_b16 a[248:249], v79 offset:8192\n"
    "ds_read_b64_tr_b16 a[250:251], v79 offset:12288\n"
    "v_mfma_f32_16x16x32_bf16 v[140:143], a[228:231], a[104:107], v[140:143]\n"
    "ds_read_b64_tr_b16 a[252:253], v80 offset:8192\n"
    "ds_read_b64_tr_b16 a[254:255], v80 offset:12288\n"
    "s_waitcnt lgkmcnt(8)\n"
    "v_mfma_f32_16x16x32_bf16 v[144:147], a[232:235], a[104:107], v[144:147]\n"
    "ds_read_b64_tr_b16 a[224:225], v81 offset:8192\n"
    "ds_read_b64_tr_b16 a[226:227], v81 offset:12288\n"
    "v_mfma_f32_16x16x32_bf16 v[148:151], a[236:239], a[104:107], v[148:151]\n"
    "ds_read_b64_tr_b16 a[228:229], v82 offset:8192\n"
    "ds_read_b64_tr_b16 a[230:231], v82 offset:12288\n"
    "s_waitcnt lgkmcnt(8)\n"
    "v_mfma_f32_16x16x32_bf16 v[152:155], a[240:243], a[104:107], v[152:155]\n"
    "ds_read_b64_tr_b16 a[232:233], v83 offset:8192\n"
    "ds_read_b64_tr_b16 a[234:235], v83 offset:12288\n"
    "v_mfma_f32_16x16x32_bf16 v[128:131], a[244:247], a[108:111], v[128:131]\n"
    "ds_read_b64_tr_b16 a[236:237], v84 offset:8192\n"
    "ds_read_b64_tr_b16 a[238:239], v84 offset:12288\n"
    "s_waitcnt lgkmcnt(8)\n"
    "s_barrier\n"
    "v_mfma_f32_16x16x32_bf16 v[132:135], a[248:251], a[108:111], v[132:135]\n"
    "ds_read_b64_tr_b16 a[240:241], v64 offset:49152\n"
    "ds_read_b64_tr_b16 a[242:243], v64 offset:53248\n"
    "s_add_u32 s76, %[rotk], 2\n"
    "s_and_b32 s76, s76, 15\n"
    "s_lshl_b32 s76, s76, 11\n"
    "buffer_load_dwordx4 %[q8], v104, s[40:43], s76 offen offset:0 sc1\n"
    "v_mfma_f32_16x16x32_bf16 v[136:139], a[252:255], a[108:111], v[136:139]\n"
    "ds_read_b64_tr_b16 a[244:245], v65 offset:49152\n"
    "ds_read_b64_tr_b16 a[246:247], v65 offset:53248\n"
    "buffer_load_dwordx4 %[q10], v104, s[40:43], s76 offen offset:1024 sc1\n"
    "s_waitcnt lgkmcnt(8)\n"
    "v_mfma_f32_16x16x32_bf16 v[140:143], a[224:227], a[108:111], v[140:143]\n"
    "ds_read_b64_tr_b16 a[248:249], v66 offset:49152\n"
    "ds_read_b64_tr_b16 a[250:251], v66 offset:53248\n"
    "buffer_load_dwordx4 %[q9], v105, s[40:43], s76 offen offset:0 sc1\n"
    "v_mfma_f32_16x16x32_bf16 v[144:147], a[228:231], a[108:111], v[144:147]\n"
    "ds_read_b64_tr_b16 a[252:253], v67 offset:49152\n"
    "ds_read_b64_tr_b16 a[254:255], v67 offset:53248\n"
    "buffer_load_dwordx4 %[q11], v105, s[40:43], s76 offen offset:1024 sc1\n"
    "s_waitcnt lgkmcnt(8)\n"
    "v_mfma_f32_16x16x32_bf16 v[148:151], a[232:235], a[108:111], v[148:151]\n"
    "ds_read_b64_tr_b16 a[224:225], v68 offset:49152\n"
    "ds_read_b64_tr_b16 a[226:227], v68 offset:53248\n"
    "s_add_u32 m0, s50, 32768\n"
    "s_add_u32 s49, %[rotk], 6\n"
    "s_and_b32 s49, s49, 15\n"
    "s_mul_i32 s49, s49, 14336\n"
    "buffer_load_dwordx4 v42, s[60:63], s49 offen offset:0 sc1 lds\n"
    "v_mfma_f32_16x16x32_bf16 v[152:155], a[236:239], a[108:111], v[152:155]\n"
    "ds_read_b64_tr_b16 a[228:229], v69 offset:49152\n"
    "ds_read_b64_tr_b16 a[230:231], v69 offset:53248\n"
    "buffer_load_dwordx4 v43, s[60:63], s49 offen offset:1024 sc1 lds\n"
    "s_waitcnt vmcnt(18)\n"
    "s_waitcnt lgkmcnt(8)\n"
    "s_mul_i32 s52, %[w], 7168\n"
    "s_add_u32 s52, s52, %[ldsb]\n"
    "s_add_u32 s52, s52, 98304\n"
    "v_add_u32 v86, s52, v33\n"
    "v_mfma_f32_16x16x32_bf16 v[184:187], a[240:243], %[q0], 0\n"
    "ds_read_b64_tr_b16 a[232:233], v70 offset:49152\n"
    "v_mfma_f32_16x16x32_bf16 v[188:191], a[240:243], %[q1], 0\n"
    "ds_read_b64_tr_b16 a[234:235], v70 offset:53248\n"
    "buffer_load_dwordx4 v44, s[60:63], s49 offen offset:2048 sc1 lds\n"
    "v_mfma_f32_16x16x32_bf16 v[192:195], a[244:247], %[q0], 0\n"
    "ds_read_b64_tr_b16 a[236:237], v64 offset:57344\n"
    "v_mfma_f32_16x16x32_bf16 v[196:199], a[244:247], %[q1], 0\n"
    "ds_read_b64_tr_b16 a[238:239], v64 offset:61440\n"
    "buffer_load_dwordx4 v45, s[60:63], s49 offen offset:3072 sc1 lds\n"
    "s_waitcnt lgkmcnt(8)\n"
    "v_mfma_f32_16x16x32_bf16 v[200:203], a[248:251], %[q0], 0\n"
    "ds_read_b64_tr_b16 a[240:241], v65 offset:57344\n"
    "v_mfma_f32_16x16x32_bf16 v[204:207], a[248:251], %[q1], 0\n"
    "ds_read_b64_tr_b16 a[242:243], v65 offset:61440\n"
    "v_mfma_f32_16x16x32_bf16 v[208:211], a[252:255], %[q0], 0\n"
    "ds_read_b64_tr_b16 a[244:245], v66 offset:57344\n"
    "ds_write_b128 v86, v[128:131] offset:0\n"
    "v_mfma_f32_16x16x32_bf16 v[212:215], a[252:255], %[q1], 0\n"
    "ds_read_b64_tr_b16 a[246:247], v66 offset:61440\n"
    "ds_write_b128 v86, v[132:135] offset:1024\n"
    "s_waitcnt lgkmcnt(10)\n"
    "v_mfma_f32_16x16x32_bf16 v[216:219], a[224:227], %[q0], 0\n"
    "ds_read_b64_tr_b16 a[248:249], v67 offset:57344\n"
    "ds_write_b128 v86, v[136:139] offset:2048\n"
    "v_mfma_f32_16x16x32_bf16 v[220:223], a[224:227], %[q1], 0\n"
    "ds_read_b64_tr_b16 a[250:251], v67 offset:61440\n"
    "ds_write_b128 v86, v[140:143] offset:3072\n"
    "v_mfma_f32_16x16x32_bf16 v[224:227], a[228:231], %[q0], 0\n"
    "ds_read_b64_tr_b16 a[252:253], v68 offset:57344\n"
    "ds_write_b128 v86, v[144:147] offset:4096\n"
    "v_mfma_f32_16x16x32_bf16 v[228:231], a[228:231], %[q1], 0\n"
    "ds_read_b64_tr_b16 a[254:255], v68 offset:61440\n"
    "ds_write_b128 v86, v[148:151] offset:5120\n"
    "s_waitcnt lgkmcnt(14)\n"
    "v_mfma_f32_16x16x32_bf16 v[232:235], a[232:235], %[q0], 0\n"
    "ds_read_b64_tr_b16 a[224:225], v69 offset:57344\n"
    "ds_write_b128 v86, v[152:155] offset:6144\n"
    "v_mfma_f32_16x16x32_bf16 v[236:239], a[232:235], %[q1], 0\n"
    "ds_read_b64_tr_b16 a[226:227], v69 offset:61440\n"
    "v_mfma_f32_16x16x32_bf16 v[184:187], a[236:239], %[q2], v[184:187]\n"
    "ds_read_b64_tr_b16 a[228:229], v70 offset:57344\n"
    "v_mfma_f32_16x16x32_bf16 v[188:191], a[236:239], %[q3], v[188:191]\n"
    "ds_read_b64_tr_b16 a[230:231], v70 offset:61440\n"
    "s_waitcnt lgkmcnt(14)\n"
    "s_barrier\n"
    "v_mfma_f32_16x16x32_bf16 v[192:195], a[240:243], %[q2], v[192:195]\n"
    "ds_read_b64_tr_b16 a[232:233], v78 offset:16384\n"
    "v_mfma_f32_16x16x32_bf16 v[196:199], a[240:243], %[q3], v[196:199]\n"
    "ds_read_b64_tr_b16 a[234:235], v78 offset:20480\n"
    "s_add_u32 s76, %[rotk], 3\n"
    "s_and_b32 s76, s76, 15\n"
    "s_lshl_b32 s76, s76, 11\n"
    "buffer_load_dwordx4 %[q12], v104, s[40:43], s76 offen offset:0 sc1\n"
    "v_mfma_f32_16x16x32_bf16 v[200:203], a[244:247], %[q2], v[200:203]\n"
    "ds_read_b64_tr_b16 a[236:237], v79 offset:16384\n"
    "v_mfma_f32_16x16x32_bf16 v[204:207], a[244:247], %[q3], v[204:207]\n"
    "ds_read_b64_tr_b16 a[238:239], v79 offset:20480\n"
    "buffer_load_dwordx4 %[q14], v104, s[40:43], s76 offen offset:1024 sc1\n"
    "s_waitcnt lgkmcnt(10)\n"
    "v_mfma_f32_16x16x32_bf16 v[208:211], a[248:251], %[q2], v[208:211]\n"
    "ds_read_b64_tr_b16 a[240:241], v80 offset:16384\n"
    "v_mfma_f32_16x16x32_bf16 v[212:215], a[248:251], %[q3], v[212:215]\n"
    "ds_read_b64_tr_b16 a[242:243], v80 offset:20480\n"
    "buffer_load_dwordx4 %[q13], v105, s[40:43], s76 offen offset:0 sc1\n"
    "v_mfma_f32_16x16x32_bf16 v[216:219], a[252:255], %[q2], v[216:219]\n"
    "ds_read_b64_tr_b16 a[244:245], v81 offset:16384\n"
    "v_mfma_f32_16x16x32_bf16 v[220:223], a[252:255], %[q3], v[220:223]\n"
    "ds_read_b64_tr_b16 a[246:247], v81 offset:20480\n"
    "buffer_load_dwordx4 %[q15], v105, s[40:43], s76 offen offset:1024 sc1\n"
    "s_waitcnt lgkmcnt(8)\n"
    "v_mfma_f32_16x16x32_bf16 v[224:227], a[224:227], %[q2], v[224:227]\n"
    "ds_read_b64_tr_b16 a[248:249], v82 offset:16384\n"
    "v_mfma_f32_16x16x32_bf16 v[228:231], a[224:227], %[q3], v[228:231]\n"
    "ds_read_b64_tr_b16 a[250:251], v82 offset:20480\n"
    "s_add_u32 m0, s50, 131072\n"
    "s_add_u32 s49, %[rotk], 7\n"
    "s_and_b32 s49, s49, 15\n"
    "s_mul_i32 s49, s49, 14336\n"
    "buffer_load_dwordx4 v42, s[60:63], s49 offen offset:0 sc1 lds\n"
    "v_mfma_f32_16x16x32_bf16 v[232:235], a[228:231], %[q2], v[232:235]\n"
    "ds_read_b64_tr_b16 a[252:253], v83 offset:16384\n"
    "v_mfma_f32_16x16x32_bf16 v[236:239], a[228:231], %[q3], v[236:239]\n"
    "ds_read_b64_tr_b16 a[254:255], v83 offset:20480\n"
    "buffer_load_dwordx4 v43, s[60:63], s49 offen offset:1024 sc1 lds\n"
    "s_waitcnt vmcnt(18)\n"
    "s_waitcnt lgkmcnt(8)\n"
    "v_mfma_f32_16x16x32_bf16 v[184:187], a[232:235], %[q4], v[184:187]\n"
    "ds_read_b64_tr_b16 a[224:225], v84 offset:16384\n"
    "v_mfma_f32_16x16x32_bf16 v[188:191], a[232:235], %[q5], v[188:191]\n"
    "ds_read_b64_tr_b16 a[226:227], v84 offset:20480\n"
    "buffer_load_dwordx4 v44, s[60:63], s49 offen offset:2048 sc1 lds\n"
    "v_mfma_f32_16x16x32_bf16 v[192:195], a[236:239], %[q4], v[192:195]\n"
    "ds_read_b64_tr_b16 a[228:229], v78 offset:24576\n"
    "v_mfma_f32_16x16x32_bf16 v[196:199], a[236:239], %[q5], v[196:199]\n"
    "ds_read_b64_tr_b16 a[230:231], v78 offset:28672\n"
    "buffer_load_dwordx4 v45, s[60:63], s49 offen offset:3072 sc1 lds\n"
    "s_waitcnt lgkmcnt(8)\n"
    "v_mfma_f32_16x16x32_bf16 v[200:203], a[240:243], %[q4], v[200:203]\n"
    "ds_read_b64_tr_b16 a[232:233], v79 offset:24576\n"
    "v_mfma_f32_16x16x32_bf16 v[204:207], a[240:243], %[q5], v[204:207]\n"
    "ds_read_b64_tr_b16 a[234:235], v79 offset:28672\n"
    "v_mfma_f32_16x16x32_bf16 v[208:211], a[244:247], %[q4], v[208:211]\n"
    "ds_read_b64_tr_b16 a[236:237], v80 offset:24576\n"
    "v_mfma_f32_16x16x32_bf16 v[212:215], a[244:247], %[q5], v[212:215]\n"
    "ds_read_b64_tr_b16 a[238:239], v80 offset:28672\n"
    "s_waitcnt lgkmcnt(8)\n"
    "v_mfma_f32_16x16x32_bf16 v[216:219], a[248:251], %[q4], v[216:219]\n"
    "ds_read_b64_tr_b16 a[240:241], v81 offset:24576\n"
    "v_mfma_f32_16x16x32_bf16 v[220:223], a[248:251], %[q5], v[220:223]\n"
    "ds_read_b64_tr_b16 a[242:243], v81 offset:28672\n"
    "v_mfma_f32_16x16x32_bf16 v[224:227], a[252:255], %[q4], v[224:227]\n"
    "ds_read_b64_tr_b16 a[244:245], v82 offset:24576\n"
    "v_mfma_f32_16x16x32_bf16 v[228:231], a[252:255], %[q5], v[228:231]\n"
    "ds_read_b64_tr_b16 a[246:247], v82 offset:28672\n"
    "s_waitcnt lgkmcnt(8)\n"
    "v_mfma_f32_16x16x32_bf16 v[232:235], a[224:227], %[q4], v[232:235]\n"
    "ds_read_b64_tr_b16 a[248:249], v83 offset:24576\n"
    "v_mfma_f32_16x16x32_bf16 v[236:239], a[224:227], %[q5], v[236:239]\n"
    "ds_read_b64_tr_b16 a[250:251], v83 offset:28672\n"
    "v_mfma_f32_16x16x32_bf16 v[184:187], a[228:231], %[q6], v[184:187]\n"
    "ds_read_b64_tr_b16 a[252:253], v84 offset:24576\n"
    "v_mfma_f32_16x16x32_bf16 v[188:191], a[228:231], %[q7], v[188:191]\n"
    "ds_read_b64_tr_b16 a[254:255], v84 offset:28672\n"
    "s_waitcnt lgkmcnt(8)\n"
    "s_barrier\n"
    "s_and_b32 s52, %[w], 1\n"
    "s_mul_i32 s52, s52, 7168\n"
    "s_lshr_b32 s53, %[w], 1\n"
    "s_lshl_b32 s53, s53, 10\n"
    "s_add_u32 s52, s52, s53\n"
    "s_add_u32 s52, s52, %[ldsb]\n"
    "s_add_u32 s52, s52, 98304\n"
    "v_add_u32 v87, s52, v33\n"
    "v_mfma_f32_16x16x32_bf16 v[192:195], a[232:235], %[q6], v[192:195]\n"
    "ds_read_b64_tr_b16 a[224:225], v64 offset:0\n"
    "ds_read_b128 v[160:163], v87 offset:0\n"
    "v_mfma_f32_16x16x32_bf16 v[196:199], a[232:235], %[q7], v[196:199]\n"
    "ds_read_b64_tr_b16 a[226:227], v64 offset:4096\n"
    "s_add_u32 s76, %[rotk], 4\n"
    "s_and_b32 s76, s76, 15\n"
    "s_lshl_b32 s76, s76, 11\n"
    "buffer_load_dwordx4 a[192:195], v104, s[40:43], s76 offen offset:0 sc1\n"
    "v_mfma_f32_16x16x32_bf16 v[200:203], a[236:239], %[q6], v[200:203]\n"
    "ds_read_b64_tr_b16 a[228:229], v65 offset:0\n"
    "ds_read_b128 v[128:131], v87 offset:14336\n"
    "v_mfma_f32_16x16x32_bf16 v[204:207], a[236:239], %[q7], v[204:207]\n"
    "ds_read_b64_tr_b16 a[230:231], v65 offset:4096\n"
    "buffer_load_dwordx4 a[200:203], v104, s[40:43], s76 offen offset:1024 sc1\n"
    "s_waitcnt lgkmcnt(10)\n"
    "v_mfma_f32_16x16x32_bf16 v[208:211], a[240:243], %[q6], v[208:211]\n"
    "ds_read_b64_tr_b16 a[232:233], v66 offset:0\n"
    "ds_read_b128 v[164:167], v87 offset:2048\n"
    "v_mfma_f32_16x16x32_bf16 v[212:215], a[240:243], %[q7], v[212:215]\n"
    "ds_read_b64_tr_b16 a[234:235], v66 offset:4096\n"
    "buffer_load_dwordx4 a[196:199], v105, s[40:43], s76 offen offset:0 sc1\n"
    "v_mfma_f32_16x16x32_bf16 v[216:219], a[244:247], %[q6], v[216:219]\n"
    "ds_read_b64_tr_b16 a[236:237], v67 offset:0\n"
    "ds_read_b128 v[132:135], v87 offset:16384\n"
    "v_mfma_f32_16x16x32_bf16 v[220:223], a[244:247], %[q7], v[220:223]\n"
    "ds_read_b64_tr_b16 a[238:239], v67 offset:4096\n"
    "buffer_load_dwordx4 a[204:207], v105, s[40:43], s76 offen offset:1024 sc1\n"
    "s_waitcnt lgkmcnt(12)\n"
    "v_mfma_f32_16x16x32_bf16 v[224:227], a[248:251], %[q6], v[224:227]\n"
    "ds_read_b64_tr_b16 a[240:241], v68 offset:0\n"
    "ds_read_b128 v[168:171], v87 offset:4096\n"
    "v_mfma_f32_16x16x32_bf16 v[228:231], a[248:251], %[q7], v[228:231]\n"
    "ds_read_b64_tr_b16 a[242:243], v68 offset:4096\n"
    "s_add_u32 m0, s50, 49152\n"
    "s_add_u32 s49, %[rotk], 8\n"
    "s_and_b32 s49, s49, 15\n"
    "s_mul_i32 s49, s49, 14336\n"
    "buffer_load_dwordx4 v42, s[60:63], s49 offen offset:0 sc1 lds\n"
    "v_mfma_f32_16x16x32_bf16 v[232:235], a[252:255], %[q6], v[232:235]\n"
    "ds_read_b64_tr_b16 a[244:245], v69 offset:0\n"
    "ds_read_b128 v[136:139], v87 offset:18432\n"
    "v_mfma_f32_16x16x32_bf16 v[236:239], a[252:255], %[q7], v[236:239]\n"
    "ds_read_b64_tr_b16 a[246:247], v69 offset:4096\n"
    "buffer_load_dwordx4 v43, s[60:63], s49 offen offset:1024 sc1 lds\n"
    "s_waitcnt vmcnt(18)\n"
    "s_waitcnt lgkmcnt(12)\n"
    "v_mfma_f32_16x16x32_bf16 v[184:187], a[224:227], %[q8], v[184:187]\n"
    "ds_read_b64_tr_b16 a[248:249], v70 offset:0\n"
    "ds_read_b128 v[172:175], v87 offset:6144\n"
    "v_mfma_f32_16x16x32_bf16 v[188:191], a[224:227], %[q9], v[188:191]\n"
    "ds_read_b64_tr_b16 a[250:251], v70 offset:4096\n"
    "buffer_load_dwordx4 v44, s[60:63], s49 offen offset:2048 sc1 lds\n"
    "v_mfma_f32_16x16x32_bf16 v[192:195], a[228:231], %[q8], v[192:195]\n"
    "ds_read_b64_tr_b16 a[252:253], v64 offset:8192\n"
    "ds_read_b128 v[140:143], v87 offset:20480\n"
    "v_mfma_f32_16x16x32_bf16 v[196:199], a[228:231], %[q9], v[196:199]\n"
    "ds_read_b64_tr_b16 a[254:255], v64 offset:12288\n"
    "buffer_load_dwordx4 v45, s[60:63], s49 offen offset:3072 sc1 lds\n"
    "s_waitcnt lgkmcnt(12)\n"
    "v_mfma_f32_16x16x32_bf16 v[200:203], a[232:235], %[q8], v[200:203]\n"
    "ds_read_b64_tr_b16 a[224:225], v65 offset:8192\n"
    "v_mfma_f32_16x16x32_bf16 v[204:207], a[232:235], %[q9], v[204:207]\n"
    "ds_read_b64_tr_b16 a[226:227], v65 offset:12288\n"
    "v_mfma_f32_16x16x32_bf16 v[208:211], a[236:239], %[q8], v[208:211]\n"
    "ds_read_b64_tr_b16 a[228:229], v66 offset:8192\n"
    "v_mfma_f32_16x16x32_bf16 v[212:215], a[236:239], %[q9], v[212:215]\n"
    "ds_read_b64_tr_b16 a[230:231], v66 offset:12288\n"
    "s_waitcnt lgkmcnt(10)\n"
    "v_mfma_f32_16x16x32_bf16 v[216:219], a[240:243], %[q8], v[216:219]\n"
    "ds_read_b64_tr_b16 a[232:233], v67 offset:8192\n"
    "v_mfma_f32_16x16x32_bf16 v[220:223], a[240:243], %[q9], v[220:223]\n"
    "ds_read_b64_tr_b16 a[234:235], v67 offset:12288\n"
    "v_mfma_f32_16x16x32_bf16 v[224:227], a[244:247], %[q8], v[224:227]\n"
    "ds_read_b64_tr_b16 a[236:237], v68 offset:8192\n"
    "v_mfma_f32_16x16x32_bf16 v[228:231], a[244:247], %[q9], v[228:231]\n"
    "ds_read_b64_tr_b16 a[238:239], v68 offset:12288\n"
    "s_waitcnt lgkmcnt(8)\n"
    "v_mfma_f32_16x16x32_bf16 v[232:235], a[248:251], %[q8], v[232:235]\n"
    "ds_read_b64_tr_b16 a[240:241], v69 offset:8192\n"
    "v_mfma_f32_16x16x32_bf16 v[236:239], a[248:251], %[q9], v[236:239]\n"
    "ds_read_b64_tr_b16 a[242:243], v69 offset:12288\n"
    "v_mfma_f32_16x16x32_bf16 v[184:187], a[252:255], %[q10], v[184:187]\n"
    "ds_read_b64_tr_b16 a[244:245], v70 offset:8192\n"
    "v_mfma_f32_16x16x32_bf16 v[188:191], a[252:255], %[q11], v[188:191]\n"
    "ds_read_b64_tr_b16 a[246:247], v70 offset:12288\n"
    "s_waitcnt lgkmcnt(8)\n"
    "s_barrier\n"
    "v_mfma_f32_16x16x32_bf16 v[192:195], a[224:227], %[q10], v[192:195]\n"
    "ds_read_b64_tr_b16 a[248:249], v64 offset:16384\n"
    "v_mfma_f32_16x16x32_bf16 v[196:199], a[224:227], %[q11], v[196:199]\n"
    "ds_read_b64_tr_b16 a[250:251], v64 offset:20480\n"
    "s_add_u32 s76, %[rotk], 5\n"
    "s_and_b32 s76, s76, 15\n"
    "s_lshl_b32 s76, s76, 11\n"
    "buffer_load_dwordx4 a[208:211], v104, s[40:43], s76 offen offset:0 sc1\n"
    "v_mfma_f32_16x16x32_bf16 v[200:203], a[228:231], %[q10], v[200:203]\n"
    "ds_read_b64_tr_b16 a[252:253], v65 offset:16384\n"
    "v_mfma_f32_16x16x32_bf16 v[204:207], a[228:231], %[q11], v[204:207]\n"
    "ds_read_b64_tr_b16 a[254:255], v65 offset:20480\n"
    "buffer_load_dwordx4 a[216:219], v104, s[40:43], s76 offen offset:1024 sc1\n"
    "s_waitcnt lgkmcnt(8)\n"
    "v_mfma_f32_16x16x32_bf16 v[208:211], a[232:235], %[q10], v[208:211]\n"
    "ds_read_b64_tr_b16 a[224:225], v66 offset:16384\n"
    "v_mfma_f32_16x16x32_bf16 v[212:215], a[232:235], %[q11], v[212:215]\n"
    "ds_read_b64_tr_b16 a[226:227], v66 offset:20480\n"
    "buffer_load_dwordx4 a[212:215], v105, s[40:43], s76 offen offset:0 sc1\n"
    "v_mfma_f32_16x16x32_bf16 v[216:219], a[236:239], %[q10], v[216:219]\n"
    "ds_read_b64_tr_b16 a[228:229], v67 offset:16384\n"
    "v_mfma_f32_16x16x32_bf16 v[220:223], a[236:239], %[q11], v[220:223]\n"
    "ds_read_b64_tr_b16 a[230:231], v67 offset:20480\n"
    "buffer_load_dwordx4 a[220:223], v105, s[40:43], s76 offen offset:1024 sc1\n"
    "s_waitcnt lgkmcnt(8)\n"
    "v_mfma_f32_16x16x32_bf16 v[224:227], a[240:243], %[q10], v[224:227]\n"
    "ds_read_b64_tr_b16 a[232:233], v68 offset:16384\n"
    "v_mfma_f32_16x16x32_bf16 v[228:231], a[240:243], %[q11], v[228:231]\n"
    "ds_read_b64_tr_b16 a[234:235], v68 offset:20480\n"
    "s_add_u32 m0, s50, 147456\n"
    "s_add_u32 s49, %[rotk], 9\n"
    "s_and_b32 s49, s49, 15\n"
    "s_mul_i32 s49, s49, 14336\n"
    "buffer_load_dwordx4 v42, s[60:63], s49 offen offset:0 sc1 lds\n"
    "v_mfma_f32_16x16x32_bf16 v[232:235], a[244:247], %[q10], v[232:235]\n"
    "ds_read_b64_tr_b16 a[236:237], v69 offset:16384\n"
    "v_mfma_f32_16x16x32_bf16 v[236:239], a[244:247], %[q11], v[236:239]\n"
    "ds_read_b64_tr_b16 a[238:239], v69 offset:20480\n"
    "buffer_load_dwordx4 v43, s[60:63], s49 offen offset:1024 sc1 lds\n"
    "s_waitcnt vmcnt(18)\n"
    "s_waitcnt lgkmcnt(8)\n"
    "v_mfma_f32_16x16x32_bf16 v[184:187], a[248:251], %[q12], v[184:187]\n"
    "ds_read_b64_tr_b16 a[240:241], v70 offset:16384\n"
    "v_mfma_f32_16x16x32_bf16 v[188:191], a[248:251], %[q13], v[188:191]\n"
    "ds_read_b64_tr_b16 a[242:243], v70 offset:20480\n"
    "buffer_load_dwordx4 v44, s[60:63], s49 offen offset:2048 sc1 lds\n"
    "v_mfma_f32_16x16x32_bf16 v[192:195], a[252:255], %[q12], v[192:195]\n"
    "ds_read_b64_tr_b16 a[244:245], v64 offset:24576\n"
    "v_mfma_f32_16x16x32_bf16 v[196:199], a[252:255], %[q13], v[196:199]\n"
    "ds_read_b64_tr_b16 a[246:247], v64 offset:28672\n"
    "buffer_load_dwordx4 v45, s[60:63], s49 offen offset:3072 sc1 lds\n"
    "s_waitcnt lgkmcnt(8)\n"
    "v_mfma_f32_16x16x32_bf16 v[200:203], a[224:227], %[q12], v[200:203]\n"
    "ds_read_b64_tr_b16 a[248:249], v65 offset:24576\n"
    "v_mfma_f32_16x16x32_bf16 v[204:207], a[224:227], %[q13], v[204:207]\n"
    "ds_read_b64_tr_b16 a[250:251], v65 offset:28672\n"
    "v_mfma_f32_16x16x32_bf16 v[208:211], a[228:231], %[q12], v[208:211]\n"
    "ds_read_b64_tr_b16 a[252:253], v66 offset:24576\n"
    "v_mfma_f32_16x16x32_bf16 v[212:215], a[228:231], %[q13], v[212:215]\n"
    "ds_read_b64_tr_b16 a[254:255], v66 offset:28672\n"
    "s_waitcnt lgkmcnt(8)\n"
    "v_mfma_f32_16x16x32_bf16 v[216:219], a[232:235], %[q12], v[216:219]\n"
    "ds_read_b64_tr_b16 a[224:225], v67 offset:24576\n"
    "v_mfma_f32_16x16x32_bf16 v[220:223], a[232:235], %[q13], v[220:223]\n"
    "ds_read_b64_tr_b16 a[226:227], v67 offset:28672\n"
    "v_mfma_f32_16x16x32_bf16 v[224:227], a[236:239], %[q12], v[224:227]\n"
    "ds_read_b64_tr_b16 a[228:229], v68 offset:24576\n"
    "v_mfma_f32_16x16x32_bf16 v[228:231], a[236:239], %[q13], v[228:231]\n"
    "ds_read_b64_tr_b16 a[230:231], v68 offset:28672\n"
    "s_waitcnt lgkmcnt(8)\n"
    "v_mfma_f32_16x16x32_bf16 v[232:235], a[240:243], %[q12], v[232:235]\n"
    "ds_read_b64_tr_b16 a[232:233], v69 offset:24576\n"
    "v_mfma_f32_16x16x32_bf16 v[236:239], a[240:243], %[q13], v[236:239]\n"
    "ds_read_b64_tr_b16 a[234:235], v69 offset:28672\n"
    "v_mfma_f32_16x16x32_bf16 v[184:187], a[244:247], %[q14], v[184:187]\n"
    "ds_read_b64_tr_b16 a[236:237], v70 offset:24576\n"
    "v_mfma_f32_16x16x32_bf16 v[188:191], a[244:247], %[q15], v[188:191]\n"
    "ds_read_b64_tr_b16 a[238:239], v70 offset:28672\n"
    "s_waitcnt lgkmcnt(8)\n"
    "s_barrier\n"
    "v_mfma_f32_16x16x32_bf16 v[192:195], a[248:251], %[q14], v[192:195]\n"
    "ds_read_b64_tr_b16 a[240:241], v71 offset:0\n"
    "v_mfma_f32_16x16x32_bf16 v[196:199], a[248:251], %[q15], v[196:199]\n"
    "ds_read_b64_tr_b16 a[242:243], v71 offset:4096\n"
    "s_add_u32 s76, %[rotk], 6\n"
    "s_and_b32 s76, s76, 15\n"
    "s_lshl_b32 s76, s76, 11\n"
    "buffer_load_dwordx4 a[112:115], v104, s[40:43], s76 offen offset:0 sc1\n"
    "v_mfma_f32_16x16x32_bf16 v[200:203], a[252:255], %[q14], v[200:203]\n"
    "ds_read_b64_tr_b16 a[244:245], v72 offset:0\n"
    "v_mfma_f32_16x16x32_bf16 v[204:207], a[252:255], %[q15], v[204:207]\n"
    "ds_read_b64_tr_b16 a[246:247], v72 offset:4096\n"
    "buffer_load_dwordx4 a[120:123], v104, s[40:43], s76 offen offset:1024 sc1\n"
    "s_waitcnt lgkmcnt(8)\n"
    "v_mfma_f32_16x16x32_bf16 v[208:211], a[224:227], %[q14], v[208:211]\n"
    "ds_read_b64_tr_b16 a[248:249], v73 offset:0\n"
    "v_mfma_f32_16x16x32_bf16 v[212:215], a[224:227], %[q15], v[212:215]\n"
    "ds_read_b64_tr_b16 a[250:251], v73 offset:4096\n"
    "buffer_load_dwordx4 a[116:119], v105, s[40:43], s76 offen offset:0 sc1\n"
    "v_mfma_f32_16x16x32_bf16 v[216:219], a[228:231], %[q14], v[216:219]\n"
    "ds_read_b64_tr_b16 a[252:253], v74 offset:0\n"
    "v_mfma_f32_16x16x32_bf16 v[220:223], a[228:231], %[q15], v[220:223]\n"
    "ds_read_b64_tr_b16 a[254:255], v74 offset:4096\n"
    "buffer_load_dwordx4 a[124:127], v105, s[40:43], s76 offen offset:1024 sc1\n"
    "s_waitcnt lgkmcnt(8)\n"
    "v_mfma_f32_16x16x32_bf16 v[224:227], a[232:235], %[q14], v[224:227]\n"
    "ds_read_b64_tr_b16 a[224:225], v75 offset:0\n"
    "v_mfma_f32_16x16x32_bf16 v[228:231], a[232:235], %[q15], v[228:231]\n"
    "ds_read_b64_tr_b16 a[226:227], v75 offset:4096\n"
    "s_add_u32 m0, s50, 0\n"
    "s_add_u32 s49, %[rotk], 10\n"
    "s_and_b32 s49, s49, 15\n"
    "s_mul_i32 s49, s49, 14336\n"
    "buffer_load_dwordx4 v42, s[60:63], s49 offen offset:0 sc1 lds\n"
    "v_mfma_f32_16x16x32_bf16 v[232:235], a[236:239], %[q14], v[232:235]\n"
    "ds_read_b64_tr_b16 a[228:229], v76 offset:0\n"
    "v_mfma_f32_16x16x32_bf16 v[236:239], a[236:239], %[q15], v[236:239]\n"
    "ds_read_b64_tr_b16 a[230:231], v76 offset:4096\n"
    "buffer_load_dwordx4 v43, s[60:63], s49 offen offset:1024 sc1 lds\n"
    "s_waitcnt vmcnt(18)\n"
    "s_waitcnt lgkmcnt(8)\n"
    "s_mov_b32 s77, 0xbfb8aa3b\n"
    "v_mfma_f32_16x16x32_bf16 v[184:187], a[240:243], a[192:195], v[184:187]\n"
    "ds_read_b64_tr_b16 a[232:233], v77 offset:0\n"
    "v_add_f32 v160, v160, v128\n"
    "v_mfma_f32_16x16x32_bf16 v[188:191], a[240:243], a[196:199], v[188:191]\n"
    "ds_read_b64_tr_b16 a[234:235], v77 offset:4096\n"
    "buffer_load_dwordx4 v44, s[60:63], s49 offen offset:2048 sc1 lds\n"
    "v_mfma_f32_16x16x32_bf16 v[192:195], a[244:247], a[192:195], v[192:195]\n"
    "ds_read_b64_tr_b16 a[236:237], v71 offset:8192\n"
    "v_add_f32 v161, v161, v129\n"
    "v_mfma_f32_16x16x32_bf16 v[196:199], a[244:247], a[196:199], v[196:199]\n"
    "ds_read_b64_tr_b16 a[238:239], v71 offset:12288\n"
    "buffer_load_dwordx4 v45, s[60:63], s49 offen offset:3072 sc1 lds\n"
    "s_waitcnt lgkmcnt(8)\n"
    "v_mfma_f32_16x16x32_bf16 v[200:203], a[248:251], a[192:195], v[200:203]\n"
    "ds_read_b64_tr_b16 a[240:241], v72 offset:8192\n"
    "v_add_f32 v162, v162, v130\n"
    "v_mfma_f32_16x16x32_bf16 v[204:207], a[248:251], a[196:199], v[204:207]\n"
    "ds_read_b64_tr_b16 a[242:243], v72 offset:12288\n"
    "v_add_f32 v163, v163, v131\n"
    "v_mfma_f32_16x16x32_bf16 v[208:211], a[252:255], a[192:195], v[208:211]\n"
    "ds_read_b64_tr_b16 a[244:245], v73 offset:8192\n"
    "v_add_f32 v164, v164, v132\n"
    "v_mfma_f32_16x16x32_bf16 v[212:215], a[252:255], a[196:199], v[212:215]\n"
    "ds_read_b64_tr_b16 a[246:247], v73 offset:12288\n"
    "v_add_f32 v165, v165, v133\n"
    "s_waitcnt lgkmcnt(8)\n"
    "v_mfma_f32_16x16x32_bf16 v[216:219], a[224:227], a[192:195], v[216:219]\n"
    "ds_read_b64_tr_b16 a[248:249], v74 offset:8192\n"
    "v_add_f32 v166, v166, v134\n"
    "v_mfma_f32_16x16x32_bf16 v[220:223], a[224:227], a[196:199], v[220:223]\n"
    "ds_read_b64_tr_b16 a[250:251], v74 offset:12288\n"
    "v_add_f32 v167, v167, v135\n"
    "v_mfma_f32_16x16x32_bf16 v[224:227], a[228:231], a[192:195], v[224:227]\n"
    "ds_read_b64_tr_b16 a[252:253], v75 offset:8192\n"
    "v_add_f32 v168, v168, v136\n"
    "v_mfma_f32_16x16x32_bf16 v[228:231], a[228:231], a[196:199], v[228:231]\n"
    "ds_read_b64_tr_b16 a[254:255], v75 offset:12288\n"
    "v_add_f32 v169, v169, v137\n"
    "s_waitcnt lgkmcnt(8)\n"
    "v_mfma_f32_16x16x32_bf16 v[232:235], a[232:235], a[192:195], v[232:235]\n"
    "ds_read_b64_tr_b16 a[224:225], v76 offset:8192\n"
    "v_add_f32 v170, v170, v138\n"
    "v_mfma_f32_16x16x32_bf16 v[236:239], a[232:235], a[196:199], v[236:239]\n"
    "ds_read_b64_tr_b16 a[226:227], v76 offset:12288\n"
    "v_add_f32 v171, v171, v139\n"
    "v_mfma_f32_16x16x32_bf16 v[184:187], a[236:239], a[200:203], v[184:187]\n"
    "ds_read_b64_tr_b16 a[228:229], v77 offset:8192\n"
    "v_add_f32 v172, v172, v140\n"
    "v_mfma_f32_16x16x32_bf16 v[188:191], a[236:239], a[204:207], v[188:191]\n"
    "ds_read_b64_tr_b16 a[230:231], v77 offset:12288\n"
    "v_add_f32 v173, v173, v141\n"
    "s_waitcnt lgkmcnt(8)\n"
    "s_barrier\n"
    "v_mfma_f32_16x16x32_bf16 v[192:195], a[240:243], a[200:203], v[192:195]\n"
    "ds_read_b64_tr_b16 a[232:233], v71 offset:16384\n"
    "v_add_f32 v174, v174, v142\n"
    "v_mfma_f32_16x16x32_bf16 v[196:199], a[240:243], a[204:207], v[196:199]\n"
    "ds_read_b64_tr_b16 a[234:235], v71 offset:20480\n"
    "s_add_u32 s76, %[rotk], 7\n"
    "s_and_b32 s76, s76, 15\n"
    "s_lshl_b32 s76, s76, 11\n"
    "buffer_load_dwordx4 a[0:3], v104, s[40:43], s76 offen offset:0 sc1\n"
    "v_mfma_f32_16x16x32_bf16 v[200:203], a[244:247], a[200:203], v[200:203]\n"
    "ds_read_b64_tr_b16 a[236:237], v72 offset:16384\n"
    "v_add_f32 v175, v175, v143\n"
    "v_mfma_f32_16x16x32_bf16 v[204:207], a[244:247], a[204:207], v[204:207]\n"
    "ds_read_b64_tr_b16 a[238:239], v72 offset:20480\n"
    "buffer_load_dwordx4 a[8:11], v104, s[40:43], s76 offen offset:1024 sc1\n"
    "s_waitcnt lgkmcnt(8)\n"
    "v_mfma_f32_16x16x32_bf16 v[208:211], a[248:251], a[200:203], v[208:211]\n"
    "ds_read_b64_tr_b16 a[240:241], v73 offset:16384\n"
    "v_mul_f32 v128, s77, v160\n"
    "v_mfma_f32_16x16x32_bf16 v[212:215], a[248:251], a[204:207], v[212:215]\n"
    "ds_read_b64_tr_b16 a[242:243], v73 offset:20480\n"
    "buffer_load_dwordx4 a[4:7], v105, s[40:43], s76 offen offset:0 sc1\n"
    "v_mfma_f32_16x16x32_bf16 v[216:219], a[252:255], a[200:203], v[216:219]\n"
    "ds_read_b64_tr_b16 a[244:245], v74 offset:16384\n"
    "v_mul_f32 v129, s77, v161\n"
    "v_mfma_f32_16x16x32_bf16 v[220:223], a[252:255], a[204:207], v[220:223]\n"
    "ds_read_b64_tr_b16 a[246:247], v74 offset:20480\n"
    "buffer_load_dwordx4 a[12:15], v105, s[40:43], s76 offen offset:1024 sc1\n"
    "s_waitcnt lgkmcnt(8)\n"
    "v_mfma_f32_16x16x32_bf16 v[224:227], a[224:227], a[200:203], v[224:227]\n"
    "ds_read_b64_tr_b16 a[248:249], v75 offset:16384\n"
    "v_mul_f32 v130, s77, v162\n"
    "v_mfma_f32_16x16x32_bf16 v[228:231], a[224:227], a[204:207], v[228:231]\n"
    "ds_read_b64_tr_b16 a[250:251], v75 offset:20480\n"
    "s_add_u32 m0, s50, 16384\n"
    "s_add_u32 s49, %[rotk], 11\n"
    "s_and_b32 s49, s49, 15\n"
    "s_mul_i32 s49, s49, 14336\n"
    "buffer_load_dwordx4 v42, s[60:63], s49 offen offset:0 sc1 lds\n"
    "v_mfma_f32_16x16x32_bf16 v[232:235], a[228:231], a[200:203], v[232:235]\n"
    "ds_read_b64_tr_b16 a[252:253], v76 offset:16384\n"
    "v_mul_f32 v131, s77, v163\n"
    "v_mfma_f32_16x16x32_bf16 v[236:239], a[228:231], a[204:207], v[236:239]\n"
    "ds_read_b64_tr_b16 a[254:255], v76 offset:20480\n"
    "buffer_load_dwordx4 v43, s[60:63], s49 offen offset:1024 sc1 lds\n"
    "s_waitcnt vmcnt(18)\n"
    "s_waitcnt lgkmcnt(8)\n"
    "v_mfma_f32_16x16x32_bf16 v[184:187], a[232:235], a[208:211], v[184:187]\n"
    "ds_read_b64_tr_b16 a[224:225], v77 offset:16384\n"
    "v_mul_f32 v132, s77, v164\n"
    "v_mfma_f32_16x16x32_bf16 v[188:191], a[232:235], a[212:215], v[188:191]\n"
    "ds_read_b64_tr_b16 a[226:227], v77 offset:20480\n"
    "buffer_load_dwordx4 v44, s[60:63], s49 offen offset:2048 sc1 lds\n"
    "v_mfma_f32_16x16x32_bf16 v[192:195], a[236:239], a[208:211], v[192:195]\n"
    "ds_read_b64_tr_b16 a[228:229], v71 offset:24576\n"
    "v_mul_f32 v133, s77, v165\n"
    "v_mfma_f32_16x16x32_bf16 v[196:199], a[236:239], a[212:215], v[196:199]\n"
    "ds_read_b64_tr_b16 a[230:231], v71 offset:28672\n"
    "buffer_load_dwordx4 v45, s[60:63], s49 offen offset:3072 sc1 lds\n"
    "s_waitcnt lgkmcnt(8)\n"
    "v_mfma_f32_16x16x32_bf16 v[200:203], a[240:243], a[208:211], v[200:203]\n"
    "ds_read_b64_tr_b16 a[232:233], v72 offset:24576\n"
    "v_mul_f32 v134, s77, v166\n"
    "v_mfma_f32_16x16x32_bf16 v[204:207], a[240:243], a[212:215], v[204:207]\n"
    "ds_read_b64_tr_b16 a[234:235], v72 offset:28672\n"
    "v_mul_f32 v135, s77, v167\n"
    "v_mfma_f32_16x16x32_bf16 v[208:211], a[244:247], a[208:211], v[208:211]\n"
    "ds_read_b64_tr_b16 a[236:237], v73 offset:24576\n"
    "v_mul_f32 v136, s77, v168\n"
    "v_mfma_f32_16x16x32_bf16 v[212:215], a[244:247], a[212:215], v[212:215]\n"
    "ds_read_b64_tr_b16 a[238:239], v73 offset:28672\n"
    "v_mul_f32 v137, s77, v169\n"
    "s_waitcnt lgkmcnt(8)\n"
    "v_mfma_f32_16x16x32_bf16 v[216:219], a[248:251], a[208:211], v[216:219]\n"
    "ds_read_b64_tr_b16 a[240:241], v74 offset:24576\n"
    "v_mul_f32 v138, s77, v170\n"
    "v_mfma_f32_16x16x32_bf16 v[220:223], a[248:251], a[212:215], v[220:223]\n"
    "ds_read_b64_tr_b16 a[242:243], v74 offset:28672\n"
    "v_mul_f32 v139, s77, v171\n"
    "v_mfma_f32_16x16x32_bf16 v[224:227], a[252:255], a[208:211], v[224:227]\n"
    "ds_read_b64_tr_b16 a[244:245], v75 offset:24576\n"
    "v_mul_f32 v140, s77, v172\n"
    "v_mfma_f32_16x16x32_bf16 v[228:231], a[252:255], a[212:215], v[228:231]\n"
    "ds_read_b64_tr_b16 a[246:247], v75 offset:28672\n"
    "v_mul_f32 v141, s77, v173\n"
    "s_waitcnt lgkmcnt(8)\n"
    "v_mfma_f32_16x16x32_bf16 v[232:235], a[224:227], a[208:211], v[232:235]\n"
    "ds_read_b64_tr_b16 a[248:249], v76 offset:24576\n"
    "v_mul_f32 v142, s77, v174\n"
    "v_mfma_f32_16x16x32_bf16 v[236:239], a[224:227], a[212:215], v[236:239]\n"
    "ds_read_b64_tr_b16 a[250:251], v76 offset:28672\n"
    "v_mul_f32 v143, s77, v175\n"
    "v_mfma_f32_16x16x32_bf16 v[184:187], a[228:231], a[216:219], v[184:187]\n"
    "ds_read_b64_tr_b16 a[252:253], v77 offset:24576\n"
    "v_exp_f32 v128, v128\n"
    "v_mfma_f32_16x16x32_bf16 v[188:191], a[228:231], a[220:223], v[188:191]\n"
    "ds_read_b64_tr_b16 a[254:255], v77 offset:28672\n"
    "v_exp_f32 v129, v129\n"
    "s_waitcnt lgkmcnt(8)\n"
    "s_barrier\n"
    "v_mfma_f32_16x16x32_bf16 v[192:195], a[232:235], a[216:219], v[192:195]\n"
    "ds_read_b64_tr_b16 a[224:225], v64 offset:32768\n"
    "v_exp_f32 v130, v130\n"
    "v_mfma_f32_16x16x32_bf16 v[196:199], a[232:235], a[220:223], v[196:199]\n"
    "ds_read_b64_tr_b16 a[226:227], v64 offset:36864\n"
    "s_add_u32 s76, %[rotk], 8\n"
    "s_and_b32 s76, s76, 15\n"
    "s_lshl_b32 s76, s76, 11\n"
    "buffer_load_dwordx4 a[16:19], v104, s[40:43], s76 offen offset:0 sc1\n"
    "v_mfma_f32_16x16x32_bf16 v[200:203], a[236:239], a[216:219], v[200:203]\n"
    "ds_read_b64_tr_b16 a[228:229], v65 offset:32768\n"
    "v_exp_f32 v131, v131\n"
    "v_mfma_f32_16x16x32_bf16 v[204:207], a[236:239], a[220:223], v[204:207]\n"
    "ds_read_b64_tr_b16 a[230:231], v65 offset:36864\n"
    "buffer_load_dwordx4 a[24:27], v104, s[40:43], s76 offen offset:1024 sc1\n"
    "s_waitcnt lgkmcnt(8)\n"
    "v_mfma_f32_16x16x32_bf16 v[208:211], a[240:243], a[216:219], v[208:211]\n"
    "ds_read_b64_tr_b16 a[232:233], v66 offset:32768\n"
    "v_exp_f32 v132, v132\n"
    "v_mfma_f32_16x16x32_bf16 v[212:215], a[240:243], a[220:223], v[212:215]\n"
    "ds_read_b64_tr_b16 a[234:235], v66 offset:36864\n"
    "buffer_load_dwordx4 a[20:23], v105, s[40:43], s76 offen offset:0 sc1\n"
    "v_mfma_f32_16x16x32_bf16 v[216:219], a[244:247], a[216:219], v[216:219]\n"
    "ds_read_b64_tr_b16 a[236:237], v67 offset:32768\n"
    "v_exp_f32 v133, v133\n"
    "v_mfma_f32_16x16x32_bf16 v[220:223], a[244:247], a[220:223], v[220:223]\n"
    "ds_read_b64_tr_b16 a[238:239], v67 offset:36864\n"
    "buffer_load_dwordx4 a[28:31], v105, s[40:43], s76 offen offset:1024 sc1\n"
    "s_waitcnt lgkmcnt(8)\n"
    "v_mfma_f32_16x16x32_bf16 v[224:227], a[248:251], a[216:219], v[224:227]\n"
    "ds_read_b64_tr_b16 a[240:241], v68 offset:32768\n"
    "v_exp_f32 v134, v134\n"
    "v_mfma_f32_16x16x32_bf16 v[228:231], a[248:251], a[220:223], v[228:231]\n"
    "ds_read_b64_tr_b16 a[242:243], v68 offset:36864\n"
    "s_add_u32 m0, s50, 65536\n"
    "s_add_u32 s49, %[rotk], 12\n"
    "s_and_b32 s49, s49, 15\n"
    "s_mul_i32 s49, s49, 14336\n"
    "buffer_load_dwordx4 v42, s[60:63], s49 offen offset:0 sc1 lds\n"
    "v_mfma_f32_16x16x32_bf16 v[232:235], a[252:255], a[216:219], v[232:235]\n"
    "ds_read_b64_tr_b16 a[244:245], v69 offset:32768\n"
    "v_exp_f32 v135, v135\n"
    "v_mfma_f32_16x16x32_bf16 v[236:239], a[252:255], a[220:223], v[236:239]\n"
    "ds_read_b64_tr_b16 a[246:247], v69 offset:36864\n"
    "buffer_load_dwordx4 v43, s[60:63], s49 offen offset:1024 sc1 lds\n"
    "s_waitcnt vmcnt(18)\n"
    "s_waitcnt lgkmcnt(8)\n"
    "v_mfma_f32_16x16x32_bf16 v[184:187], a[224:227], a[112:115], v[184:187]\n"
    "ds_read_b64_tr_b16 a[248:249], v70 offset:32768\n"
    "v_exp_f32 v136, v136\n"
    "v_mfma_f32_16x16x32_bf16 v[188:191], a[224:227], a[116:119], v[188:191]\n"
    "ds_read_b64_tr_b16 a[250:251], v70 offset:36864\n"
    "buffer_load_dwordx4 v44, s[60:63], s49 offen offset:2048 sc1 lds\n"
    "v_mfma_f32_16x16x32_bf16 v[192:195], a[228:231], a[112:115], v[192:195]\n"
    "ds_read_b64_tr_b16 a[252:253], v64 offset:40960\n"
    "v_exp_f32 v137, v137\n"
    "v_mfma_f32_16x16x32_bf16 v[196:199], a[228:231], a[116:119], v[196:199]\n"
    "ds_read_b64_tr_b16 a[254:255], v64 offset:45056\n"
    "buffer_load_dwordx4 v45, s[60:63], s49 offen offset:3072 sc1 lds\n"
    "s_waitcnt lgkmcnt(8)\n"
    "v_mfma_f32_16x16x32_bf16 v[200:203], a[232:235], a[112:115], v[200:203]\n"
    "ds_read_b64_tr_b16 a[224:225], v65 offset:40960\n"
    "v_exp_f32 v138, v138\n"
    "v_mfma_f32_16x16x32_bf16 v[204:207], a[232:235], a[116:119], v[204:207]\n"
    "ds_read_b64_tr_b16 a[226:227], v65 offset:45056\n"
    "v_exp_f32 v139, v139\n"
    "v_mfma_f32_16x16x32_bf16 v[208:211], a[236:239], a[112:115], v[208:211]\n"
    "ds_read_b64_tr_b16 a[228:229], v66 offset:40960\n"
    "v_exp_f32 v140, v140\n"
    "v_mfma_f32_16x16x32_bf16 v[212:215], a[236:239], a[116:119], v[212:215]\n"
    "ds_read_b64_tr_b16 a[230:231], v66 offset:45056\n"
    "v_exp_f32 v141, v141\n"
    "s_waitcnt lgkmcnt(8)\n"
    "v_mfma_f32_16x16x32_bf16 v[216:219], a[240:243], a[112:115], v[216:219]\n"
    "ds_read_b64_tr_b16 a[232:233], v67 offset:40960\n"
    "v_exp_f32 v142, v142\n"
    "v_mfma_f32_16x16x32_bf16 v[220:223], a[240:243], a[116:119], v[220:223]\n"
    "ds_read_b64_tr_b16 a[234:235], v67 offset:45056\n"
    "v_exp_f32 v143, v143\n"
    "v_mfma_f32_16x16x32_bf16 v[224:227], a[244:247], a[112:115], v[224:227]\n"
    "ds_read_b64_tr_b16 a[236:237], v68 offset:40960\n"
    "v_add_f32 v128, 1.0, v128\n"
    "v_mfma_f32_16x16x32_bf16 v[228:231], a[244:247], a[116:119], v[228:231]\n"
    "ds_read_b64_tr_b16 a[238:239], v68 offset:45056\n"
    "v_add_f32 v129, 1.0, v129\n"
    "s_waitcnt lgkmcnt(8)\n"
    "v_mfma_f32_16x16x32_bf16 v[232:235], a[248:251], a[112:115], v[232:235]\n"
    "ds_read_b64_tr_b16 a[240:241], v69 offset:40960\n"
    "v_add_f32 v130, 1.0, v130\n"
    "v_mfma_f32_16x16x32_bf16 v[236:239], a[248:251], a[116:119], v[236:239]\n"
    "ds_read_b64_tr_b16 a[242:243], v69 offset:45056\n"
    "v_add_f32 v131, 1.0, v131\n"
    "v_mfma_f32_16x16x32_bf16 v[184:187], a[252:255], a[120:123], v[184:187]\n"
    "ds_read_b64_tr_b16 a[244:245], v70 offset:40960\n"
    "v_add_f32 v132, 1.0, v132\n"
    "v_mfma_f32_16x16x32_bf16 v[188:191], a[252:255], a[124:127], v[188:191]\n"
    "ds_read_b64_tr_b16 a[246:247], v70 offset:45056\n"
    "v_add_f32 v133, 1.0, v133\n"
    "s_waitcnt lgkmcnt(8)\n"
    "s_barrier\n"
    "v_mfma_f32_16x16x32_bf16 v[192:195], a[224:227], a[120:123], v[192:195]\n"
    "ds_read_b64_tr_b16 a[248:249], v78 offset:0\n"
    "v_add_f32 v134, 1.0, v134\n"
    "v_mfma_f32_16x16x32_bf16 v[196:199], a[224:227], a[124:127], v[196:199]\n"
    "ds_read_b64_tr_b16 a[250:251], v78 offset:4096\n"
    "s_add_u32 s76, %[rotk], 9\n"
    "s_and_b32 s76, s76, 15\n"
    "s_lshl_b32 s76, s76, 11\n"
    "buffer_load_dwordx4 a[32:35], v104, s[40:43], s76 offen offset:0 sc1\n"
    "v_mfma_f32_16x16x32_bf16 v[200:203], a[228:231], a[120:123], v[200:203]\n"
    "ds_read_b64_tr_b16 a[252:253], v79 offset:0\n"
    "v_add_f32 v135, 1.0, v135\n"
    "v_mfma_f32_16x16x32_bf16 v[204:207], a[228:231], a[124:127], v[204:207]\n"
    "ds_read_b64_tr_b16 a[254:255], v79 offset:4096\n"
    "buffer_load_dwordx4 a[40:43], v104, s[40:43], s76 offen offset:1024 sc1\n"
    "s_waitcnt lgkmcnt(8)\n"
    "v_mfma_f32_16x16x32_bf16 v[208:211], a[232:235], a[120:123], v[208:211]\n"
    "ds_read_b64_tr_b16 a[224:225], v80 offset:0\n"
    "v_add_f32 v136, 1.0, v136\n"
    "v_mfma_f32_16x16x32_bf16 v[212:215], a[232:235], a[124:127], v[212:215]\n"
    "ds_read_b64_tr_b16 a[226:227], v80 offset:4096\n"
    "buffer_load_dwordx4 a[36:39], v105, s[40:43], s76 offen offset:0 sc1\n"
    "v_mfma_f32_16x16x32_bf16 v[216:219], a[236:239], a[120:123], v[216:219]\n"
    "ds_read_b64_tr_b16 a[228:229], v81 offset:0\n"
    "v_add_f32 v137, 1.0, v137\n"
    "v_mfma_f32_16x16x32_bf16 v[220:223], a[236:239], a[124:127], v[220:223]\n"
    "ds_read_b64_tr_b16 a[230:231], v81 offset:4096\n"
    "buffer_load_dwordx4 a[44:47], v105, s[40:43], s76 offen offset:1024 sc1\n"
    "s_waitcnt lgkmcnt(8)\n"
    "v_mfma_f32_16x16x32_bf16 v[224:227], a[240:243], a[120:123], v[224:227]\n"
    "ds_read_b64_tr_b16 a[232:233], v82 offset:0\n"
    "v_add_f32 v138, 1.0, v138\n"
    "v_mfma_f32_16x16x32_bf16 v[228:231], a[240:243], a[124:127], v[228:231]\n"
    "ds_read_b64_tr_b16 a[234:235], v82 offset:4096\n"
    "s_add_u32 m0, s50, 81920\n"
    "s_add_u32 s49, %[rotk], 13\n"
    "s_and_b32 s49, s49, 15\n"
    "s_mul_i32 s49, s49, 14336\n"
    "buffer_load_dwordx4 v42, s[60:63], s49 offen offset:0 sc1 lds\n"
    "v_mfma_f32_16x16x32_bf16 v[232:235], a[244:247], a[120:123], v[232:235]\n"
    "ds_read_b64_tr_b16 a[236:237], v83 offset:0\n"
    "v_add_f32 v139, 1.0, v139\n"
    "v_mfma_f32_16x16x32_bf16 v[236:239], a[244:247], a[124:127], v[236:239]\n"
    "ds_read_b64_tr_b16 a[238:239], v83 offset:4096\n"
    "buffer_load_dwordx4 v43, s[60:63], s49 offen offset:1024 sc1 lds\n"
    "s_waitcnt vmcnt(18)\n"
    "s_waitcnt lgkmcnt(8)\n"
    "v_mfma_f32_16x16x32_bf16 v[184:187], a[248:251], a[0:3], v[184:187]\n"
    "ds_read_b64_tr_b16 a[240:241], v84 offset:0\n"
    "v_add_f32 v140, 1.0, v140\n"
    "v_mfma_f32_16x16x32_bf16 v[188:191], a[248:251], a[4:7], v[188:191]\n"
    "ds_read_b64_tr_b16 a[242:243], v84 offset:4096\n"
    "buffer_load_dwordx4 v44, s[60:63], s49 offen offset:2048 sc1 lds\n"
    "v_mfma_f32_16x16x32_bf16 v[192:195], a[252:255], a[0:3], v[192:195]\n"
    "ds_read_b64_tr_b16 a[244:245], v78 offset:8192\n"
    "v_add_f32 v141, 1.0, v141\n"
    "v_mfma_f32_16x16x32_bf16 v[196:199], a[252:255], a[4:7], v[196:199]\n"
    "ds_read_b64_tr_b16 a[246:247], v78 offset:12288\n"
    "buffer_load_dwordx4 v45, s[60:63], s49 offen offset:3072 sc1 lds\n"
    "s_waitcnt lgkmcnt(8)\n"
    "v_mfma_f32_16x16x32_bf16 v[200:203], a[224:227], a[0:3], v[200:203]\n"
    "ds_read_b64_tr_b16 a[248:249], v79 offset:8192\n"
    "v_add_f32 v142, 1.0, v142\n"
    "v_mfma_f32_16x16x32_bf16 v[204:207], a[224:227], a[4:7], v[204:207]\n"
    "ds_read_b64_tr_b16 a[250:251], v79 offset:12288\n"
    "v_add_f32 v143, 1.0, v143\n"
    "v_mfma_f32_16x16x32_bf16 v[208:211], a[228:231], a[0:3], v[208:211]\n"
    "ds_read_b64_tr_b16 a[252:253], v80 offset:8192\n"
    "v_rcp_f32 v128, v128\n"
    "v_mfma_f32_16x16x32_bf16 v[212:215], a[228:231], a[4:7], v[212:215]\n"
    "ds_read_b64_tr_b16 a[254:255], v80 offset:12288\n"
    "v_rcp_f32 v129, v129\n"
    "s_waitcnt lgkmcnt(8)\n"
    "v_mfma_f32_16x16x32_bf16 v[216:219], a[232:235], a[0:3], v[216:219]\n"
    "ds_read_b64_tr_b16 a[224:225], v81 offset:8192\n"
    "v_rcp_f32 v130, v130\n"
    "v_mfma_f32_16x16x32_bf16 v[220:223], a[232:235], a[4:7], v[220:223]\n"
    "ds_read_b64_tr_b16 a[226:227], v81 offset:12288\n"
    "v_rcp_f32 v131, v131\n"
    "v_mfma_f32_16x16x32_bf16 v[224:227], a[236:239], a[0:3], v[224:227]\n"
    "ds_read_b64_tr_b16 a[228:229], v82 offset:8192\n"
    "v_rcp_f32 v132, v132\n"
    "v_mfma_f32_16x16x32_bf16 v[228:231], a[236:239], a[4:7], v[228:231]\n"
    "ds_read_b64_tr_b16 a[230:231], v82 offset:12288\n"
    "v_rcp_f32 v133, v133\n"
    "s_waitcnt lgkmcnt(8)\n"
    "v_mfma_f32_16x16x32_bf16 v[232:235], a[240:243], a[0:3], v[232:235]\n"
    "ds_read_b64_tr_b16 a[232:233], v83 offset:8192\n"
    "v_rcp_f32 v134, v134\n"
    "v_mfma_f32_16x16x32_bf16 v[236:239], a[240:243], a[4:7], v[236:239]\n"
    "ds_read_b64_tr_b16 a[234:235], v83 offset:12288\n"
    "v_rcp_f32 v135, v135\n"
    "v_mfma_f32_16x16x32_bf16 v[184:187], a[244:247], a[8:11], v[184:187]\n"
    "ds_read_b64_tr_b16 a[236:237], v84 offset:8192\n"
    "v_rcp_f32 v136, v136\n"
    "v_mfma_f32_16x16x32_bf16 v[188:191], a[244:247], a[12:15], v[188:191]\n"
    "ds_read_b64_tr_b16 a[238:239], v84 offset:12288\n"
    "v_rcp_f32 v137, v137\n"
    "s_waitcnt lgkmcnt(8)\n"
    "s_barrier\n"
    "v_mfma_f32_16x16x32_bf16 v[192:195], a[248:251], a[8:11], v[192:195]\n"
    "ds_read_b64_tr_b16 a[240:241], v64 offset:49152\n"
    "v_rcp_f32 v138, v138\n"
    "v_mfma_f32_16x16x32_bf16 v[196:199], a[248:251], a[12:15], v[196:199]\n"
    "ds_read_b64_tr_b16 a[242:243], v64 offset:53248\n"
    "s_add_u32 s76, %[rotk], 10\n"
    "s_and_b32 s76, s76, 15\n"
    "s_lshl_b32 s76, s76, 11\n"
    "buffer_load_dwordx4 a[48:51], v104, s[40:43], s76 offen offset:0 sc1\n"
    "v_mfma_f32_16x16x32_bf16 v[200:203], a[252:255], a[8:11], v[200:203]\n"
    "ds_read_b64_tr_b16 a[244:245], v65 offset:49152\n"
    "v_rcp_f32 v139, v139\n"
    "v_mfma_f32_16x16x32_bf16 v[204:207], a[252:255], a[12:15], v[204:207]\n"
    "ds_read_b64_tr_b16 a[246:247], v65 offset:53248\n"
    "buffer_load_dwordx4 a[56:59], v104, s[40:43], s76 offen offset:1024 sc1\n"
    "s_waitcnt lgkmcnt(8)\n"
    "v_mfma_f32_16x16x32_bf16 v[208:211], a[224:227], a[8:11], v[208:211]\n"
    "ds_read_b64_tr_b16 a[248:249], v66 offset:49152\n"
    "v_rcp_f32 v140, v140\n"
    "v_mfma_f32_16x16x32_bf16 v[212:215], a[224:227], a[12:15], v[212:215]\n"
    "ds_read_b64_tr_b16 a[250:251], v66 offset:53248\n"
    "buffer_load_dwordx4 a[52:55], v105, s[40:43], s76 offen offset:0 sc1\n"
    "v_mfma_f32_16x16x32_bf16 v[216:219], a[228:231], a[8:11], v[216:219]\n"
    "ds_read_b64_tr_b16 a[252:253], v67 offset:49152\n"
    "v_rcp_f32 v141, v141\n"
    "v_mfma_f32_16x16x32_bf16 v[220:223], a[228:231], a[12:15], v[220:223]\n"
    "ds_read_b64_tr_b16 a[254:255], v67 offset:53248\n"
    "buffer_load_dwordx4 a[60:63], v105, s[40:43], s76 offen offset:1024 sc1\n"
    "s_waitcnt lgkmcnt(8)\n"
    "v_mfma_f32_16x16x32_bf16 v[224:227], a[232:235], a[8:11], v[224:227]\n"
    "ds_read_b64_tr_b16 a[224:225], v68 offset:49152\n"
    "v_rcp_f32 v142, v142\n"
    "v_mfma_f32_16x16x32_bf16 v[228:231], a[232:235], a[12:15], v[228:231]\n"
    "ds_read_b64_tr_b16 a[226:227], v68 offset:53248\n"
    "s_add_u32 m0, s50, 32768\n"
    "s_add_u32 s49, %[rotk], 14\n"
    "s_and_b32 s49, s49, 15\n"
    "s_mul_i32 s49, s49, 14336\n"
    "buffer_load_dwordx4 v42, s[60:63], s49 offen offset:0 sc1 lds\n"
    "v_mfma_f32_16x16x32_bf16 v[232:235], a[236:239], a[8:11], v[232:235]\n"
    "ds_read_b64_tr_b16 a[228:229], v69 offset:49152\n"
    "v_rcp_f32 v143, v143\n"
    "v_mfma_f32_16x16x32_bf16 v[236:239], a[236:239], a[12:15], v[236:239]\n"
    "ds_read_b64_tr_b16 a[230:231], v69 offset:53248\n"
    "buffer_load_dwordx4 v43, s[60:63], s49 offen offset:1024 sc1 lds\n"
    "s_waitcnt vmcnt(18)\n"
    "s_waitcnt lgkmcnt(8)\n"
    "v_mfma_f32_16x16x32_bf16 v[184:187], a[240:243], a[16:19], v[184:187]\n"
    "ds_read_b64_tr_b16 a[232:233], v70 offset:49152\n"
    "v_mul_f32 v160, v160, v128\n"
    "v_mfma_f32_16x16x32_bf16 v[188:191], a[240:243], a[20:23], v[188:191]\n"
    "ds_read_b64_tr_b16 a[234:235], v70 offset:53248\n"
    "buffer_load_dwordx4 v44, s[60:63], s49 offen offset:2048 sc1 lds\n"
    "v_mfma_f32_16x16x32_bf16 v[192:195], a[244:247], a[16:19], v[192:195]\n"
    "ds_read_b64_tr_b16 a[236:237], v64 offset:57344\n"
    "v_mul_f32 v161, v161, v129\n"
    "v_mfma_f32_16x16x32_bf16 v[196:199], a[244:247], a[20:23], v[196:199]\n"
    "ds_read_b64_tr_b16 a[238:239], v64 offset:61440\n"
    "buffer_load_dwordx4 v45, s[60:63], s49 offen offset:3072 sc1 lds\n"
    "s_waitcnt lgkmcnt(8)\n"
    "v_mfma_f32_16x16x32_bf16 v[200:203], a[248:251], a[16:19], v[200:203]\n"
    "ds_read_b64_tr_b16 a[240:241], v65 offset:57344\n"
    "v_mul_f32 v162, v162, v130\n"
    "v_mfma_f32_16x16x32_bf16 v[204:207], a[248:251], a[20:23], v[204:207]\n"
    "ds_read_b64_tr_b16 a[242:243], v65 offset:61440\n"
    "v_mul_f32 v163, v163, v131\n"
    "v_mfma_f32_16x16x32_bf16 v[208:211], a[252:255], a[16:19], v[208:211]\n"
    "ds_read_b64_tr_b16 a[244:245], v66 offset:57344\n"
    "v_mul_f32 v164, v164, v132\n"
    "v_mfma_f32_16x16x32_bf16 v[212:215], a[252:255], a[20:23], v[212:215]\n"
    "ds_read_b64_tr_b16 a[246:247], v66 offset:61440\n"
    "v_mul_f32 v165, v165, v133\n"
    "s_waitcnt lgkmcnt(8)\n"
    "v_mfma_f32_16x16x32_bf16 v[216:219], a[224:227], a[16:19], v[216:219]\n"
    "ds_read_b64_tr_b16 a[248:249], v67 offset:57344\n"
    "v_mul_f32 v166, v166, v134\n"
    "v_mfma_f32_16x16x32_bf16 v[220:223], a[224:227], a[20:23], v[220:223]\n"
    "ds_read_b64_tr_b16 a[250:251], v67 offset:61440\n"
    "v_mul_f32 v167, v167, v135\n"
    "v_mfma_f32_16x16x32_bf16 v[224:227], a[228:231], a[16:19], v[224:227]\n"
    "ds_read_b64_tr_b16 a[252:253], v68 offset:57344\n"
    "v_mul_f32 v168, v168, v136\n"
    "v_mfma_f32_16x16x32_bf16 v[228:231], a[228:231], a[20:23], v[228:231]\n"
    "ds_read_b64_tr_b16 a[254:255], v68 offset:61440\n"
    "v_mul_f32 v169, v169, v137\n"
    "s_waitcnt lgkmcnt(8)\n"
    "v_mfma_f32_16x16x32_bf16 v[232:235], a[232:235], a[16:19], v[232:235]\n"
    "ds_read_b64_tr_b16 a[224:225], v69 offset:57344\n"
    "v_mul_f32 v170, v170, v138\n"
    "v_mfma_f32_16x16x32_bf16 v[236:239], a[232:235], a[20:23], v[236:239]\n"
    "ds_read_b64_tr_b16 a[226:227], v69 offset:61440\n"
    "v_mul_f32 v171, v171, v139\n"
    "v_mfma_f32_16x16x32_bf16 v[184:187], a[236:239], a[24:27], v[184:187]\n"
    "ds_read_b64_tr_b16 a[228:229], v70 offset:57344\n"
    "v_mul_f32 v172, v172, v140\n"
    "v_mfma_f32_16x16x32_bf16 v[188:191], a[236:239], a[28:31], v[188:191]\n"
    "ds_read_b64_tr_b16 a[230:231], v70 offset:61440\n"
    "v_mul_f32 v173, v173, v141\n"
    "s_waitcnt lgkmcnt(8)\n"
    "s_barrier\n"
    "v_mfma_f32_16x16x32_bf16 v[192:195], a[240:243], a[24:27], v[192:195]\n"
    "ds_read_b64_tr_b16 a[232:233], v78 offset:16384\n"
    "v_mul_f32 v174, v174, v142\n"
    "v_mfma_f32_16x16x32_bf16 v[196:199], a[240:243], a[28:31], v[196:199]\n"
    "ds_read_b64_tr_b16 a[234:235], v78 offset:20480\n"
    "s_add_u32 s76, %[rotk], 11\n"
    "s_and_b32 s76, s76, 15\n"
    "s_lshl_b32 s76, s76, 11\n"
    "buffer_load_dwordx4 a[64:67], v104, s[40:43], s76 offen offset:0 sc1\n"
    "v_mfma_f32_16x16x32_bf16 v[200:203], a[244:247], a[24:27], v[200:203]\n"
    "ds_read_b64_tr_b16 a[236:237], v79 offset:16384\n"
    "v_mul_f32 v175, v175, v143\n"
    "v_mfma_f32_16x16x32_bf16 v[204:207], a[244:247], a[28:31], v[204:207]\n"
    "ds_read_b64_tr_b16 a[238:239], v79 offset:20480\n"
    "buffer_load_dwordx4 a[72:75], v104, s[40:43], s76 offen offset:1024 sc1\n"
    "s_waitcnt lgkmcnt(8)\n"
    "v_mfma_f32_16x16x32_bf16 v[208:211], a[248:251], a[24:27], v[208:211]\n"
    "ds_read_b64_tr_b16 a[240:241], v80 offset:16384\n"
    "v_mfma_f32_16x16x32_bf16 v[212:215], a[248:251], a[28:31], v[212:215]\n"
    "ds_read_b64_tr_b16 a[242:243], v80 offset:20480\n"
    "buffer_load_dwordx4 a[68:71], v105, s[40:43], s76 offen offset:0 sc1\n"
    "v_mfma_f32_16x16x32_bf16 v[216:219], a[252:255], a[24:27], v[216:219]\n"
    "ds_read_b64_tr_b16 a[244:245], v81 offset:16384\n"
    "v_mfma_f32_16x16x32_bf16 v[220:223], a[252:255], a[28:31], v[220:223]\n"
    "ds_read_b64_tr_b16 a[246:247], v81 offset:20480\n"
    "buffer_load_dwordx4 a[76:79], v105, s[40:43], s76 offen offset:1024 sc1\n"
    "s_waitcnt lgkmcnt(8)\n"
    "v_mfma_f32_16x16x32_bf16 v[224:227], a[224:227], a[24:27], v[224:227]\n"
    "ds_read_b64_tr_b16 a[248:249], v82 offset:16384\n"
    "v_mfma_f32_16x16x32_bf16 v[228:231], a[224:227], a[28:31], v[228:231]\n"
    "ds_read_b64_tr_b16 a[250:251], v82 offset:20480\n"
    "s_add_u32 m0, s50, 131072\n"
    "s_add_u32 s49, %[rotk], 15\n"
    "s_and_b32 s49, s49, 15\n"
    "s_mul_i32 s49, s49, 14336\n"
    "buffer_load_dwordx4 v42, s[60:63], s49 offen offset:0 sc1 lds\n"
    "v_mfma_f32_16x16x32_bf16 v[232:235], a[228:231], a[24:27], v[232:235]\n"
    "ds_read_b64_tr_b16 a[252:253], v83 offset:16384\n"
    "v_mfma_f32_16x16x32_bf16 v[236:239], a[228:231], a[28:31], v[236:239]\n"
    "ds_read_b64_tr_b16 a[254:255], v83 offset:20480\n"
    "buffer_load_dwordx4 v43, s[60:63], s49 offen offset:1024 sc1 lds\n"
    "s_waitcnt vmcnt(18)\n"
    "s_waitcnt lgkmcnt(8)\n"
    "v_mfma_f32_16x16x32_bf16 v[184:187], a[232:235], a[32:35], v[184:187]\n"
    "ds_read_b64_tr_b16 a[224:225], v84 offset:16384\n"
    "v_mfma_f32_16x16x32_bf16 v[188:191], a[232:235], a[36:39], v[188:191]\n"
    "ds_read_b64_tr_b16 a[226:227], v84 offset:20480\n"
    "buffer_load_dwordx4 v44, s[60:63], s49 offen offset:2048 sc1 lds\n"
    "v_mfma_f32_16x16x32_bf16 v[192:195], a[236:239], a[32:35], v[192:195]\n"
    "ds_read_b64_tr_b16 a[228:229], v78 offset:24576\n"
    "v_mfma_f32_16x16x32_bf16 v[196:199], a[236:239], a[36:39], v[196:199]\n"
    "ds_read_b64_tr_b16 a[230:231], v78 offset:28672\n"
    "buffer_load_dwordx4 v45, s[60:63], s49 offen offset:3072 sc1 lds\n"
    "s_waitcnt lgkmcnt(8)\n"
    "v_mfma_f32_16x16x32_bf16 v[200:203], a[240:243], a[32:35], v[200:203]\n"
    "ds_read_b64_tr_b16 a[232:233], v79 offset:24576\n"
    "v_mfma_f32_16x16x32_bf16 v[204:207], a[240:243], a[36:39], v[204:207]\n"
    "ds_read_b64_tr_b16 a[234:235], v79 offset:28672\n"
    "v_mfma_f32_16x16x32_bf16 v[208:211], a[244:247], a[32:35], v[208:211]\n"
    "ds_read_b64_tr_b16 a[236:237], v80 offset:24576\n"
    "v_mfma_f32_16x16x32_bf16 v[212:215], a[244:247], a[36:39], v[212:215]\n"
    "ds_read_b64_tr_b16 a[238:239], v80 offset:28672\n"
    "s_waitcnt lgkmcnt(8)\n"
    "v_mfma_f32_16x16x32_bf16 v[216:219], a[248:251], a[32:35], v[216:219]\n"
    "ds_read_b64_tr_b16 a[240:241], v81 offset:24576\n"
    "v_mfma_f32_16x16x32_bf16 v[220:223], a[248:251], a[36:39], v[220:223]\n"
    "ds_read_b64_tr_b16 a[242:243], v81 offset:28672\n"
    "v_mfma_f32_16x16x32_bf16 v[224:227], a[252:255], a[32:35], v[224:227]\n"
    "ds_read_b64_tr_b16 a[244:245], v82 offset:24576\n"
    "v_mfma_f32_16x16x32_bf16 v[228:231], a[252:255], a[36:39], v[228:231]\n"
    "ds_read_b64_tr_b16 a[246:247], v82 offset:28672\n"
    "s_waitcnt lgkmcnt(8)\n"
    "v_mfma_f32_16x16x32_bf16 v[232:235], a[224:227], a[32:35], v[232:235]\n"
    "ds_read_b64_tr_b16 a[248:249], v83 offset:24576\n"
    "v_mfma_f32_16x16x32_bf16 v[236:239], a[224:227], a[36:39], v[236:239]\n"
    "ds_read_b64_tr_b16 a[250:251], v83 offset:28672\n"
    "v_mfma_f32_16x16x32_bf16 v[184:187], a[228:231], a[40:43], v[184:187]\n"
    "ds_read_b64_tr_b16 a[252:253], v84 offset:24576\n"
    "v_mfma_f32_16x16x32_bf16 v[188:191], a[228:231], a[44:47], v[188:191]\n"
    "ds_read_b64_tr_b16 a[254:255], v84 offset:28672\n"
    "s_waitcnt lgkmcnt(8)\n"
    "s_barrier\n"
    "v_mfma_f32_16x16x32_bf16 v[192:195], a[232:235], a[40:43], v[192:195]\n"
    "ds_read_b64_tr_b16 a[224:225], v64 offset:0\n"
    "v_mfma_f32_16x16x32_bf16 v[196:199], a[232:235], a[44:47], v[196:199]\n"
    "ds_read_b64_tr_b16 a[226:227], v64 offset:4096\n"
    "s_add_u32 s76, %[rotk], 12\n"
    "s_and_b32 s76, s76, 15\n"
    "s_lshl_b32 s76, s76, 11\n"
    "buffer_load_dwordx4 a[80:83], v104, s[40:43], s76 offen offset:0 sc1\n"
    "v_mfma_f32_16x16x32_bf16 v[200:203], a[236:239], a[40:43], v[200:203]\n"
    "ds_read_b64_tr_b16 a[228:229], v65 offset:0\n"
    "v_mfma_f32_16x16x32_bf16 v[204:207], a[236:239], a[44:47], v[204:207]\n"
    "ds_read_b64_tr_b16 a[230:231], v65 offset:4096\n"
    "buffer_load_dwordx4 a[88:91], v104, s[40:43], s76 offen offset:1024 sc1\n"
    "s_waitcnt lgkmcnt(8)\n"
    "v_mfma_f32_16x16x32_bf16 v[208:211], a[240:243], a[40:43], v[208:211]\n"
    "ds_read_b64_tr_b16 a[232:233], v66 offset:0\n"
    "v_mfma_f32_16x16x32_bf16 v[212:215], a[240:243], a[44:47], v[212:215]\n"
    "ds_read_b64_tr_b16 a[234:235], v66 offset:4096\n"
    "buffer_load_dwordx4 a[84:87], v105, s[40:43], s76 offen offset:0 sc1\n"
    "v_mfma_f32_16x16x32_bf16 v[216:219], a[244:247], a[40:43], v[216:219]\n"
    "ds_read_b64_tr_b16 a[236:237], v67 offset:0\n"
    "v_mfma_f32_16x16x32_bf16 v[220:223], a[244:247], a[44:47], v[220:223]\n"
    "ds_read_b64_tr_b16 a[238:239], v67 offset:4096\n"
    "buffer_load_dwordx4 a[92:95], v105, s[40:43], s76 offen offset:1024 sc1\n"
    "s_waitcnt lgkmcnt(8)\n"
    "v_mfma_f32_16x16x32_bf16 v[224:227], a[248:251], a[40:43], v[224:227]\n"
    "ds_read_b64_tr_b16 a[240:241], v68 offset:0\n"
    "v_mfma_f32_16x16x32_bf16 v[228:231], a[248:251], a[44:47], v[228:231]\n"
    "ds_read_b64_tr_b16 a[242:243], v68 offset:4096\n"
    "v_mfma_f32_16x16x32_bf16 v[232:235], a[252:255], a[40:43], v[232:235]\n"
    "ds_read_b64_tr_b16 a[244:245], v69 offset:0\n"
    "v_mfma_f32_16x16x32_bf16 v[236:239], a[252:255], a[44:47], v[236:239]\n"
    "ds_read_b64_tr_b16 a[246:247], v69 offset:4096\n"
    "s_waitcnt vmcnt(16)\n"
    "s_waitcnt lgkmcnt(8)\n"
    "s_lshl_b32 s52, %[tp2], 7\n"
    "s_lshl_b32 s53, %[kq], 5\n"
    "s_add_u32 s52, s52, s53\n"
    "s_and_b32 s53, %[w], 1\n"
    "s_lshl_b32 s53, s53, 4\n"
    "s_add_u32 s52, s52, s53\n"
    "s_mul_i32 s52, s52, 896\n"
    "s_mul_i32 s53, %[nq], 224\n"
    "s_add_u32 s52, s52, s53\n"
    "s_lshr_b32 s53, %[w], 1\n"
    "s_lshl_b32 s53, s53, 5\n"
    "s_add_u32 s78, s52, s53\n"
    "s_add_u32 s68, %[xlo], s78\n"
    "s_addc_u32 s69, %[xhi], 0\n"
    "s_and_b32 s69, s69, 0xffff\n"
    "s_mov_b32 s70, 13664\n"
    "s_mov_b32 s71, 0x20000\n"
    "s_add_u32 s44, %[outlo], s78\n"
    "s_addc_u32 s45, %[outhi], 0\n"
    "s_and_b32 s45, s45, 0xffff\n"
    "s_mov_b32 s46, 13664\n"
    "s_mov_b32 s47, 0x20000\n"
    "s_sub_u32 s70, s70, s53\n"
    "s_sub_u32 s46, s46, s53\n"
    "v_mul_u32_u24 v88, 896, v31\n"
    "v_lshl_add_u32 v88, v32, 3, v88\n"
    "buffer_load_dwordx2 v[108:109], v88, s[68:71], 0 offen offset:0\n"
    "buffer_load_dwordx2 v[110:111], v88, s[68:71], 0 offen offset:64\n"
    "buffer_load_dwordx2 v[112:113], v88, s[68:71], 0 offen offset:128\n"
    "buffer_load_dwordx2 v[114:115], v88, s[68:71], 0 offen offset:192\n"
    "v_mfma_f32_16x16x32_bf16 v[184:187], a[224:227], a[48:51], v[184:187]\n"
    "ds_read_b64_tr_b16 a[248:249], v70 offset:0\n"
    "v_mfma_f32_16x16x32_bf16 v[188:191], a[224:227], a[52:55], v[188:191]\n"
    "ds_read_b64_tr_b16 a[250:251], v70 offset:4096\n"
    "v_mfma_f32_16x16x32_bf16 v[192:195], a[228:231], a[48:51], v[192:195]\n"
    "ds_read_b64_tr_b16 a[252:253], v64 offset:8192\n"
    "v_mfma_f32_16x16x32_bf16 v[196:199], a[228:231], a[52:55], v[196:199]\n"
    "ds_read_b64_tr_b16 a[254:255], v64 offset:12288\n"
    "s_waitcnt lgkmcnt(8)\n"
    "v_mfma_f32_16x16x32_bf16 v[200:203], a[232:235], a[48:51], v[200:203]\n"
    "ds_read_b64_tr_b16 a[224:225], v65 offset:8192\n"
    "v_mfma_f32_16x16x32_bf16 v[204:207], a[232:235], a[52:55], v[204:207]\n"
    "ds_read_b64_tr_b16 a[226:227], v65 offset:12288\n"
    "v_mfma_f32_16x16x32_bf16 v[208:211], a[236:239], a[48:51], v[208:211]\n"
    "ds_read_b64_tr_b16 a[228:229], v66 offset:8192\n"
    "v_mfma_f32_16x16x32_bf16 v[212:215], a[236:239], a[52:55], v[212:215]\n"
    "ds_read_b64_tr_b16 a[230:231], v66 offset:12288\n"
    "s_waitcnt lgkmcnt(8)\n"
    "v_mfma_f32_16x16x32_bf16 v[216:219], a[240:243], a[48:51], v[216:219]\n"
    "ds_read_b64_tr_b16 a[232:233], v67 offset:8192\n"
    "v_mfma_f32_16x16x32_bf16 v[220:223], a[240:243], a[52:55], v[220:223]\n"
    "ds_read_b64_tr_b16 a[234:235], v67 offset:12288\n"
    "v_mfma_f32_16x16x32_bf16 v[224:227], a[244:247], a[48:51], v[224:227]\n"
    "ds_read_b64_tr_b16 a[236:237], v68 offset:8192\n"
    "v_mfma_f32_16x16x32_bf16 v[228:231], a[244:247], a[52:55], v[228:231]\n"
    "ds_read_b64_tr_b16 a[238:239], v68 offset:12288\n"
    "s_waitcnt lgkmcnt(8)\n"
    "v_mfma_f32_16x16x32_bf16 v[232:235], a[248:251], a[48:51], v[232:235]\n"
    "ds_read_b64_tr_b16 a[240:241], v69 offset:8192\n"
    "v_mfma_f32_16x16x32_bf16 v[236:239], a[248:251], a[52:55], v[236:239]\n"
    "ds_read_b64_tr_b16 a[242:243], v69 offset:12288\n"
    "v_mfma_f32_16x16x32_bf16 v[184:187], a[252:255], a[56:59], v[184:187]\n"
    "ds_read_b64_tr_b16 a[244:245], v70 offset:8192\n"
    "v_mfma_f32_16x16x32_bf16 v[188:191], a[252:255], a[60:63], v[188:191]\n"
    "ds_read_b64_tr_b16 a[246:247], v70 offset:12288\n"
    "s_waitcnt lgkmcnt(8)\n"
    "s_barrier\n"
    "v_mfma_f32_16x16x32_bf16 v[192:195], a[224:227], a[56:59], v[192:195]\n"
    "ds_read_b64_tr_b16 a[248:249], v64 offset:16384\n"
    "v_mfma_f32_16x16x32_bf16 v[196:199], a[224:227], a[60:63], v[196:199]\n"
    "ds_read_b64_tr_b16 a[250:251], v64 offset:20480\n"
    "s_add_u32 s76, %[rotk], 13\n"
    "s_and_b32 s76, s76, 15\n"
    "s_lshl_b32 s76, s76, 11\n"
    "buffer_load_dwordx4 a[96:99], v104, s[40:43], s76 offen offset:0 sc1\n"
    "v_mfma_f32_16x16x32_bf16 v[200:203], a[228:231], a[56:59], v[200:203]\n"
    "ds_read_b64_tr_b16 a[252:253], v65 offset:16384\n"
    "v_mfma_f32_16x16x32_bf16 v[204:207], a[228:231], a[60:63], v[204:207]\n"
    "ds_read_b64_tr_b16 a[254:255], v65 offset:20480\n"
    "buffer_load_dwordx4 a[104:107], v104, s[40:43], s76 offen offset:1024 sc1\n"
    "s_waitcnt lgkmcnt(8)\n"
    "v_mfma_f32_16x16x32_bf16 v[208:211], a[232:235], a[56:59], v[208:211]\n"
    "ds_read_b64_tr_b16 a[224:225], v66 offset:16384\n"
    "v_mfma_f32_16x16x32_bf16 v[212:215], a[232:235], a[60:63], v[212:215]\n"
    "ds_read_b64_tr_b16 a[226:227], v66 offset:20480\n"
    "buffer_load_dwordx4 a[100:103], v105, s[40:43], s76 offen offset:0 sc1\n"
    "v_mfma_f32_16x16x32_bf16 v[216:219], a[236:239], a[56:59], v[216:219]\n"
    "ds_read_b64_tr_b16 a[228:229], v67 offset:16384\n"
    "v_mfma_f32_16x16x32_bf16 v[220:223], a[236:239], a[60:63], v[220:223]\n"
    "ds_read_b64_tr_b16 a[230:231], v67 offset:20480\n"
    "buffer_load_dwordx4 a[108:111], v105, s[40:43], s76 offen offset:1024 sc1\n"
    "s_waitcnt lgkmcnt(8)\n"
    "v_mfma_f32_16x16x32_bf16 v[224:227], a[240:243], a[56:59], v[224:227]\n"
    "ds_read_b64_tr_b16 a[232:233], v68 offset:16384\n"
    "v_mfma_f32_16x16x32_bf16 v[228:231], a[240:243], a[60:63], v[228:231]\n"
    "ds_read_b64_tr_b16 a[234:235], v68 offset:20480\n"
    "v_mfma_f32_16x16x32_bf16 v[232:235], a[244:247], a[56:59], v[232:235]\n"
    "ds_read_b64_tr_b16 a[236:237], v69 offset:16384\n"
    "v_mfma_f32_16x16x32_bf16 v[236:239], a[244:247], a[60:63], v[236:239]\n"
    "ds_read_b64_tr_b16 a[238:239], v69 offset:20480\n"
    "s_waitcnt vmcnt(16)\n"
    "s_waitcnt lgkmcnt(8)\n"
    "v_mfma_f32_16x16x32_bf16 v[184:187], a[248:251], a[64:67], v[184:187]\n"
    "ds_read_b64_tr_b16 a[240:241], v70 offset:16384\n"
    "v_mfma_f32_16x16x32_bf16 v[188:191], a[248:251], a[68:71], v[188:191]\n"
    "ds_read_b64_tr_b16 a[242:243], v70 offset:20480\n"
    "v_mfma_f32_16x16x32_bf16 v[192:195], a[252:255], a[64:67], v[192:195]\n"
    "ds_read_b64_tr_b16 a[244:245], v64 offset:24576\n"
    "v_mfma_f32_16x16x32_bf16 v[196:199], a[252:255], a[68:71], v[196:199]\n"
    "ds_read_b64_tr_b16 a[246:247], v64 offset:28672\n"
    "s_waitcnt lgkmcnt(8)\n"
    "v_mfma_f32_16x16x32_bf16 v[200:203], a[224:227], a[64:67], v[200:203]\n"
    "ds_read_b64_tr_b16 a[248:249], v65 offset:24576\n"
    "v_mfma_f32_16x16x32_bf16 v[204:207], a[224:227], a[68:71], v[204:207]\n"
    "ds_read_b64_tr_b16 a[250:251], v65 offset:28672\n"
    "v_mfma_f32_16x16x32_bf16 v[208:211], a[228:231], a[64:67], v[208:211]\n"
    "ds_read_b64_tr_b16 a[252:253], v66 offset:24576\n"
    "v_mfma_f32_16x16x32_bf16 v[212:215], a[228:231], a[68:71], v[212:215]\n"
    "ds_read_b64_tr_b16 a[254:255], v66 offset:28672\n"
    "s_waitcnt lgkmcnt(8)\n"
    "v_mfma_f32_16x16x32_bf16 v[216:219], a[232:235], a[64:67], v[216:219]\n"
    "ds_read_b64_tr_b16 a[224:225], v67 offset:24576\n"
    "v_mfma_f32_16x16x32_bf16 v[220:223], a[232:235], a[68:71], v[220:223]\n"
    "ds_read_b64_tr_b16 a[226:227], v67 offset:28672\n"
    "v_mfma_f32_16x16x32_bf16 v[224:227], a[236:239], a[64:67], v[224:227]\n"
    "ds_read_b64_tr_b16 a[228:229], v68 offset:24576\n"
    "v_mfma_f32_16x16x32_bf16 v[228:231], a[236:239], a[68:71], v[228:231]\n"
    "ds_read_b64_tr_b16 a[230:231], v68 offset:28672\n"
    "s_waitcnt lgkmcnt(8)\n"
    "v_mfma_f32_16x16x32_bf16 v[232:235], a[240:243], a[64:67], v[232:235]\n"
    "ds_read_b64_tr_b16 a[232:233], v69 offset:24576\n"
    "v_mfma_f32_16x16x32_bf16 v[236:239], a[240:243], a[68:71], v[236:239]\n"
    "ds_read_b64_tr_b16 a[234:235], v69 offset:28672\n"
    "v_mfma_f32_16x16x32_bf16 v[184:187], a[244:247], a[72:75], v[184:187]\n"
    "ds_read_b64_tr_b16 a[236:237], v70 offset:24576\n"
    "v_mfma_f32_16x16x32_bf16 v[188:191], a[244:247], a[76:79], v[188:191]\n"
    "ds_read_b64_tr_b16 a[238:239], v70 offset:28672\n"
    "s_waitcnt lgkmcnt(8)\n"
    "s_barrier\n"
    "v_mfma_f32_16x16x32_bf16 v[192:195], a[248:251], a[72:75], v[192:195]\n"
    "ds_read_b64_tr_b16 a[240:241], v71 offset:0\n"
    "v_mfma_f32_16x16x32_bf16 v[196:199], a[248:251], a[76:79], v[196:199]\n"
    "ds_read_b64_tr_b16 a[242:243], v71 offset:4096\n"
    "s_add_u32 s76, %[rotk], 14\n"
    "s_and_b32 s76, s76, 15\n"
    "s_lshl_b32 s76, s76, 11\n"
    "buffer_load_dwordx4 %[q0], v104, s[40:43], s76 offen offset:0 sc1\n"
    "v_mfma_f32_16x16x32_bf16 v[200:203], a[252:255], a[72:75], v[200:203]\n"
    "ds_read_b64_tr_b16 a[244:245], v72 offset:0\n"
    "v_mfma_f32_16x16x32_bf16 v[204:207], a[252:255], a[76:79], v[204:207]\n"
    "ds_read_b64_tr_b16 a[246:247], v72 offset:4096\n"
    "buffer_load_dwordx4 %[q2], v104, s[40:43], s76 offen offset:1024 sc1\n"
    "s_waitcnt lgkmcnt(8)\n"
    "v_mfma_f32_16x16x32_bf16 v[208:211], a[224:227], a[72:75], v[208:211]\n"
    "ds_read_b64_tr_b16 a[248:249], v73 offset:0\n"
    "v_mfma_f32_16x16x32_bf16 v[212:215], a[224:227], a[76:79], v[212:215]\n"
    "ds_read_b64_tr_b16 a[250:251], v73 offset:4096\n"
    "buffer_load_dwordx4 %[q1], v105, s[40:43], s76 offen offset:0 sc1\n"
    "v_mfma_f32_16x16x32_bf16 v[216:219], a[228:231], a[72:75], v[216:219]\n"
    "ds_read_b64_tr_b16 a[252:253], v74 offset:0\n"
    "v_mfma_f32_16x16x32_bf16 v[220:223], a[228:231], a[76:79], v[220:223]\n"
    "ds_read_b64_tr_b16 a[254:255], v74 offset:4096\n"
    "buffer_load_dwordx4 %[q3], v105, s[40:43], s76 offen offset:1024 sc1\n"
    "s_waitcnt lgkmcnt(8)\n"
    "v_mfma_f32_16x16x32_bf16 v[224:227], a[232:235], a[72:75], v[224:227]\n"
    "ds_read_b64_tr_b16 a[224:225], v75 offset:0\n"
    "v_mfma_f32_16x16x32_bf16 v[228:231], a[232:235], a[76:79], v[228:231]\n"
    "ds_read_b64_tr_b16 a[226:227], v75 offset:4096\n"
    "v_mfma_f32_16x16x32_bf16 v[232:235], a[236:239], a[72:75], v[232:235]\n"
    "ds_read_b64_tr_b16 a[228:229], v76 offset:0\n"
    "v_mfma_f32_16x16x32_bf16 v[236:239], a[236:239], a[76:79], v[236:239]\n"
    "ds_read_b64_tr_b16 a[230:231], v76 offset:4096\n"
    "s_waitcnt vmcnt(12)\n"
    "s_waitcnt lgkmcnt(8)\n"
    "v_mfma_f32_16x16x32_bf16 v[184:187], a[240:243], a[80:83], v[184:187]\n"
    "ds_read_b64_tr_b16 a[232:233], v77 offset:0\n"
    "v_mfma_f32_16x16x32_bf16 v[188:191], a[240:243], a[84:87], v[188:191]\n"
    "ds_read_b64_tr_b16 a[234:235], v77 offset:4096\n"
    "v_mfma_f32_16x16x32_bf16 v[192:195], a[244:247], a[80:83], v[192:195]\n"
    "ds_read_b64_tr_b16 a[236:237], v71 offset:8192\n"
    "v_mfma_f32_16x16x32_bf16 v[196:199], a[244:247], a[84:87], v[196:199]\n"
    "ds_read_b64_tr_b16 a[238:239], v71 offset:12288\n"
    "s_waitcnt lgkmcnt(8)\n"
    "v_mfma_f32_16x16x32_bf16 v[200:203], a[248:251], a[80:83], v[200:203]\n"
    "ds_read_b64_tr_b16 a[240:241], v72 offset:8192\n"
    "v_mfma_f32_16x16x32_bf16 v[204:207], a[248:251], a[84:87], v[204:207]\n"
    "ds_read_b64_tr_b16 a[242:243], v72 offset:12288\n"
    "v_mfma_f32_16x16x32_bf16 v[208:211], a[252:255], a[80:83], v[208:211]\n"
    "ds_read_b64_tr_b16 a[244:245], v73 offset:8192\n"
    "v_mfma_f32_16x16x32_bf16 v[212:215], a[252:255], a[84:87], v[212:215]\n"
    "ds_read_b64_tr_b16 a[246:247], v73 offset:12288\n"
    "s_waitcnt lgkmcnt(8)\n"
    "v_mfma_f32_16x16x32_bf16 v[216:219], a[224:227], a[80:83], v[216:219]\n"
    "ds_read_b64_tr_b16 a[248:249], v74 offset:8192\n"
    "v_mfma_f32_16x16x32_bf16 v[220:223], a[224:227], a[84:87], v[220:223]\n"
    "ds_read_b64_tr_b16 a[250:251], v74 offset:12288\n"
    "v_mfma_f32_16x16x32_bf16 v[224:227], a[228:231], a[80:83], v[224:227]\n"
    "ds_read_b64_tr_b16 a[252:253], v75 offset:8192\n"
    "v_mfma_f32_16x16x32_bf16 v[228:231], a[228:231], a[84:87], v[228:231]\n"
    "ds_read_b64_tr_b16 a[254:255], v75 offset:12288\n"
    "s_waitcnt lgkmcnt(8)\n"
    "v_mfma_f32_16x16x32_bf16 v[232:235], a[232:235], a[80:83], v[232:235]\n"
    "ds_read_b64_tr_b16 a[224:225], v76 offset:8192\n"
    "v_mfma_f32_16x16x32_bf16 v[236:239], a[232:235], a[84:87], v[236:239]\n"
    "ds_read_b64_tr_b16 a[226:227], v76 offset:12288\n"
    "v_mfma_f32_16x16x32_bf16 v[184:187], a[236:239], a[88:91], v[184:187]\n"
    "ds_read_b64_tr_b16 a[228:229], v77 offset:8192\n"
    "v_mfma_f32_16x16x32_bf16 v[188:191], a[236:239], a[92:95], v[188:191]\n"
    "ds_read_b64_tr_b16 a[230:231], v77 offset:12288\n"
    "s_waitcnt lgkmcnt(8)\n"
    "s_barrier\n"
    "v_mfma_f32_16x16x32_bf16 v[192:195], a[240:243], a[88:91], v[192:195]\n"
    "ds_read_b64_tr_b16 a[232:233], v71 offset:16384\n"
    "v_mfma_f32_16x16x32_bf16 v[196:199], a[240:243], a[92:95], v[196:199]\n"
    "ds_read_b64_tr_b16 a[234:235], v71 offset:20480\n"
    "s_add_u32 s76, %[rotk], 15\n"
    "s_and_b32 s76, s76, 15\n"
    "s_lshl_b32 s76, s76, 11\n"
    "buffer_load_dwordx4 %[q4], v104, s[40:43], s76 offen offset:0 sc1\n"
    "v_mfma_f32_16x16x32_bf16 v[200:203], a[244:247], a[88:91], v[200:203]\n"
    "ds_read_b64_tr_b16 a[236:237], v72 offset:16384\n"
    "v_mfma_f32_16x16x32_bf16 v[204:207], a[244:247], a[92:95], v[204:207]\n"
    "ds_read_b64_tr_b16 a[238:239], v72 offset:20480\n"
    "buffer_load_dwordx4 %[q6], v104, s[40:43], s76 offen offset:1024 sc1\n"
    "s_waitcnt lgkmcnt(8)\n"
    "v_mfma_f32_16x16x32_bf16 v[208:211], a[248:251], a[88:91], v[208:211]\n"
    "ds_read_b64_tr_b16 a[240:241], v73 offset:16384\n"
    "v_mfma_f32_16x16x32_bf16 v[212:215], a[248:251], a[92:95], v[212:215]\n"
    "ds_read_b64_tr_b16 a[242:243], v73 offset:20480\n"
    "buffer_load_dwordx4 %[q5], v105, s[40:43], s76 offen offset:0 sc1\n"
    "v_mfma_f32_16x16x32_bf16 v[216:219], a[252:255], a[88:91], v[216:219]\n"
    "ds_read_b64_tr_b16 a[244:245], v74 offset:16384\n"
    "v_mfma_f32_16x16x32_bf16 v[220:223], a[252:255], a[92:95], v[220:223]\n"
    "ds_read_b64_tr_b16 a[246:247], v74 offset:20480\n"
    "buffer_load_dwordx4 %[q7], v105, s[40:43], s76 offen offset:1024 sc1\n"
    "s_waitcnt lgkmcnt(8)\n"
    "v_mfma_f32_16x16x32_bf16 v[224:227], a[224:227], a[88:91], v[224:227]\n"
    "ds_read_b64_tr_b16 a[248:249], v75 offset:16384\n"
    "v_mfma_f32_16x16x32_bf16 v[228:231], a[224:227], a[92:95], v[228:231]\n"
    "ds_read_b64_tr_b16 a[250:251], v75 offset:20480\n"
    "v_mfma_f32_16x16x32_bf16 v[232:235], a[228:231], a[88:91], v[232:235]\n"
    "ds_read_b64_tr_b16 a[252:253], v76 offset:16384\n"
    "v_mfma_f32_16x16x32_bf16 v[236:239], a[228:231], a[92:95], v[236:239]\n"
    "ds_read_b64_tr_b16 a[254:255], v76 offset:20480\n"
    "s_waitcnt vmcnt(8)\n"
    "s_waitcnt lgkmcnt(8)\n"
    "v_mfma_f32_16x16x32_bf16 v[184:187], a[232:235], a[96:99], v[184:187]\n"
    "ds_read_b64_tr_b16 a[224:225], v77 offset:16384\n"
    "v_mfma_f32_16x16x32_bf16 v[188:191], a[232:235], a[100:103], v[188:191]\n"
    "ds_read_b64_tr_b16 a[226:227], v77 offset:20480\n"
    "v_mfma_f32_16x16x32_bf16 v[192:195], a[236:239], a[96:99], v[192:195]\n"
    "ds_read_b64_tr_b16 a[228:229], v71 offset:24576\n"
    "v_mfma_f32_16x16x32_bf16 v[196:199], a[236:239], a[100:103], v[196:199]\n"
    "ds_read_b64_tr_b16 a[230:231], v71 offset:28672\n"
    "s_waitcnt lgkmcnt(8)\n"
    "v_mfma_f32_16x16x32_bf16 v[200:203], a[240:243], a[96:99], v[200:203]\n"
    "ds_read_b64_tr_b16 a[232:233], v72 offset:24576\n"
    "v_mfma_f32_16x16x32_bf16 v[204:207], a[240:243], a[100:103], v[204:207]\n"
    "ds_read_b64_tr_b16 a[234:235], v72 offset:28672\n"
    "v_mfma_f32_16x16x32_bf16 v[208:211], a[244:247], a[96:99], v[208:211]\n"
    "ds_read_b64_tr_b16 a[236:237], v73 offset:24576\n"
    "v_mfma_f32_16x16x32_bf16 v[212:215], a[244:247], a[100:103], v[212:215]\n"
    "ds_read_b64_tr_b16 a[238:239], v73 offset:28672\n"
    "s_waitcnt lgkmcnt(8)\n"
    "v_mfma_f32_16x16x32_bf16 v[216:219], a[248:251], a[96:99], v[216:219]\n"
    "ds_read_b64_tr_b16 a[240:241], v74 offset:24576\n"
    "v_mfma_f32_16x16x32_bf16 v[220:223], a[248:251], a[100:103], v[220:223]\n"
    "ds_read_b64_tr_b16 a[242:243], v74 offset:28672\n"
    "v_mfma_f32_16x16x32_bf16 v[224:227], a[252:255], a[96:99], v[224:227]\n"
    "ds_read_b64_tr_b16 a[244:245], v75 offset:24576\n"
    "v_mfma_f32_16x16x32_bf16 v[228:231], a[252:255], a[100:103], v[228:231]\n"
    "ds_read_b64_tr_b16 a[246:247], v75 offset:28672\n"
    "s_waitcnt lgkmcnt(8)\n"
    "v_mfma_f32_16x16x32_bf16 v[232:235], a[224:227], a[96:99], v[232:235]\n"
    "ds_read_b64_tr_b16 a[248:249], v76 offset:24576\n"
    "v_mfma_f32_16x16x32_bf16 v[236:239], a[224:227], a[100:103], v[236:239]\n"
    "ds_read_b64_tr_b16 a[250:251], v76 offset:28672\n"
    "v_mfma_f32_16x16x32_bf16 v[184:187], a[228:231], a[104:107], v[184:187]\n"
    "ds_read_b64_tr_b16 a[252:253], v77 offset:24576\n"
    "v_mfma_f32_16x16x32_bf16 v[188:191], a[228:231], a[108:111], v[188:191]\n"
    "ds_read_b64_tr_b16 a[254:255], v77 offset:28672\n"
    "s_waitcnt lgkmcnt(8)\n"
    "s_barrier\n"
    "v_mfma_f32_16x16x32_bf16 v[192:195], a[232:235], a[104:107], v[192:195]\n"
    "ds_read_b64_tr_b16 a[224:225], v64 offset:32768\n"
    "v_mfma_f32_16x16x32_bf16 v[196:199], a[232:235], a[108:111], v[196:199]\n"
    "ds_read_b64_tr_b16 a[226:227], v64 offset:36864\n"
    "v_mfma_f32_16x16x32_bf16 v[200:203], a[236:239], a[104:107], v[200:203]\n"
    "ds_read_b64_tr_b16 a[228:229], v65 offset:32768\n"
    "v_mfma_f32_16x16x32_bf16 v[204:207], a[236:239], a[108:111], v[204:207]\n"
    "ds_read_b64_tr_b16 a[230:231], v65 offset:36864\n"
    "s_waitcnt lgkmcnt(8)\n"
    "v_mfma_f32_16x16x32_bf16 v[208:211], a[240:243], a[104:107], v[208:211]\n"
    "ds_read_b64_tr_b16 a[232:233], v66 offset:32768\n"
    "v_mfma_f32_16x16x32_bf16 v[212:215], a[240:243], a[108:111], v[212:215]\n"
    "ds_read_b64_tr_b16 a[234:235], v66 offset:36864\n"
    "v_mfma_f32_16x16x32_bf16 v[216:219], a[244:247], a[104:107], v[216:219]\n"
    "ds_read_b64_tr_b16 a[236:237], v67 offset:32768\n"
    "v_mfma_f32_16x16x32_bf16 v[220:223], a[244:247], a[108:111], v[220:223]\n"
    "ds_read_b64_tr_b16 a[238:239], v67 offset:36864\n"
    "s_waitcnt lgkmcnt(8)\n"
    "v_mfma_f32_16x16x32_bf16 v[224:227], a[248:251], a[104:107], v[224:227]\n"
    "ds_read_b64_tr_b16 a[240:241], v68 offset:32768\n"
    "v_mfma_f32_16x16x32_bf16 v[228:231], a[248:251], a[108:111], v[228:231]\n"
    "ds_read_b64_tr_b16 a[242:243], v68 offset:36864\n"
    "v_mfma_f32_16x16x32_bf16 v[232:235], a[252:255], a[104:107], v[232:235]\n"
    "ds_read_b64_tr_b16 a[244:245], v69 offset:32768\n"
    "v_mfma_f32_16x16x32_bf16 v[236:239], a[252:255], a[108:111], v[236:239]\n"
    "ds_read_b64_tr_b16 a[246:247], v69 offset:36864\n"
    "s_waitcnt vmcnt(4)\n"
    "s_waitcnt lgkmcnt(8)\n"
    "v_mfma_f32_16x16x32_bf16 v[184:187], a[224:227], %[q0], v[184:187]\n"
    "ds_read_b64_tr_b16 a[248:249], v70 offset:32768\n"
    "v_mfma_f32_16x16x32_bf16 v[188:191], a[224:227], %[q1], v[188:191]\n"
    "ds_read_b64_tr_b16 a[250:251], v70 offset:36864\n"
    "v_mfma_f32_16x16x32_bf16 v[192:195], a[228:231], %[q0], v[192:195]\n"
    "ds_read_b64_tr_b16 a[252:253], v64 offset:40960\n"
    "v_mfma_f32_16x16x32_bf16 v[196:199], a[228:231], %[q1], v[196:199]\n"
    "ds_read_b64_tr_b16 a[254:255], v64 offset:45056\n"
    "s_waitcnt lgkmcnt(8)\n"
    "v_mfma_f32_16x16x32_bf16 v[200:203], a[232:235], %[q0], v[200:203]\n"
    "ds_read_b64_tr_b16 a[224:225], v65 offset:40960\n"
    "v_mfma_f32_16x16x32_bf16 v[204:207], a[232:235], %[q1], v[204:207]\n"
    "ds_read_b64_tr_b16 a[226:227], v65 offset:45056\n"
    "v_mfma_f32_16x16x32_bf16 v[208:211], a[236:239], %[q0], v[208:211]\n"
    "ds_read_b64_tr_b16 a[228:229], v66 offset:40960\n"
    "v_mfma_f32_16x16x32_bf16 v[212:215], a[236:239], %[q1], v[212:215]\n"
    "ds_read_b64_tr_b16 a[230:231], v66 offset:45056\n"
    "s_waitcnt lgkmcnt(8)\n"
    "v_mfma_f32_16x16x32_bf16 v[216:219], a[240:243], %[q0], v[216:219]\n"
    "ds_read_b64_tr_b16 a[232:233], v67 offset:40960\n"
    "v_mfma_f32_16x16x32_bf16 v[220:223], a[240:243], %[q1], v[220:223]\n"
    "ds_read_b64_tr_b16 a[234:235], v67 offset:45056\n"
    "v_mfma_f32_16x16x32_bf16 v[224:227], a[244:247], %[q0], v[224:227]\n"
    "ds_read_b64_tr_b16 a[236:237], v68 offset:40960\n"
    "v_mfma_f32_16x16x32_bf16 v[228:231], a[244:247], %[q1], v[228:231]\n"
    "ds_read_b64_tr_b16 a[238:239], v68 offset:45056\n"
    "s_waitcnt lgkmcnt(8)\n"
    "v_mfma_f32_16x16x32_bf16 v[232:235], a[248:251], %[q0], v[232:235]\n"
    "ds_read_b64_tr_b16 a[240:241], v69 offset:40960\n"
    "v_mfma_f32_16x16x32_bf16 v[236:239], a[248:251], %[q1], v[236:239]\n"
    "ds_read_b64_tr_b16 a[242:243], v69 offset:45056\n"
    "v_mfma_f32_16x16x32_bf16 v[184:187], a[252:255], %[q2], v[184:187]\n"
    "ds_read_b64_tr_b16 a[244:245], v70 offset:40960\n"
    "v_mfma_f32_16x16x32_bf16 v[188:191], a[252:255], %[q3], v[188:191]\n"
    "ds_read_b64_tr_b16 a[246:247], v70 offset:45056\n"
    "s_waitcnt lgkmcnt(8)\n"
    "s_barrier\n"
    "v_mfma_f32_16x16x32_bf16 v[192:195], a[224:227], %[q2], v[192:195]\n"
    "ds_read_b64_tr_b16 a[248:249], v78 offset:0\n"
    "v_mfma_f32_16x16x32_bf16 v[196:199], a[224:227], %[q3], v[196:199]\n"
    "ds_read_b64_tr_b16 a[250:251], v78 offset:4096\n"
    "v_mfma_f32_16x16x32_bf16 v[200:203], a[228:231], %[q2], v[200:203]\n"
    "ds_read_b64_tr_b16 a[252:253], v79 offset:0\n"
    "v_mfma_f32_16x16x32_bf16 v[204:207], a[228:231], %[q3], v[204:207]\n"
    "ds_read_b64_tr_b16 a[254:255], v79 offset:4096\n"
    "s_waitcnt lgkmcnt(8)\n"
    "v_mfma_f32_16x16x32_bf16 v[208:211], a[232:235], %[q2], v[208:211]\n"
    "ds_read_b64_tr_b16 a[224:225], v80 offset:0\n"
    "v_mfma_f32_16x16x32_bf16 v[212:215], a[232:235], %[q3], v[212:215]\n"
    "ds_read_b64_tr_b16 a[226:227], v80 offset:4096\n"
    "v_mfma_f32_16x16x32_bf16 v[216:219], a[236:239], %[q2], v[216:219]\n"
    "ds_read_b64_tr_b16 a[228:229], v81 offset:0\n"
    "v_mfma_f32_16x16x32_bf16 v[220:223], a[236:239], %[q3], v[220:223]\n"
    "ds_read_b64_tr_b16 a[230:231], v81 offset:4096\n"
    "s_waitcnt lgkmcnt(8)\n"
    "v_mfma_f32_16x16x32_bf16 v[224:227], a[240:243], %[q2], v[224:227]\n"
    "ds_read_b64_tr_b16 a[232:233], v82 offset:0\n"
    "v_mfma_f32_16x16x32_bf16 v[228:231], a[240:243], %[q3], v[228:231]\n"
    "ds_read_b64_tr_b16 a[234:235], v82 offset:4096\n"
    "v_mfma_f32_16x16x32_bf16 v[232:235], a[244:247], %[q2], v[232:235]\n"
    "ds_read_b64_tr_b16 a[236:237], v83 offset:0\n"
    "v_mfma_f32_16x16x32_bf16 v[236:239], a[244:247], %[q3], v[236:239]\n"
    "ds_read_b64_tr_b16 a[238:239], v83 offset:4096\n"
    "s_waitcnt vmcnt(0)\n"
    "s_waitcnt lgkmcnt(8)\n"
    "v_mfma_f32_16x16x32_bf16 v[184:187], a[248:251], %[q4], v[184:187]\n"
    "ds_read_b64_tr_b16 a[240:241], v84 offset:0\n"
    "v_mfma_f32_16x16x32_bf16 v[188:191], a[248:251], %[q5], v[188:191]\n"
    "ds_read_b64_tr_b16 a[242:243], v84 offset:4096\n"
    "v_mfma_f32_16x16x32_bf16 v[192:195], a[252:255], %[q4], v[192:195]\n"
    "ds_read_b64_tr_b16 a[244:245], v78 offset:8192\n"
    "v_mfma_f32_16x16x32_bf16 v[196:199], a[252:255], %[q5], v[196:199]\n"
    "ds_read_b64_tr_b16 a[246:247], v78 offset:12288\n"
    "s_waitcnt lgkmcnt(8)\n"
    "v_mfma_f32_16x16x32_bf16 v[200:203], a[224:227], %[q4], v[200:203]\n"
    "ds_read_b64_tr_b16 a[248:249], v79 offset:8192\n"
    "v_mfma_f32_16x16x32_bf16 v[204:207], a[224:227], %[q5], v[204:207]\n"
    "ds_read_b64_tr_b16 a[250:251], v79 offset:12288\n"
    "v_mfma_f32_16x16x32_bf16 v[208:211], a[228:231], %[q4], v[208:211]\n"
    "ds_read_b64_tr_b16 a[252:253], v80 offset:8192\n"
    "v_mfma_f32_16x16x32_bf16 v[212:215], a[228:231], %[q5], v[212:215]\n"
    "ds_read_b64_tr_b16 a[254:255], v80 offset:12288\n"
    "s_waitcnt lgkmcnt(8)\n"
    "v_mfma_f32_16x16x32_bf16 v[216:219], a[232:235], %[q4], v[216:219]\n"
    "ds_read_b64_tr_b16 a[224:225], v81 offset:8192\n"
    "v_mfma_f32_16x16x32_bf16 v[220:223], a[232:235], %[q5], v[220:223]\n"
    "ds_read_b64_tr_b16 a[226:227], v81 offset:12288\n"
    "v_mfma_f32_16x16x32_bf16 v[224:227], a[236:239], %[q4], v[224:227]\n"
    "ds_read_b64_tr_b16 a[228:229], v82 offset:8192\n"
    "v_mfma_f32_16x16x32_bf16 v[228:231], a[236:239], %[q5], v[228:231]\n"
    "ds_read_b64_tr_b16 a[230:231], v82 offset:12288\n"
    "s_waitcnt lgkmcnt(8)\n"
    "v_mfma_f32_16x16x32_bf16 v[232:235], a[240:243], %[q4], v[232:235]\n"
    "ds_read_b64_tr_b16 a[232:233], v83 offset:8192\n"
    "v_mfma_f32_16x16x32_bf16 v[236:239], a[240:243], %[q5], v[236:239]\n"
    "ds_read_b64_tr_b16 a[234:235], v83 offset:12288\n"
    "v_mfma_f32_16x16x32_bf16 v[184:187], a[244:247], %[q6], v[184:187]\n"
    "ds_read_b64_tr_b16 a[236:237], v84 offset:8192\n"
    "v_mfma_f32_16x16x32_bf16 v[188:191], a[244:247], %[q7], v[188:191]\n"
    "ds_read_b64_tr_b16 a[238:239], v84 offset:12288\n"
    "s_waitcnt lgkmcnt(8)\n"
    "v_mfma_f32_16x16x32_bf16 v[192:195], a[248:251], %[q6], v[192:195]\n"
    "v_mfma_f32_16x16x32_bf16 v[196:199], a[248:251], %[q7], v[196:199]\n"
    "v_mfma_f32_16x16x32_bf16 v[200:203], a[252:255], %[q6], v[200:203]\n"
    "v_mfma_f32_16x16x32_bf16 v[204:207], a[252:255], %[q7], v[204:207]\n"
    "s_waitcnt lgkmcnt(4)\n"
    "v_mfma_f32_16x16x32_bf16 v[208:211], a[224:227], %[q6], v[208:211]\n"
    "v_mfma_f32_16x16x32_bf16 v[212:215], a[224:227], %[q7], v[212:215]\n"
    "v_mfma_f32_16x16x32_bf16 v[216:219], a[228:231], %[q6], v[216:219]\n"
    "v_mfma_f32_16x16x32_bf16 v[220:223], a[228:231], %[q7], v[220:223]\n"
    "s_waitcnt lgkmcnt(0)\n"
    "v_mfma_f32_16x16x32_bf16 v[224:227], a[232:235], %[q6], v[224:227]\n"
    "v_mfma_f32_16x16x32_bf16 v[228:231], a[232:235], %[q7], v[228:231]\n"
    "v_mfma_f32_16x16x32_bf16 v[232:235], a[236:239], %[q6], v[232:235]\n"
    "v_mfma_f32_16x16x32_bf16 v[236:239], a[236:239], %[q7], v[236:239]\n"
    "s_lshl_b32 s78, %[tp2], 2\n"
    "s_add_u32 s78, s78, %[nq]\n"
    "s_lshl_b32 s52, s78, 2\n"
    "s_add_u32 s52, s52, %[w]\n"
    "s_lshl_b32 s52, s52, 2\n"
    "s_add_u32 s52, s52, %[kq]\n"
    "s_lshl_b32 s52, s52, 13\n"
    "s_add_u32 s52, s52, 50331648\n"
    "s_add_u32 s64, %[wslo], s52\n"
    "s_addc_u32 s65, %[wshi], 0\n"
    "s_and_b32 s65, s65, 0xffff\n"
    "s_mov_b32 s66, 8192\n"
    "s_mov_b32 s67, 0x20000\n"
    "s_add_u32 s52, %[ldsb], 98304\n"
    "v_add_u32 v86, s52, v33\n"
    "v_cvt_pk_bf16_f32 v128, v184, v185\n"
    "v_cvt_pk_bf16_f32 v129, v186, v187\n"
    "v_cvt_pk_bf16_f32 v130, v200, v201\n"
    "v_cvt_pk_bf16_f32 v131, v202, v203\n"
    "v_cvt_pk_bf16_f32 v132, v216, v217\n"
    "v_cvt_pk_bf16_f32 v133, v218, v219\n"
    "v_cvt_pk_bf16_f32 v134, v232, v233\n"
    "v_cvt_pk_bf16_f32 v135, v234, v235\n"
    "v_cvt_pk_bf16_f32 v136, v188, v189\n"
    "v_cvt_pk_bf16_f32 v137, v190, v191\n"
    "v_cvt_pk_bf16_f32 v138, v204, v205\n"
    "v_cvt_pk_bf16_f32 v139, v206, v207\n"
    "v_cvt_pk_bf16_f32 v140, v220, v221\n"
    "v_cvt_pk_bf16_f32 v141, v222, v223\n"
    "v_cvt_pk_bf16_f32 v142, v236, v237\n"
    "v_cvt_pk_bf16_f32 v143, v238, v239\n"
    "v_cvt_pk_bf16_f32 v144, v192, v193\n"
    "v_cvt_pk_bf16_f32 v145, v194, v195\n"
    "v_cvt_pk_bf16_f32 v146, v208, v209\n"
    "v_cvt_pk_bf16_f32 v147, v210, v211\n"
    "v_cvt_pk_bf16_f32 v148, v224, v225\n"
    "v_cvt_pk_bf16_f32 v149, v226, v227\n"
    "v_cvt_pk_bf16_f32 v150, v224, v225\n"
    "v_cvt_pk_bf16_f32 v151, v226, v227\n"
    "v_cvt_pk_bf16_f32 v152, v196, v197\n"
    "v_cvt_pk_bf16_f32 v153, v198, v199\n"
    "v_cvt_pk_bf16_f32 v154, v212, v213\n"
    "v_cvt_pk_bf16_f32 v155, v214, v215\n"
    "v_cvt_pk_bf16_f32 v156, v228, v229\n"
    "v_cvt_pk_bf16_f32 v157, v230, v231\n"
    "v_cvt_pk_bf16_f32 v158, v228, v229\n"
    "v_cvt_pk_bf16_f32 v159, v230, v231\n"
    "s_cmp_eq_u32 %[w], %[kq]\n"
    "s_cbranch_scc1 .Lt8_%=\n"
    "buffer_store_dwordx4 v[128:131], v33, s[64:67], 0 offen offset:0 sc1\n"
    "buffer_store_dwordx4 v[132:135], v33, s[64:67], 0 offen offset:1024 sc1\n"
    "buffer_store_dwordx4 v[136:139], v33, s[64:67], 0 offen offset:2048 sc1\n"
    "buffer_store_dwordx4 v[140:143], v33, s[64:67], 0 offen offset:3072 sc1\n"
    "buffer_store_dwordx4 v[144:147], v33, s[64:67], s72 offen offset:0 sc1\n"
    "buffer_store_dwordx4 v[148:151], v33, s[64:67], s72 offen offset:1024 sc1\n"
    "buffer_store_dwordx4 v[152:155], v33, s[64:67], s72 offen offset:2048 sc1\n"
    "buffer_store_dwordx4 v[156:159], v33, s[64:67], s72 offen offset:3072 sc1\n"
    "s_lshl_b32 s75, s78, 2\n"
    "s_add_u32 s75, s75, %[w]\n"
    "s_lshl_b32 s75, s75, 6\n"
    "s_add_u32 s75, s75, 32768\n"
    "s_lshl_b32 s52, %[kq], 3\n"
    "s_add_u32 s75, s75, s52\n"
    "s_waitcnt vmcnt(0)\n"
    "s_mov_b64 exec, 1\n"
    "buffer_store_dwordx2 v[20:21], off, s[80:83], s75 sc1\n"
    "s_mov_b64 exec, -1\n"
    "s_branch .Lt9_%=\n"
    ".Lt8_%=:\n"
    "ds_write_b128 v86, v[128:131] offset:0\n"
    "ds_write_b128 v86, v[132:135] offset:1024\n"
    "ds_write_b128 v86, v[136:139] offset:2048\n"
    "ds_write_b128 v86, v[140:143] offset:3072\n"
    "ds_write_b128 v86, v[144:147] offset:4096\n"
    "ds_write_b128 v86, v[148:151] offset:5120\n"
    "ds_write_b128 v86, v[152:155] offset:6144\n"
    "ds_write_b128 v86, v[156:159] offset:7168\n"
    "s_waitcnt vmcnt(0)\n"
    ".Lt9_%=:\n"
    "s_lshl_b32 s75, s78, 2\n"
    "s_add_u32 s75, s75, %[kq]\n"
    "s_lshl_b32 s75, s75, 6\n"
    "s_add_u32 s75, s75, 32768\n"
    "s_lshl_b32 s37, 1, %[kq]\n"
    "s_xor_b32 s37, s37, 15\n"
    "s_lshl_b32 s52, s78, 2\n"
    "s_add_u32 s52, s52, %[kq]\n"
    "s_lshl_b32 s52, s52, 15\n"
    "s_lshl_b32 s53, %[w], 11\n"
    "s_add_u32 s52, s52, s53\n"
    "s_add_u32 s52, s52, 50331648\n"
    "s_add_u32 s56, %[wslo], s52\n"
    "s_addc_u32 s57, %[wshi], 0\n"
    "s_and_b32 s57, s57, 0xffff\n"
    "s_mov_b32 s58, 32768\n"
    "s_mov_b32 s59, 0x20000\n"
    "v_add_u32 v87, s53, v86\n"
    "buffer_load_dwordx2 v[46:47], v22, s[80:83], s75 offen sc1\n"
    "s_waitcnt lgkmcnt(0)\n"
    "s_barrier\n"
    "ds_read_b128 v[208:211], v87 offset:0\n"
    "ds_read_b128 v[212:215], v87 offset:1024\n"
    "v_lshlrev_b32 v216, 16, v108\n"
    "v_and_b32 v217, 0xffff0000, v108\n"
    "v_lshlrev_b32 v218, 16, v109\n"
    "v_and_b32 v219, 0xffff0000, v109\n"
    "v_lshlrev_b32 v220, 16, v110\n"
    "v_and_b32 v221, 0xffff0000, v110\n"
    "v_lshlrev_b32 v222, 16, v111\n"
    "v_and_b32 v223, 0xffff0000, v111\n"
    "v_lshlrev_b32 v224, 16, v112\n"
    "v_and_b32 v225, 0xffff0000, v112\n"
    "v_lshlrev_b32 v226, 16, v113\n"
    "v_and_b32 v227, 0xffff0000, v113\n"
    "v_lshlrev_b32 v228, 16, v114\n"
    "v_and_b32 v229, 0xffff0000, v114\n"
    "v_lshlrev_b32 v230, 16, v115\n"
    "v_and_b32 v231, 0xffff0000, v115\n"
    "s_waitcnt vmcnt(0)\n"
    "v_cmp_eq_u64 s[52:53], v[46:47], v[20:21]\n"
    "s_orn2_b32 s52, s52, s37\n"
    "s_mov_b32 s53, -1\n"
    "s_cmp_eq_u64 s[52:53], -1\n"
    "s_cbranch_scc1 .Lt10_%=\n"
    "s_mov_b32 s38, 0\n"
    ".Lt11_%=:\n"
    "s_sleep 2\n"
    "buffer_load_dwordx2 v[46:47], v22, s[80:83], s75 offen sc1\n"
    "s_waitcnt vmcnt(0)\n"
    "v_cmp_eq_u64 s[52:53], v[46:47], v[20:21]\n"
    "s_orn2_b32 s52, s52, s37\n"
    "s_mov_b32 s53, -1\n"
    "s_cmp_eq_u64 s[52:53], -1\n"
    "s_cbranch_scc1 .Lt10_%=\n"
    "s_add_u32 s38, s38, 1\n"
    "s_cmp_lt_u32 s38, 0x200000\n"
    "s_cbranch_scc1 .Lt11_%=\n"
    ".Lt10_%=:\n"
    "s_add_u32 s52, %[kq], 1\n"
    "s_and_b32 s52, s52, 3\n"
    "s_lshl_b32 s52, s52, 13\n"
    "buffer_load_dwordx4 v[184:187], v33, s[56:59], s52 offen offset:0 sc1\n"
    "buffer_load_dwordx4 v[188:191], v33, s[56:59], s52 offen offset:1024 sc1\n"
    "s_add_u32 s52, %[kq], 2\n"
    "s_and_b32 s52, s52, 3\n"
    "s_lshl_b32 s52, s52, 13\n"
    "buffer_load_dwordx4 v[192:195], v33, s[56:59], s52 offen offset:0 sc1\n"
    "buffer_load_dwordx4 v[196:199], v33, s[56:59], s52 offen offset:1024 sc1\n"
    "s_add_u32 s52, %[kq], 3\n"
    "s_and_b32 s52, s52, 3\n"
    "s_lshl_b32 s52, s52, 13\n"
    "buffer_load_dwordx4 v[200:203], v33, s[56:59], s52 offen offset:0 sc1\n"
    "buffer_load_dwordx4 v[204:207], v33, s[56:59], s52 offen offset:1024 sc1\n"
    "s_waitcnt lgkmcnt(0)\n"
    "v_lshlrev_b32 v128, 16, v208\n"
    "v_and_b32 v129, 0xffff0000, v208\n"
    "v_lshlrev_b32 v130, 16, v209\n"
    "v_and_b32 v131, 0xffff0000, v209\n"
    "v_lshlrev_b32 v132, 16, v210\n"
    "v_and_b32 v133, 0xffff0000, v210\n"
    "v_lshlrev_b32 v134, 16, v211\n"
    "v_and_b32 v135, 0xffff0000, v211\n"
    "v_lshlrev_b32 v136, 16, v212\n"
    "v_and_b32 v137, 0xffff0000, v212\n"
    "v_lshlrev_b32 v138, 16, v213\n"
    "v_and_b32 v139, 0xffff0000, v213\n"
    "v_lshlrev_b32 v140, 16, v214\n"
    "v_and_b32 v141, 0xffff0000, v214\n"
    "v_lshlrev_b32 v142, 16, v215\n"
    "v_and_b32 v143, 0xffff0000, v215\n"
    "s_waitcnt vmcnt(4)\n"
    "v_lshlrev_b32 v144, 16, v184\n"
    "v_and_b32 v145, 0xffff0000, v184\n"
    "v_pk_add_f32 v[128:129], v[128:129], v[144:145]\n"
    "v_lshlrev_b32 v146, 16, v185\n"
    "v_and_b32 v147, 0xffff0000, v185\n"
    "v_pk_add_f32 v[130:131], v[130:131], v[146:147]\n"
    "v_lshlrev_b32 v148, 16, v186\n"
    "v_and_b32 v149, 0xffff0000, v186\n"
    "v_pk_add_f32 v[132:133], v[132:133], v[148:149]\n"
    "v_lshlrev_b32 v150, 16, v187\n"
    "v_and_b32 v151, 0xffff0000, v187\n"
    "v_pk_add_f32 v[134:135], v[134:135], v[150:151]\n"
    "v_lshlrev_b32 v152, 16, v188\n"
    "v_and_b32 v153, 0xffff0000, v188\n"
    "v_pk_add_f32 v[136:137], v[136:137], v[152:153]\n"
    "v_lshlrev_b32 v154, 16, v189\n"
    "v_and_b32 v155, 0xffff0000, v189\n"
    "v_pk_add_f32 v[138:139], v[138:139], v[154:155]\n"
    "v_lshlrev_b32 v156, 16, v190\n"
    "v_and_b32 v157, 0xffff0000, v190\n"
    "v_pk_add_f32 v[140:141], v[140:141], v[156:157]\n"
    "v_lshlrev_b32 v158, 16, v191\n"
    "v_and_b32 v159, 0xffff0000, v191\n"
    "v_pk_add_f32 v[142:143], v[142:143], v[158:159]\n"
    "s_waitcnt vmcnt(2)\n"
    "v_lshlrev_b32 v144, 16, v192\n"
    "v_and_b32 v145, 0xffff0000, v192\n"
    "v_pk_add_f32 v[128:129], v[128:129], v[144:145]\n"
    "v_lshlrev_b32 v146, 16, v193\n"
    "v_and_b32 v147, 0xffff0000, v193\n"
    "v_pk_add_f32 v[130:131], v[130:131], v[146:147]\n"
    "v_lshlrev_b32 v148, 16, v194\n"
    "v_and_b32 v149, 0xffff0000, v194\n"
    "v_pk_add_f32 v[132:133], v[132:133], v[148:149]\n"
    "v_lshlrev_b32 v150, 16, v195\n"
    "v_and_b32 v151, 0xffff0000, v195\n"
    "v_pk_add_f32 v[134:135], v[134:135], v[150:151]\n"
    "v_lshlrev_b32 v152, 16, v196\n"
    "v_and_b32 v153, 0xffff0000, v196\n"
    "v_pk_add_f32 v[136:137], v[136:137], v[152:153]\n"
    "v_lshlrev_b32 v154, 16, v197\n"
    "v_and_b32 v155, 0xffff0000, v197\n"
    "v_pk_add_f32 v[138:139], v[138:139], v[154:155]\n"
    "v_lshlrev_b32 v156, 16, v198\n"
    "v_and_b32 v157, 0xffff0000, v198\n"
    "v_pk_add_f32 v[140:141], v[140:141], v[156:157]\n"
    "v_lshlrev_b32 v158, 16, v199\n"
    "v_and_b32 v159, 0xffff0000, v199\n"
    "v_pk_add_f32 v[142:143], v[142:143], v[158:159]\n"
    "s_waitcnt vmcnt(0)\n"
    "v_lshlrev_b32 v144, 16, v200\n"
    "v_and_b32 v145, 0xffff0000, v200\n"
    "v_pk_add_f32 v[128:129], v[128:129], v[144:145]\n"
    "v_lshlrev_b32 v146, 16, v201\n"
    "v_and_b32 v147, 0xffff0000, v201\n"
    "v_pk_add_f32 v[130:131], v[130:131], v[146:147]\n"
    "v_lshlrev_b32 v148, 16, v202\n"
    "v_and_b32 v149, 0xffff0000, v202\n"
    "v_pk_add_f32 v[132:133], v[132:133], v[148:149]\n"
    "v_lshlrev_b32 v150, 16, v203\n"
    "v_and_b32 v151, 0xffff0000, v203\n"
    "v_pk_add_f32 v[134:135], v[134:135], v[150:151]\n"
    "v_lshlrev_b32 v152, 16, v204\n"
    "v_and_b32 v153, 0xffff0000, v204\n"
    "v_pk_add_f32 v[136:137], v[136:137], v[152:153]\n"
    "v_lshlrev_b32 v154, 16, v205\n"
    "v_and_b32 v155, 0xffff0000, v205\n"
    "v_pk_add_f32 v[138:139], v[138:139], v[154:155]\n"
    "v_lshlrev_b32 v156, 16, v206\n"
    "v_and_b32 v157, 0xffff0000, v206\n"
    "v_pk_add_f32 v[140:141], v[140:141], v[156:157]\n"
    "v_lshlrev_b32 v158, 16, v207\n"
    "v_and_b32 v159, 0xffff0000, v207\n"
    "v_pk_add_f32 v[142:143], v[142:143], v[158:159]\n"
    "v_fmac_f32 v216, v128, v160\n"
    "v_fmac_f32 v217, v129, v161\n"
    "v_fmac_f32 v218, v130, v162\n"
    "v_fmac_f32 v219, v131, v163\n"
    "v_cvt_pk_bf16_f32 v108, v216, v217\n"
    "v_cvt_pk_bf16_f32 v109, v218, v219\n"
    "buffer_store_dwordx2 v[108:109], v88, s[44:47], 0 offen offset:0\n"
    "v_fmac_f32 v220, v132, v164\n"
    "v_fmac_f32 v221, v133, v165\n"
    "v_fmac_f32 v222, v134, v166\n"
    "v_fmac_f32 v223, v135, v167\n"
    "v_cvt_pk_bf16_f32 v110, v220, v221\n"
    "v_cvt_pk_bf16_f32 v111, v222, v223\n"
    "buffer_store_dwordx2 v[110:111], v88, s[44:47], 0 offen offset:64\n"
    "v_fmac_f32 v224, v136, v168\n"
    "v_fmac_f32 v225, v137, v169\n"
    "v_fmac_f32 v226, v138, v170\n"
    "v_fmac_f32 v227, v139, v171\n"
    "v_cvt_pk_bf16_f32 v112, v224, v225\n"
    "v_cvt_pk_bf16_f32 v113, v226, v227\n"
    "buffer_store_dwordx2 v[112:113], v88, s[44:47], 0 offen offset:128\n"
    "v_fmac_f32 v228, v140, v172\n"
    "v_fmac_f32 v229, v141, v173\n"
    "v_fmac_f32 v230, v142, v174\n"
    "v_fmac_f32 v231, v143, v175\n"
    "v_cvt_pk_bf16_f32 v114, v228, v229\n"
    "v_cvt_pk_bf16_f32 v115, v230, v231\n"
    "s_cmp_ge_u32 %[w], 2\n"
    "s_cbranch_scc1 .Lt12_%=\n"
    "buffer_store_dwordx2 v[114:115], v88, s[44:47], 0 offen offset:192\n"
    ".Lt12_%=:\n"
    "s_branch .Lend_%=\n"
    ".Lrare0_%=:\n"
    "s_call_b64 s[54:55], .Lresc1_%=\n"
    "s_branch .Lret0_%=\n"
    ".Lrare1_%=:\n"
    "s_call_b64 s[54:55], .Lresc0_%=\n"
    "s_branch .Lret1_%=\n"
    ".Lrare2_%=:\n"
    "s_call_b64 s[54:55], .Lresc1_%=\n"
    "s_branch .Lret2_%=\n"
    ".Lrare3_%=:\n"
    "s_call_b64 s[54:55], .Lresc0_%=\n"
    "s_branch .Lret3_%=\n"
    ".LrareP0T24_%=:\n"
    "s_call_b64 s[54:55], .Lresc1_%=\n"
    "s_branch .LretP0T24_%=\n"
    ".LrareP0T25_%=:\n"
    "s_call_b64 s[54:55], .Lresc0_%=\n"
    "s_branch .LretP0T25_%=\n"
    ".LrareP0T26_%=:\n"
    "s_call_b64 s[54:55], .Lresc1_%=\n"
    "s_branch .LretP0T26_%=\n"
    ".LrareP0T27_%=:\n"
    "s_call_b64 s[54:55], .Lresc0_%=\n"
    "s_branch .LretP0T27_%=\n"
    ".LrareP0T28_%=:\n"
    "s_call_b64 s[54:55], .Lresc1_%=\n"
    "s_branch .LretP0T28_%=\n"
    ".LrareS_%=:\n"
    "s_call_b64 s[54:55], .Lresc0_%=\n"
    "s_branch .LretS_%=\n"
    ".LrareP1T28_%=:\n"
    "s_call_b64 s[54:55], .Lresc1_%=\n"
    "s_branch .LretP1T28_%=\n"
    ".LrareP1T29_%=:\n"
    "s_call_b64 s[54:55], .Lresc0_%=\n"
    "s_branch .LretP1T29_%=\n"
    ".LrareP1T30_%=:\n"
    "s_call_b64 s[54:55], .Lresc1_%=\n"
    "s_branch .LretP1T30_%=\n"
    ".LrareP1T31_%=:\n"
    "s_call_b64 s[54:55], .Lresc0_%=\n"
    "s_branch .LretP1T31_%=\n"
    ".LrareP1T32_%=:\n"
    "s_call_b64 s[54:55], .Lresc1_%=\n"
    "s_branch .LretP1T32_%=\n"
    ".Lresc0_%=:\n"
    "v_max3_f32 v122, v64, v65, v66\n"
    "v_max3_f32 v122, v122, v67, v72\n"
    "v_max3_f32 v122, v122, v73, v74\n"
    "v_max_f32 v122, v122, v75\n"
    "v_mov_b32 v120, v122\n"
    "s_nop 1\n"
    "v_permlane32_swap_b32 v122, v120\n"
    "s_nop 1\n"
    "v_max_f32 v122, v122, v120\n"
    "v_mov_b32 v120, v122\n"
    "s_nop 1\n"
    "v_permlane16_swap_b32 v122, v120\n"
    "s_nop 1\n"
    "v_max_f32 v122, v122, v120\n"
    "v_max_f32 v122, 0, v122\n"
    "v_add_f32 v24, v24, v122\n"
    "v_exp_f32 v124, -v122\n"
    "v_sub_f32 v64, v64, v122\n"
    "v_sub_f32 v72, v72, v122\n"
    "v_sub_f32 v65, v65, v122\n"
    "v_sub_f32 v73, v73, v122\n"
    "v_sub_f32 v66, v66, v122\n"
    "v_sub_f32 v74, v74, v122\n"
    "v_sub_f32 v67, v67, v122\n"
    "v_sub_f32 v75, v75, v122\n"
    "v_sub_f32 v96, 0, v24\n"
    "v_sub_f32 v97, 0, v24\n"
    "v_sub_f32 v98, 0, v24\n"
    "v_sub_f32 v99, 0, v24\n"
    "v_mul_f32 v26, v26, v124\n"
    "v_mul_f32 v27, v27, v124\n"
    "v_max3_f32 v123, v68, v69, v70\n"
    "v_max3_f32 v123, v123, v71, v76\n"
    "v_max3_f32 v123, v123, v77, v78\n"
    "v_max_f32 v123, v123, v79\n"
    "v_mov_b32 v120, v123\n"
    "s_nop 1\n"
    "v_permlane32_swap_b32 v123, v120\n"
    "s_nop 1\n"
    "v_max_f32 v123, v123, v120\n"
    "v_mov_b32 v120, v123\n"
    "s_nop 1\n"
    "v_permlane16_swap_b32 v123, v120\n"
    "s_nop 1\n"
    "v_max_f32 v123, v123, v120\n"
    "v_max_f32 v123, 0, v123\n"
    "v_add_f32 v25, v25, v123\n"
    "v_exp_f32 v125, -v123\n"
    "v_sub_f32 v68, v68, v123\n"
    "v_sub_f32 v76, v76, v123\n"
    "v_sub_f32 v69, v69, v123\n"
    "v_sub_f32 v77, v77, v123\n"
    "v_sub_f32 v70, v70, v123\n"
    "v_sub_f32 v78, v78, v123\n"
    "v_sub_f32 v71, v71, v123\n"
    "v_sub_f32 v79, v79, v123\n"
    "v_sub_f32 v100, 0, v25\n"
    "v_sub_f32 v101, 0, v25\n"
    "v_sub_f32 v102, 0, v25\n"
    "v_sub_f32 v103, 0, v25\n"
    "v_mul_f32 v28, v28, v125\n"
    "v_mul_f32 v29, v29, v125\n"
    "s_branch .LrescO_%=\n"
    ".Lresc1_%=:\n"
    "v_max3_f32 v122, v80, v81, v82\n"
    "v_max3_f32 v122, v122, v83, v88\n"
    "v_max3_f32 v122, v122, v89, v90\n"
    "v_max_f32 v122, v122, v91\n"
    "v_mov_b32 v120, v122\n"
    "s_nop 1\n"
    "v_permlane32_swap_b32 v122, v120\n"
    "s_nop 1\n"
    "v_max_f32 v122, v122, v120\n"
    "v_mov_b32 v120, v122\n"
    "s_nop 1\n"
    "v_permlane16_swap_b32 v122, v120\n"
    "s_nop 1\n"
    "v_max_f32 v122, v122, v120\n"
    "v_max_f32 v122, 0, v122\n"
    "v_add_f32 v24, v24, v122\n"
    "v_exp_f32 v124, -v122\n"
    "v_sub_f32 v80, v80, v122\n"
    "v_sub_f32 v88, v88, v122\n"
    "v_sub_f32 v81, v81, v122\n"
    "v_sub_f32 v89, v89, v122\n"
    "v_sub_f32 v82, v82, v122\n"
    "v_sub_f32 v90, v90, v122\n"
    "v_sub_f32 v83, v83, v122\n"
    "v_sub_f32 v91, v91, v122\n"
    "v_sub_f32 v96, 0, v24\n"
    "v_sub_f32 v97, 0, v24\n"
    "v_sub_f32 v98, 0, v24\n"
    "v_sub_f32 v99, 0, v24\n"
    "v_mul_f32 v26, v26, v124\n"
    "v_mul_f32 v27, v27, v124\n"
    "v_max3_f32 v123, v84, v85, v86\n"
    "v_max3_f32 v123, v123, v87, v92\n"
    "v_max3_f32 v123, v123, v93, v94\n"
    "v_max_f32 v123, v123, v95\n"
    "v_mov_b32 v120, v123\n"
    "s_nop 1\n"
    "v_permlane32_swap_b32 v123, v120\n"
    "s_nop 1\n"
    "v_max_f32 v123, v123, v120\n"
    "v_mov_b32 v120, v123\n"
    "s_nop 1\n"
    "v_permlane16_swap_b32 v123, v120\n"
    "s_nop 1\n"
    "v_max_f32 v123, v123, v120\n"
    "v_max_f32 v123, 0, v123\n"
    "v_add_f32 v25, v25, v123\n"
    "v_exp_f32 v125, -v123\n"
    "v_sub_f32 v84, v84, v123\n"
    "v_sub_f32 v92, v92, v123\n"
    "v_sub_f32 v85, v85, v123\n"
    "v_sub_f32 v93, v93, v123\n"
    "v_sub_f32 v86, v86, v123\n"
    "v_sub_f32 v94, v94, v123\n"
    "v_sub_f32 v87, v87, v123\n"
    "v_sub_f32 v95, v95, v123\n"
    "v_sub_f32 v100, 0, v25\n"
    "v_sub_f32 v101, 0, v25\n"
    "v_sub_f32 v102, 0, v25\n"
    "v_sub_f32 v103, 0, v25\n"
    "v_mul_f32 v28, v28, v125\n"
    "v_mul_f32 v29, v29, v125\n"
    "s_branch .LrescO_%=\n"
    ".LrescO_%=:\n"
    "v_mul_f32 v128, v128, v124\n"
    "v_mul_f32 v129, v129, v124\n"
    "v_mul_f32 v130, v130, v124\n"
    "v_mul_f32 v131, v131, v124\n"
    "v_mul_f32 v132, v132, v125\n"
    "v_mul_f32 v133, v133, v125\n"
    "v_mul_f32 v134, v134, v125\n"
    "v_mul_f32 v135, v135, v125\n"
    "v_mul_f32 v136, v136, v124\n"
    "v_mul_f32 v137, v137, v124\n"
    "v_mul_f32 v138, v138, v124\n"
    "v_mul_f32 v139, v139, v124\n"
    "v_mul_f32 v140, v140, v125\n"
    "v_mul_f32 v141, v141, v125\n"
    "v_mul_f32 v142, v142, v125\n"
    "v_mul_f32 v143, v143, v125\n"
    "v_mul_f32 v144, v144, v124\n"
    "v_mul_f32 v145, v145, v124\n"
    "v_mul_f32 v146, v146, v124\n"
    "v_mul_f32 v147, v147, v124\n"
    "v_mul_f32 v148, v148, v125\n"
    "v_mul_f32 v149, v149, v125\n"
    "v_mul_f32 v150, v150, v125\n"
    "v_mul_f32 v151, v151, v125\n"
    "v_mul_f32 v152, v152, v124\n"
    "v_mul_f32 v153, v153, v124\n"
    "v_mul_f32 v154, v154, v124\n"
    "v_mul_f32 v155, v155, v124\n"
    "v_mul_f32 v156, v156, v125\n"
    "v_mul_f32 v157, v157, v125\n"
    "v_mul_f32 v158, v158, v125\n"
    "v_mul_f32 v159, v159, v125\n"
    "v_mul_f32 v160, v160, v124\n"
    "v_mul_f32 v161, v161, v124\n"
    "v_mul_f32 v162, v162, v124\n"
    "v_mul_f32 v163, v163, v124\n"
    "v_mul_f32 v164, v164, v125\n"
    "v_mul_f32 v165, v165, v125\n"
    "v_mul_f32 v166, v166, v125\n"
    "v_mul_f32 v167, v167, v125\n"
    "v_mul_f32 v168, v168, v124\n"
    "v_mul_f32 v169, v169, v124\n"
    "v_mul_f32 v170, v170, v124\n"
    "v_mul_f32 v171, v171, v124\n"
    "v_mul_f32 v172, v172, v125\n"
    "v_mul_f32 v173, v173, v125\n"
    "v_mul_f32 v174, v174, v125\n"
    "v_mul_f32 v175, v175, v125\n"
    "v_mul_f32 v176, v176, v124\n"
    "v_mul_f32 v177, v177, v124\n"
    "v_mul_f32 v178, v178, v124\n"
    "v_mul_f32 v179, v179, v124\n"
    "v_mul_f32 v180, v180, v125\n"
    "v_mul_f32 v181, v181, v125\n"
    "v_mul_f32 v182, v182, v125\n"
    "v_mul_f32 v183, v183, v125\n"
    "v_mul_f32 v184, v184, v124\n"
    "v_mul_f32 v185, v185, v124\n"
    "v_mul_f32 v186, v186, v124\n"
    "v_mul_f32 v187, v187, v124\n"
    "v_mul_f32 v188, v188, v125\n"
    "v_mul_f32 v189, v189, v125\n"
    "v_mul_f32 v190, v190, v125\n"
    "v_mul_f32 v191, v191, v125\n"
    "v_mul_f32 v192, v192, v124\n"
    "v_mul_f32 v193, v193, v124\n"
    "v_mul_f32 v194, v194, v124\n"
    "v_mul_f32 v195, v195, v124\n"
    "v_mul_f32 v196, v196, v125\n"
    "v_mul_f32 v197, v197, v125\n"
    "v_mul_f32 v198, v198, v125\n"
    "v_mul_f32 v199, v199, v125\n"
    "v_mul_f32 v200, v200, v124\n"
    "v_mul_f32 v201, v201, v124\n"
    "v_mul_f32 v202, v202, v124\n"
    "v_mul_f32 v203, v203, v124\n"
    "v_mul_f32 v204, v204, v125\n"
    "v_mul_f32 v205, v205, v125\n"
    "v_mul_f32 v206, v206, v125\n"
    "v_mul_f32 v207, v207, v125\n"
    "v_mul_f32 v208, v208, v124\n"
    "v_mul_f32 v209, v209, v124\n"
    "v_mul_f32 v210, v210, v124\n"
    "v_mul_f32 v211, v211, v124\n"
    "v_mul_f32 v212, v212, v125\n"
    "v_mul_f32 v213, v213, v125\n"
    "v_mul_f32 v214, v214, v125\n"
    "v_mul_f32 v215, v215, v125\n"
    "v_mul_f32 v216, v216, v124\n"
    "v_mul_f32 v217, v217, v124\n"
    "v_mul_f32 v218, v218, v124\n"
    "v_mul_f32 v219, v219, v124\n"
    "v_mul_f32 v220, v220, v125\n"
    "v_mul_f32 v221, v221, v125\n"
    "v_mul_f32 v222, v222, v125\n"
    "v_mul_f32 v223, v223, v125\n"
    "v_mul_f32 v224, v224, v124\n"
    "v_mul_f32 v225, v225, v124\n"
    "v_mul_f32 v226, v226, v124\n"
    "v_mul_f32 v227, v227, v124\n"
    "v_mul_f32 v228, v228, v125\n"
    "v_mul_f32 v229, v229, v125\n"
    "v_mul_f32 v230, v230, v125\n"
    "v_mul_f32 v231, v231, v125\n"
    "v_mul_f32 v232, v232, v124\n"
    "v_mul_f32 v233, v233, v124\n"
    "v_mul_f32 v234, v234, v124\n"
    "v_mul_f32 v235, v235, v124\n"
    "v_mul_f32 v236, v236, v125\n"
    "v_mul_f32 v237, v237, v125\n"
    "v_mul_f32 v238, v238, v125\n"
    "v_mul_f32 v239, v239, v125\n"
    "v_mul_f32 v240, v240, v124\n"
    "v_mul_f32 v241, v241, v124\n"
    "v_mul_f32 v242, v242, v124\n"
    "v_mul_f32 v243, v243, v124\n"
    "v_mul_f32 v244, v244, v125\n"
    "v_mul_f32 v245, v245, v125\n"
    "v_mul_f32 v246, v246, v125\n"
    "v_mul_f32 v247, v247, v125\n"
    "v_mul_f32 v248, v248, v124\n"
    "v_mul_f32 v249, v249, v124\n"
    "v_mul_f32 v250, v250, v124\n"
    "v_mul_f32 v251, v251, v124\n"
    "v_mul_f32 v252, v252, v125\n"
    "v_mul_f32 v253, v253, v125\n"
    "v_mul_f32 v254, v254, v125\n"
    "v_mul_f32 v255, v255, v125\n"
    "s_nop 1\n"
    "s_setpc_b64 s[54:55]\n"
    ".Ljobs_%=:\n"
    "s_lshl_b32 s75, %[tp], 4\n"
    "s_lshl_b32 s52, %[fs], 1\n"
    "s_add_u32 s75, s75, s52\n"
    "s_add_u32 s75, s75, %[kh]\n"
    "s_lshl_b32 s75, s75, 3\n"
    "s_mul_i32 s52, s74, 4096\n"
    "s_add_u32 s75, s75, s52\n"
    "s_add_u32 s75, s75, 4096\n"
    "s_cmp_lg_u32 %[w], 0\n"
    "s_cbranch_scc1 .Lt13_%=\n"
    "s_mov_b64 exec, 1\n"
    "buffer_store_dwordx2 v[20:21], off, s[80:83], s75 sc1\n"
    "s_mov_b64 exec, -1\n"
    ".Lt13_%=:\n"
    "s_branch .Ljobret_%=\n"
    ".Lend_%=:\n"
    ".Lk1_code_end:\n"
    : [q0]"+a"(qf[0]), [q1]"+a"(qf[1]), [q2]"+a"(qf[2]), [q3]"+a"(qf[3]), [q4]"+a"(qf[4]), [q5]"+a"(qf[5]), [q6]"+a"(qf[6]), [q7]"+a"(qf[7]), [q8]"+a"(qf[8]), [q9]"+a"(qf[9]), [q10]"+a"(qf[10]), [q11]"+a"(qf[11]), [q12]"+a"(qf[12]), [q13]"+a"(qf[13]), [q14]"+a"(qf[14]), [q15]"+a"(qf[15])
    : [vk0]"v"(vk0), [vv0]"v"(vv0),
      [vok0]"v"(vok[0]), [vok1]"v"(vok[1]), [vok2]"v"(vok[2]), [vok3]"v"(vok[3]), [vov0]"v"(vov[0]), [vov1]"v"(vov[1]), [vov2]"v"(vov[2]), [vov3]"v"(vov[3]),
      [m0b]"s"(m0b), [ldsb]"s"(ldsb), [w]"s"(w), [qb]"s"(qb), [head]"s"(head), [kh]"s"(kh), [tp]"s"(tp), [fs]"s"(fs), [kq]"s"(kq), [tp2]"s"(tp2), [nq]"s"(nq), [rot]"s"(rot2), [rotk]"s"(rotk), [n0]"s"(n0), [tlo]"s"(tlo), [thi]"s"(thi),
      [klo]"s"(PLO(kp)), [khi]"s"(PHI(kp)), [vlo]"s"(PLO(vp)), [vhi]"s"(PHI(vp)), [wslo]"s"(PLO(ws)), [wshi]"s"(PHI(ws)), [splo]"s"(PLO(w_sp)), [sphi]"s"(PHI(w_sp)),
      [xlo]"s"(PLO(x)), [xhi]"s"(PHI(x)), [outlo]"s"(PLO(out)), [outhi]"s"(PHI(out)),
      [bslo]"s"(PLO(b_sp)), [bshi]"s"(PHI(b_sp))
    : "v20","v21","v22","v23","v24","v25","v26","v27","v28","v29","v30","v31","v32","v33","v34","v35","v36","v37","v38","v39","v40","v41","v42","v43","v44","v45","v46","v47","v48","v49","v50","v51","v52","v53","v54","v55","v56","v57","v58","v59","v60","v61","v62","v63","v64","v65","v66","v67","v68","v69","v70","v71","v72","v73","v74","v75","v76","v77","v78","v79","v80","v81","v82","v83","v84","v85","v86","v87","v88","v89","v90","v91","v92","v93","v94","v95","v96","v97","v98","v99","v100","v101","v102","v103","v104","v105","v106","v107","v108","v109","v110","v111","v112","v113","v114","v115","v116","v117","v118","v119","v120","v121","v122","v123","v124","v125","v126","v127","v128","v129","v130","v131","v132","v133","v134","v135","v136","v137","v138","v139","v140","v141","v142","v143","v144","v145","v146","v147","v148","v149","v150","v151","v152","v153","v154","v155","v156","v157","v158","v159","v160","v161","v162","v163","v164","v165","v166","v167","v168","v169","v170","v171","v172","v173","v174","v175","v176","v177","v178","v179","v180","v181","v182","v183","v184","v185","v186","v187","v188","v189","v190","v191","v192","v193","v194","v195","v196","v197","v198","v199","v200","v201","v202","v203","v204","v205","v206","v207","v208","v209","v210","v211","v212","v213","v214","v215","v216","v217","v218","v219","v220","v221","v222","v223","v224","v225","v226","v227","v228","v229","v230","v231","v232","v233","v234","v235","v236","v237","v238","v239","v240","v241","v242","v243","v244","v245","v246","v247","v248","v249","v250","v251","v252","v253","v254","v255","a0","a1","a2","a3","a4","a5","a6","a7","a8","a9","a10","a11","a12","a13","a14","a15","a16","a17","a18","a19","a20","a21","a22","a23","a24","a25","a26","a27","a28","a29","a30","a31","a32","a33","a34","a35","a36","a37","a38","a39","a40","a41","a42","a43","a44","a45","a46","a47","a48","a49","a50","a51","a52","a53","a54","a55","a56","a57","a58","a59","a60","a61","a62","a63","a64","a65","a66","a67","a68","a69","a70","a71","a72","a73","a74","a75","a76","a77","a78","a79","a80","a81","a82","a83","a84","a85","a86","a87","a88","a89","a90","a91","a92","a93","a94","a95","a96","a97","a98","a99","a100","a101","a102","a103","a104","a105","a106","a107","a108","a109","a110","a111","a112","a113","a114","a115","a116","a117","a118","a119","a120","a121","a122","a123","a124","a125","a126","a127","a192","a193","a194","a195","a196","a197","a198","a199","a200","a201","a202","a203","a204","a205","a206","a207","a208","a209","a210","a211","a212","a213","a214","a215","a216","a217","a218","a219","a220","a221","a222","a223","a224","a225","a226","a227","a228","a229","a230","a231","a232","a233","a234","a235","a236","a237","a238","a239","a240","a241","a242","a243","a244","a245","a246","a247","a248","a249","a250","a251","a252","a253","a254","a255","s37","s38","s39","s40","s41","s42","s43","s44","s45","s46","s47","s48","s49","s50","s51","s52","s53","s54","s55","s56","s57","s58","s59","s60","s61","s62","s63","s64","s65","s66","s67","s68","s69","s70","s71","s72","s73","s74","s75","s76","s77","s78","s79","s80","s81","s82","s83","vcc","scc","memory");
}

extern "C" void kernel_launch(void* const* d_in, const int* in_sizes, int n_in, void* d_out, int out_size, void* d_ws, size_t ws_size, hipStream_t stream) {
  const u16* x = (const u16*)d_in[0]; const u16* kc = (const u16*)d_in[1]; const u16* vc = (const u16*)d_in[2];
  const u16* wq = (const u16*)d_in[3]; const u16* wo = (const u16*)d_in[4];
  const u16* w_in = (const u16*)d_in[6]; const u16* w_sp = (const u16*)d_in[7]; const u16* b_sp = (const u16*)d_in[8]; const u16* w_out = (const u16*)d_in[9];
  char* ws = (char*)d_ws;
  (void)hipFuncSetAttribute((const void*)k1, hipFuncAttributeMaxDynamicSharedMemorySize, 163840);
  k1<<<256, 256, 163840, stream>>>(kc, vc, wq, w_in, ws, w_sp, b_sp, wo, w_out, x, (u16*)d_out);
}
```
